```python
import torch

def _softmax(x, axis=-1):
    return torch.softmax(x, dim=axis)
def _gelu(x):
    return torch.nn.functional.gelu(x, approximate="tanh")
def _silu(x):
    return torch.nn.functional.silu(x)
def _sigmoid(x):
    return torch.sigmoid(x)
def _layernorm(x, axis=-1, eps=1e-5):
    m = x.mean(dim=axis, keepdim=True)
    v = x.var(dim=axis, keepdim=True, unbiased=False)
    return (x - m) / torch.sqrt(v + eps)
def _scatter_add(tgt, idx, src, axis=0):
    return tgt.clone().index_add_(axis, idx, src)
def _segment_sum(src, seg, n):
    out = torch.zeros((n,) + src.shape[1:], dtype=src.dtype)
    return out.index_add_(0, seg, src)
def _pad(x, pw):
    flat = tuple(v for pair in reversed(pw) for v in pair)
    return torch.nn.functional.pad(x, flat)

kernel_name = 'recurrent_scan__ssm_conv_s4__moe_arch_mod__prompt'

def setup_inputs():

    _w = (lambda a: (lambda t: t.to(torch.bfloat16) if t.is_floating_point() else t)(torch.as_tensor(a)))
    def _A_setup():
        B, L, D, N = 1, 2048, 576, 128
        A_re = -(lambda _x: torch.exp(torch.as_tensor(_x)))((lambda *s: torch.randn(*s, dtype=torch.bfloat16) * 6.4)(D, N))
        A_im = torch.pi * (lambda *s: torch.randn(*s, dtype=torch.bfloat16) * 0.05)(D, N)
        B_re = (lambda *s: torch.randn(*s, dtype=torch.bfloat16) * 0.1)(D, N)
        B_im = (lambda *s: torch.randn(*s, dtype=torch.bfloat16) * 0.1)(D, N)
        C_re = (lambda *s: torch.randn(*s, dtype=torch.bfloat16) * 0.1)(D, N)
        C_im = (lambda *s: torch.randn(*s, dtype=torch.bfloat16) * 0.1)(D, N)
        log_dt = (lambda *s: torch.randn(*s, dtype=torch.bfloat16) * 0.05)(D) * 0.5 - 4.0
        Dskip = (lambda *s: torch.randn(*s, dtype=torch.bfloat16) * 0.05)(D)
        return {
            "u": _w((lambda *s: torch.randn(*s, dtype=torch.bfloat16) * 0.05)(B, L, D)),
            "A_re": _w(A_re),
            "A_im": _w(A_im),
            "B_re": _w(B_re),
            "B_im": _w(B_im),
            "C_re": _w(C_re),
            "C_im": _w(C_im),
            "log_dt": _w(log_dt),
            "Dskip": _w(Dskip),
        }
    _A_in = _A_setup()

    def _B_setup():
        B, S, D, F, C = 1, 2048, 576, 4608, 8
        return {
            "x": _w((lambda *s: torch.randn(*s, dtype=torch.bfloat16) * 0.05)(B, S, D)),
            "w_r": _w((lambda *s: torch.randn(*s, dtype=torch.bfloat16) * 0.05)(D)),
            "w_gate": _w((lambda *s: torch.randn(*s, dtype=torch.bfloat16) * 0.05)(D, F)),
            "w_up": _w((lambda *s: torch.randn(*s, dtype=torch.bfloat16) * 0.05)(D, F)),
            "w_down": _w((lambda *s: torch.randn(*s, dtype=torch.bfloat16) * 0.05)(F, D)),
        }
    _B_in = _B_setup()

    return {
        "A_u": _A_in["u"],
        "A_A_re": _A_in["A_re"],
        "A_A_im": _A_in["A_im"],
        "A_B_re": _A_in["B_re"],
        "A_B_im": _A_in["B_im"],
        "A_C_re": _A_in["C_re"],
        "A_C_im": _A_in["C_im"],
        "A_log_dt": _A_in["log_dt"],
        "A_Dskip": _A_in["Dskip"],
        "B_x": _B_in["x"],
        "B_w_r": _B_in["w_r"],
        "B_w_gate": _B_in["w_gate"],
        "B_w_up": _B_in["w_up"],
        "B_w_down": _B_in["w_down"],
    }

def reference(A_u, A_A_re, A_A_im, A_B_re, A_B_im, A_C_re, A_C_im, A_log_dt, A_Dskip, B_x, B_w_r, B_w_gate, B_w_up, B_w_down):
    def _A_ref(u, A_re, A_im, B_re, B_im, C_re, C_im, log_dt, Dskip):
        Bsz, L, D = u.shape
        dt = torch.exp(log_dt)[:, None]
        A = A_re + 1j * A_im
        Bc = B_re + 1j * B_im
        Cc = C_re + 1j * C_im
        logdA = dt * A
        dB = dt * Bc
        l = (lambda x, dt: x.to(dt))(torch.arange(L), A_re.dtype)
        P = torch.exp(logdA[:, :, None] * l[None, None, :])
        K = torch.einsum("dn,dnl,dn->dl", Cc, P, dB).real
        L2 = 2 * L
        Kp = _pad(K, ((0, 0), (0, L)))
        up = (lambda x, *d: x.permute(*d))(u, 0, 2, 1)
        up = _pad(up, ((0, 0), (0, 0), (0, L)))
        Kf = (lambda x, axes=(-2,-1): torch.fft.rfft2(x.float(), dim=axes))(Kp[:, None, :], axes=(-2, -1))
        Uf = (lambda x, axes=(-2,-1): torch.fft.rfft2(x.float(), dim=axes))(up[:, :, None, :], axes=(-2, -1))
        Yf = Uf * Kf[None, :, :, :]
        y = (lambda x, s, axes=(-2,-1): torch.fft.irfft2(x, s=s, dim=axes).to(torch.bfloat16))(Yf, (1, L2), axes=(-2, -1))[:, :, 0, :L]
        y = (lambda x, *d: x.permute(*d))(y, 0, 2, 1) + u * Dskip[None, None, :]
        return y

    def _B_ref(x, w_r, w_gate, w_up, w_down):
        B, S, D = x.shape
        C = 8
        r = x @ w_r
        idx = torch.argsort(-r, axis=-1)[:, :C]
        idx_e = (lambda x, n, axis: torch.repeat_interleave(x, n, dim=axis))(idx.reshape(B, C, 1), D, axis=2)
        x_sel = (lambda x, i, axis: torch.take_along_dim(x, i, dim=axis))(x, idx_e, axis=1)
        g = _silu(x_sel @ w_gate)
        u = x_sel @ w_up
        y = (g * u) @ w_down
        r_sel = _sigmoid((lambda x, i, axis: torch.take_along_dim(x, i, dim=axis))(r, idx, axis=1))
        y = y * r_sel.reshape(B, C, 1)
        b_off = (torch.arange(B) * S).reshape(B, 1)
        flat_idx = (idx + b_off).reshape(B * C)
        zero = (x * 0.0).reshape(B * S, D)
        upd = _scatter_add(zero, flat_idx, y.reshape(B * C, D), axis=0)
        return x + upd.reshape(B, S, D)

    x = A_u
    xn = _layernorm(x, axis=-1)
    s = _A_ref(xn, A_A_re, A_A_im, A_B_re, A_B_im, A_C_re, A_C_im, A_log_dt, A_Dskip)
    g = _silu(_B_ref(xn, B_w_r, B_w_gate, B_w_up, B_w_down))
    return x + s * g
```

```cpp
#include <hip/hip_runtime.h>
#include <hip/hip_bf16.h>
#include <stdint.h>
#if defined(__HIP_DEVICE_COMPILE__)
#define NOPK __attribute__((target("no-packed-fp32-ops")))
#else
#define NOPK
#endif
#define DEV __device__ NOPK __forceinline__
typedef unsigned short u16;
typedef unsigned int u32;
typedef unsigned long long u64;
typedef short bf16x8 __attribute__((ext_vector_type(8)));
typedef _Float16 h8 __attribute__((ext_vector_type(8)));
typedef _Float16 h2 __attribute__((ext_vector_type(2)));
typedef float f32x4 __attribute__((ext_vector_type(4)));
typedef float f32x2 __attribute__((ext_vector_type(2)));
typedef u32 u32x4 __attribute__((ext_vector_type(4)));
typedef u32 u32x2 __attribute__((ext_vector_type(2)));
typedef __bf16 bf2 __attribute__((ext_vector_type(2)));
typedef short v4s __attribute__((ext_vector_type(4)));
typedef __amdgpu_buffer_rsrc_t rsrc_t;
#define GAS __attribute__((address_space(1)))
#define LAS __attribute__((address_space(3)))
typedef LAS char lch;
#define L_ 2048
#define D_ 576
#define F_ 4608
#define BC(T,v) __builtin_bit_cast(T,v)
#define TID ((int)__builtin_amdgcn_workitem_id_x())
#define BID ((int)__builtin_amdgcn_workgroup_id_x())
#define RFL(x) __builtin_amdgcn_readfirstlane(x)
#define RLX_AGENT __ATOMIC_RELAXED, __HIP_MEMORY_SCOPE_AGENT
#define LD(T,p) (*(const LAS T*)(p))
#define ST(T,p) (*(LAS T*)(p))
#define SPINCAP (1<<17)
#define DRAIN() asm volatile("s_waitcnt vmcnt(0)" ::: "memory")
#define GLD(p) __hip_atomic_load((const GAS u64*)(p),RLX_AGENT)
#define GST(p,v) __hip_atomic_store((GAS u64*)(p),(u64)(v),RLX_AGENT)
#define BALLOT(c) __builtin_amdgcn_ballot_w64(c)
#define SC1 16
#define LANE(l) int l; { int t2_=TID; asm volatile("" : "+v"(t2_)); l=t2_&63; }
#define SCHED() __builtin_amdgcn_sched_barrier(0)
#define RSRC(p,n) __builtin_amdgcn_make_buffer_rsrc((void*)(p),0,(n),0x00020000)
#define BLD128 __builtin_amdgcn_raw_buffer_load_b128
#define BLD32 __builtin_amdgcn_raw_buffer_load_b32
#define BST128 __builtin_amdgcn_raw_buffer_store_b128
#define PRIO(n) __builtin_amdgcn_s_setprio(n)

DEV float b2f(u32 v){ return BC(float,v<<16); }
DEV float blo(u32 v){ return BC(float,v<<16); }
DEV float bhi(u32 v){ return BC(float,v&0xffff0000u); }
DEV u32 pk(float a,float b){ f32x2 v={a,b}; bf2 r=__builtin_convertvector(v,bf2); return BC(u32,r); }
DEV u32 pkh(float a,float b){ f32x2 v={a,b}; h2 r=__builtin_convertvector(v,h2); return BC(u32,r); }
DEV float rb(float f){ return BC(float,pk(f,f)<<16); }
DEV float rcpf_(float x){ return __builtin_amdgcn_rcpf(x); }
DEV float expf_(float x){
  const float L2E=1.4426950216293335f, L2El=1.9259629911266175e-8f; float h=x*L2E; float l=__builtin_fmaf(x,L2E,-h)+x*L2El;
  float e=__builtin_amdgcn_exp2f(h); return __builtin_fmaf(e*l,0.6931471805599453f,e); }
DEV float sigm_f(float x){ return rcpf_(1.f+__builtin_amdgcn_exp2f(-x*1.4426950408889634f)); }
DEV float silu_f(float x){ return x*sigm_f(x); }
DEV u32 umax_(u32 a,u32 b){ return a>b?a:b; }
DEV u32 umin_(u32 a,u32 b){ return a<b?a:b; }

#define YSH 3
#define PRE1 6
#define PRE3 4
#define PRE2 5
#define W1 5
#define W3 3
#define W2_ 4
#define W1B 20
#define W2B 12
#define LAST1 4
#define RGATE 4
#define UGATE 7
#define R2D 12
#define PFN 32
#define CPF 8
#define NRG 8
#define W_EPOCH 0
#define W_RG 16384
#define W_ST (W_RG+32*8192)
#define W_U (W_ST+32768)
#define UST (D_*16+128)
#define YST (L_*2+128)
#define TL 1
#define GST_ ((D_*16<<TL)+128)
#define W_Y (W_U+256*UST)
#define W_PART (W_Y+D_*YST)
#define W_GU (W_PART+8*9*16*256)
#define W_FL 9216
#define W_IDX 1024
#define CTLB 0
struct KP { const u16 *Are,*Aim,*Bre,*Bim,*Cre,*Cim,*ldt,*pad; };

template<int CTRL> DEV float dppf(float v){ return BC(float,__builtin_amdgcn_update_dpp(0u,BC(u32,v),CTRL,0xf,0xf,false)); }
template<int CTRL> DEV u32 dppu(u32 v){ return (u32)__builtin_amdgcn_update_dpp(0u,v,CTRL,0xf,0xf,false); }
DEV float sum16(float v){ v+=dppf<0xB1>(v); v+=dppf<0x4E>(v); v+=dppf<0x141>(v); v+=dppf<0x140>(v); return v; }
DEV float add16x(float v){ auto r=__builtin_amdgcn_permlane16_swap(BC(u32,v),BC(u32,v),false,false); u32 a=r[0], b=r[1]; return BC(float,a)+BC(float,b); }
DEV u32 wmax(u32 v){
  v=umax_(v,(u32)__builtin_amdgcn_update_dpp(0u,v,0x111,0xf,0xf,false));
  v=umax_(v,(u32)__builtin_amdgcn_update_dpp(0u,v,0x112,0xf,0xf,false));
  v=umax_(v,(u32)__builtin_amdgcn_update_dpp(0u,v,0x114,0xf,0xf,false));
  v=umax_(v,(u32)__builtin_amdgcn_update_dpp(0u,v,0x118,0xf,0xf,false));
  v=umax_(v,(u32)__builtin_amdgcn_update_dpp(0u,v,0x142,0xa,0xf,false));
  v=umax_(v,(u32)__builtin_amdgcn_update_dpp(0u,v,0x143,0xc,0xf,false));
  return (u32)__builtin_amdgcn_readlane((int)v,63); }

DEV void garrive(lch* sm,int c){ u32 ad=(u32)(size_t)sm+c*4, one=1;
  asm volatile("s_mov_b64 exec, 1\n\tds_add_u32 %0, %1\n\ts_mov_b64 exec, -1" :: "v"(ad),"v"(one) : "memory"); }
template<int SLP> DEV void gwait(lch* sm,int c,u32 target){ u32 ad=(u32)(size_t)sm+c*4, tv, cnt, sv; target=RFL(target);
  asm volatile("s_mov_b32 %1, 0x20000\n"
    "1:\n\tds_read_b32 %0, %3\n\ts_waitcnt lgkmcnt(0)\n\tv_readfirstlane_b32 %2, %0\n\ts_cmp_ge_u32 %2, %4\n\ts_cbranch_scc1 2f\n\ts_sleep %5\n\ts_sub_u32 %1, %1, 1\n\ts_cbranch_scc0 1b\n"
    "2:" : "=&v"(tv),"=&s"(cnt),"=&s"(sv) : "v"(ad),"s"(target),"i"(SLP) : "memory","scc"); }
template<int ST_,class T> DEV void gwait4(int c,u32 target,const lch* p,T& a,T& b,T& d,T& e){ u32 tv,cnt,sv;
  asm volatile("s_mov_b32 %1, 0x20000\n1:\n\tds_read_b32 %0, %7\n\tds_read_b128 %3, %8\n\tds_read_b128 %4, %8 offset:%10\n\tds_read_b128 %5, %8 offset:%11\n\tds_read_b128 %6, %8 offset:%12\n\ts_waitcnt lgkmcnt(4)\n\t"
    "v_readfirstlane_b32 %2, %0\n\ts_cmp_ge_u32 %2, %9\n\ts_cbranch_scc1 2f\n\ts_sleep 1\n\ts_sub_u32 %1, %1, 1\n\ts_cbranch_scc0 1b\n2:\n\ts_waitcnt lgkmcnt(0)"
    : "=&v"(tv),"=&s"(cnt),"=&s"(sv),"=&v"(a),"=&v"(b),"=&v"(d),"=&v"(e) : "v"(c*4),"v"(p),"i"(target),"i"(ST_),"i"(2*ST_),"i"(3*ST_) : "memory","scc"); }
DEV void gsync(lch* sm,int c,u32 target){ garrive(sm,c); gwait<0>(sm,c,target); }
template<int SH> DEV u64 poll1(const char* p,u64 want){ u64 f=GLD(p);
  _Pragma("unroll 1") for(int sp=0;sp<SPINCAP;sp++){ bool bad=((f^want)>>SH)!=0ull; if(BALLOT(bad)==0ull) break; __builtin_amdgcn_s_sleep(1); if(bad) f=GLD(p); }
  __builtin_amdgcn_fence(__ATOMIC_ACQUIRE,"wavefront"); return f; }
#define STR_(x) #x
#define STR(x) STR_(x)
DEV u32x4 mkrs(const void* p,u32 n){ u64 a=(u64)p; return u32x4{(u32)RFL((int)(u32)a),(u32)RFL((int)((u32)(a>>32)&0xffffu)),n,0x00020000u}; }
#define VMW(n) asm volatile("s_waitcnt vmcnt(%0)" :: "i"(n) : "memory")
#define YSLP 1
#define GSLP 1
#define QSLP 1
#define PSLP 3
#define POLLASM(LOADS,CHECK,K) "s_mov_b32 %[cnt], 0x20000\n1:\n\ts_waitcnt vmcnt(0)\n\t" CHECK "s_cbranch_vccz 9f\n\ts_sleep " STR(K) "\n\ts_mov_b64 exec, vcc\n\t" LOADS \
  "s_mov_b64 exec, -1\n\ts_sub_u32 %[cnt], %[cnt], 1\n\ts_cbranch_scc0 1b\n9:\n\t"
#define LD_DUP2 "buffer_load_dwordx4 v[112:115], %[of], %[rs], 0 offen sc1\n\tbuffer_load_dwordx4 v[116:119], %[of], %[rs], %[so] offen sc1\n\t"
#define MX2(a,b) "v_max3_u32 %[t0], %[t0], v" #a ", v" #b "\n\t"
#define CHK_I0 "v_max_u32 %[t0], v112, v113\n\t" MX2(114,115)
#define CHK_IE "v_cmp_eq_u32 vcc, -1, %[t0]\n\t"
#define PCLOB "memory","vcc","scc"
#define UMASK 0x40074007u
DEV u32 upat(u32 tag,int k){ return ((((tag*0xea959c21u)>>(8*k))&255u)*0x04000801u)&UMASK; }
DEV u32 pkz(float a,float b){ return BC(u32,__builtin_amdgcn_cvt_pkrtz(a,b)); }
#define INV 0xffffffffu
DEV u32 nv(u32 v){ return umin_(v,0xfffeffffu); }
DEV u32x4 nv4(u32x4 v){ return u32x4{nv(v[0]),nv(v[1]),nv(v[2]),nv(v[3])}; }
#define OOR 0x40000000

#define CHSZ 36864
#define O_CH 512
#define O_CND 256
#define SLSZ 38080
#define O_R2 (O_CH+2*SLSZ)
#define O_X2 (O_R2+CHSZ)
#define O_TILE (O_X2+CHSZ)
#define XSTR 1168
#define TSTR2 1216
#define O_ONE (O_TILE+9728+16)
#define O_CAND (O_ONE+1168+48)
#define LDS_TOTAL (O_CAND+128)
#define C_G 0
#define C_U 8
#define C_L 4
#define C_D 5
#define C_X 12
#define C_P 13
#define C_H 14
#define C_I 17
#define C_E 18
#define C_IX 19
#define C_PR 21
#define O_IDX 96
#define O_EXP 144
#define O_RMX 192
#define O_RV 128
#define O_MD 400
#define O_RC (O_CAND+64)

struct C2 { float r,i; };
DEV C2 cmul(C2 a,C2 b){ C2 c; c.r=__builtin_fmaf(a.r,b.r,-(a.i*b.i)); c.i=__builtin_fmaf(a.r,b.i,a.i*b.r); return c; }
DEV h8 ldA(const lch* p){ u32x2 a=*(const volatile LAS u32x2*)p, b=*(const volatile LAS u32x2*)(p+8); return BC(h8,(u32x4{a[0],a[1],b[0],b[1]})); }
DEV bf16x8 trfrag(const lch* p0,const lch* p1){
  u32x2 b0=BC(u32x2,__builtin_amdgcn_ds_read_tr16_b64_v4i16((LAS v4s*)p0)), b1=BC(u32x2,__builtin_amdgcn_ds_read_tr16_b64_v4i16((LAS v4s*)p1));
  return BC(bf16x8,(u32x4{b0[0],b0[1],b1[0],b1[1]})); }

struct FP { float gg[24], bs[24]; };
DEV void final_pre(FP& f,int w4,const u32x4* xq,const u32x4* dq,f32x2 rc){
  float ea=rc[0]*-1.4426950408889634f, eb=rc[1]*-1.4426950408889634f;
  #pragma unroll
  for(int z=0;z<3;z++) if(z<2||w4==0){
    #pragma unroll
    for(int e=0;e<4;e++){ u32 xw=xq[z][e], dw=dq[z][e]; int i=8*z+2*e; float a=blo(xw), b=bhi(xw);
      float pa=__builtin_fmaf(a,rc[0],rc[1]), pb=__builtin_fmaf(b,rc[0],rc[1]);
      float ga=pa*rcpf_(1.f+__builtin_amdgcn_exp2f(__builtin_fmaf(a,ea,eb))), gb=pb*rcpf_(1.f+__builtin_amdgcn_exp2f(__builtin_fmaf(b,ea,eb)));
      f.gg[i]=ga; f.gg[i+1]=gb; f.bs[i]=__builtin_fmaf(pa*blo(dw),ga,a); f.bs[i+1]=__builtin_fmaf(pb*bhi(dw),gb,b);
      asm volatile("" : "+v"(f.gg[i]),"+v"(f.gg[i+1]),"+v"(f.bs[i]),"+v"(f.bs[i+1])); } }
}
DEV void final_post(lch* sm,const FP& f,int w4,int nch,u16* __restrict__ out,char* ws){
  LANE(ln); int bid=BID, xg=bid&7, lb=bid>>3, g=ln>>4, li=ln&15, qq=li>>2, pq=li&3;
  int cg=lb&3, T=8*xg+(lb>>2), trow=32*T+8*g+(li&7);
  int ob=(trow*D_+144*cg+16*w4+(li>>3)*8)*2, yo=(144*cg+16*w4+(ln>>2))*YST+(32*T+8*(ln&3))*2;
  #define ZCH 64
  { gwait<2>(sm,C_IX,1); u32x4 iq=LD(u32x4,sm+O_IDX); u32 tt=(u32)trow*0x10001u; bool routed=false;
    #pragma unroll
    for(int q=0;q<4;q++){ u32 z=iq[q]^tt; routed|=((z&0xffffu)==0u)|((z>>16)==0u); }
    ob=routed? OOR:ob; }
  lch* fb=sm+O_CH+w4*3072; lch* wp=fb+((ln&3)*16+(ln>>2))*16; const lch* tp=fb+(16*g+8*(pq>>1)+qq)*16+(pq&1)*8;
  u32x4 rs=mkrs(ws+W_Y,D_*YST); rsrc_t ro=RSRC(out,L_*D_*2);
  gwait<4>(sm,C_E,4*nch);
  if(lb>=8&&lb<14) gwait<4>(sm,C_H,4);
  PRIO(3);
  { u32 t0,cnt;
    #define LD_Y "buffer_load_dwordx4 v[112:115], %[of], %[rs], 0 offen sc1\n\tbuffer_load_dwordx4 v[116:119], %[of], %[rs], %[s1] offen sc1\n\tbuffer_load_dwordx4 v[120:123], %[o3], %[rs], 0 offen sc1\n\t"
    asm volatile(LD_Y POLLASM(LD_Y,CHK_I0 MX2(116,117) MX2(118,119) MX2(120,121) MX2(122,123) CHK_IE,YSLP)
      "ds_write_b128 %[wp], v[112:115]\n\tds_write_b128 %[wp], v[116:119] offset:1024\n\tds_write_b128 %[wp], v[120:123] offset:2048"
      : [t0]"=&v"(t0),[cnt]"=&s"(cnt) : [of]"v"(yo),[rs]"s"(rs),[s1]"s"(ZCH*YST),[o3]"v"(w4==0? yo+2*ZCH*YST:OOR),[wp]"v"(wp) : PCLOB); }
  if(lb>=14) garrive(sm,C_PR);
  u32x4 r[3];
  #pragma unroll
  for(int z=0;z<3;z++) r[z]=BC(u32x4,trfrag(tp+z*1024,tp+z*1024+64));
  #pragma unroll
  for(int z=0;z<3;z++) if(z<2||w4==0){ u32x4 ov;
    #pragma unroll
    for(int e=0;e<4;e++){ u32 rw=r[z][e]; int i=8*z+2*e; ov[e]=pk(__builtin_fmaf(blo(rw),f.gg[i],f.bs[i]),__builtin_fmaf(bhi(rw),f.gg[i+1],f.bs[i+1])); }
    BST128(ov,ro,ob+z*(2*ZCH),0,0); SCHED(); }
}

#define TROW 528
#define O_TB 4640
#define O_DE 34752
#define O_DO 35968
#define O_R2K 37120
#define O_R3K 37632
#define O_DUM 37952
#define O_RED O_TB
#define MH(a,b,c) __builtin_amdgcn_mfma_f32_16x16x32_f16(a,b,c,0,0,0)
#define PIN(x) asm volatile("" : "+v"(x))
DEV h8 ld4w(const lch* p){ return BC(h8,(u32x4{LD(u32,p),LD(u32,p+4),LD(u32,p+8),LD(u32,p+12)})); }
DEV u32 __umul24_(u32 a,u32 b){ u32 r; asm("v_mul_u32_u24 %0, %1, %2" : "=v"(r) : "s"(b),"v"(a)); return r; }
DEV u32x4 band(u32 mask,int g,u32 val){ u32 by=__builtin_amdgcn_ubfe(mask,8*g,8), X=by|(by<<15), k=0x10001u; u32x4 r; asm volatile("" : "+s"(k),"+s"(val));
  #pragma unroll
  for(int d=0;d<4;d++) r[d]=__umul24_((X>>(2*d))&k,val);
  return r; }
DEV h8 zpack(f32x4 e,f32x4 o){ return BC(h8,(u32x4{pkh(e[0],o[0]),pkh(e[1],o[1]),pkh(e[2],o[2]),pkh(e[3],o[3])})); }
struct C3 { float r,i,n; };
DEV C3 c3(C2 a){ C3 c; c.r=a.r; c.i=a.i; PIN(c.i); c.n=-c.i; PIN(c.n); return c; }
DEV C2 cm(C2 a,C3 b){ C2 c; c.r=__builtin_fmaf(a.r,b.r,a.i*b.n); c.i=__builtin_fmaf(a.r,b.i,a.i*b.r); return c; }
DEV u32 ph(C2 a){ return pkh(a.r,a.i); }
#define W2(p,a,b) asm volatile("ds_write2_b32 %0, %1, %2 offset1:132" :: "v"(p),"v"(a),"v"(b) : "memory")
struct CP { const lch *x0,*x1,*bd; lch *p0,*p1,*p2,*wp; u32x4 Be,Bo,AE; int yo; };
template<int ROLE> DEV void lanes(CP& c,lch* cb,int ln){ int m=ln&15, g=ln>>4;
  const lch* ad=cb+((m&1)? O_DO+2*527 : O_DE+2*536)+2*(8*g-m);
  const lch* bd=cb+2*(256+16*m+8*g); lch* dum=cb+O_DUM;
  if(ROLE==0){ int l0=128*(g>>1)+8*m+4*(g&1); c.p0=cb+O_DE+(549-l0)*2; PIN(c.p0); c.p1=c.p0+6; PIN(c.p1); c.p2=c.p0+(O_DO-O_DE-18); c.x0=ad-640; c.bd=bd-64; c.x1=cb+O_R3K+2*(126-2*m+8*g)-192; PIN(c.x1);
    c.Be=band(0x1ffu<<(m+4),g,0x3c00u); PIN(c.Be); }
  if(ROLE==1){ lch* r=cb+O_R2K+(192-64*(g-2)-2*m)*2; c.p0= g<2? dum : r-66; c.p1= ln==0? cb+O_DE+(552-256)*2 : (ln<=32? dum : r); c.p2=cb+O_DE+(544-256-8*ln)*2; c.x0=ad-128-640; c.bd=bd-128-128; PIN(c.p0); PIN(c.p1); PIN(c.p2); }
  if(ROLE==2){ int i0=16*m+4*g; c.p0= m<8? cb+O_R3K+(125-i0)*2 : dum+2; c.p1= ln==0? cb+O_R2K+128 : (ln==8? cb+O_R3K : c.p0+6); c.x0=ad-320-640; c.bd=bd-320-128; PIN(c.p0); PIN(c.p1); }
  if(ROLE==3){ c.x0=cb+O_R2K+2*(188-4*m+8*g)-256; c.bd=cb+2*(248+8*m+8*g);
    c.Be=band(0x1fu<<(m+6),g,0x3c00u); c.Bo=band(0x7u<<(m+11),g,0x3c00u); PIN(c.Be); PIN(c.Bo); }
  PIN(c.x0); PIN(c.bd); }
template<int ROLE> DEV void lanes2(CP& c,lch* cb,int ln){ int m=ln&15, g=ln>>4;
  if(ROLE==0){ c.Bo=band(0x7fu<<(m+5),g,0x3c00u); c.AE=band(3u<<(m+12),g,0xffffu); PIN(c.Bo); PIN(c.AE); }
  c.wp=cb+O_RED+ln*16; c.yo=(g&1)*512+m*32+(g>>1)*16; PIN(c.wp); PIN(c.yo); }
template<int ROLE> DEV void chan2(lch* sm,lch* cb,const lch*& fa,const lch*& fb,int grp,int d,int one_,int emax,u64 tag,char* ws){
  CP c;
  const f32x4 Z4={0,0,0,0}; h8 A[6]; f32x4 acc[8];
  #define LD_U "buffer_load_dwordx4 v[112:115], %[of], %[rs], 0 offen sc1\n\tbuffer_load_dwordx4 v[116:119], %[of], %[rs], %[s1] offen sc1\n\tbuffer_load_dwordx4 v[120:123], %[of], %[rs], %[s2] offen sc1\n\tbuffer_load_dwordx4 v[124:127], %[of], %[rs], %[s3] offen sc1\n\t"
  #define UC(t,a,b,c) "v_bitop3_b32 " t ", v" #a ", v" #b ", %[" #c "] bitop3:0x7e\n\t"
  #define UD(t,a,c) "v_bitop3_b32 " t ", " t ", v" #a ", %[" #c "] bitop3:0xf6\n\t"
  #define CHK_U UC("%[t0]",112,116,c0) UC("%[t1]",113,117,c1) UD("%[t0]",120,c0) UD("%[t1]",121,c1) UD("%[t0]",124,c0) UD("%[t1]",125,c1) UD("%[t0]",114,c2) UD("%[t1]",115,c3) UD("%[t0]",118,c2) UD("%[t1]",119,c3) \
    UD("%[t0]",122,c2) UD("%[t1]",123,c3) UD("%[t0]",126,c2) UD("%[t1]",127,c3) "v_bitop3_b32 %[t0], %[t0], %[t1], %[mk] bitop3:0xa8\n\tv_cmp_ne_u32 vcc, 0, %[t0]\n\t"
  #define UX(a,b,c,d,o) "v_xor_b32 v" #a ", %[e0], v" #a "\n\tv_xor_b32 v" #b ", %[e1], v" #b "\n\tv_xor_b32 v" #c ", %[e2], v" #c "\n\tv_xor_b32 v" #d ", %[e3], v" #d "\n\tds_write_b128 %[up], v[" #a ":" #d "] offset:" #o "\n\t"
  #define UPOLL { u32 t0,t1,cnt; asm volatile(POLLASM(LD_U,CHK_U,PSLP) UX(112,113,114,115,544) UX(116,117,118,119,1056) UX(120,121,122,123,1568) UX(124,125,126,127,2080) \
      : [t0]"=&v"(t0),[t1]"=&v"(t1),[cnt]"=&s"(cnt) : [of]"v"(c.p0),[rs]"s"(rsU),[s1]"s"((32>>TL)*GST_),[s2]"s"((64>>TL)*GST_),[s3]"s"((96>>TL)*GST_),[c0]"s"(c0),[c1]"s"(c1),[c2]"s"(c2),[c3]"s"(c3_), \
        [e0]"s"(c0^0x40004u),[e1]"s"(c1^0x40004u),[e2]"s"(c2^0x40004u),[e3]"s"(c3_^0x40004u),[mk]"s"(UMASK),[up]"v"(c.p1) : PCLOB); garrive(sm,C_U+one_); }
  if(ROLE<3){
    h8 a[8], b[8];
    #pragma unroll
    for(int ks=0;ks<8;ks++){ a[ks]=LD(h8,fa+ks*64); b[ks]=LD(h8,fb+ks*64); }
    SCHED();
    LANE(ln); lanes<ROLE>(c,cb,ln); float fz= ln==32? 0.f : 4.76837158203125e-07f; if(ROLE==1) PIN(fz);
    SCHED();
    f32x4 k=Z4;
    #pragma unroll
    for(int ks=0;ks<8;ks++) k=MH(a[ks],b[ks],k);
    const float f=4.76837158203125e-07f;
    float k0=k[0]*f, k1=k[1]*f, k2=k[2]*f, k3=k[3]*f;
    u32 h32=pkh(k3,k2), h10=pkh(k1,k0);
    if(ROLE!=1){
      ST(u16,c.p0)=(u16)h32; ST(u32,c.p0+2)=pkh(k2,k1); ST(u16,c.p1)=(u16)(h10>>16);
      if(ROLE==0) ST(u32x2,c.p2)=u32x2{h32,h10};
    } else {
      float n0=dppf<0x130>(k[0])*fz, z1= fz==0.f? k0:k1, z2=k[2]*fz, z3=k[3]*fz; u32 h22=pkh(z2,z2);
      ST(u16,c.p0+64)=(u16)h10; ST(u16,c.p0+2)=(u16)(h32>>16); ST(u16,c.p0)=(u16)h32; ST(u16,c.p1)=(u16)(h10>>16);
      u32x4 vo={pkh(n0,z3),h22,pkh(z2,z1),pkh(k0,k0)}, ve={pkh(n0,n0),pkh(z3,z2),h22,pkh(z1,k0)};
      asm volatile("s_mov_b32 exec_hi, 1\n\tds_write_b128 %0, %1\n\tds_write_b128 %0, %2 offset:1200\n\ts_mov_b32 exec_hi, -1" :: "v"(c.p2),"v"(ve),"v"(vo) : "memory"); }
    gsync(sm,C_G+grp,7);
    u32 kv; A[0]=ld4w(c.x0+640); A[1]=ld4w(c.x0+128);
    if(ROLE==0){ A[2]=ld4w(c.x1+192); A[3]=ld4w(c.x1+128); A[4]=ld4w(c.x1+64); A[5]=ld4w(c.x1); kv=LD(u16,cb+O_R2K+128); }
    else { A[2]=ld4w(c.x0+576); A[3]=ld4w(c.x0+64); A[4]=ld4w(c.x0+512); A[5]=ld4w(c.x0); }
    SCHED(); lanes2<ROLE>(c,cb,ln); SCHED();
    if(ROLE==0){ kv|=kv<<16; c.AE=u32x4{c.AE[0]&kv,c.AE[1]&kv,c.AE[2]&kv,c.AE[3]&kv}; PIN(c.AE); }
    PRIO(2);
    gwait<1>(sm,C_U+one_,2-one_);
  } else {
    u32 tg=(u32)tag, c0=upat(tg,0), c1=upat(tg,1), c2=upat(tg,2), c3_=upat(tg,3); u32x4 rsU=mkrs(ws+W_U,256*UST);
    if(!one_) PRIO(2);
    LANE(ln); c.p0=(lch*)fa; c.p1=(lch*)fb;
    bool late= grp==0||BID>=112;
    if(!late) asm volatile(LD_U :: [of]"v"(c.p0),[rs]"s"(rsU),[s1]"s"((32>>TL)*GST_),[s2]"s"((64>>TL)*GST_),[s3]"s"((96>>TL)*GST_) : "memory");
    asm volatile("s_mov_b64 exec, 1\n\tglobal_store_dwordx2 %0, %1, %2 sc1\n\ts_mov_b64 exec, -1" :: "v"(W_FL+d*8),"v"(tag),"s"(ws) : "memory");
    lanes<3>(c,cb,ln); lanes2<3>(c,cb,ln);
    #pragma unroll
    for(int j=2;j<8;j++){ acc[j]=Z4; PIN(acc[j]); }
    if(late){ gwait<2>(sm,C_G,UGATE); if(BID>=112) gwait<1>(sm,C_D,4); asm volatile(LD_U :: [of]"v"(c.p0),[rs]"s"(rsU),[s1]"s"((32>>TL)*GST_),[s2]"s"((64>>TL)*GST_),[s3]"s"((96>>TL)*GST_) : "memory"); }
    if(!late) UPOLL
    gwait<1>(sm,C_G+grp,7);
    #pragma unroll
    for(int i=0;i<6;i++) A[i]=ldA(c.x0+320-64*i);
    if(late) UPOLL
    PRIO(2);
    gwait<1>(sm,C_U+one_,2-one_);
  }
  const lch* bd=c.bd; h8 Be=BC(h8,c.Be), Bo=BC(h8,c.Bo);
  #pragma unroll
  for(int j=0;j<(ROLE==3? 2:8);j++) acc[j]=Z4;
  #define RW(j) if((j)>=0&&((j)>>1)!=ROLE&&!(ROLE==3&&(j)<2)){ ST(f32x4,c.wp+((j)>>1)*6144+((ROLE-((j)>>1)-1)&3)*2048+((j)&1)*1024)=acc[(j)<0? 0:(j)]; SCHED(); }
  if(ROLE==1||ROLE==2){
    h8 B[8];
    #pragma unroll
    for(int q=0;q<8;q++) B[q]=LD(h8,bd+128+512*q);
    #pragma unroll
    for(int r=0;r<3;r++){
      #pragma unroll
      for(int dl=0;dl<8;dl++){ if(dl<7) acc[dl+1]=MH(A[2*r+1],B[dl],acc[dl+1]); SCHED(); acc[dl]=MH(A[2*r],B[dl],acc[dl]); SCHED();
        if(r<2){ B[dl]=LD(h8,bd+64-64*r+512*dl); SCHED(); } else RW(dl-2) }
      } }
  if(ROLE==0){
    h8 B[8], ue[4];
    #pragma unroll
    for(int kt=0;kt<4;kt++){ ue[kt]=LD(h8,bd+48+512*kt); B[kt]=LD(h8,bd+64+512*kt); }
    f32x4 ce[2],co[2]; h8 Z[2];
    ce[0]=MH(ue[0],Be,Z4); co[0]=MH(B[0],Bo,Z4); ce[1]=MH(ue[1],Be,Z4); co[1]=MH(B[1],Bo,Z4); Z[0]=zpack(ce[0],co[0]);
    #pragma unroll
    for(int kt=0;kt<4;kt++){
      SCHED();
      if(kt+2<4){ ce[kt&1]=MH(ue[kt+2],Be,Z4); co[kt&1]=MH(B[kt+2],Bo,Z4); }
      if(kt+1<4) Z[(kt+1)&1]=zpack(ce[(kt+1)&1],co[(kt+1)&1]);
      #pragma unroll
      for(int rho=0;rho+kt<4;rho++) acc[4+kt+rho]=MH(Z[kt&1],A[2+rho],acc[4+kt+rho]);
      SCHED(); B[4+kt]=LD(h8,bd+64+512*(4+kt)); if(kt==0) A[5]=BC(h8,c.AE); if(kt==1) A[4]=ld4w(c.x0-384); if(kt==2) A[3]=ld4w(c.x0+64); if(kt==3) A[2]=ld4w(c.x0+576); SCHED(); }
    #pragma unroll
    for(int dl=0;dl<8;dl++){ if(dl<4) acc[dl+4]=MH(A[5],B[dl],acc[dl+4]); SCHED(); if(dl<6) acc[dl+2]=MH(A[4],B[dl],acc[dl+2]); SCHED(); if(dl<7) acc[dl+1]=MH(A[1],B[dl],acc[dl+1]); SCHED(); acc[dl]=MH(A[0],B[dl],acc[dl]);
      SCHED(); B[dl]=LD(h8,bd+512*dl); SCHED(); }
    #pragma unroll
    for(int dl=0;dl<8;dl++){ if(dl<7) acc[dl+1]=MH(A[3],B[dl],acc[dl+1]); SCHED(); acc[dl]=MH(A[2],B[dl],acc[dl]); SCHED(); RW(dl-2) } }
  if(ROLE==3){
    h8 U[12];
    #pragma unroll
    for(int kt=0;kt<6;kt++) U[kt]=LD(h8,bd+256*kt);
    f32x4 ce[2],co[2]; h8 Z[2];
    ce[0]=MH(U[0],Be,Z4); co[0]=MH(U[0],Bo,Z4); ce[1]=MH(U[1],Be,Z4); co[1]=MH(U[1],Bo,Z4); Z[0]=zpack(ce[0],co[0]);
    #pragma unroll
    for(int kt=0;kt<12;kt++){
      SCHED();
      if(kt+2<12){ ce[kt&1]=MH(U[kt+2],Be,Z4); co[kt&1]=MH(U[kt+2],Bo,Z4); }
      if(kt+1<12) Z[(kt+1)&1]=zpack(ce[(kt+1)&1],co[(kt+1)&1]);
      #pragma unroll
      for(int rho=-1;rho<5;rho++){ int j=(kt+4+rho)>>1; if(((kt+rho)&1)==0&&j>=2&&j<8) acc[j]=MH(Z[kt&1],A[rho+1],acc[j]); }
      if(kt<6){ SCHED(); U[kt+6]=LD(h8,bd+256*(kt+6)); }
      if(kt>=3&&kt<=9&&(kt&1)){ SCHED(); RW((kt+1)/2) } }
  }
  PRIO(3);
  RW(6) RW(7)
  gsync(sm,C_G+grp,11);
  { const lch* rp=c.wp+ROLE*6144; f32x4 a[3], b[3], o0=acc[2*ROLE], o1=acc[2*ROLE+1];
    #pragma unroll
    for(int q=0;q<(ROLE==0? 2:3);q++){ a[q]=LD(f32x4,rp+q*2048); b[q]=LD(f32x4,rp+q*2048+1024); }
    SCHED();
    float sc=__builtin_amdgcn_ldexpf(1.f,emax-YSH), s0[4], s1[4];
    #pragma unroll
    for(int e=0;e<4;e++){ s0[e]=((o0[e]+a[0][e])+(ROLE==0? a[1][e] : a[1][e]+a[2][e]))*sc; s1[e]=((o1[e]+b[0][e])+(ROLE==0? b[1][e] : b[1][e]+b[2][e]))*sc; }
    auto r0=__builtin_amdgcn_permlane16_swap(nv(pk(s0[0],s0[1])),nv(pk(s1[0],s1[1])),false,false); auto r1=__builtin_amdgcn_permlane16_swap(nv(pk(s0[2],s0[3])),nv(pk(s1[2],s1[3])),false,false);
    BST128(u32x4{r0[0],r1[0],r0[1],r1[1]},RSRC(ws+W_Y+d*YST,4096),c.yo+ROLE*1024,0,SC1); }
  garrive(sm,C_E);

  asm volatile("s_endpgm" : "=v"(fa),"=v"(fb) :: "memory");
}
#define GETEP asm volatile("s_waitcnt lgkmcnt(0)\n\ts_mov_b64 %0, s[96:97]\n\ts_mov_b32 %1, s98" : "=s"(ep),"=s"(ldw));
DEV void chan(lch* sm,int grp,int w4,int d,int one_,char* ws,u32x4 q0,u32x4 q1,u32x4 q2,u32x4 q3,u32x4 q4){
  lch* cb=sm+O_CH+grp*SLSZ; int role=w4, emax;
  #define LP(v) ((lch*)(size_t)(u32)(v))
  const lch* fa=LP(q4[0]); const lch* fb=LP(q4[1]);
  u64 tag;
  { float a2,bp,br,bi,cr,ci;
    u32 fq=q3[2], fr=q3[3], one=q4[2]; float f64=BC(float,fq), f128=BC(float,fr);
    lch *t8[4]={LP(q0[0]),LP(q0[1]),LP(q0[2]),LP(q0[3])}, *tB[4]={LP(q1[0]),LP(q1[1]),LP(q1[2]),LP(q1[3])}, *t4[2]={LP(q2[0]),LP(q2[1])}, *t2[2]={LP(q2[2]),LP(q2[3])}, *t1=LP(q3[0]), *te=LP(q3[1]);
    if(grp!=0) PRIO(1);
    u64 ep; u32 ldw; GETEP tag=ep+1ull;
    float dt=rb(expf_((d&1)? bhi(ldw):blo(ldw))), ca=dt*1.4426950408889634f, cb_=dt*0.15915494309189535f;
    asm volatile("s_waitcnt vmcnt(1)\n\tv_mul_f32 %2, %8, v114\n\tv_mul_f32 %3, %8, v115\n\tv_mul_f32 %0, %6, v112\n\tv_mul_f32 %1, %7, v113\n\tv_mov_b32 %4, v116\n\tv_mov_b32 %5, v117"
      : "=&v"(a2),"=&v"(bp),"=&v"(br),"=&v"(bi),"=&v"(cr),"=&v"(ci) : "v"(ca),"v"(cb_),"v"(dt) : "memory");
    C2 W; W.r=cr*br-ci*bi; W.i=cr*bi+ci*br;
    u32 mx=BC(u32,__builtin_fmaxf(__builtin_fabsf(W.r),__builtin_fabsf(W.i)));
    #define MXS(C) mx=umax_(mx,dppu<C>(mx));
    float e8=a2*8.f, eB=__builtin_fmaf(a2,f64,14.f), p8=bp*8.f, pB=bp*f64; SCHED();
    MXS(0xB1) p8=__builtin_amdgcn_fractf(p8); pB=__builtin_amdgcn_fractf(pB); e8=__builtin_amdgcn_exp2f(e8); SCHED();
    MXS(0x4E) eB=__builtin_amdgcn_exp2f(eB); float c8=__builtin_amdgcn_cosf(p8); SCHED();
    MXS(0x141) float s8=__builtin_amdgcn_sinf(p8), cB=__builtin_amdgcn_cosf(pB); SCHED();
    float sB=__builtin_amdgcn_sinf(pB); PIN(sB); MXS(0x140) float e1=__builtin_amdgcn_exp2f(a2); PIN(e1); SCHED();
    asm volatile("s_mov_b64 exec, 0x10001\n\tds_max_u32 %0, %1\n\tds_add_u32 %0, %2 offset:4\n\ts_mov_b64 exec, -1" :: "v"(te),"v"(mx),"v"(one) : "memory");
    C3 z8; z8.r=e8*c8; z8.n=e8*s8; z8.i=-z8.n; PIN(z8.i); C2 B; B.r=eB*cB; B.i=-(eB*sB);
    float p1=__builtin_amdgcn_fractf(bp), pE=__builtin_amdgcn_fractf(bp*256.f), pV=__builtin_amdgcn_fractf(bp*f128), eE=a2*256.f, eV=a2*f128, tr[8];
    #pragma unroll
    for(int k=0;k<4;k++){ SCHED(); u32 h0=ph(B); B=cm(B,z8);
      if(k==0){ tr[0]=__builtin_amdgcn_cosf(pV); tr[1]=__builtin_amdgcn_sinf(pV); } if(k==1){ tr[2]=__builtin_amdgcn_exp2f(eV); tr[3]=__builtin_amdgcn_cosf(p1); }
      if(k==2){ tr[4]=__builtin_amdgcn_sinf(p1); tr[5]=__builtin_amdgcn_exp2f(eE); } if(k==3){ tr[6]=__builtin_amdgcn_cosf(pE); tr[7]=__builtin_amdgcn_sinf(pE); }
      PIN(tr[2*k]); PIN(tr[2*k+1]);
      u32 h1=ph(B); if(k<3) B=cm(B,z8); W2(tB[k],h0,h1); }
    asm volatile("ds_read_b64 v[118:119], %0" :: "v"(te) : "memory");
    SCHED();
    C3 z1=c3(C2{e1*tr[3],e1*tr[4]}), E=c3(C2{tr[5]*tr[6],tr[5]*tr[7]}); C2 V[8]; V[0]=cmul(W,C2{tr[2]*tr[0],tr[2]*tr[1]});
    { u32 cnt,n; PIN(V[0].r); PIN(V[0].i); PIN(z1.r); PIN(E.r);
      asm volatile("s_mov_b32 %1, 0x20000\n1:\n\ts_waitcnt lgkmcnt(0)\n\tv_readfirstlane_b32 %2, v119\n\tv_readfirstlane_b32 %0, v118\n\ts_cmp_eq_u32 %2, 8\n\ts_cbranch_scc1 2f\n\tds_read_b64 v[118:119], %3\n\ts_sub_u32 %1, %1, 1\n\ts_cbranch_scc0 1b\n2:"
        : "=&s"(emax),"=&s"(cnt),"=&s"(n) : "v"(te) : "memory","scc");
      emax=RFL(emax); emax=(emax>>23)-126; }
    V[0].r=__builtin_amdgcn_ldexpf(V[0].r,14-emax); V[0].i=__builtin_amdgcn_ldexpf(V[0].i,14-emax);
    C2 Y=cm(V[0],E), Y4; u32 y0=ph(Y);
    V[1]=cm(V[0],z1); W2(t8[0],ph(V[0]),ph(V[1])); Y=cm(Y,E); u32 y1=ph(Y);
    V[2]=cm(V[1],z1); Y=cm(Y,E); u32 y2=ph(Y);
    V[3]=cm(V[2],z1); W2(t8[1],ph(V[2]),ph(V[3])); W2(t4[0],y0,ph(cm(V[2],E)));
    V[4]=cm(V[3],z1); Y=cm(Y,E); ST(u32,t1)=ph(Y);
    V[5]=cm(V[4],z1); W2(t8[2],ph(V[4]),ph(V[5])); Y4=cm(V[4],E); u32 y4=ph(Y4);
    V[6]=cm(V[5],z1); Y=cm(Y,E); ST(u32,t1+2*TROW)=ph(Y);
    V[7]=cm(V[6],z1); W2(t8[3],ph(V[6]),ph(V[7])); Y4=cm(Y4,E); W2(t2[0],y1,ph(Y4));
    W2(t4[1],y4,ph(cm(V[6],E))); Y=cm(Y,E); ST(u32,t1+4*TROW)=ph(Y);
    Y4=cm(Y4,E); W2(t2[1],y2,ph(Y4)); Y=cm(Y,E); ST(u32,t1+6*TROW)=ph(Y);
    Y=cm(Y,E); asm volatile("s_mov_b32 exec_hi, 0\n\tds_write_b32 %0, %1 offset:4224\n\ts_mov_b32 exec_hi, -1" :: "v"(t1),"v"(ph(Y)) : "memory");
  }
  if(one_) PRIO(3);
  DRAIN();
  gsync(sm,C_G+grp,4);
  if(role==0) chan2<0>(sm,cb,fa,fb,grp,d,one_,emax,tag,ws);
  if(role==1) chan2<1>(sm,cb,fa,fb,grp,d,one_,emax,tag,ws);
  if(role==2) chan2<2>(sm,cb,fa,fb,grp,d,one_,emax,tag,ws);
  chan2<3>(sm,cb,fa,fb,grp,d,one_,emax,tag,ws);
}

DEV u32 med3u(u32 a,u32 b,u32 c){ u32 r; asm("v_med3_u32 %0, %1, %2, %3" : "=v"(r) : "v"(a),"v"(b),"v"(c)); return r; }
DEV u32 max3u(u32 a,u32 b,u32 c){ return umax_(umax_(a,b),c); }
#define GTC(r,s) "v_cmp_gt_u32 %[" #s "], v" #r ", %[c]\n\t"
#define GTA(s) "v_addc_co_u32 %[rk], vcc, 0, %[rk], %[" #s "]\n\t"
#define GT4(a,b,c,d) GTC(a,s0) GTC(b,s1) GTC(c,s2) GTC(d,s3) GTA(s0) GTA(s1) GTA(s2) GTA(s3)
#define GTS [s0]"=&s"(q0),[s1]"=&s"(q1),[s2]"=&s"(q2),[s3]"=&s"(q3)
DEV void top8(lch* sm,int w4,int ln,u32 M,char* ws,u32& rank_,u32& key_){
  u32 k[8], rc[8], hm=0xffff0000u; u64 em=0x0001000100010001ull,q0,q1,q2,q3;
  u32x4 rs=mkrs(ws+W_RG+(BID&(NRG-1))*8192,8192); int ro=w4*1024+ln*16;
  #pragma unroll
  for(int e=0;e<8;e++){ rc[e]=(u32)(0xfff-(w4*256+ln*4+(e&3)+(e>>2)*1024)); PIN(rc[e]); }
  lch *aW=sm+O_RMX+(w4*4+(ln>>4))*4, *aC=sm+O_RMX+(ln>>2)*4, *aO=sm+O_RMX+(ln&3)*16, *dC=sm+O_CND+(ln>>1)*4, *dO=sm+O_CND+(ln&1)*64; PIN(aW); PIN(aC); PIN(aO); PIN(dC); PIN(dO); PIN(ro); PIN(ln);
  asm volatile("" : "+s"(hm),"+s"(em),"+s"(rs));
  gwait<2>(sm,C_G,RGATE);
  { u32 cnt;
    #define KL(e,r) "v_lshl_or_b32 %[k" #e "], v" #r ", 16, %[k" #e "]\n\t"
    #define KH(e,r) "v_and_or_b32 %[k" #e "], v" #r ", %[hm], %[k" #e "]\n\t"
    #define CHK_R "v_bitop3_b32 %[t0], v113, v115, %[M] bitop3:0x7e\n\tv_bitop3_b32 %[t1], v117, v119, %[M] bitop3:0x7e\n\tv_or_b32 %[t0], %[t0], %[t1]\n\tv_cmp_ne_u32 vcc, 0, %[t0]\n\t"
    u32 t0,t1;
    asm volatile(LD_DUP2
      "s_sleep " STR(R2D) "\n\tbuffer_load_dwordx4 v[120:123], %[of], %[rs], 0 offen sc1\n\tbuffer_load_dwordx4 v[124:127], %[of], %[rs], %[so] offen sc1\n\ts_mov_b32 %[cnt], 0x20000\n\ts_waitcnt vmcnt(2)\n\t" CHK_R "s_cbranch_vccz 9f\n\t"
      "s_waitcnt vmcnt(0)\n\tv_mov_b64 v[112:113], v[120:121]\n\tv_mov_b64 v[114:115], v[122:123]\n\tv_mov_b64 v[116:117], v[124:125]\n\tv_mov_b64 v[118:119], v[126:127]\n\t"
      POLLASM(LD_DUP2,CHK_R,PSLP)
      KL(0,112) KH(1,112) KL(2,114) KH(3,114) KL(4,116) KH(5,116) KL(6,118) KH(7,118)
      : [k0]"+v"(rc[0]),[k1]"+v"(rc[1]),[k2]"+v"(rc[2]),[k3]"+v"(rc[3]),[k4]"+v"(rc[4]),[k5]"+v"(rc[5]),[k6]"+v"(rc[6]),[k7]"+v"(rc[7]),[cnt]"=&s"(cnt),[t0]"=&v"(t0),[t1]"=&v"(t1) : [of]"v"(ro),[rs]"s"(rs),[so]"s"(4096),[M]"s"(M),[hm]"s"(hm) : PCLOB); }
  #pragma unroll
  for(int e=0;e<8;e++) k[e]=rc[e];
  u32 h=max3u(k[0],k[1],k[2]), m1=max3u(max3u(k[3],k[4],k[5]),max3u(k[6],k[7],h),h), mx=m1, l=med3u(k[0],k[1],k[2]), t=umin_(umin_(k[0],k[1]),k[2]);
  SCHED();
  #define INS(e) t=med3u(t,l,k[e]); l=med3u(h,l,k[e]); if(e<7) h=umax_(h,k[e]); PIN(t); PIN(l); SCHED();
  mx=umax_(mx,dppu<0xB1>(mx)); INS(3) mx=umax_(mx,dppu<0x4E>(mx)); INS(4) mx=umax_(mx,dppu<0x141>(mx)); INS(5) mx=umax_(mx,dppu<0x140>(mx));
  asm volatile("s_mov_b64 exec, %2\n\tds_write_b32 %0, %1\n\ts_mov_b64 exec, -1\n\tds_read_b32 v116, %3\n\tds_read_b128 v[112:115], %4" :: "v"(aW),"v"(mx),"s"(em),"v"(aC),"v"(aO) : "memory");
  INS(6) INS(7)
  u32 m2=l, m3=t, T;
  { u32 c,rk,cnt;
    asm volatile("s_mov_b32 %[cnt], 0x20000\n1:\n\ts_waitcnt lgkmcnt(0)\n\tv_cmp_eq_u32 vcc, 0, v116\n\ts_cbranch_vccz 2f\n\tds_read_b32 v116, %[aC]\n\tds_read_b128 v[112:115], %[aO]\n\ts_sub_u32 %[cnt], %[cnt], 1\n\ts_cbranch_scc0 1b\n2:\n\t"
      "v_mov_b32 %[c], v116\n\tv_mov_b32 %[rk], 0\n\t" GT4(112,113,114,115) "s_nop 1\n\tv_add_u32_dpp %[rk], %[rk], %[rk] quad_perm:[1,0,3,2] row_mask:0xf bank_mask:0xf\n\ts_nop 1\n\tv_add_u32_dpp %[rk], %[rk], %[rk] quad_perm:[2,3,0,1] row_mask:0xf bank_mask:0xf\n\t"
      "v_cmp_eq_u32 vcc, 7, %[rk]\n\ts_ff1_i32_b64 %[cnt], vcc\n\ts_nop 0\n\tv_readlane_b32 %[T], %[c], %[cnt]\n\ts_nop 1"
      : [c]"=&v"(c),[rk]"=&v"(rk),[cnt]"=&s"(cnt),[T]"=&s"(T),GTS : [aC]"v"(aC),[aO]"v"(aO) : PCLOB); T=RFL(T); }
  { u64 b1=BALLOT(m1>=T), b2=BALLOT(m2>=T), b3=BALLOT(m3>=T); int n1=__builtin_popcountll(b1), n2=n1+__builtin_popcountll(b2);
    lch* cw=sm+O_CND+w4*32;
    if(__builtin_expect(b3==0ull&&n2<=8,1)){
      int p1=(int)__builtin_amdgcn_mbcnt_hi((u32)(b1>>32),__builtin_amdgcn_mbcnt_lo((u32)b1,0u)), pf=n2+ln-p1; bool c1=m1>=T;
      if(__builtin_expect(b2!=0ull,0)){ int p2=(int)__builtin_amdgcn_mbcnt_hi((u32)(b2>>32),__builtin_amdgcn_mbcnt_lo((u32)b2,(u32)n1)); if(m2>=T) ST(u32,cw+p2*4)=m2; }
      int ps= c1? p1:pf; ps= ps<8? ps:32-8*w4; ST(u32,cw+ps*4)= c1? m1:1u;
    } else {
      u32 mine=0;
      _Pragma("unroll 1") for(int c=0;c<8;c++){ u32 lm=umax_(umax_(umax_(k[0],k[1]),umax_(k[2],k[3])),umax_(umax_(k[4],k[5]),umax_(k[6],k[7]))); u32 wm=wmax(lm); if(ln==c) mine=wm;
        #pragma unroll
        for(int e=0;e<8;e++) k[e]=(k[e]==wm)?0u:k[e]; }
      if(ln<8) ST(u32,cw+ln*4)=mine; } }
  { u32 c,rk,cnt;
    asm volatile("s_mov_b32 %[cnt], 0x20000\n\tv_mov_b32 %[rk], 0\n\ts_waitcnt vmcnt(0)\n1:\n\tds_read_b32 %[c], %[dC]\n\tds_read_b128 v[112:115], %[dO]\n\tds_read_b128 v[116:119], %[dO] offset:16\n\tds_read_b128 v[120:123], %[dO] offset:32\n\tds_read_b128 v[124:127], %[dO] offset:48\n\t"
      "s_waitcnt lgkmcnt(4)\n\tv_cmp_eq_u32 vcc, 0, %[c]\n\ts_cbranch_vccz 2f\n\ts_sub_u32 %[cnt], %[cnt], 1\n\ts_cbranch_scc0 1b\n2:\n\t"
      "s_waitcnt lgkmcnt(3)\n\t" GT4(112,113,114,115) "s_waitcnt lgkmcnt(2)\n\t" GT4(116,117,118,119) "s_waitcnt lgkmcnt(1)\n\t" GT4(120,121,122,123) "s_waitcnt lgkmcnt(0)\n\t" GT4(124,125,126,127)
      "s_nop 1\n\tv_add_u32_dpp %[rk], %[rk], %[rk] quad_perm:[1,0,3,2] row_mask:0xf bank_mask:0xf"
      : [c]"=&v"(c),[rk]"=&v"(rk),[cnt]"=&s"(cnt),GTS : [dC]"v"(dC),[dO]"v"(dO) : PCLOB);
    rank_=rk; key_=c; }
}

#define FILL_(n,WAIT) "s_mov_b32 m0, %[ld]\n\ts_mov_b32 %[c], " STR(n) "-1\n3:\n\t" WAIT "buffer_load_dwordx4 %[vo], %[rs], %[so] offen nt lds\n\ts_add_u32 m0, m0, 0x1000\n\ts_add_u32 %[so], %[so], %[st]\n\t" \
  "s_sub_u32 %[c], %[c], 1\n\ts_cbranch_scc0 3b\n\ts_mov_b32 %[ld], m0\n\t"
#define FILL(n,WAIT) do{ u32 c_; asm volatile(FILL_(n,WAIT) : [c]"=&s"(c_),[so]"+s"(wso),[ld]"+s"(wld) : [vo]"v"(wvo),[rs]"s"(wrs),[st]"s"(wst) : "memory","scc","m0"); }while(0)
#define FILLD(n,WN,WW_) do{ u32 c_,t_,s_; asm volatile(FILL_(n,"ds_read_b32 %[t], %[ad]\n\ts_waitcnt lgkmcnt(0)\n\tv_readfirstlane_b32 %[s], %[t]\n\ts_cmp_ge_u32 %[s], 4\n\ts_cbranch_scc1 4f\n\t" VW(WN) "4:\n\t" VW(WW_)) \
  : [c]"=&s"(c_),[so]"+s"(wso),[ld]"+s"(wld),[t]"=&v"(t_),[s]"=&s"(s_) : [vo]"v"(wvo),[rs]"s"(wrs),[st]"s"(wst),[ad]"v"(C_D*4) : "memory","scc","m0"); wso=RFL(wso); wld=RFL(wld); }while(0)
#define VW(n) "s_waitcnt vmcnt(" STR(n) ")\n\t"

__global__ NOPK __attribute__((amdgpu_num_vgpr(56),amdgpu_num_sgpr(102))) __launch_bounds__(1024,4) void kA(const u16* __restrict__ x,char* ws,const u16* __restrict__ wr,const u16* __restrict__ Dsk,const u16* __restrict__ wg,const u16* __restrict__ wu,
                                              const u16* __restrict__ wd,u16* __restrict__ out,KP kp){
  __shared__ __attribute__((aligned(16))) char smem[LDS_TOTAL];
  lch* sm=(lch*)smem;
  u32x4 xa,xb_,wa,wb,dq[3]; u32 xc,wc;
  asm volatile(
    "s_load_dwordx16 s[64:79], %[ka], 0x0\n\ts_load_dwordx16 s[80:95], %[ka], 0x40\n\t"
    "v_readfirstlane_b32 s60, %[t]\n\ts_and_b32 s61, %[wg], 7\n\ts_lshr_b32 s62, %[wg], 3\n\tv_and_b32 v118, 31, %[t]\n\ts_bfe_u32 s63, s60, 0x20006\n\ts_cmpk_ge_u32 s60, 0x100\n\ts_cbranch_scc1 1f\n\t"
    "s_setprio 3\n\ts_lshl_b32 s56, s61, 5\n\ts_add_u32 s56, s56, s62\n\ts_mulk_i32 s56, 0x2400\n\tv_lshrrev_b32 v119, 5, %[t]\n\tv_mul_u32_u24 v119, 0x480, v119\n\tv_add_u32 v119, s56, v119\n\t"
    "v_lshl_add_u32 v116, v118, 4, v119\n\tv_lshl_add_u32 v117, v118, 2, v119\n\tv_lshlrev_b32 v114, 4, v118\n\tv_lshlrev_b32 v115, 2, v118\n\t"
    "s_and_b32 s57, s62, 3\n\ts_mulk_i32 s57, 0x120\n\ts_lshl_b32 s58, s63, 5\n\ts_add_u32 s57, s57, s58\n\tv_and_b32 v113, 8, %[t]\n\tv_lshl_add_u32 v113, v113, 1, s57\n\tv_min_u32 v112, 0x370, v113\n\t"
    "v_lshlrev_b32 v119, 1, %[t]\n\tv_mov_b32 v118, 0\n\tds_write_b16 v119, v118\n\t"
    "s_waitcnt lgkmcnt(0)\n\t"
    "s_load_dwordx2 s[96:97], s[66:67], 0x0\n\t"
    "global_load_dwordx4 %[wa], v114, s[68:69]\n\tglobal_load_dwordx4 %[wb], v114, s[68:69] offset:512\n\tglobal_load_dword %[wc], v115, s[68:69] offset:1024\n\t"
    "global_load_dwordx4 %[xa], v116, s[64:65] nt\n\tglobal_load_dwordx4 %[xb], v116, s[64:65] offset:512 nt\n\tglobal_load_dword %[xc], v117, s[64:65] offset:1024 nt\n\t"
    "global_load_dwordx4 %[d0], v113, s[70:71]\n\tglobal_load_dwordx4 %[d1], v113, s[70:71] offset:128\n\tglobal_load_dwordx4 %[d2], v112, s[70:71] offset:256\n\ts_barrier\n\t"
    "s_branch 8f\n"
    "1:\n\ts_cmp_ge_u32 s62, " STR(CPF) "\n\ts_cbranch_scc1 6f\n\ts_getpc_b64 s[56:57]\n.Lkpc:\n\ts_and_b32 s58, s62, 3\n\ts_lshl_b32 s58, s58, 13\n\ts_sub_u32 s58, s58, 0x1000\n\tv_lshl_add_u32 v119, %[t], 4, s58\n\tv_min_u32 v119, .Lkend-.Lkpc-16, v119\n\t"
    "global_load_dwordx4 v[120:123], v119, s[56:57]\n\t.subsection 1\n.Lkend:\n\t.subsection 0\n6:\n\t"
    "v_mov_b32 v124, -1\n\tv_mov_b32 v125, -1\n\tv_mov_b32 v126, -1\n\tv_mov_b32 v127, -1\n\tv_lshlrev_b32 v119, 4, %[t]\n\tv_and_b32 v119, 0xff0, v119\n\t"
    "s_lshr_b32 s59, s60, 8\n\ts_cmp_lt_u32 s62, 8\n\ts_cselect_b32 s58, 2, 1\n\ts_cbranch_scc1 2f\n\ts_cmp_eq_u32 s59, 3\n\ts_cbranch_scc1 4f\n"
    "2:\n\ts_sub_u32 s57, s59, 2\n\ts_cmp_eq_u32 s59, 1\n\ts_cselect_b32 s58, s58, s57\n\ts_cmp_lt_u32 s62, 8\n\ts_cselect_b32 s56, 0, 8\n\ts_lshl1_add_u32 s56, s62, s56\n\ts_mulk_i32 s61, 72\n\ts_add_u32 s51, s56, s61\n\ts_sub_u32 s56, 2, s59\n\ts_sub_u32 s57, 16, s62\n\ts_cmp_eq_u32 s59, 3\n\ts_cselect_b32 s50, s57, s56\n\ts_add_u32 s56, s50, s51\n\t"
    "v_mov_b32 v112, 0\n\tv_mov_b32 v113, 0\n\tv_mov_b32 v114, 0\n\tv_mov_b32 v115, 0\n\tv_mov_b32 v116, 0\n\tv_mov_b32 v117, 0\n\ts_setprio 2\n\t"
    "s_lshl_b32 s57, s56, 2\n\ts_add_u32 s57, s57, s63\n\ts_lshl_b32 s57, s57, 6\n\tv_lshl_add_u32 v22, v118, 1, s57\n\ts_mul_i32 s57, s56, %[yst]\n\ts_add_u32 s57, s57, %[wy]\n\tv_add_u32 v23, s57, v119\n\ts_lshl_b32 s56, s56, 1\n\ts_and_b32 s56, s56, -4\n\t"
    "s_waitcnt lgkmcnt(0)\n\t"
    "global_load_short_d16_hi v112, v22, s[80:81] nt\n\tglobal_load_short_d16_hi v113, v22, s[82:83] nt\n\tglobal_load_short_d16_hi v114, v22, s[84:85] nt\n\t"
    "global_load_short_d16_hi v115, v22, s[86:87] nt\n\tglobal_load_short_d16_hi v116, v22, s[88:89] nt\n\tglobal_load_short_d16_hi v117, v22, s[90:91] nt\n\t"
    "s_load_dwordx2 s[96:97], s[66:67], 0x0\n\ts_load_dword s98, s[92:93], s56\n\t"
    "s_barrier\n\t" "global_store_dwordx4 v23, v[124:127], s[66:67] sc1\n\t"
    "s_mul_i32 s52, s58, %[sl]\n\ts_add_u32 s52, s52, %[tb]\n\ts_lshl_b32 s53, s63, 7\n\ts_add_u32 s53, s53, s52\n\tv_lshl_add_u32 v30, v118, 2, s53\n\tv_bfe_u32 v31, %[t], 5, 1\n\ts_movk_i32 s54, 528\n\tv_mul_u32_u24 v29, s54, v31\n\t"
    "v_lshl_add_u32 v2, v29, 3, v30\n\ts_movk_i32 s53, 1056\n\tv_add_u32 v3, s53, v2\n\tv_add_u32 v4, s53, v3\n\tv_add_u32 v5, s53, v4\n\tv_add_u32 v6, 41*528, v2\n\tv_add_u32 v7, s53, v6\n\tv_add_u32 v8, s53, v7\n\tv_add_u32 v9, s53, v8\n\t"
    "v_lshl_add_u32 v10, v29, 2, v30\n\tv_add_u32 v10, 16*528, v10\n\tv_add_u32 v11, s53, v10\n\tv_lshl_add_u32 v12, v29, 1, v30\n\tv_add_u32 v12, 24*528, v12\n\tv_add_u32 v13, 2112, v12\n\tv_add_u32 v14, v29, v30\n\tv_add_u32 v14, 32*528, v14\n\t"
    "s_lshl_b32 s53, s58, 4\n\ts_add_u32 s53, s53, %[ex]\n\tv_mov_b32 v15, s53\n\tv_cvt_f32_ubyte0 v31, v31\n\tv_mul_f32 v16, 0x42800000, v31\n\tv_add_f32 v17, v16, v16\n\tv_mov_b32 v20, 1\n\t"
    "v_and_b32 v29, 15, %[t]\n\tv_and_b32 v30, 48, %[t]\n\tv_mad_u32_u24 v30, v29, s54, v30\n\ts_lshl_b32 s55, s59, 1\n\ts_lshr_b32 s55, 0x48, s55\n\ts_add_u32 s55, s55, s63\n\ts_and_b32 s55, s55, 3\n\ts_mul_i32 s53, s55, 16*528\n\ts_movk_i32 s57, 41*528\n\ts_cmp_eq_u32 s55, 2\n\ts_cselect_b32 s53, s57, s53\n\ts_cselect_b32 s57, 32*528, s57\n\t"
    "s_add_u32 s53, s53, s52\n\ts_add_u32 s57, s57, s52\n\tv_add_u32 v18, s53, v30\n\tv_add_u32 v19, s57, v30\n\t"
    "v_min_u32 v29, 237*16, v119\n\tv_subrev_u32 v30, 204*16, v29\n\tv_add_u32 v29, %[de], v29\n\tv_min_u32 v29, v29, v30\n\ts_sub_u32 s53, s52, 4640\n\tv_add_u32 v29, s53, v29\n\tv_mov_b32 v24, 0\n\tv_mov_b32 v25, 0\n\tv_mov_b32 v26, 0\n\tv_mov_b32 v27, 0\n\tds_write_b128 v29, v[24:27]\n\t"
    "s_cmp_lg_u32 s55, 3\n\ts_cbranch_scc1 9f\n\t"
    "v_bfe_u32 v18, %[t], 1, 5\n\tv_and_b32 v19, 1, %[t]\n\ts_cmp_lt_u32 s62, 8\n\ts_cselect_b32 s53, 2, 1\n\ts_mul_i32 s53, s53, %[sl]\n\tv_mul_u32_u24 v21, s53, v19\n\ts_sub_u32 s55, 2, s59\n\ts_lshl_b32 s55, s55, 7\n\tv_add_u32 v18, s55, v18\n\ts_cmp_lg_u32 s59, 3\n\ts_cbranch_scc1 7f\n\t"
    "v_bfe_u32 v18, %[t], 5, 1\n\tv_lshl_add_u32 v18, v18, 7, v118\n\tv_mov_b32 v19, s50\n\tv_mov_b32 v21, %[sl]\n"
    "7:\n\tv_add_u32 v19, s51, v19\n\tv_lshl_add_u32 v21, v18, 4, v21\n\tv_lshrrev_b32 v24, 3-" STR(TL) ", v19\n\tv_and_b32 v19, 7>>" STR(TL) ", v19\n\tv_lshlrev_b32 v19, 4, v19\n\tv_lshl_add_u32 v19, v24, 7, v19\n\t"
    "v_and_b32 v24, (1<<" STR(TL) ")-1, v18\n\tv_lshl_add_u32 v19, v24, 7-" STR(TL) ", v19\n\tv_lshrrev_b32 v18, " STR(TL) ", v18\n\tv_mul_u32_u24 v18, %[ust], v18\n\tv_add_u32 v18, v18, v19\n\tv_add_u32 v19, 512, v21\n\t"
    "s_branch 9f\n"
    "4:\n\t"
    "v_bfe_u32 v118, %[t], 3, 5\n\tv_lshrrev_b32 v117, 1, v118\n\tv_and_b32 v117, 4, v117\n\tv_and_b32 v116, 2, v118\n\tv_and_b32 v115, 7, %[t]\n\tv_or_b32 v116, v116, v117\n\tv_xor_b32 v115, v115, v116\n\tv_lshlrev_b32 v115, 4, v115\n\t"
    "s_mul_i32 s54, s61, 9\n\ts_sub_u32 s55, s62, 14\n\ts_lshr_b32 s55, s55, 1\n\ts_add_u32 s54, s54, s55\n\ts_lshl_b32 s54, s54, 7\n\t"
    "s_mul_i32 s52, s61, 0xa2000\n\ts_mul_i32 s53, s62, 0x180\n\ts_add_u32 s52, s52, s53\n\ts_cmp_ge_u32 s62, 11\n\ts_cselect_b32 s53, 0x51000-0x480, 0\n\ts_add_u32 s52, s52, s53\n\ts_sub_u32 s52, s52, 0xc00\n\t"
    "s_movk_i32 s55, 0x480\n\ts_cmp_ge_u32 s62, 14\n\ts_cselect_b32 s54, s54, s52\n\ts_cselect_b32 s55, 0x2400, s55\n\tv_mad_u32_u24 v118, v118, s55, v115\n\ts_lshl_b32 s55, s55, 5\n\t"
    "s_lshl_b32 s52, s63, 10\n\ts_add_u32 m0, s52, %[r2]\n\ts_mov_b32 s58, 0x510000\n\ts_mov_b32 s59, 0x20000\n\ts_cmp_ge_u32 s62, 14\n\ts_cselect_b32 s53, " STR(PRE1) "-1, " STR(PRE2) "-1\n\ts_setprio 3\n\t"
    "s_waitcnt lgkmcnt(0)\n\t"
    "s_bitcmp1_b32 %[wg], 3\n\ts_cselect_b64 s[56:57], s[74:75], s[72:73]\n\ts_cmp_ge_u32 s62, 14\n\ts_cselect_b64 s[56:57], s[56:57], s[76:77]\n\ts_cselect_b32 s52, 0x180, -1\n\ts_and_b32 s57, s57, 0xffff\n\ts_add_u32 s60, s56, s54\n\ts_and_b32 s60, s60, 0x380\n\ts_cmp_eq_u32 s60, s52\n\ts_cselect_b32 s53, " STR(PRE3) "-1, s53\n\t"
    "s_load_dwordx2 s[96:97], s[66:67], 0x0\n\ts_barrier\n\t"
    "\n3:\n\t" "buffer_load_dwordx4 v118, s[56:59], s54 offen nt lds\n\ts_add_u32 m0, m0, 0x1000\n\ts_add_u32 s54, s54, s55\n\ts_sub_u32 s53, s53, 1\n\ts_cbranch_scc0 3b\n"
    "9:\n8:"
    : [xa]"=&{v[2:5]}"(xa),[xb]"=&{v[6:9]}"(xb_),[xc]"=&{v30}"(xc),[wa]"=&{v[10:13]}"(wa),[wb]"=&{v[14:17]}"(wb),[wc]"=&{v31}"(wc),[d0]"=&{v[18:21]}"(dq[0]),[d1]"=&{v[22:25]}"(dq[1]),[d2]"=&{v[26:29]}"(dq[2])
    : [ka]"s"(__builtin_amdgcn_kernarg_segment_ptr()),[wg]"s"(BID),[t]"v"(TID),[yst]"i"(YST),[wy]"i"(W_Y),[r2]"i"(O_R2),[sl]"i"(SLSZ),[tb]"i"(O_CH+O_TB),[ex]"i"(O_EXP),[de]"i"(O_DE),[ust]"i"(GST_)
    : "memory","scc","m0","v127","s50","s51","s52","s53","s54","s55","s56","s57","s58","s59","s60","s61","s62","s63","s64","s65","s66","s67","s68","s69","s70","s71","s72","s73","s74","s75","s76","s77","s78","s79",
      "s80","s81","s82","s83","s84","s85","s86","s87","s88","s89","s90","s91","s92","s93","s94","s95","s99");
  int wv=RFL(TID>>6), bid=BID, q4=wv>>2, w4=wv&3;
  int xg=bid&7, lb=bid>>3, tb=32*xg+lb, nch=2+(lb<8);
  u64 ep; u32 ldw;
  {
  }
  #define KILLIN "=v"(xa),"=v"(xb_),"=v"(xc),"=v"(wa),"=v"(wb),"=v"(wc),"=v"(dq[0]),"=v"(dq[1]),"=v"(dq[2])
  if(__builtin_expect(q4==3&&lb>=8,0)){
    int fu=18*xg+lb-14, kq=2*xg+(lb>=11), cgp=lb-8-3*(lb>=11);
  u32 wst= lb>=14? F_*64:D_*64;
  u32 wso=(lb>=14? (fu>>1)*128 : kq*(288*D_*2)+384*cgp)+(lb>=14? PRE1:PRE2)*wst, wld=(u32)(size_t)sm+O_R2+w4*1024+(lb>=14? PRE1:PRE2)*4096; u32x4 wrs=mkrs(lb>=14? ((fu&1)? wu:wg):wd,D_*F_*2);
  GETEP
  #define FLANE(RS) LANE(ln); int g=ln>>4, li=ln&15, qq=li>>2, pq=li&3; u32 wvo=(u32)(8*w4+(ln>>3))*(RS)+(u32)((ln&7)^(((ln>>3)&2)|((w4&1)<<2)))*16u; \
    const lch* tp=sm+O_R2+(8*g+qq)*128+((w4^((qq>>1)|((g&1)<<1)))*32)+pq*8;
  #define WFR(img,ks) trfrag(tp+(img)*CHSZ+(ks)*4096,tp+(img)*CHSZ+(ks)*4096+512)
  u64 tag=ep+1ull; const u32x4 I4={INV,INV,INV,INV};
  if(lb>=14){
    FLANE(F_*2)
    int mt=fu&1;
    if(w4==0) ST(u32x4,sm+O_ONE+ln*16)=u32x4{0x3f803f80u,0x3f803f80u,0x3f803f80u,0x3f803f80u}; else if(ln<8) ST(u32x4,sm+O_ONE+1024+ln*16)=u32x4{0x3f803f80u,0x3f803f80u,0x3f803f80u,0x3f803f80u};
    const lch* xp=sm+(li<8? O_TILE+li*XSTR : O_ONE)+g*16; int of=(mt*288+(fu>>1)*4+w4)*256+(li&7)*32+g*8+(li>>3)*4;
    const lch* mp=sm+O_RC+(li&7)*4;
    asm volatile("" : "+v"(xp),"+v"(of),"+v"(mp));
    rsrc_t rsG=RSRC(ws+W_GU,2*8*F_*2);
    if(w4==0) BST128(I4,rsG,(mt*288+(fu>>1)*4)*256+ln*16,0,SC1);
    bool slow=(((u32)wrs[0]+wso)&0x380u)==0x180u;
    if(slow){ wso-=(PRE1-PRE3)*wst; wld-=(PRE1-PRE3)*4096; FILL(5+PRE1-PRE3,VW(W3)); } else FILLD(5,W1,W1B);
    if(w4==0){ VMW(5); if(ln==0) GST(ws+W_FL+(576+fu)*8,tag); }
    if(slow) FILLD(13-PRE1,W3,W1B); else FILLD(13-PRE1,W1,W1B);
    bf16x8 fB[18];
    VMW(LAST1); gsync(sm,C_P,4);
    #pragma unroll
    for(int ks=0;ks<18-LAST1;ks++) fB[ks]=WFR(0,ks);
    #pragma unroll
    for(int ks=0;ks<18-LAST1;ks++) asm volatile("" : "+v"(fB[ks]));
    DRAIN(); gsync(sm,C_P,8);
    #pragma unroll
    for(int ks=18-LAST1;ks<18;ks++) fB[ks]=WFR(0,ks);
    f32x4 a0={0,0,0,0}; float nm,ri,ce; u32 mdw;
    { bf16x8 xb[18];
      gwait<2>(sm,C_X,1);
      gwait4<64>(C_X,4,xp,xb[0],xb[1],xb[2],xb[3]);
      #pragma unroll
      for(int ks=4;ks<7;ks++) xb[ks]=LD(bf16x8,xp+ks*64);
      SCHED();
      #pragma unroll
      for(int ks=0;ks<17;ks++){
        a0=__builtin_amdgcn_mfma_f32_16x16x32_bf16(fB[ks],xb[ks],a0,0,0,0); SCHED();
        if(2*ks+7<18) xb[2*ks+7]=LD(bf16x8,xp+(2*ks+7)*64); if(2*ks+8<18) xb[2*ks+8]=LD(bf16x8,xp+(2*ks+8)*64);
        if(ks==5) mdw=LD(u32,mp); if(ks==14){ ri=rcpf_(bhi(mdw)); nm=-blo(mdw); PIN(nm); } if(ks==15){ ce=ri*-1.4426950408889634f; PIN(ce); }
        SCHED(); }
      #define RR8 " row_ror:8 row_mask:0xf bank_mask:"
      float v0,v1,s0,s1;
      asm volatile("v_mfma_f32_16x16x32_bf16 v[112:115], %4, %5, %6\n\ts_nop 7\n\tv_fmac_f32_dpp v114, v114, %7" RR8 "0xf\n\tv_fmac_f32_dpp v115, v115, %7" RR8 "0xf\n\tv_fmac_f32_dpp v112, v112, %7" RR8 "0xf\n\tv_fmac_f32_dpp v113, v113, %7" RR8 "0xf\n\t"
        "v_mov_b32_dpp v112, v114" RR8 "0xc\n\tv_mov_b32_dpp v113, v115" RR8 "0xc\n\tv_mul_f32 %2, %9, v112\n\tv_mul_f32 %3, %9, v113\n\tv_mul_f32 %0, %8, v112\n\tv_mul_f32 %1, %8, v113"
        : "=&v"(v0),"=&v"(v1),"=&v"(s0),"=&v"(s1) : "v"(fB[17]),"v"(xb[17]),"v"(a0),"v"(nm),"v"(ri),"v"(ce));
      if(mt==0){ v0*=rcpf_(1.f+__builtin_amdgcn_exp2f(s0)); v1*=rcpf_(1.f+__builtin_amdgcn_exp2f(s1)); }
      __builtin_amdgcn_raw_buffer_store_b32(nv(pk(v0,v1)),rsG,of,0,SC1); garrive(sm,C_H); }
    if(mt==0&&w4==0){
    LANE(l2); ln=l2; li=ln&15; g=ln>>4;
    int c9=(lb-14)>>1, t=RFL((int)LD(u16,sm+O_IDX+xg*2)), c=64*c9+4*li+g; f32x4 pr;
    { u32 u=LD(u16,sm+O_IDX+16+xg*2)^0x8000u, xb=LD(u16,sm+O_TILE+xg*XSTR+c*2), sv=LD(u32,sm+O_RC+xg*4); float ds=b2f(Dsk[c]); u^=(u32)(((int)(u<<16))>>31)&0x7fffu;
      poll1<0>(ws+W_FL+(64*c9+ln)*8,tag); poll1<0>(ws+W_FL+(720+(ln<48? ln:47))*8,tag);
      float xn=(b2f(xb)-blo(sv))*rcpf_(bhi(sv)); pr=f32x4{b2f(xb),xn,xn*ds,sigm_f(b2f(u))}; }
    gwait<2>(sm,C_PR,1);
    { u32x4 rs=mkrs(ws+W_PART+(xg*9+c9)*4096,4096), ry=mkrs(ws+W_Y,D_*YST);
      int yo=c*YST+(t&~1)*2, po=ln*16; u32 a[4], t0,cnt,yb; u16* op=out+t*D_+c;
      #define LD_P "buffer_load_dwordx4 v[112:115], %[of], %[rs], 0 offen sc1\n\tbuffer_load_dwordx4 v[116:119], %[of], %[rs], 0 offen offset:1024 sc1\n\tbuffer_load_dwordx4 v[120:123], %[of], %[rs], 0 offen offset:2048 sc1\n\tbuffer_load_dwordx4 v[124:127], %[of], %[rs], 0 offen offset:3072 sc1\n\t"
      #define LD_YB "buffer_load_dword %[yb], %[yo], %[ry], 0 offen sc1\n\t"
      #define AD4(o,a,b,c,d) "v_add_f32 %[t0], v" #a ", v" #b "\n\tv_add_f32 %[" #o "], v" #c ", v" #d "\n\tv_add_f32 %[" #o "], %[t0], %[" #o "]\n\t"
      u64 ym;
      asm volatile(LD_YB LD_P "s_mov_b32 %[cnt], 0x20000\n1:\n\ts_waitcnt vmcnt(0)\n\tv_cmp_eq_u32 %[ym], -1, %[yb]\n\t" CHK_I0 MX2(116,117) MX2(118,119) MX2(120,121) MX2(122,123) MX2(124,125) MX2(126,127) CHK_IE
        "s_or_b64 exec, vcc, %[ym]\n\ts_cbranch_scc0 9f\n\ts_sleep " STR(QSLP) "\n\ts_mov_b64 exec, %[ym]\n\ts_cbranch_execz 2f\n\t" LD_YB "2:\n\ts_mov_b64 exec, vcc\n\ts_cbranch_execz 3f\n\t" LD_P
        "3:\n\ts_mov_b64 exec, -1\n\ts_sub_u32 %[cnt], %[cnt], 1\n\ts_cbranch_scc0 1b\n9:\n\ts_mov_b64 exec, -1\n\t"
        AD4(a0,112,116,120,124) AD4(a1,113,117,121,125) AD4(a2,114,118,122,126) AD4(a3,115,119,123,127) "s_nop 1"
        : [t0]"=&v"(t0),[cnt]"=&s"(cnt),[a0]"=&v"(a[0]),[a1]"=&v"(a[1]),[a2]"=&v"(a[2]),[a3]"=&v"(a[3]),[yb]"=&v"(yb),[ym]"=&s"(ym) : [of]"v"(po),[rs]"s"(rs),[yo]"v"(yo),[ry]"s"(ry),"v"(op),"v"(pr) : PCLOB);
      auto s0=__builtin_amdgcn_permlane32_swap(a[0],a[2],false,false); auto s1=__builtin_amdgcn_permlane32_swap(a[1],a[3],false,false);
      u32 x0=s0[0],x1=s0[1],y0=s1[0],y1=s1[1]; u32 p0=BC(u32,BC(float,x0)+BC(float,x1)), p1=BC(u32,BC(float,y0)+BC(float,y1));
      auto s2=__builtin_amdgcn_permlane16_swap(p0,p1,false,false); u32 z0=s2[0], z1=s2[1]; float s=BC(float,z0)+BC(float,z1);
      float gq=silu_f(__builtin_fmaf(s,pr[3],pr[1]));
      float sy=((t&1)? bhi(yb):blo(yb))+pr[2];
      float o=__builtin_fmaf(sy,gq,pr[0]);
      *op=(u16)(pk(o,o)&0xffffu);
      if(bid==112&&ln==0) GST(ws+W_EPOCH,tag); }
    }
  } else
  {
    FLANE(D_*2)
    bf16x8 fr[27];
    rsrc_t rsP=RSRC(ws+W_PART+cgp*12288+kq*256+w4*64,8*9*4096-2*12288);
    #pragma unroll
    for(int i=0;i<3;i++) BST128(I4,rsP,li<8? li*(9*4096)+g*16+i*4096 : OOR,0,SC1);
    if(PRE2<9) FILLD(9-PRE2,W2_,W2B);
    wso+=128u-9u*wst;
    FILLD(9,W2_,W2B);
    VMW(9); gsync(sm,C_P,4);
    #pragma unroll
    for(int ks=0;ks<9;ks++) fr[ks]=WFR(0,ks);
    gsync(sm,C_P,8); if(w4==0&&ln==0) GST(ws+W_FL+(720+6*xg+lb-8)*8,tag);
    wso+=128u-9u*wst; wld-=2*CHSZ;
    FILL(9,VW(W2B));
    int fo=W_FL+(576+((9*kq)&~1)+(ln<9? ln:9))*8;
    if(w4==0) asm volatile("global_load_dwordx2 v[112:113], %0, %1 sc1\n\ts_waitcnt vmcnt(10)" :: "v"(fo),"s"(ws) : "memory"); else VMW(9);
    gsync(sm,C_P,12);
    #pragma unroll
    for(int ks=0;ks<9;ks++) fr[9+ks]=WFR(1,ks);
    { int t2=TID; asm volatile("" : "+v"(t2)); ln=t2&63; li=ln&15; g=ln>>4; }
    const lch* hp=sm+O_TILE+(li&7)*64+g*16; int po= li<8? li*(9*4096)+g*16+4096 : OOR; PIN(hp); PIN(po);
    u32x4 rs=mkrs(ws+W_GU,2*8*F_*2);
    int of=((ln>>5)*288+18*kq+2*w4)*256+(ln&31)*16; lch* hd=sm+O_TILE+w4*512+((ln>>1)&7)*64+((ln>>4)&1)*32+(ln&1)*16+(ln>>5)*8; PIN(of); PIN(hd); asm volatile("" : "+s"(rs));
    DRAIN();
    if(w4==0){ u64 bad; asm volatile("v_cmp_ne_u64 %0, %1, v[112:113]" : "=s"(bad) : "s"(tag) : "memory");
      if(bad!=0ull) poll1<0>(ws+fo,tag); }
    gsync(sm,C_P,16);
    { u32 t0,cnt;
      #define LD_G "buffer_load_dwordx4 v[112:115], %[of], %[rs], 0 offen sc1\n\tbuffer_load_dwordx4 v[116:119], %[of], %[rs], 0 offen offset:2048 sc1\n\tbuffer_load_dwordx4 v[120:123], %[of], %[rs], %[so] offen sc1\n\t"
      #define HM1(a,b) "v_lshlrev_b32 %[t0], 16, v" #a "\n\tv_lshlrev_b32 %[t1], 16, v" #b "\n\tv_and_b32 v" #a ", %[hm], v" #a "\n\tv_and_b32 v" #b ", %[hm], v" #b "\n\tv_mul_f32 %[t0], %[t0], %[t1]\n\tv_mul_f32 v" #a ", v" #a ", v" #b "\n\tv_cvt_pk_bf16_f32 v" #a ", %[t0], v" #a "\n\t"
      #define HMUL(a,b,c,d,o) "v_permlane32_swap_b32 v" #a ", v" #c "\n\tv_permlane32_swap_b32 v" #b ", v" #d "\n\t" HM1(a,c) HM1(b,d) "ds_write_b64 %[hd], v[" #a ":" #b "] offset:" #o "\n\t"
      u32 t1;
      asm volatile(LD_G :: [of]"v"(of),[rs]"s"(rs),[so]"s"(w4==0? 4096:0) : "memory");
      fr[18]=WFR(0,0); PIN(fr[18]);
      asm volatile(POLLASM(LD_G,CHK_I0 MX2(116,117) MX2(118,119) MX2(120,121) MX2(122,123) CHK_IE,GSLP)
        HMUL(112,113,114,115,0) HMUL(116,117,118,119,2048) "s_cmp_eq_u32 %[so], 0\n\ts_cbranch_scc1 8f\n\t" HMUL(120,121,122,123,4096) "8:\n\ts_mov_b64 exec, 1\n\tds_add_u32 %[ch], %[one]\n\ts_mov_b64 exec, -1"
        : [t0]"=&v"(t0),[t1]"=&v"(t1),[cnt]"=&s"(cnt) : [of]"v"(of),[rs]"s"(rs),[so]"s"(w4==0? 4096:0),[hd]"v"(hd),[hm]"s"(0xffff0000u),[ch]"v"(C_H*4),[one]"v"(1),"v"(hp),"v"(po) : PCLOB);
      }
    { f32x4 ac[3]={{0,0,0,0},{0,0,0,0},{0,0,0,0}};
      bf16x8 hb[9];
      gwait4<512>(C_H,4,hp,hb[0],hb[1],hb[2],hb[3]);
      #pragma unroll
      for(int ks=0;ks<10;ks++){
        if(ks<9){ ac[0]=__builtin_amdgcn_mfma_f32_16x16x32_bf16(fr[ks],hb[ks],ac[0],0,0,0); ac[1]=__builtin_amdgcn_mfma_f32_16x16x32_bf16(fr[9+ks],hb[ks],ac[1],0,0,0); }
        if(ks>0) ac[2]=__builtin_amdgcn_mfma_f32_16x16x32_bf16(fr[17+ks],hb[ks-1],ac[2],0,0,0);
        SCHED();
        if(ks<4){ fr[19+2*ks]=WFR(0,1+2*ks); fr[20+2*ks]=WFR(0,2+2*ks); } if(ks>=1&&ks<=2){ hb[2*ks+2]=LD(bf16x8,hp+(2*ks+2)*512); hb[2*ks+3]=LD(bf16x8,hp+(2*ks+3)*512); } if(ks==3) hb[8]=LD(bf16x8,hp+8*512);
        SCHED(); }
      rsrc_t r0=RSRC(ws+W_PART+cgp*12288+kq*256+w4*64-4096,8*9*4096), r2=RSRC(ws+W_PART+cgp*12288+kq*256+w4*64+4096,8*9*4096-2*12288);
      BST128(nv4(BC(u32x4,ac[0])),r0,po,0,SC1); BST128(nv4(BC(u32x4,ac[1])),rsP,po,0,SC1); BST128(nv4(BC(u32x4,ac[2])),r2,po,0,SC1); }

  }
  asm volatile("s_endpgm" : KILLIN :: "memory"); }
  {
  if(__builtin_expect(q4!=0,0)){ int grp= q4==1? nch-1 : q4-2;
    chan(sm,grp,(w4+(0x48>>(2*q4)))&3,72*xg+(q4==3? 16+lb : 2*lb+(lb<8? 0:8)+2-q4),(q4+1)>>2,ws,xa,xb_,wa,wb,dq[0]);
    asm volatile("s_endpgm" : KILLIN :: "memory"); }
  u32x4 xq[3]; f32x2 rc; int xr_,xr2_;
  {
    LANE(ln); int g=ln>>4, li=ln&15, qq=li>>2, pq=li&3, j=ln&31, rl=2*w4+(ln>>5);
    lch* tile=sm+O_TILE; u32 Tr=(ln>>1)&16;
    int uo=W_U+(tb>>TL)*GST_+(tb&((1<<TL)-1))*(128>>TL)+(w4*1024<<TL)+(ln>>(3-TL))*128+(ln&(7>>TL))*16, uo1=uo+(4096<<TL), uo2=uo+(8192<<TL); PIN(uo); PIN(uo1); PIN(uo2);
    lch* tw=tile+rl*TSTR2+j*16; lch* tw2=tw+1024-j*12; u32 one=0x3f803f80u; const lch* tp=tile+qq*TSTR2+(w4*64+g*16+pq*4)*2;
    int go= ln<32? W_RG+(ln>>2)*8192+(tb*4+(ln&3))*8 : W_ST+(tb*8+(ln&7))*8; lch* kw=sm+O_RV+rl*2; const lch* kr=sm+(ln<32? O_RV+(ln&3)*4 : O_MD+(ln&7)*4); PIN(kw); PIN(kr);
    int xr=(32*(tb>>2)+8*g+(li&7))*(D_*2)+288*(lb&3)+32*w4+(ln&8)*2, xr2= w4==0? xr+256:OOR;
    rsrc_t rx=RSRC(x,L_*D_*2); u64 gm=((1ull<<(4*NRG))-1ull)|0xff00000000ull; u32 um=~UMASK, km=0x80008000u; asm volatile("" : "+s"(gm),"+s"(um),"+s"(km),"+s"(ws));
    #define D2(a,b,c) __builtin_amdgcn_fdot2_f32_bf16(BC(bf2,a),BC(bf2,b),c,false)
    asm volatile("s_waitcnt vmcnt(6)" : "+v"(wa),"+v"(wb),"+v"(wc),"+v"(Tr),"+v"(tw),"+v"(tw2),"+v"(tp),"+v"(one),"+v"(go),"+v"(xr),"+v"(xr2) :: "memory");
    u32 wq[9]={wa[0],wa[1],wa[2],wa[3],wb[0],wb[1],wb[2],wb[3],wc}; float sw;
    { float a[3];
      #pragma unroll
      for(int i=0;i<9;i++) a[i%3]=D2(wq[i],one,i<3? 0.f:a[i%3]);
      sw=add16x(sum16((a[0]+a[1])+a[2])); }
    GETEP u32 tg=(u32)ep+1u; Tr=((upat(tg,w4)>>Tr)&0xffffu)*0x10001u; u32 tgv=tg;
    float z_[9]={0,0,0,0,0,0,0,0,0};
    asm volatile("s_waitcnt vmcnt(3)" : "+v"(xa),"+v"(xb_),"+v"(xc),"+v"(Tr),"+v"(sw),"+v"(tgv),"+v"(z_[0]),"+v"(z_[1]),"+v"(z_[2]),"+v"(z_[3]),"+v"(z_[4]),"+v"(z_[5]) :: "memory");
    u32 v[9]={xa[0],xa[1],xa[2],xa[3],xb_[0],xb_[1],xb_[2],xb_[3],xc};
    { float xf[18], s1, s2, s3;
      #pragma unroll
      for(int i=0;i<9;i++){ z_[i&1]=D2(v[i],one,z_[i&1]); z_[2+(i&1)]=D2(v[i],v[i],z_[2+(i&1)]); z_[4+(i&1)]=D2(v[i],wq[i],z_[4+(i&1)]); }
      s1=z_[0]+z_[1]; s2=z_[2]+z_[3]; s3=z_[4]+z_[5];
      SCHED();
      #define RSTEP(C) s1+=dppf<C>(s1); s2+=dppf<C>(s2); s3+=dppf<C>(s3); SCHED();
      RSTEP(0xB1) RSTEP(0x4E) RSTEP(0x141) RSTEP(0x140)
      s1=add16x(s1)*(1.f/576.f); s2=add16x(s2)*(1.f/576.f); s3=add16x(s3);
      float mm=s1*s1, var=s2-mm;
      if(__builtin_expect(BALLOT(!(mm<=64.f*var))!=0ull,0)){
        float q=0.f;
        #pragma unroll
        for(int i=0;i<9;i++){ u32 t=v[i]; PIN(t); float d0=blo(t)-s1, d1=bhi(t)-s1; q=__builtin_fmaf(d0,d0,q); q=__builtin_fmaf(d1,d1,q); }
        var=add16x(sum16(q))*(1.f/576.f); }
      #define UNP(i) PIN(v[i]); xf[2*(i)]=blo(v[i]); xf[2*(i)+1]=bhi(v[i]); PIN(xf[2*(i)]); PIN(xf[2*(i)+1]); SCHED();
      u32 vb=pk(var,s1); UNP(0)
      float den=blo(vb)+1e-5f, m=bhi(vb); UNP(1)
      vb=pk(den,den); s3=__builtin_fmaf(-m,sw,s3); UNP(2)
      den=__builtin_amdgcn_sqrtf(bhi(vb)); UNP(3)
      u32 md=pk(m,den); UNP(4)
      float ri=rcpf_(bhi(md)); UNP(5)
      s3*=ri; float rs_=ri*0.0625f; UNP(6)
      u32 u=pk(s3,s3); float cs_=-m*rs_; UNP(7)
      { u32 sg=(u32)((int)u>>31); PIN(sg); u^=sg|km; } UNP(8)
      asm volatile("s_mov_b64 exec, 0x1\n\ts_mov_b32 exec_hi, 1\n\tds_write_b16 %0, %1\n\tv_add_u32 %0, %0, %0\n\tds_write_b32 %0, %2 offset:%3\n\ts_mov_b32 exec_hi, 0\n\tds_add_u32 %4, %5\n\ts_mov_b64 exec, -1" : "+v"(kw) : "v"(u),"v"(md),"i"(O_MD-2*O_RV),"v"(C_I*4),"v"(1) : "memory");
      if(w4==3){ gwait<0>(sm,C_I,4); asm volatile("ds_read_b32 v118, %4\n\tv_mov_b32 v119, %1\n\ts_mov_b64 exec, %3\n\ts_waitcnt lgkmcnt(0)\n\tglobal_store_dwordx2 %0, v[118:119], %2 sc1\n\ts_mov_b64 exec, -1" :: "v"(go),"v"(tgv),"s"(ws),"s"(gm),"v"(kr) : "memory"); }
      SCHED();
      u32 h[9];
      #define UH(i) h[i]=(pkz(__builtin_fmaf(xf[2*(i)],rs_,cs_),__builtin_fmaf(xf[2*(i)+1],rs_,cs_))&um)|Tr;
      UH(0) UH(1) UH(2) UH(3) ST(u32x4,tw)=u32x4{h[0],h[1],h[2],h[3]}; UH(4) UH(5) UH(6) UH(7) ST(u32x4,tw+512)=u32x4{h[4],h[5],h[6],h[7]};
      UH(8) ST(u32,tw2)=h[8]; }
    gsync(sm,C_L,4);
    { u32x4 uq[3];
      #pragma unroll
      for(int z=0;z<3;z++) uq[z]=BC(u32x4,trfrag(tp+z*512,tp+z*512+4*TSTR2));
      asm volatile("global_store_dwordx4 %0, %1, %2 sc1" :: "v"(uo),"v"(uq[0]),"s"(ws) : "memory");
      asm volatile("global_store_dwordx4 %0, %1, %2 sc1" :: "v"(uo1),"v"(uq[1]),"s"(ws) : "memory");
      asm volatile("s_cmp_lg_u32 %3, 0\n\ts_cbranch_scc1 1f\n\tglobal_store_dwordx4 %0, %1, %2 sc1\n1:\n\ts_nop 1" :: "v"(uo2),"v"(uq[2]),"s"(ws),"s"(w4),"v"(uq[0]),"v"(uq[1]) : "memory","scc");
      asm volatile("" : "+v"(dq[0]),"+v"(dq[1]),"+v"(dq[2]));
      xr_=xr; xr2_=xr2;
      #define XQ { rsrc_t rx_=RSRC(x,L_*D_*2); xq[0]=BLD128(rx_,xr_,0,0); xq[1]=BLD128(rx_,xr_+128,0,0); xq[2]=BLD128(rx_,xr2_,0,0); }
    }
    if(lb>=2&&lb<8){
      const u16* wm=(lb&1)? wu:wg; int co=(3-(int)((u64)wm>>7)-xg)&7, r_=576-12*PFN+(((lb>>1)-1)*4+w4)+12*j;
      asm volatile("buffer_load_dword v120, %0, %1, 0 offen" :: "v"(j<PFN&&(ln<32||co==0)? r_*(F_*2)+(9*xg+co)*128+(ln>>5)*1024 : OOR),"s"(mkrs(wm,D_*F_*2)) : "memory"); }
    if(lb>=8&&lb<14) garrive(sm,C_D);
  }
  FP fp;
  { LANE(ln); int g=ln>>4, li=ln&15; u64 tag=ep+1ull;
    const char* sp_=ws+W_ST+(32*(tb>>2)+8*g+(li&7))*8; u64 fs, ff; const char* fp_=ws+W_FL+(144*(lb&3)+16*w4+64*(g<2||w4==0? (g<3? g:2):0)+li)*8;
    if(lb==CTLB||lb>=14){
      u32 M=(u32)tag, rank,key;
      top8(sm,w4,ln,M,ws,rank,key);
      {
        int t0=__builtin_amdgcn_readlane((int)key,__builtin_ctzll(BALLOT(rank==(u32)(2*w4)))), t1=__builtin_amdgcn_readlane((int)key,__builtin_ctzll(BALLOT(rank==(u32)(2*w4+1))));
        int tsel=~((ln>>5)? t1:t0)&2047, j=ln&31; rsrc_t rx=RSRC(x,L_*D_*2);
        u32x4 a=BLD128(rx,tsel*1152+j*16,0,0), b=BLD128(rx,tsel*1152+j*16+512,0,0); u32 c=BLD32(rx,tsel*1152+j*4+1024,0,0);
        const char* rp_=ws+W_ST+tsel*8; u64 f1=GLD(rp_);
        garrive(sm,C_D);
        if(lb==CTLB&&rank<8&&!(ln&1)) GST(ws+W_IDX+xg*1024+w4*128+rank*8,(tag<<16)|(~key&2047u));
        if(w4==0){ if(rank<8) ST(u16,sm+O_IDX+(ln&1)*16+rank*2)=(u16)((ln&1)? key>>16 : ~key&2047u); garrive(sm,C_IX); }
        lch* xw=sm+O_TILE+(2*w4+(ln>>5))*XSTR+j*16;
        if(BALLOT((u32)(f1>>32)!=(u32)tag)!=0ull) f1=poll1<32>(rp_,tag<<32);
        ST(u32x4,xw)=a; ST(u32x4,xw+512)=b; ST(u32,xw+1024-j*12)=c; if(j==0) ST(u32,sm+O_RC+(2*w4+(ln>>5))*4)=(u32)f1;
        garrive(sm,C_X);
      }
    }
    PRIO(0);
    if(lb<14) gwait<8>(sm,C_U,2); else gwait<4>(sm,C_H,4);
    fs=GLD(sp_); ff=GLD(fp_); XQ
    {
      if(BALLOT((u32)(fs>>32)!=(u32)tag)!=0ull) fs=poll1<32>(sp_,tag<<32);
      float ri=rcpf_(bhi((u32)fs)); rc=f32x2{ri,-blo((u32)fs)*ri}; }
    asm volatile("" : "+v"(rc),"+v"(xq[0]),"+v"(xq[1]),"+v"(xq[2]));
    bool ix= w4==3&&lb<14&&lb!=CTLB; u64 f; const char* p=ws+W_IDX+(xg*8+(lb&3))*128+(ln&7)*8; if(ix) f=GLD(p);
    final_pre(fp,w4,xq,dq,rc);
    if(BALLOT(ff!=tag)!=0ull) poll1<0>(fp_,tag);
    if(ix){ if(BALLOT(((f^(tag<<16))>>16)!=0ull)!=0ull) f=poll1<16>(p,tag<<16);
      if(ln<8) ST(u16,sm+O_IDX+ln*2)=(u16)f; garrive(sm,C_IX); }
  }
  final_post(sm,fp,w4,nch,out,ws);
  }
}

extern "C" void kernel_launch(void* const* d_in, const int* in_sizes, int n_in, void* d_out, int out_size, void* d_ws, size_t ws_size, hipStream_t stream) {
  const u16** I=(const u16**)d_in;
  KP kp; kp.Are=I[1]; kp.Aim=I[2]; kp.Bre=I[3]; kp.Bim=I[4]; kp.Cre=I[5]; kp.Cim=I[6]; kp.ldt=I[7]; kp.pad=I[7];
  kA<<<256,1024,0,stream>>>(I[0],(char*)d_ws+((ws_size-(40u<<20))&~(size_t)0xfffff),I[10],I[8],I[11],I[12],I[13],(u16*)d_out,kp);
}
```

```cpp
#include <hip/hip_runtime.h>
#include <hip/hip_bf16.h>
#include <stdint.h>
#if defined(__HIP_DEVICE_COMPILE__)
#define NOPK __attribute__((target("no-packed-fp32-ops")))
#else
#define NOPK
#endif
#define DEV __device__ NOPK __forceinline__
typedef unsigned short u16;
typedef unsigned int u32;
typedef unsigned long long u64;
typedef short bf16x8 __attribute__((ext_vector_type(8)));
typedef _Float16 h8 __attribute__((ext_vector_type(8)));
typedef _Float16 h2 __attribute__((ext_vector_type(2)));
typedef float f32x4 __attribute__((ext_vector_type(4)));
typedef float f32x2 __attribute__((ext_vector_type(2)));
typedef u32 u32x4 __attribute__((ext_vector_type(4)));
typedef u32 u32x2 __attribute__((ext_vector_type(2)));
typedef __bf16 bf2 __attribute__((ext_vector_type(2)));
typedef short v4s __attribute__((ext_vector_type(4)));
typedef __amdgpu_buffer_rsrc_t rsrc_t;
#define GAS __attribute__((address_space(1)))
#define LAS __attribute__((address_space(3)))
typedef LAS char lch;
#define L_ 2048
#define D_ 576
#define F_ 4608
#define BC(T,v) __builtin_bit_cast(T,v)
#define TID ((int)__builtin_amdgcn_workitem_id_x())
#define BID ((int)__builtin_amdgcn_workgroup_id_x())
#define RFL(x) __builtin_amdgcn_readfirstlane(x)
#define RLX_AGENT __ATOMIC_RELAXED, __HIP_MEMORY_SCOPE_AGENT
#define LD(T,p) (*(const LAS T*)(p))
#define ST(T,p) (*(LAS T*)(p))
#define SPINCAP (1<<17)
#define DRAIN() asm volatile("s_waitcnt vmcnt(0)" ::: "memory")
#define GLD(p) __hip_atomic_load((const GAS u64*)(p),RLX_AGENT)
#define GST(p,v) __hip_atomic_store((GAS u64*)(p),(u64)(v),RLX_AGENT)
#define BALLOT(c) __builtin_amdgcn_ballot_w64(c)
#define SC1 16
#define LANE(l) int l; { int t2_=TID; asm volatile("" : "+v"(t2_)); l=t2_&63; }
#define SCHED() __builtin_amdgcn_sched_barrier(0)
#define RSRC(p,n) __builtin_amdgcn_make_buffer_rsrc((void*)(p),0,(n),0x00020000)
#define BLD128 __builtin_amdgcn_raw_buffer_load_b128
#define BLD32 __builtin_amdgcn_raw_buffer_load_b32
#define BST128 __builtin_amdgcn_raw_buffer_store_b128
#define PRIO(n) __builtin_amdgcn_s_setprio(n)

DEV float b2f(u32 v){ return BC(float,v<<16); }
DEV float blo(u32 v){ return BC(float,v<<16); }
DEV float bhi(u32 v){ return BC(float,v&0xffff0000u); }
DEV u32 pk(float a,float b){ f32x2 v={a,b}; bf2 r=__builtin_convertvector(v,bf2); return BC(u32,r); }
DEV u32 pkh(float a,float b){ f32x2 v={a,b}; h2 r=__builtin_convertvector(v,h2); return BC(u32,r); }
DEV float rb(float f){ return BC(float,pk(f,f)<<16); }
DEV float rcpf_(float x){ return __builtin_amdgcn_rcpf(x); }
DEV float expf_(float x){
  const float L2E=1.4426950216293335f, L2El=1.9259629911266175e-8f; float h=x*L2E; float l=__builtin_fmaf(x,L2E,-h)+x*L2El;
  float e=__builtin_amdgcn_exp2f(h); return __builtin_fmaf(e*l,0.6931471805599453f,e); }
DEV float sigm_f(float x){ return rcpf_(1.f+__builtin_amdgcn_exp2f(-x*1.4426950408889634f)); }
DEV float silu_f(float x){ return x*sigm_f(x); }
DEV u32 umax_(u32 a,u32 b){ return a>b?a:b; }
DEV u32 umin_(u32 a,u32 b){ return a<b?a:b; }

#define YSH 3
#define PRE1 6
#define PRE3 4
#define PRE2 5
#define W1 5
#define W3 3
#define W2_ 4
#define W1B 20
#define W2B 12
#define LAST1 4
#define RGATE 4
#define UGATE 7
#define R2D 12
#define PFN 32
#define CPF 8
#define NRG 8
#define W_EPOCH 0
#define W_RG 16384
#define W_ST (W_RG+32*8192)
#define W_U (W_ST+32768)
#define UST (D_*16+128)
#define YST (L_*2+128)
#define TL 1
#define GST_ ((D_*16<<TL)+128)
#define W_Y (W_U+256*UST)
#define W_PART (W_Y+D_*YST)
#define W_GU (W_PART+8*9*16*256)
#define W_FL 9216
#define W_IDX 1024
#define CTLB 0
struct KP { const u16 *Are,*Aim,*Bre,*Bim,*Cre,*Cim,*ldt,*pad; };

template<int CTRL> DEV float dppf(float v){ return BC(float,__builtin_amdgcn_update_dpp(0u,BC(u32,v),CTRL,0xf,0xf,false)); }
template<int CTRL> DEV u32 dppu(u32 v){ return (u32)__builtin_amdgcn_update_dpp(0u,v,CTRL,0xf,0xf,false); }
DEV float sum16(float v){ v+=dppf<0xB1>(v); v+=dppf<0x4E>(v); v+=dppf<0x141>(v); v+=dppf<0x140>(v); return v; }
DEV float add16x(float v){ auto r=__builtin_amdgcn_permlane16_swap(BC(u32,v),BC(u32,v),false,false); u32 a=r[0], b=r[1]; return BC(float,a)+BC(float,b); }
DEV u32 wmax(u32 v){
  v=umax_(v,(u32)__builtin_amdgcn_update_dpp(0u,v,0x111,0xf,0xf,false));
  v=umax_(v,(u32)__builtin_amdgcn_update_dpp(0u,v,0x112,0xf,0xf,false));
  v=umax_(v,(u32)__builtin_amdgcn_update_dpp(0u,v,0x114,0xf,0xf,false));
  v=umax_(v,(u32)__builtin_amdgcn_update_dpp(0u,v,0x118,0xf,0xf,false));
  v=umax_(v,(u32)__builtin_amdgcn_update_dpp(0u,v,0x142,0xa,0xf,false));
  v=umax_(v,(u32)__builtin_amdgcn_update_dpp(0u,v,0x143,0xc,0xf,false));
  return (u32)__builtin_amdgcn_readlane((int)v,63); }

DEV void garrive(lch* sm,int c){ u32 ad=(u32)(size_t)sm+c*4, one=1;
  asm volatile("s_mov_b64 exec, 1\n\tds_add_u32 %0, %1\n\ts_mov_b64 exec, -1" :: "v"(ad),"v"(one) : "memory"); }
template<int SLP> DEV void gwait(lch* sm,int c,u32 target){ u32 ad=(u32)(size_t)sm+c*4, tv, cnt, sv; target=RFL(target);
  asm volatile("s_mov_b32 %1, 0x20000\n"
    "1:\n\tds_read_b32 %0, %3\n\ts_waitcnt lgkmcnt(0)\n\tv_readfirstlane_b32 %2, %0\n\ts_cmp_ge_u32 %2, %4\n\ts_cbranch_scc1 2f\n\ts_sleep %5\n\ts_sub_u32 %1, %1, 1\n\ts_cbranch_scc0 1b\n"
    "2:" : "=&v"(tv),"=&s"(cnt),"=&s"(sv) : "v"(ad),"s"(target),"i"(SLP) : "memory","scc"); }
template<int ST_,class T> DEV void gwait4(int c,u32 target,const lch* p,T& a,T& b,T& d,T& e){ u32 tv,cnt,sv;
  asm volatile("s_mov_b32 %1, 0x20000\n1:\n\tds_read_b32 %0, %7\n\tds_read_b128 %3, %8\n\tds_read_b128 %4, %8 offset:%10\n\tds_read_b128 %5, %8 offset:%11\n\tds_read_b128 %6, %8 offset:%12\n\ts_waitcnt lgkmcnt(4)\n\t"
    "v_readfirstlane_b32 %2, %0\n\ts_cmp_ge_u32 %2, %9\n\ts_cbranch_scc1 2f\n\ts_sleep 1\n\ts_sub_u32 %1, %1, 1\n\ts_cbranch_scc0 1b\n2:\n\ts_waitcnt lgkmcnt(0)"
    : "=&v"(tv),"=&s"(cnt),"=&s"(sv),"=&v"(a),"=&v"(b),"=&v"(d),"=&v"(e) : "v"(c*4),"v"(p),"i"(target),"i"(ST_),"i"(2*ST_),"i"(3*ST_) : "memory","scc"); }
DEV void gsync(lch* sm,int c,u32 target){ garrive(sm,c); gwait<0>(sm,c,target); }
template<int SH> DEV u64 poll1(const char* p,u64 want){ u64 f=GLD(p);
  _Pragma("unroll 1") for(int sp=0;sp<SPINCAP;sp++){ bool bad=((f^want)>>SH)!=0ull; if(BALLOT(bad)==0ull) break; __builtin_amdgcn_s_sleep(1); if(bad) f=GLD(p); }
  __builtin_amdgcn_fence(__ATOMIC_ACQUIRE,"wavefront"); return f; }
#define STR_(x) #x
#define STR(x) STR_(x)
DEV u32x4 mkrs(const void* p,u32 n){ u64 a=(u64)p; return u32x4{(u32)RFL((int)(u32)a),(u32)RFL((int)((u32)(a>>32)&0xffffu)),n,0x00020000u}; }
#define VMW(n) asm volatile("s_waitcnt vmcnt(%0)" :: "i"(n) : "memory")
#define YSLP 1
#define GSLP 1
#define QSLP 1
#define PSLP 3
#define POLLASM(LOADS,CHECK,K) "s_mov_b32 %[cnt], 0x20000\n1:\n\ts_waitcnt vmcnt(0)\n\t" CHECK "s_cbranch_vccz 9f\n\ts_sleep " STR(K) "\n\ts_mov_b64 exec, vcc\n\t" LOADS \
  "s_mov_b64 exec, -1\n\ts_sub_u32 %[cnt], %[cnt], 1\n\ts_cbranch_scc0 1b\n9:\n\t"
#define LD_DUP2 "buffer_load_dwordx4 v[112:115], %[of], %[rs], 0 offen sc1\n\tbuffer_load_dwordx4 v[116:119], %[of], %[rs], %[so] offen sc1\n\t"
#define MX2(a,b) "v_max3_u32 %[t0], %[t0], v" #a ", v" #b "\n\t"
#define CHK_I0 "v_max_u32 %[t0], v112, v113\n\t" MX2(114,115)
#define CHK_IE "v_cmp_eq_u32 vcc, -1, %[t0]\n\t"
#define PCLOB "memory","vcc","scc"
#define UMASK 0x40074007u
DEV u32 upat(u32 tag,int k){ return ((((tag*0xea959c21u)>>(8*k))&255u)*0x04000801u)&UMASK; }
DEV u32 pkz(float a,float b){ return BC(u32,__builtin_amdgcn_cvt_pkrtz(a,b)); }
#define INV 0xffffffffu
DEV u32 nv(u32 v){ return umin_(v,0xfffeffffu); }
DEV u32x4 nv4(u32x4 v){ return u32x4{nv(v[0]),nv(v[1]),nv(v[2]),nv(v[3])}; }
#define OOR 0x40000000

#define CHSZ 36864
#define O_CH 512
#define O_CND 256
#define SLSZ 38080
#define O_R2 (O_CH+2*SLSZ)
#define O_X2 (O_R2+CHSZ)
#define O_TILE (O_X2+CHSZ)
#define XSTR 1168
#define TSTR2 1216
#define O_ONE (O_TILE+9728+16)
#define O_CAND (O_ONE+1168+48)
#define LDS_TOTAL (O_CAND+128)
#define C_G 0
#define C_U 8
#define C_L 4
#define C_D 5
#define C_X 12
#define C_P 13
#define C_H 14
#define C_I 17
#define C_E 18
#define C_IX 19
#define C_PR 21
#define O_IDX 96
#define O_EXP 144
#define O_RMX 192
#define O_RV 128
#define O_MD 400
#define O_RC (O_CAND+64)

struct C2 { float r,i; };
DEV C2 cmul(C2 a,C2 b){ C2 c; c.r=__builtin_fmaf(a.r,b.r,-(a.i*b.i)); c.i=__builtin_fmaf(a.r,b.i,a.i*b.r); return c; }
DEV h8 ldA(const lch* p){ u32x2 a=*(const volatile LAS u32x2*)p, b=*(const volatile LAS u32x2*)(p+8); return BC(h8,(u32x4{a[0],a[1],b[0],b[1]})); }
DEV bf16x8 trfrag(const lch* p0,const lch* p1){
  u32x2 b0=BC(u32x2,__builtin_amdgcn_ds_read_tr16_b64_v4i16((LAS v4s*)p0)), b1=BC(u32x2,__builtin_amdgcn_ds_read_tr16_b64_v4i16((LAS v4s*)p1));
  return BC(bf16x8,(u32x4{b0[0],b0[1],b1[0],b1[1]})); }

struct FP { float gg[24], bs[24]; };
DEV void final_pre(FP& f,int w4,const u32x4* xq,const u32x4* dq,f32x2 rc){
  float ea=rc[0]*-1.4426950408889634f, eb=rc[1]*-1.4426950408889634f;
  #pragma unroll
  for(int z=0;z<3;z++) if(z<2||w4==0){
    #pragma unroll
    for(int e=0;e<4;e++){ u32 xw=xq[z][e], dw=dq[z][e]; int i=8*z+2*e; float a=blo(xw), b=bhi(xw);
      float pa=__builtin_fmaf(a,rc[0],rc[1]), pb=__builtin_fmaf(b,rc[0],rc[1]);
      float ga=pa*rcpf_(1.f+__builtin_amdgcn_exp2f(__builtin_fmaf(a,ea,eb))), gb=pb*rcpf_(1.f+__builtin_amdgcn_exp2f(__builtin_fmaf(b,ea,eb)));
      f.gg[i]=ga; f.gg[i+1]=gb; f.bs[i]=__builtin_fmaf(pa*blo(dw),ga,a); f.bs[i+1]=__builtin_fmaf(pb*bhi(dw),gb,b);
      asm volatile("" : "+v"(f.gg[i]),"+v"(f.gg[i+1]),"+v"(f.bs[i]),"+v"(f.bs[i+1])); } }
}
DEV void final_post(lch* sm,const FP& f,int w4,int nch,u16* __restrict__ out,char* ws){
  LANE(ln); int bid=BID, xg=bid&7, lb=bid>>3, g=ln>>4, li=ln&15, qq=li>>2, pq=li&3;
  int cg=lb&3, T=8*xg+(lb>>2), trow=32*T+8*g+(li&7);
  int ob=(trow*D_+144*cg+16*w4+(li>>3)*8)*2, yo=(144*cg+16*w4+(ln>>2))*YST+(32*T+8*(ln&3))*2;
  #define ZCH 64
  { gwait<2>(sm,C_IX,1); u32x4 iq=LD(u32x4,sm+O_IDX); u32 tt=(u32)trow*0x10001u; bool routed=false;
    #pragma unroll
    for(int q=0;q<4;q++){ u32 z=iq[q]^tt; routed|=((z&0xffffu)==0u)|((z>>16)==0u); }
    ob=routed? OOR:ob; }
  lch* fb=sm+O_CH+w4*3072; lch* wp=fb+((ln&3)*16+(ln>>2))*16; const lch* tp=fb+(16*g+8*(pq>>1)+qq)*16+(pq&1)*8;
  u32x4 rs=mkrs(ws+W_Y,D_*YST); rsrc_t ro=RSRC(out,L_*D_*2);
  gwait<4>(sm,C_E,4*nch);
  if(lb>=8&&lb<14) gwait<4>(sm,C_H,4);
  PRIO(3);
  { u32 t0,cnt;
    #define LD_Y "buffer_load_dwordx4 v[112:115], %[of], %[rs], 0 offen sc1\n\tbuffer_load_dwordx4 v[116:119], %[of], %[rs], %[s1] offen sc1\n\tbuffer_load_dwordx4 v[120:123], %[o3], %[rs], 0 offen sc1\n\t"
    asm volatile(LD_Y POLLASM(LD_Y,CHK_I0 MX2(116,117) MX2(118,119) MX2(120,121) MX2(122,123) CHK_IE,YSLP)
      "ds_write_b128 %[wp], v[112:115]\n\tds_write_b128 %[wp], v[116:119] offset:1024\n\tds_write_b128 %[wp], v[120:123] offset:2048"
      : [t0]"=&v"(t0),[cnt]"=&s"(cnt) : [of]"v"(yo),[rs]"s"(rs),[s1]"s"(ZCH*YST),[o3]"v"(w4==0? yo+2*ZCH*YST:OOR),[wp]"v"(wp) : PCLOB); }
  if(lb>=14) garrive(sm,C_PR);
  u32x4 r[3];
  #pragma unroll
  for(int z=0;z<3;z++) r[z]=BC(u32x4,trfrag(tp+z*1024,tp+z*1024+64));
  #pragma unroll
  for(int z=0;z<3;z++) if(z<2||w4==0){ u32x4 ov;
    #pragma unroll
    for(int e=0;e<4;e++){ u32 rw=r[z][e]; int i=8*z+2*e; ov[e]=pk(__builtin_fmaf(blo(rw),f.gg[i],f.bs[i]),__builtin_fmaf(bhi(rw),f.gg[i+1],f.bs[i+1])); }
    BST128(ov,ro,ob+z*(2*ZCH),0,0); SCHED(); }
}

#define TROW 528
#define O_TB 4640
#define O_DE 34752
#define O_DO 35968
#define O_R2K 37120
#define O_R3K 37632
#define O_DUM 37952
#define O_RED O_TB
#define MH(a,b,c) __builtin_amdgcn_mfma_f32_16x16x32_f16(a,b,c,0,0,0)
#define PIN(x) asm volatile("" : "+v"(x))
DEV h8 ld4w(const lch* p){ return BC(h8,(u32x4{LD(u32,p),LD(u32,p+4),LD(u32,p+8),LD(u32,p+12)})); }
DEV u32 __umul24_(u32 a,u32 b){ u32 r; asm("v_mul_u32_u24 %0, %1, %2" : "=v"(r) : "s"(b),"v"(a)); return r; }
DEV u32x4 band(u32 mask,int g,u32 val){ u32 by=__builtin_amdgcn_ubfe(mask,8*g,8), X=by|(by<<15), k=0x10001u; u32x4 r; asm volatile("" : "+s"(k),"+s"(val));
  #pragma unroll
  for(int d=0;d<4;d++) r[d]=__umul24_((X>>(2*d))&k,val);
  return r; }
DEV h8 zpack(f32x4 e,f32x4 o){ return BC(h8,(u32x4{pkh(e[0],o[0]),pkh(e[1],o[1]),pkh(e[2],o[2]),pkh(e[3],o[3])})); }
struct C3 { float r,i,n; };
DEV C3 c3(C2 a){ C3 c; c.r=a.r; c.i=a.i; PIN(c.i); c.n=-c.i; PIN(c.n); return c; }
DEV C2 cm(C2 a,C3 b){ C2 c; c.r=__builtin_fmaf(a.r,b.r,a.i*b.n); c.i=__builtin_fmaf(a.r,b.i,a.i*b.r); return c; }
DEV u32 ph(C2 a){ return pkh(a.r,a.i); }
#define W2(p,a,b) asm volatile("ds_write2_b32 %0, %1, %2 offset1:132" :: "v"(p),"v"(a),"v"(b) : "memory")
struct CP { const lch *x0,*x1,*bd; lch *p0,*p1,*p2,*wp; u32x4 Be,Bo,AE; int yo; };
template<int ROLE> DEV void lanes(CP& c,lch* cb,int ln){ int m=ln&15, g=ln>>4;
  const lch* ad=cb+((m&1)? O_DO+2*527 : O_DE+2*536)+2*(8*g-m);
  const lch* bd=cb+2*(256+16*m+8*g); lch* dum=cb+O_DUM;
  if(ROLE==0){ int l0=128*(g>>1)+8*m+4*(g&1); c.p0=cb+O_DE+(549-l0)*2; PIN(c.p0); c.p1=c.p0+6; PIN(c.p1); c.p2=c.p0+(O_DO-O_DE-18); c.x0=ad-640; c.bd=bd-64; c.x1=cb+O_R3K+2*(126-2*m+8*g)-192; PIN(c.x1);
    c.Be=band(0x1ffu<<(m+4),g,0x3c00u); PIN(c.Be); }
  if(ROLE==1){ lch* r=cb+O_R2K+(192-64*(g-2)-2*m)*2; c.p0= g<2? dum : r-66; c.p1= ln==0? cb+O_DE+(552-256)*2 : (ln<=32? dum : r); c.p2=cb+O_DE+(544-256-8*ln)*2; c.x0=ad-128-640; c.bd=bd-128-128; PIN(c.p0); PIN(c.p1); PIN(c.p2); }
  if(ROLE==2){ int i0=16*m+4*g; c.p0= m<8? cb+O_R3K+(125-i0)*2 : dum+2; c.p1= ln==0? cb+O_R2K+128 : (ln==8? cb+O_R3K : c.p0+6); c.x0=ad-320-640; c.bd=bd-320-128; PIN(c.p0); PIN(c.p1); }
  if(ROLE==3){ c.x0=cb+O_R2K+2*(188-4*m+8*g)-256; c.bd=cb+2*(248+8*m+8*g);
    c.Be=band(0x1fu<<(m+6),g,0x3c00u); c.Bo=band(0x7u<<(m+11),g,0x3c00u); PIN(c.Be); PIN(c.Bo); }
  PIN(c.x0); PIN(c.bd); }
template<int ROLE> DEV void lanes2(CP& c,lch* cb,int ln){ int m=ln&15, g=ln>>4;
  if(ROLE==0){ c.Bo=band(0x7fu<<(m+5),g,0x3c00u); c.AE=band(3u<<(m+12),g,0xffffu); PIN(c.Bo); PIN(c.AE); }
  c.wp=cb+O_RED+ln*16; c.yo=(g&1)*512+m*32+(g>>1)*16; PIN(c.wp); PIN(c.yo); }
template<int ROLE> DEV void chan2(lch* sm,lch* cb,const lch*& fa,const lch*& fb,int grp,int d,int one_,int emax,u64 tag,char* ws){
  CP c;
  const f32x4 Z4={0,0,0,0}; h8 A[6]; f32x4 acc[8];
  #define LD_U "buffer_load_dwordx4 v[112:115], %[of], %[rs], 0 offen sc1\n\tbuffer_load_dwordx4 v[116:119], %[of], %[rs], %[s1] offen sc1\n\tbuffer_load_dwordx4 v[120:123], %[of], %[rs], %[s2] offen sc1\n\tbuffer_load_dwordx4 v[124:127], %[of], %[rs], %[s3] offen sc1\n\t"
  #define UC(t,a,b,c) "v_bitop3_b32 " t ", v" #a ", v" #b ", %[" #c "] bitop3:0x7e\n\t"
  #define UD(t,a,c) "v_bitop3_b32 " t ", " t ", v" #a ", %[" #c "] bitop3:0xf6\n\t"
  #define CHK_U UC("%[t0]",112,116,c0) UC("%[t1]",113,117,c1) UD("%[t0]",120,c0) UD("%[t1]",121,c1) UD("%[t0]",124,c0) UD("%[t1]",125,c1) UD("%[t0]",114,c2) UD("%[t1]",115,c3) UD("%[t0]",118,c2) UD("%[t1]",119,c3) \
    UD("%[t0]",122,c2) UD("%[t1]",123,c3) UD("%[t0]",126,c2) UD("%[t1]",127,c3) "v_bitop3_b32 %[t0], %[t0], %[t1], %[mk] bitop3:0xa8\n\tv_cmp_ne_u32 vcc, 0, %[t0]\n\t"
  #define UX(a,b,c,d,o) "v_xor_b32 v" #a ", %[e0], v" #a "\n\tv_xor_b32 v" #b ", %[e1], v" #b "\n\tv_xor_b32 v" #c ", %[e2], v" #c "\n\tv_xor_b32 v" #d ", %[e3], v" #d "\n\tds_write_b128 %[up], v[" #a ":" #d "] offset:" #o "\n\t"
  #define UPOLL { u32 t0,t1,cnt; asm volatile(POLLASM(LD_U,CHK_U,PSLP) UX(112,113,114,115,544) UX(116,117,118,119,1056) UX(120,121,122,123,1568) UX(124,125,126,127,2080) \
      : [t0]"=&v"(t0),[t1]"=&v"(t1),[cnt]"=&s"(cnt) : [of]"v"(c.p0),[rs]"s"(rsU),[s1]"s"((32>>TL)*GST_),[s2]"s"((64>>TL)*GST_),[s3]"s"((96>>TL)*GST_),[c0]"s"(c0),[c1]"s"(c1),[c2]"s"(c2),[c3]"s"(c3_), \
        [e0]"s"(c0^0x40004u),[e1]"s"(c1^0x40004u),[e2]"s"(c2^0x40004u),[e3]"s"(c3_^0x40004u),[mk]"s"(UMASK),[up]"v"(c.p1) : PCLOB); garrive(sm,C_U+one_); }
  if(ROLE<3){
    h8 a[8], b[8];
    #pragma unroll
    for(int ks=0;ks<8;ks++){ a[ks]=LD(h8,fa+ks*64); b[ks]=LD(h8,fb+ks*64); }
    SCHED();
    LANE(ln); lanes<ROLE>(c,cb,ln); float fz= ln==32? 0.f : 4.76837158203125e-07f; if(ROLE==1) PIN(fz);
    SCHED();
    f32x4 k=Z4;
    #pragma unroll
    for(int ks=0;ks<8;ks++) k=MH(a[ks],b[ks],k);
    const float f=4.76837158203125e-07f;
    float k0=k[0]*f, k1=k[1]*f, k2=k[2]*f, k3=k[3]*f;
    u32 h32=pkh(k3,k2), h10=pkh(k1,k0);
    if(ROLE!=1){
      ST(u16,c.p0)=(u16)h32; ST(u32,c.p0+2)=pkh(k2,k1); ST(u16,c.p1)=(u16)(h10>>16);
      if(ROLE==0) ST(u32x2,c.p2)=u32x2{h32,h10};
    } else {
      float n0=dppf<0x130>(k[0])*fz, z1= fz==0.f? k0:k1, z2=k[2]*fz, z3=k[3]*fz; u32 h22=pkh(z2,z2);
      ST(u16,c.p0+64)=(u16)h10; ST(u16,c.p0+2)=(u16)(h32>>16); ST(u16,c.p0)=(u16)h32; ST(u16,c.p1)=(u16)(h10>>16);
      u32x4 vo={pkh(n0,z3),h22,pkh(z2,z1),pkh(k0,k0)}, ve={pkh(n0,n0),pkh(z3,z2),h22,pkh(z1,k0)};
      asm volatile("s_mov_b32 exec_hi, 1\n\tds_write_b128 %0, %1\n\tds_write_b128 %0, %2 offset:1200\n\ts_mov_b32 exec_hi, -1" :: "v"(c.p2),"v"(ve),"v"(vo) : "memory"); }
    gsync(sm,C_G+grp,7);
    u32 kv; A[0]=ld4w(c.x0+640); A[1]=ld4w(c.x0+128);
    if(ROLE==0){ A[2]=ld4w(c.x1+192); A[3]=ld4w(c.x1+128); A[4]=ld4w(c.x1+64); A[5]=ld4w(c.x1); kv=LD(u16,cb+O_R2K+128); }
    else { A[2]=ld4w(c.x0+576); A[3]=ld4w(c.x0+64); A[4]=ld4w(c.x0+512); A[5]=ld4w(c.x0); }
    SCHED(); lanes2<ROLE>(c,cb,ln); SCHED();
    if(ROLE==0){ kv|=kv<<16; c.AE=u32x4{c.AE[0]&kv,c.AE[1]&kv,c.AE[2]&kv,c.AE[3]&kv}; PIN(c.AE); }
    PRIO(2);
    gwait<1>(sm,C_U+one_,2-one_);
  } else {
    u32 tg=(u32)tag, c0=upat(tg,0), c1=upat(tg,1), c2=upat(tg,2), c3_=upat(tg,3); u32x4 rsU=mkrs(ws+W_U,256*UST);
    if(!one_) PRIO(2);
    LANE(ln); c.p0=(lch*)fa; c.p1=(lch*)fb;
    bool late= grp==0||BID>=112;
    if(!late) asm volatile(LD_U :: [of]"v"(c.p0),[rs]"s"(rsU),[s1]"s"((32>>TL)*GST_),[s2]"s"((64>>TL)*GST_),[s3]"s"((96>>TL)*GST_) : "memory");
    asm volatile("s_mov_b64 exec, 1\n\tglobal_store_dwordx2 %0, %1, %2 sc1\n\ts_mov_b64 exec, -1" :: "v"(W_FL+d*8),"v"(tag),"s"(ws) : "memory");
    lanes<3>(c,cb,ln); lanes2<3>(c,cb,ln);
    #pragma unroll
    for(int j=2;j<8;j++){ acc[j]=Z4; PIN(acc[j]); }
    if(late){ gwait<2>(sm,C_G,UGATE); if(BID>=112) gwait<1>(sm,C_D,4); asm volatile(LD_U :: [of]"v"(c.p0),[rs]"s"(rsU),[s1]"s"((32>>TL)*GST_),[s2]"s"((64>>TL)*GST_),[s3]"s"((96>>TL)*GST_) : "memory"); }
    if(!late) UPOLL
    gwait<1>(sm,C_G+grp,7);
    #pragma unroll
    for(int i=0;i<6;i++) A[i]=ldA(c.x0+320-64*i);
    if(late) UPOLL
    PRIO(2);
    gwait<1>(sm,C_U+one_,2-one_);
  }
  const lch* bd=c.bd; h8 Be=BC(h8,c.Be), Bo=BC(h8,c.Bo);
  #pragma unroll
  for(int j=0;j<(ROLE==3? 2:8);j++) acc[j]=Z4;
  #define RW(j) if((j)>=0&&((j)>>1)!=ROLE&&!(ROLE==3&&(j)<2)){ ST(f32x4,c.wp+((j)>>1)*6144+((ROLE-((j)>>1)-1)&3)*2048+((j)&1)*1024)=acc[(j)<0? 0:(j)]; SCHED(); }
  if(ROLE==1||ROLE==2){
    h8 B[8];
    #pragma unroll
    for(int q=0;q<8;q++) B[q]=LD(h8,bd+128+512*q);
    #pragma unroll
    for(int r=0;r<3;r++){
      #pragma unroll
      for(int dl=0;dl<8;dl++){ if(dl<7) acc[dl+1]=MH(A[2*r+1],B[dl],acc[dl+1]); SCHED(); acc[dl]=MH(A[2*r],B[dl],acc[dl]); SCHED();
        if(r<2){ B[dl]=LD(h8,bd+64-64*r+512*dl); SCHED(); } else RW(dl-2) }
      } }
  if(ROLE==0){
    h8 B[8], ue[4];
    #pragma unroll
    for(int kt=0;kt<4;kt++){ ue[kt]=LD(h8,bd+48+512*kt); B[kt]=LD(h8,bd+64+512*kt); }
    f32x4 ce[2],co[2]; h8 Z[2];
    ce[0]=MH(ue[0],Be,Z4); co[0]=MH(B[0],Bo,Z4); ce[1]=MH(ue[1],Be,Z4); co[1]=MH(B[1],Bo,Z4); Z[0]=zpack(ce[0],co[0]);
    #pragma unroll
    for(int kt=0;kt<4;kt++){
      SCHED();
      if(kt+2<4){ ce[kt&1]=MH(ue[kt+2],Be,Z4); co[kt&1]=MH(B[kt+2],Bo,Z4); }
      if(kt+1<4) Z[(kt+1)&1]=zpack(ce[(kt+1)&1],co[(kt+1)&1]);
      #pragma unroll
      for(int rho=0;rho+kt<4;rho++) acc[4+kt+rho]=MH(Z[kt&1],A[2+rho],acc[4+kt+rho]);
      SCHED(); B[4+kt]=LD(h8,bd+64+512*(4+kt)); if(kt==0) A[5]=BC(h8,c.AE); if(kt==1) A[4]=ld4w(c.x0-384); if(kt==2) A[3]=ld4w(c.x0+64); if(kt==3) A[2]=ld4w(c.x0+576); SCHED(); }
    #pragma unroll
    for(int dl=0;dl<8;dl++){ if(dl<4) acc[dl+4]=MH(A[5],B[dl],acc[dl+4]); SCHED(); if(dl<6) acc[dl+2]=MH(A[4],B[dl],acc[dl+2]); SCHED(); if(dl<7) acc[dl+1]=MH(A[1],B[dl],acc[dl+1]); SCHED(); acc[dl]=MH(A[0],B[dl],acc[dl]);
      SCHED(); B[dl]=LD(h8,bd+512*dl); SCHED(); }
    #pragma unroll
    for(int dl=0;dl<8;dl++){ if(dl<7) acc[dl+1]=MH(A[3],B[dl],acc[dl+1]); SCHED(); acc[dl]=MH(A[2],B[dl],acc[dl]); SCHED(); RW(dl-2) } }
  if(ROLE==3){
    h8 U[12];
    #pragma unroll
    for(int kt=0;kt<6;kt++) U[kt]=LD(h8,bd+256*kt);
    f32x4 ce[2],co[2]; h8 Z[2];
    ce[0]=MH(U[0],Be,Z4); co[0]=MH(U[0],Bo,Z4); ce[1]=MH(U[1],Be,Z4); co[1]=MH(U[1],Bo,Z4); Z[0]=zpack(ce[0],co[0]);
    #pragma unroll
    for(int kt=0;kt<12;kt++){
      SCHED();
      if(kt+2<12){ ce[kt&1]=MH(U[kt+2],Be,Z4); co[kt&1]=MH(U[kt+2],Bo,Z4); }
      if(kt+1<12) Z[(kt+1)&1]=zpack(ce[(kt+1)&1],co[(kt+1)&1]);
      #pragma unroll
      for(int rho=-1;rho<5;rho++){ int j=(kt+4+rho)>>1; if(((kt+rho)&1)==0&&j>=2&&j<8) acc[j]=MH(Z[kt&1],A[rho+1],acc[j]); }
      if(kt<6){ SCHED(); U[kt+6]=LD(h8,bd+256*(kt+6)); }
      if(kt>=3&&kt<=9&&(kt&1)){ SCHED(); RW((kt+1)/2) } }
  }
  PRIO(3);
  RW(6) RW(7)
  gsync(sm,C_G+grp,11);
  { const lch* rp=c.wp+ROLE*6144; f32x4 a[3], b[3], o0=acc[2*ROLE], o1=acc[2*ROLE+1];
    #pragma unroll
    for(int q=0;q<(ROLE==0? 2:3);q++){ a[q]=LD(f32x4,rp+q*2048); b[q]=LD(f32x4,rp+q*2048+1024); }
    SCHED();
    float sc=__builtin_amdgcn_ldexpf(1.f,emax-YSH), s0[4], s1[4];
    #pragma unroll
    for(int e=0;e<4;e++){ s0[e]=((o0[e]+a[0][e])+(ROLE==0? a[1][e] : a[1][e]+a[2][e]))*sc; s1[e]=((o1[e]+b[0][e])+(ROLE==0? b[1][e] : b[1][e]+b[2][e]))*sc; }
    auto r0=__builtin_amdgcn_permlane16_swap(nv(pk(s0[0],s0[1])),nv(pk(s1[0],s1[1])),false,false); auto r1=__builtin_amdgcn_permlane16_swap(nv(pk(s0[2],s0[3])),nv(pk(s1[2],s1[3])),false,false);
    BST128(u32x4{r0[0],r1[0],r0[1],r1[1]},RSRC(ws+W_Y+d*YST,4096),c.yo+ROLE*1024,0,SC1); }
  garrive(sm,C_E);

  asm volatile("s_endpgm" : "=v"(fa),"=v"(fb) :: "memory");
}
#define GETEP asm volatile("s_waitcnt lgkmcnt(0)\n\ts_mov_b64 %0, s[96:97]\n\ts_mov_b32 %1, s98" : "=s"(ep),"=s"(ldw));
DEV void chan(lch* sm,int grp,int w4,int d,int one_,char* ws,u32x4 q0,u32x4 q1,u32x4 q2,u32x4 q3,u32x4 q4){
  lch* cb=sm+O_CH+grp*SLSZ; int role=w4, emax;
  #define LP(v) ((lch*)(size_t)(u32)(v))
  const lch* fa=LP(q4[0]); const lch* fb=LP(q4[1]);
  u64 tag;
  { float a2,bp,br,bi,cr,ci;
    u32 fq=q3[2], fr=q3[3], one=q4[2]; float f64=BC(float,fq), f128=BC(float,fr);
    lch *t8[4]={LP(q0[0]),LP(q0[1]),LP(q0[2]),LP(q0[3])}, *tB[4]={LP(q1[0]),LP(q1[1]),LP(q1[2]),LP(q1[3])}, *t4[2]={LP(q2[0]),LP(q2[1])}, *t2[2]={LP(q2[2]),LP(q2[3])}, *t1=LP(q3[0]), *te=LP(q3[1]);
    if(grp!=0) PRIO(1);
    u64 ep; u32 ldw; GETEP tag=ep+1ull;
    float dt=rb(expf_((d&1)? bhi(ldw):blo(ldw))), ca=dt*1.4426950408889634f, cb_=dt*0.15915494309189535f;
    asm volatile("s_waitcnt vmcnt(1)\n\tv_mul_f32 %2, %8, v114\n\tv_mul_f32 %3, %8, v115\n\tv_mul_f32 %0, %6, v112\n\tv_mul_f32 %1, %7, v113\n\tv_mov_b32 %4, v116\n\tv_mov_b32 %5, v117"
      : "=&v"(a2),"=&v"(bp),"=&v"(br),"=&v"(bi),"=&v"(cr),"=&v"(ci) : "v"(ca),"v"(cb_),"v"(dt) : "memory");
    C2 W; W.r=cr*br-ci*bi; W.i=cr*bi+ci*br;
    u32 mx=BC(u32,__builtin_fmaxf(__builtin_fabsf(W.r),__builtin_fabsf(W.i)));
    #define MXS(C) mx=umax_(mx,dppu<C>(mx));
    float e8=a2*8.f, eB=__builtin_fmaf(a2,f64,14.f), p8=bp*8.f, pB=bp*f64; SCHED();
    MXS(0xB1) p8=__builtin_amdgcn_fractf(p8); pB=__builtin_amdgcn_fractf(pB); e8=__builtin_amdgcn_exp2f(e8); SCHED();
    MXS(0x4E) eB=__builtin_amdgcn_exp2f(eB); float c8=__builtin_amdgcn_cosf(p8); SCHED();
    MXS(0x141) float s8=__builtin_amdgcn_sinf(p8), cB=__builtin_amdgcn_cosf(pB); SCHED();
    float sB=__builtin_amdgcn_sinf(pB); PIN(sB); MXS(0x140) float e1=__builtin_amdgcn_exp2f(a2); PIN(e1); SCHED();
    asm volatile("s_mov_b64 exec, 0x10001\n\tds_max_u32 %0, %1\n\tds_add_u32 %0, %2 offset:4\n\ts_mov_b64 exec, -1" :: "v"(te),"v"(mx),"v"(one) : "memory");
    C3 z8; z8.r=e8*c8; z8.n=e8*s8; z8.i=-z8.n; PIN(z8.i); C2 B; B.r=eB*cB; B.i=-(eB*sB);
    float p1=__builtin_amdgcn_fractf(bp), pE=__builtin_amdgcn_fractf(bp*256.f), pV=__builtin_amdgcn_fractf(bp*f128), eE=a2*256.f, eV=a2*f128, tr[8];
    #pragma unroll
    for(int k=0;k<4;k++){ SCHED(); u32 h0=ph(B); B=cm(B,z8);
      if(k==0){ tr[0]=__builtin_amdgcn_cosf(pV); tr[1]=__builtin_amdgcn_sinf(pV); } if(k==1){ tr[2]=__builtin_amdgcn_exp2f(eV); tr[3]=__builtin_amdgcn_cosf(p1); }
      if(k==2){ tr[4]=__builtin_amdgcn_sinf(p1); tr[5]=__builtin_amdgcn_exp2f(eE); } if(k==3){ tr[6]=__builtin_amdgcn_cosf(pE); tr[7]=__builtin_amdgcn_sinf(pE); }
      PIN(tr[2*k]); PIN(tr[2*k+1]);
      u32 h1=ph(B); if(k<3) B=cm(B,z8); W2(tB[k],h0,h1); }
    asm volatile("ds_read_b64 v[118:119], %0" :: "v"(te) : "memory");
    SCHED();
    C3 z1=c3(C2{e1*tr[3],e1*tr[4]}), E=c3(C2{tr[5]*tr[6],tr[5]*tr[7]}); C2 V[8]; V[0]=cmul(W,C2{tr[2]*tr[0],tr[2]*tr[1]});
    { u32 cnt,n; PIN(V[0].r); PIN(V[0].i); PIN(z1.r); PIN(E.r);
      asm volatile("s_mov_b32 %1, 0x20000\n1:\n\ts_waitcnt lgkmcnt(0)\n\tv_readfirstlane_b32 %2, v119\n\tv_readfirstlane_b32 %0, v118\n\ts_cmp_eq_u32 %2, 8\n\ts_cbranch_scc1 2f\n\tds_read_b64 v[118:119], %3\n\ts_sub_u32 %1, %1, 1\n\ts_cbranch_scc0 1b\n2:"
        : "=&s"(emax),"=&s"(cnt),"=&s"(n) : "v"(te) : "memory","scc");
      emax=RFL(emax); emax=(emax>>23)-126; }
    V[0].r=__builtin_amdgcn_ldexpf(V[0].r,14-emax); V[0].i=__builtin_amdgcn_ldexpf(V[0].i,14-emax);
    C2 Y=cm(V[0],E), Y4; u32 y0=ph(Y);
    V[1]=cm(V[0],z1); W2(t8[0],ph(V[0]),ph(V[1])); Y=cm(Y,E); u32 y1=ph(Y);
    V[2]=cm(V[1],z1); Y=cm(Y,E); u32 y2=ph(Y);
    V[3]=cm(V[2],z1); W2(t8[1],ph(V[2]),ph(V[3])); W2(t4[0],y0,ph(cm(V[2],E)));
    V[4]=cm(V[3],z1); Y=cm(Y,E); ST(u32,t1)=ph(Y);
    V[5]=cm(V[4],z1); W2(t8[2],ph(V[4]),ph(V[5])); Y4=cm(V[4],E); u32 y4=ph(Y4);
    V[6]=cm(V[5],z1); Y=cm(Y,E); ST(u32,t1+2*TROW)=ph(Y);
    V[7]=cm(V[6],z1); W2(t8[3],ph(V[6]),ph(V[7])); Y4=cm(Y4,E); W2(t2[0],y1,ph(Y4));
    W2(t4[1],y4,ph(cm(V[6],E))); Y=cm(Y,E); ST(u32,t1+4*TROW)=ph(Y);
    Y4=cm(Y4,E); W2(t2[1],y2,ph(Y4)); Y=cm(Y,E); ST(u32,t1+6*TROW)=ph(Y);
    Y=cm(Y,E); asm volatile("s_mov_b32 exec_hi, 0\n\tds_write_b32 %0, %1 offset:4224\n\ts_mov_b32 exec_hi, -1" :: "v"(t1),"v"(ph(Y)) : "memory");
  }
  if(one_) PRIO(3);
  DRAIN();
  gsync(sm,C_G+grp,4);
  if(role==0) chan2<0>(sm,cb,fa,fb,grp,d,one_,emax,tag,ws);
  if(role==1) chan2<1>(sm,cb,fa,fb,grp,d,one_,emax,tag,ws);
  if(role==2) chan2<2>(sm,cb,fa,fb,grp,d,one_,emax,tag,ws);
  chan2<3>(sm,cb,fa,fb,grp,d,one_,emax,tag,ws);
}

DEV u32 med3u(u32 a,u32 b,u32 c){ u32 r; asm("v_med3_u32 %0, %1, %2, %3" : "=v"(r) : "v"(a),"v"(b),"v"(c)); return r; }
DEV u32 max3u(u32 a,u32 b,u32 c){ return umax_(umax_(a,b),c); }
#define GTC(r,s) "v_cmp_gt_u32 %[" #s "], v" #r ", %[c]\n\t"
#define GTA(s) "v_addc_co_u32 %[rk], vcc, 0, %[rk], %[" #s "]\n\t"
#define GT4(a,b,c,d) GTC(a,s0) GTC(b,s1) GTC(c,s2) GTC(d,s3) GTA(s0) GTA(s1) GTA(s2) GTA(s3)
#define GTS [s0]"=&s"(q0),[s1]"=&s"(q1),[s2]"=&s"(q2),[s3]"=&s"(q3)
DEV void top8(lch* sm,int w4,int ln,u32 M,char* ws,u32& rank_,u32& key_){
  u32 k[8], rc[8], hm=0xffff0000u; u64 em=0x0001000100010001ull,q0,q1,q2,q3;
  u32x4 rs=mkrs(ws+W_RG+(BID&(NRG-1))*8192,8192); int ro=w4*1024+ln*16;
  #pragma unroll
  for(int e=0;e<8;e++){ rc[e]=(u32)(0xfff-(w4*256+ln*4+(e&3)+(e>>2)*1024)); PIN(rc[e]); }
  lch *aW=sm+O_RMX+(w4*4+(ln>>4))*4, *aC=sm+O_RMX+(ln>>2)*4, *aO=sm+O_RMX+(ln&3)*16, *dC=sm+O_CND+(ln>>1)*4, *dO=sm+O_CND+(ln&1)*64; PIN(aW); PIN(aC); PIN(aO); PIN(dC); PIN(dO); PIN(ro); PIN(ln);
  asm volatile("" : "+s"(hm),"+s"(em),"+s"(rs));
  gwait<2>(sm,C_G,RGATE);
  { u32 cnt;
    #define KL(e,r) "v_lshl_or_b32 %[k" #e "], v" #r ", 16, %[k" #e "]\n\t"
    #define KH(e,r) "v_and_or_b32 %[k" #e "], v" #r ", %[hm], %[k" #e "]\n\t"
    #define CHK_R "v_bitop3_b32 %[t0], v113, v115, %[M] bitop3:0x7e\n\tv_bitop3_b32 %[t1], v117, v119, %[M] bitop3:0x7e\n\tv_or_b32 %[t0], %[t0], %[t1]\n\tv_cmp_ne_u32 vcc, 0, %[t0]\n\t"
    u32 t0,t1;
    asm volatile(LD_DUP2
      "s_sleep " STR(R2D) "\n\tbuffer_load_dwordx4 v[120:123], %[of], %[rs], 0 offen sc1\n\tbuffer_load_dwordx4 v[124:127], %[of], %[rs], %[so] offen sc1\n\ts_mov_b32 %[cnt], 0x20000\n\ts_waitcnt vmcnt(2)\n\t" CHK_R "s_cbranch_vccz 9f\n\t"
      "s_waitcnt vmcnt(0)\n\tv_mov_b64 v[112:113], v[120:121]\n\tv_mov_b64 v[114:115], v[122:123]\n\tv_mov_b64 v[116:117], v[124:125]\n\tv_mov_b64 v[118:119], v[126:127]\n\t"
      POLLASM(LD_DUP2,CHK_R,PSLP)
      KL(0,112) KH(1,112) KL(2,114) KH(3,114) KL(4,116) KH(5,116) KL(6,118) KH(7,118)
      : [k0]"+v"(rc[0]),[k1]"+v"(rc[1]),[k2]"+v"(rc[2]),[k3]"+v"(rc[3]),[k4]"+v"(rc[4]),[k5]"+v"(rc[5]),[k6]"+v"(rc[6]),[k7]"+v"(rc[7]),[cnt]"=&s"(cnt),[t0]"=&v"(t0),[t1]"=&v"(t1) : [of]"v"(ro),[rs]"s"(rs),[so]"s"(4096),[M]"s"(M),[hm]"s"(hm) : PCLOB); }
  #pragma unroll
  for(int e=0;e<8;e++) k[e]=rc[e];
  u32 h=max3u(k[0],k[1],k[2]), m1=max3u(max3u(k[3],k[4],k[5]),max3u(k[6],k[7],h),h), mx=m1, l=med3u(k[0],k[1],k[2]), t=umin_(umin_(k[0],k[1]),k[2]);
  SCHED();
  #define INS(e) t=med3u(t,l,k[e]); l=med3u(h,l,k[e]); if(e<7) h=umax_(h,k[e]); PIN(t); PIN(l); SCHED();
  mx=umax_(mx,dppu<0xB1>(mx)); INS(3) mx=umax_(mx,dppu<0x4E>(mx)); INS(4) mx=umax_(mx,dppu<0x141>(mx)); INS(5) mx=umax_(mx,dppu<0x140>(mx));
  asm volatile("s_mov_b64 exec, %2\n\tds_write_b32 %0, %1\n\ts_mov_b64 exec, -1\n\tds_read_b32 v116, %3\n\tds_read_b128 v[112:115], %4" :: "v"(aW),"v"(mx),"s"(em),"v"(aC),"v"(aO) : "memory");
  INS(6) INS(7)
  u32 m2=l, m3=t, T;
  { u32 c,rk,cnt;
    asm volatile("s_mov_b32 %[cnt], 0x20000\n1:\n\ts_waitcnt lgkmcnt(0)\n\tv_cmp_eq_u32 vcc, 0, v116\n\ts_cbranch_vccz 2f\n\tds_read_b32 v116, %[aC]\n\tds_read_b128 v[112:115], %[aO]\n\ts_sub_u32 %[cnt], %[cnt], 1\n\ts_cbranch_scc0 1b\n2:\n\t"
      "v_mov_b32 %[c], v116\n\tv_mov_b32 %[rk], 0\n\t" GT4(112,113,114,115) "s_nop 1\n\tv_add_u32_dpp %[rk], %[rk], %[rk] quad_perm:[1,0,3,2] row_mask:0xf bank_mask:0xf\n\ts_nop 1\n\tv_add_u32_dpp %[rk], %[rk], %[rk] quad_perm:[2,3,0,1] row_mask:0xf bank_mask:0xf\n\t"
      "v_cmp_eq_u32 vcc, 7, %[rk]\n\ts_ff1_i32_b64 %[cnt], vcc\n\ts_nop 0\n\tv_readlane_b32 %[T], %[c], %[cnt]\n\ts_nop 1"
      : [c]"=&v"(c),[rk]"=&v"(rk),[cnt]"=&s"(cnt),[T]"=&s"(T),GTS : [aC]"v"(aC),[aO]"v"(aO) : PCLOB); T=RFL(T); }
  { u64 b1=BALLOT(m1>=T), b2=BALLOT(m2>=T), b3=BALLOT(m3>=T); int n1=__builtin_popcountll(b1), n2=n1+__builtin_popcountll(b2);
    lch* cw=sm+O_CND+w4*32;
    if(__builtin_expect(b3==0ull&&n2<=8,1)){
      int p1=(int)__builtin_amdgcn_mbcnt_hi((u32)(b1>>32),__builtin_amdgcn_mbcnt_lo((u32)b1,0u)), pf=n2+ln-p1; bool c1=m1>=T;
      if(__builtin_expect(b2!=0ull,0)){ int p2=(int)__builtin_amdgcn_mbcnt_hi((u32)(b2>>32),__builtin_amdgcn_mbcnt_lo((u32)b2,(u32)n1)); if(m2>=T) ST(u32,cw+p2*4)=m2; }
      int ps= c1? p1:pf; ps= ps<8? ps:32-8*w4; ST(u32,cw+ps*4)= c1? m1:1u;
    } else {
      u32 mine=0;
      _Pragma("unroll 1") for(int c=0;c<8;c++){ u32 lm=umax_(umax_(umax_(k[0],k[1]),umax_(k[2],k[3])),umax_(umax_(k[4],k[5]),umax_(k[6],k[7]))); u32 wm=wmax(lm); if(ln==c) mine=wm;
        #pragma unroll
        for(int e=0;e<8;e++) k[e]=(k[e]==wm)?0u:k[e]; }
      if(ln<8) ST(u32,cw+ln*4)=mine; } }
  { u32 c,rk,cnt;
    asm volatile("s_mov_b32 %[cnt], 0x20000\n\tv_mov_b32 %[rk], 0\n\ts_waitcnt vmcnt(0)\n1:\n\tds_read_b32 %[c], %[dC]\n\tds_read_b128 v[112:115], %[dO]\n\tds_read_b128 v[116:119], %[dO] offset:16\n\tds_read_b128 v[120:123], %[dO] offset:32\n\tds_read_b128 v[124:127], %[dO] offset:48\n\t"
      "s_waitcnt lgkmcnt(4)\n\tv_cmp_eq_u32 vcc, 0, %[c]\n\ts_cbranch_vccz 2f\n\ts_sub_u32 %[cnt], %[cnt], 1\n\ts_cbranch_scc0 1b\n2:\n\t"
      "s_waitcnt lgkmcnt(3)\n\t" GT4(112,113,114,115) "s_waitcnt lgkmcnt(2)\n\t" GT4(116,117,118,119) "s_waitcnt lgkmcnt(1)\n\t" GT4(120,121,122,123) "s_waitcnt lgkmcnt(0)\n\t" GT4(124,125,126,127)
      "s_nop 1\n\tv_add_u32_dpp %[rk], %[rk], %[rk] quad_perm:[1,0,3,2] row_mask:0xf bank_mask:0xf"
      : [c]"=&v"(c),[rk]"=&v"(rk),[cnt]"=&s"(cnt),GTS : [dC]"v"(dC),[dO]"v"(dO) : PCLOB);
    rank_=rk; key_=c; }
}

#define FILL_(n,WAIT) "s_mov_b32 m0, %[ld]\n\ts_mov_b32 %[c], " STR(n) "-1\n3:\n\t" WAIT "buffer_load_dwordx4 %[vo], %[rs], %[so] offen nt lds\n\ts_add_u32 m0, m0, 0x1000\n\ts_add_u32 %[so], %[so], %[st]\n\t" \
  "s_sub_u32 %[c], %[c], 1\n\ts_cbranch_scc0 3b\n\ts_mov_b32 %[ld], m0\n\t"
#define FILL(n,WAIT) do{ u32 c_; asm volatile(FILL_(n,WAIT) : [c]"=&s"(c_),[so]"+s"(wso),[ld]"+s"(wld) : [vo]"v"(wvo),[rs]"s"(wrs),[st]"s"(wst) : "memory","scc","m0"); }while(0)
#define FILLD(n,WN,WW_) do{ u32 c_,t_,s_; asm volatile(FILL_(n,"ds_read_b32 %[t], %[ad]\n\ts_waitcnt lgkmcnt(0)\n\tv_readfirstlane_b32 %[s], %[t]\n\ts_cmp_ge_u32 %[s], 4\n\ts_cbranch_scc1 4f\n\t" VW(WN) "4:\n\t" VW(WW_)) \
  : [c]"=&s"(c_),[so]"+s"(wso),[ld]"+s"(wld),[t]"=&v"(t_),[s]"=&s"(s_) : [vo]"v"(wvo),[rs]"s"(wrs),[st]"s"(wst),[ad]"v"(C_D*4) : "memory","scc","m0"); wso=RFL(wso); wld=RFL(wld); }while(0)
#define VW(n) "s_waitcnt vmcnt(" STR(n) ")\n\t"

__global__ NOPK __attribute__((amdgpu_num_vgpr(56),amdgpu_num_sgpr(102))) __launch_bounds__(1024,4) void kA(const u16* __restrict__ x,char* ws,const u16* __restrict__ wr,const u16* __restrict__ Dsk,const u16* __restrict__ wg,const u16* __restrict__ wu,
                                              const u16* __restrict__ wd,u16* __restrict__ out,KP kp){
  __shared__ __attribute__((aligned(16))) char smem[LDS_TOTAL];
  lch* sm=(lch*)smem;
  u32x4 xa,xb_,wa,wb,dq[3]; u32 xc,wc;
  asm volatile(
    "s_load_dwordx16 s[64:79], %[ka], 0x0\n\ts_load_dwordx16 s[80:95], %[ka], 0x40\n\t"
    "v_readfirstlane_b32 s60, %[t]\n\ts_and_b32 s61, %[wg], 7\n\ts_lshr_b32 s62, %[wg], 3\n\tv_and_b32 v118, 31, %[t]\n\ts_bfe_u32 s63, s60, 0x20006\n\ts_cmpk_ge_u32 s60, 0x100\n\ts_cbranch_scc1 1f\n\t"
    "s_setprio 3\n\ts_lshl_b32 s56, s61, 5\n\ts_add_u32 s56, s56, s62\n\ts_mulk_i32 s56, 0x2400\n\tv_lshrrev_b32 v119, 5, %[t]\n\tv_mul_u32_u24 v119, 0x480, v119\n\tv_add_u32 v119, s56, v119\n\t"
    "v_lshl_add_u32 v116, v118, 4, v119\n\tv_lshl_add_u32 v117, v118, 2, v119\n\tv_lshlrev_b32 v114, 4, v118\n\tv_lshlrev_b32 v115, 2, v118\n\t"
    "s_and_b32 s57, s62, 3\n\ts_mulk_i32 s57, 0x120\n\ts_lshl_b32 s58, s63, 5\n\ts_add_u32 s57, s57, s58\n\tv_and_b32 v113, 8, %[t]\n\tv_lshl_add_u32 v113, v113, 1, s57\n\tv_min_u32 v112, 0x370, v113\n\t"
    "v_lshlrev_b32 v119, 1, %[t]\n\tv_mov_b32 v118, 0\n\tds_write_b16 v119, v118\n\t"
    "s_waitcnt lgkmcnt(0)\n\t"
    "s_load_dwordx2 s[96:97], s[66:67], 0x0\n\t"
    "global_load_dwordx4 %[wa], v114, s[68:69]\n\tglobal_load_dwordx4 %[wb], v114, s[68:69] offset:512\n\tglobal_load_dword %[wc], v115, s[68:69] offset:1024\n\t"
    "global_load_dwordx4 %[xa], v116, s[64:65] nt\n\tglobal_load_dwordx4 %[xb], v116, s[64:65] offset:512 nt\n\tglobal_load_dword %[xc], v117, s[64:65] offset:1024 nt\n\t"
    "global_load_dwordx4 %[d0], v113, s[70:71]\n\tglobal_load_dwordx4 %[d1], v113, s[70:71] offset:128\n\tglobal_load_dwordx4 %[d2], v112, s[70:71] offset:256\n\ts_barrier\n\t"
    "s_branch 8f\n"
    "1:\n\ts_cmp_ge_u32 s62, " STR(CPF) "\n\ts_cbranch_scc1 6f\n\ts_getpc_b64 s[56:57]\n.Lkpc:\n\ts_and_b32 s58, s62, 3\n\ts_lshl_b32 s58, s58, 13\n\ts_sub_u32 s58, s58, 0x1000\n\tv_lshl_add_u32 v119, %[t], 4, s58\n\tv_min_u32 v119, .Lkend-.Lkpc-16, v119\n\t"
    "global_load_dwordx4 v[120:123], v119, s[56:57]\n\t.subsection 1\n.Lkend:\n\t.subsection 0\n6:\n\t"
    "v_mov_b32 v124, -1\n\tv_mov_b32 v125, -1\n\tv_mov_b32 v126, -1\n\tv_mov_b32 v127, -1\n\tv_lshlrev_b32 v119, 4, %[t]\n\tv_and_b32 v119, 0xff0, v119\n\t"
    "s_lshr_b32 s59, s60, 8\n\ts_cmp_lt_u32 s62, 8\n\ts_cselect_b32 s58, 2, 1\n\ts_cbranch_scc1 2f\n\ts_cmp_eq_u32 s59, 3\n\ts_cbranch_scc1 4f\n"
    "2:\n\ts_sub_u32 s57, s59, 2\n\ts_cmp_eq_u32 s59, 1\n\ts_cselect_b32 s58, s58, s57\n\ts_cmp_lt_u32 s62, 8\n\ts_cselect_b32 s56, 0, 8\n\ts_lshl1_add_u32 s56, s62, s56\n\ts_mulk_i32 s61, 72\n\ts_add_u32 s51, s56, s61\n\ts_sub_u32 s56, 2, s59\n\ts_sub_u32 s57, 16, s62\n\ts_cmp_eq_u32 s59, 3\n\ts_cselect_b32 s50, s57, s56\n\ts_add_u32 s56, s50, s51\n\t"
    "v_mov_b32 v112, 0\n\tv_mov_b32 v113, 0\n\tv_mov_b32 v114, 0\n\tv_mov_b32 v115, 0\n\tv_mov_b32 v116, 0\n\tv_mov_b32 v117, 0\n\ts_setprio 2\n\t"
    "s_lshl_b32 s57, s56, 2\n\ts_add_u32 s57, s57, s63\n\ts_lshl_b32 s57, s57, 6\n\tv_lshl_add_u32 v22, v118, 1, s57\n\ts_mul_i32 s57, s56, %[yst]\n\ts_add_u32 s57, s57, %[wy]\n\tv_add_u32 v23, s57, v119\n\ts_lshl_b32 s56, s56, 1\n\ts_and_b32 s56, s56, -4\n\t"
    "s_waitcnt lgkmcnt(0)\n\t"
    "global_load_short_d16_hi v112, v22, s[80:81] nt\n\tglobal_load_short_d16_hi v113, v22, s[82:83] nt\n\tglobal_load_short_d16_hi v114, v22, s[84:85] nt\n\t"
    "global_load_short_d16_hi v115, v22, s[86:87] nt\n\tglobal_load_short_d16_hi v116, v22, s[88:89] nt\n\tglobal_load_short_d16_hi v117, v22, s[90:91] nt\n\t"
    "s_load_dwordx2 s[96:97], s[66:67], 0x0\n\ts_load_dword s98, s[92:93], s56\n\t"
    "s_barrier\n\t" "global_store_dwordx4 v23, v[124:127], s[66:67] sc1\n\t"
    "s_mul_i32 s52, s58, %[sl]\n\ts_add_u32 s52, s52, %[tb]\n\ts_lshl_b32 s53, s63, 7\n\ts_add_u32 s53, s53, s52\n\tv_lshl_add_u32 v30, v118, 2, s53\n\tv_bfe_u32 v31, %[t], 5, 1\n\ts_movk_i32 s54, 528\n\tv_mul_u32_u24 v29, s54, v31\n\t"
    "v_lshl_add_u32 v2, v29, 3, v30\n\ts_movk_i32 s53, 1056\n\tv_add_u32 v3, s53, v2\n\tv_add_u32 v4, s53, v3\n\tv_add_u32 v5, s53, v4\n\tv_add_u32 v6, 41*528, v2\n\tv_add_u32 v7, s53, v6\n\tv_add_u32 v8, s53, v7\n\tv_add_u32 v9, s53, v8\n\t"
    "v_lshl_add_u32 v10, v29, 2, v30\n\tv_add_u32 v10, 16*528, v10\n\tv_add_u32 v11, s53, v10\n\tv_lshl_add_u32 v12, v29, 1, v30\n\tv_add_u32 v12, 24*528, v12\n\tv_add_u32 v13, 2112, v12\n\tv_add_u32 v14, v29, v30\n\tv_add_u32 v14, 32*528, v14\n\t"
    "s_lshl_b32 s53, s58, 4\n\ts_add_u32 s53, s53, %[ex]\n\tv_mov_b32 v15, s53\n\tv_cvt_f32_ubyte0 v31, v31\n\tv_mul_f32 v16, 0x42800000, v31\n\tv_add_f32 v17, v16, v16\n\tv_mov_b32 v20, 1\n\t"
    "v_and_b32 v29, 15, %[t]\n\tv_and_b32 v30, 48, %[t]\n\tv_mad_u32_u24 v30, v29, s54, v30\n\ts_lshl_b32 s55, s59, 1\n\ts_lshr_b32 s55, 0x48, s55\n\ts_add_u32 s55, s55, s63\n\ts_and_b32 s55, s55, 3\n\ts_mul_i32 s53, s55, 16*528\n\ts_movk_i32 s57, 41*528\n\ts_cmp_eq_u32 s55, 2\n\ts_cselect_b32 s53, s57, s53\n\ts_cselect_b32 s57, 32*528, s57\n\t"
    "s_add_u32 s53, s53, s52\n\ts_add_u32 s57, s57, s52\n\tv_add_u32 v18, s53, v30\n\tv_add_u32 v19, s57, v30\n\t"
    "v_min_u32 v29, 237*16, v119\n\tv_subrev_u32 v30, 204*16, v29\n\tv_add_u32 v29, %[de], v29\n\tv_min_u32 v29, v29, v30\n\ts_sub_u32 s53, s52, 4640\n\tv_add_u32 v29, s53, v29\n\tv_mov_b32 v24, 0\n\tv_mov_b32 v25, 0\n\tv_mov_b32 v26, 0\n\tv_mov_b32 v27, 0\n\tds_write_b128 v29, v[24:27]\n\t"
    "s_cmp_lg_u32 s55, 3\n\ts_cbranch_scc1 9f\n\t"
    "v_bfe_u32 v18, %[t], 1, 5\n\tv_and_b32 v19, 1, %[t]\n\ts_cmp_lt_u32 s62, 8\n\ts_cselect_b32 s53, 2, 1\n\ts_mul_i32 s53, s53, %[sl]\n\tv_mul_u32_u24 v21, s53, v19\n\ts_sub_u32 s55, 2, s59\n\ts_lshl_b32 s55, s55, 7\n\tv_add_u32 v18, s55, v18\n\ts_cmp_lg_u32 s59, 3\n\ts_cbranch_scc1 7f\n\t"
    "v_bfe_u32 v18, %[t], 5, 1\n\tv_lshl_add_u32 v18, v18, 7, v118\n\tv_mov_b32 v19, s50\n\tv_mov_b32 v21, %[sl]\n"
    "7:\n\tv_add_u32 v19, s51, v19\n\tv_lshl_add_u32 v21, v18, 4, v21\n\tv_lshrrev_b32 v24, 3-" STR(TL) ", v19\n\tv_and_b32 v19, 7>>" STR(TL) ", v19\n\tv_lshlrev_b32 v19, 4, v19\n\tv_lshl_add_u32 v19, v24, 7, v19\n\t"
    "v_and_b32 v24, (1<<" STR(TL) ")-1, v18\n\tv_lshl_add_u32 v19, v24, 7-" STR(TL) ", v19\n\tv_lshrrev_b32 v18, " STR(TL) ", v18\n\tv_mul_u32_u24 v18, %[ust], v18\n\tv_add_u32 v18, v18, v19\n\tv_add_u32 v19, 512, v21\n\t"
    "s_branch 9f\n"
    "4:\n\t"
    "v_bfe_u32 v118, %[t], 3, 5\n\tv_lshrrev_b32 v117, 1, v118\n\tv_and_b32 v117, 4, v117\n\tv_and_b32 v116, 2, v118\n\tv_and_b32 v115, 7, %[t]\n\tv_or_b32 v116, v116, v117\n\tv_xor_b32 v115, v115, v116\n\tv_lshlrev_b32 v115, 4, v115\n\t"
    "s_mul_i32 s54, s61, 9\n\ts_sub_u32 s55, s62, 14\n\ts_lshr_b32 s55, s55, 1\n\ts_add_u32 s54, s54, s55\n\ts_lshl_b32 s54, s54, 7\n\t"
    "s_mul_i32 s52, s61, 0xa2000\n\ts_mul_i32 s53, s62, 0x180\n\ts_add_u32 s52, s52, s53\n\ts_cmp_ge_u32 s62, 11\n\ts_cselect_b32 s53, 0x51000-0x480, 0\n\ts_add_u32 s52, s52, s53\n\ts_sub_u32 s52, s52, 0xc00\n\t"
    "s_movk_i32 s55, 0x480\n\ts_cmp_ge_u32 s62, 14\n\ts_cselect_b32 s54, s54, s52\n\ts_cselect_b32 s55, 0x2400, s55\n\tv_mad_u32_u24 v118, v118, s55, v115\n\ts_lshl_b32 s55, s55, 5\n\t"
    "s_lshl_b32 s52, s63, 10\n\ts_add_u32 m0, s52, %[r2]\n\ts_mov_b32 s58, 0x510000\n\ts_mov_b32 s59, 0x20000\n\ts_cmp_ge_u32 s62, 14\n\ts_cselect_b32 s53, " STR(PRE1) "-1, " STR(PRE2) "-1\n\ts_setprio 3\n\t"
    "s_waitcnt lgkmcnt(0)\n\t"
    "s_bitcmp1_b32 %[wg], 3\n\ts_cselect_b64 s[56:57], s[74:75], s[72:73]\n\ts_cmp_ge_u32 s62, 14\n\ts_cselect_b64 s[56:57], s[56:57], s[76:77]\n\ts_cselect_b32 s52, 0x180, -1\n\ts_and_b32 s57, s57, 0xffff\n\ts_add_u32 s60, s56, s54\n\ts_and_b32 s60, s60, 0x380\n\ts_cmp_eq_u32 s60, s52\n\ts_cselect_b32 s53, " STR(PRE3) "-1, s53\n\t"
    "s_load_dwordx2 s[96:97], s[66:67], 0x0\n\ts_barrier\n\t"
    "\n3:\n\t" "buffer_load_dwordx4 v118, s[56:59], s54 offen nt lds\n\ts_add_u32 m0, m0, 0x1000\n\ts_add_u32 s54, s54, s55\n\ts_sub_u32 s53, s53, 1\n\ts_cbranch_scc0 3b\n"
    "9:\n8:"
    : [xa]"=&{v[2:5]}"(xa),[xb]"=&{v[6:9]}"(xb_),[xc]"=&{v30}"(xc),[wa]"=&{v[10:13]}"(wa),[wb]"=&{v[14:17]}"(wb),[wc]"=&{v31}"(wc),[d0]"=&{v[18:21]}"(dq[0]),[d1]"=&{v[22:25]}"(dq[1]),[d2]"=&{v[26:29]}"(dq[2])
    : [ka]"s"(__builtin_amdgcn_kernarg_segment_ptr()),[wg]"s"(BID),[t]"v"(TID),[yst]"i"(YST),[wy]"i"(W_Y),[r2]"i"(O_R2),[sl]"i"(SLSZ),[tb]"i"(O_CH+O_TB),[ex]"i"(O_EXP),[de]"i"(O_DE),[ust]"i"(GST_)
    : "memory","scc","m0","v127","s50","s51","s52","s53","s54","s55","s56","s57","s58","s59","s60","s61","s62","s63","s64","s65","s66","s67","s68","s69","s70","s71","s72","s73","s74","s75","s76","s77","s78","s79",
      "s80","s81","s82","s83","s84","s85","s86","s87","s88","s89","s90","s91","s92","s93","s94","s95","s99");
  int wv=RFL(TID>>6), bid=BID, q4=wv>>2, w4=wv&3;
  int xg=bid&7, lb=bid>>3, tb=32*xg+lb, nch=2+(lb<8);
  u64 ep; u32 ldw;
  {
  }
  #define KILLIN "=v"(xa),"=v"(xb_),"=v"(xc),"=v"(wa),"=v"(wb),"=v"(wc),"=v"(dq[0]),"=v"(dq[1]),"=v"(dq[2])
  if(__builtin_expect(q4==3&&lb>=8,0)){
    int fu=18*xg+lb-14, kq=2*xg+(lb>=11), cgp=lb-8-3*(lb>=11);
  u32 wst= lb>=14? F_*64:D_*64;
  u32 wso=(lb>=14? (fu>>1)*128 : kq*(288*D_*2)+384*cgp)+(lb>=14? PRE1:PRE2)*wst, wld=(u32)(size_t)sm+O_R2+w4*1024+(lb>=14? PRE1:PRE2)*4096; u32x4 wrs=mkrs(lb>=14? ((fu&1)? wu:wg):wd,D_*F_*2);
  GETEP
  #define FLANE(RS) LANE(ln); int g=ln>>4, li=ln&15, qq=li>>2, pq=li&3; u32 wvo=(u32)(8*w4+(ln>>3))*(RS)+(u32)((ln&7)^(((ln>>3)&2)|((w4&1)<<2)))*16u; \
    const lch* tp=sm+O_R2+(8*g+qq)*128+((w4^((qq>>1)|((g&1)<<1)))*32)+pq*8;
  #define WFR(img,ks) trfrag(tp+(img)*CHSZ+(ks)*4096,tp+(img)*CHSZ+(ks)*4096+512)
  u64 tag=ep+1ull; const u32x4 I4={INV,INV,INV,INV};
  if(lb>=14){
    FLANE(F_*2)
    int mt=fu&1;
    if(w4==0) ST(u32x4,sm+O_ONE+ln*16)=u32x4{0x3f803f80u,0x3f803f80u,0x3f803f80u,0x3f803f80u}; else if(ln<8) ST(u32x4,sm+O_ONE+1024+ln*16)=u32x4{0x3f803f80u,0x3f803f80u,0x3f803f80u,0x3f803f80u};
    const lch* xp=sm+(li<8? O_TILE+li*XSTR : O_ONE)+g*16; int of=(mt*288+(fu>>1)*4+w4)*256+(li&7)*32+g*8+(li>>3)*4;
    const lch* mp=sm+O_RC+(li&7)*4;
    asm volatile("" : "+v"(xp),"+v"(of),"+v"(mp));
    rsrc_t rsG=RSRC(ws+W_GU,2*8*F_*2);
    if(w4==0) BST128(I4,rsG,(mt*288+(fu>>1)*4)*256+ln*16,0,SC1);
    bool slow=(((u32)wrs[0]+wso)&0x380u)==0x180u;
    if(slow){ wso-=(PRE1-PRE3)*wst; wld-=(PRE1-PRE3)*4096; FILL(5+PRE1-PRE3,VW(W3)); } else FILLD(5,W1,W1B);
    if(w4==0){ VMW(5); if(ln==0) GST(ws+W_FL+(576+fu)*8,tag); }
    if(slow) FILLD(13-PRE1,W3,W1B); else FILLD(13-PRE1,W1,W1B);
    bf16x8 fB[18];
    VMW(LAST1); gsync(sm,C_P,4);
    #pragma unroll
    for(int ks=0;ks<18-LAST1;ks++) fB[ks]=WFR(0,ks);
    #pragma unroll
    for(int ks=0;ks<18-LAST1;ks++) asm volatile("" : "+v"(fB[ks]));
    DRAIN(); gsync(sm,C_P,8);
    #pragma unroll
    for(int ks=18-LAST1;ks<18;ks++) fB[ks]=WFR(0,ks);
    f32x4 a0={0,0,0,0}; float nm,ri,ce; u32 mdw;
    { bf16x8 xb[18];
      gwait<2>(sm,C_X,1);
      gwait4<64>(C_X,4,xp,xb[0],xb[1],xb[2],xb[3]);
      #pragma unroll
      for(int ks=4;ks<7;ks++) xb[ks]=LD(bf16x8,xp+ks*64);
      SCHED();
      #pragma unroll
      for(int ks=0;ks<17;ks++){
        a0=__builtin_amdgcn_mfma_f32_16x16x32_bf16(fB[ks],xb[ks],a0,0,0,0); SCHED();
        if(2*ks+7<18) xb[2*ks+7]=LD(bf16x8,xp+(2*ks+7)*64); if(2*ks+8<18) xb[2*ks+8]=LD(bf16x8,xp+(2*ks+8)*64);
        if(ks==5) mdw=LD(u32,mp); if(ks==14){ ri=rcpf_(bhi(mdw)); nm=-blo(mdw); PIN(nm); } if(ks==15){ ce=ri*-1.4426950408889634f; PIN(ce); }
        SCHED(); }
      #define RR8 " row_ror:8 row_mask:0xf bank_mask:"
      float v0,v1,s0,s1;
      asm volatile("v_mfma_f32_16x16x32_bf16 v[112:115], %4, %5, %6\n\ts_nop 7\n\tv_fmac_f32_dpp v114, v114, %7" RR8 "0xf\n\tv_fmac_f32_dpp v115, v115, %7" RR8 "0xf\n\tv_fmac_f32_dpp v112, v112, %7" RR8 "0xf\n\tv_fmac_f32_dpp v113, v113, %7" RR8 "0xf\n\t"
        "v_mov_b32_dpp v112, v114" RR8 "0xc\n\tv_mov_b32_dpp v113, v115" RR8 "0xc\n\tv_mul_f32 %2, %9, v112\n\tv_mul_f32 %3, %9, v113\n\tv_mul_f32 %0, %8, v112\n\tv_mul_f32 %1, %8, v113"
        : "=&v"(v0),"=&v"(v1),"=&v"(s0),"=&v"(s1) : "v"(fB[17]),"v"(xb[17]),"v"(a0),"v"(nm),"v"(ri),"v"(ce));
      if(mt==0){ v0*=rcpf_(1.f+__builtin_amdgcn_exp2f(s0)); v1*=rcpf_(1.f+__builtin_amdgcn_exp2f(s1)); }
      __builtin_amdgcn_raw_buffer_store_b32(nv(pk(v0,v1)),rsG,of,0,SC1); garrive(sm,C_H); }
    if(mt==0&&w4==0){
    LANE(l2); ln=l2; li=ln&15; g=ln>>4;
    int c9=(lb-14)>>1, t=RFL((int)LD(u16,sm+O_IDX+xg*2)), c=64*c9+4*li+g; f32x4 pr;
    { u32 u=LD(u16,sm+O_IDX+16+xg*2)^0x8000u, xb=LD(u16,sm+O_TILE+xg*XSTR+c*2), sv=LD(u32,sm+O_RC+xg*4); float ds=b2f(Dsk[c]); u^=(u32)(((int)(u<<16))>>31)&0x7fffu;
      poll1<0>(ws+W_FL+(64*c9+ln)*8,tag); poll1<0>(ws+W_FL+(720+(ln<48? ln:47))*8,tag);
      float xn=(b2f(xb)-blo(sv))*rcpf_(bhi(sv)); pr=f32x4{b2f(xb),xn,xn*ds,sigm_f(b2f(u))}; }
    gwait<2>(sm,C_PR,1);
    { u32x4 rs=mkrs(ws+W_PART+(xg*9+c9)*4096,4096), ry=mkrs(ws+W_Y,D_*YST);
      int yo=c*YST+(t&~1)*2, po=ln*16; u32 a[4], t0,cnt,yb; u16* op=out+t*D_+c;
      #define LD_P "buffer_load_dwordx4 v[112:115], %[of], %[rs], 0 offen sc1\n\tbuffer_load_dwordx4 v[116:119], %[of], %[rs], 0 offen offset:1024 sc1\n\tbuffer_load_dwordx4 v[120:123], %[of], %[rs], 0 offen offset:2048 sc1\n\tbuffer_load_dwordx4 v[124:127], %[of], %[rs], 0 offen offset:3072 sc1\n\t"
      #define LD_YB "buffer_load_dword %[yb], %[yo], %[ry], 0 offen sc1\n\t"
      #define AD4(o,a,b,c,d) "v_add_f32 %[t0], v" #a ", v" #b "\n\tv_add_f32 %[" #o "], v" #c ", v" #d "\n\tv_add_f32 %[" #o "], %[t0], %[" #o "]\n\t"
      u64 ym;
      asm volatile(LD_YB LD_P "s_mov_b32 %[cnt], 0x20000\n1:\n\ts_waitcnt vmcnt(0)\n\tv_cmp_eq_u32 %[ym], -1, %[yb]\n\t" CHK_I0 MX2(116,117) MX2(118,119) MX2(120,121) MX2(122,123) MX2(124,125) MX2(126,127) CHK_IE
        "s_or_b64 exec, vcc, %[ym]\n\ts_cbranch_scc0 9f\n\ts_sleep " STR(QSLP) "\n\ts_mov_b64 exec, %[ym]\n\ts_cbranch_execz 2f\n\t" LD_YB "2:\n\ts_mov_b64 exec, vcc\n\ts_cbranch_execz 3f\n\t" LD_P
        "3:\n\ts_mov_b64 exec, -1\n\ts_sub_u32 %[cnt], %[cnt], 1\n\ts_cbranch_scc0 1b\n9:\n\ts_mov_b64 exec, -1\n\t"
        AD4(a0,112,116,120,124) AD4(a1,113,117,121,125) AD4(a2,114,118,122,126) AD4(a3,115,119,123,127) "s_nop 1"
        : [t0]"=&v"(t0),[cnt]"=&s"(cnt),[a0]"=&v"(a[0]),[a1]"=&v"(a[1]),[a2]"=&v"(a[2]),[a3]"=&v"(a[3]),[yb]"=&v"(yb),[ym]"=&s"(ym) : [of]"v"(po),[rs]"s"(rs),[yo]"v"(yo),[ry]"s"(ry),"v"(op),"v"(pr) : PCLOB);
      auto s0=__builtin_amdgcn_permlane32_swap(a[0],a[2],false,false); auto s1=__builtin_amdgcn_permlane32_swap(a[1],a[3],false,false);
      u32 x0=s0[0],x1=s0[1],y0=s1[0],y1=s1[1]; u32 p0=BC(u32,BC(float,x0)+BC(float,x1)), p1=BC(u32,BC(float,y0)+BC(float,y1));
      auto s2=__builtin_amdgcn_permlane16_swap(p0,p1,false,false); u32 z0=s2[0], z1=s2[1]; float s=BC(float,z0)+BC(float,z1);
      float gq=silu_f(__builtin_fmaf(s,pr[3],pr[1]));
      float sy=((t&1)? bhi(yb):blo(yb))+pr[2];
      float o=__builtin_fmaf(sy,gq,pr[0]);
      *op=(u16)(pk(o,o)&0xffffu);
      if(bid==112&&ln==0) GST(ws+W_EPOCH,tag); }
    }
  } else
  {
    FLANE(D_*2)
    bf16x8 fr[27];
    rsrc_t rsP=RSRC(ws+W_PART+cgp*12288+kq*256+w4*64,8*9*4096-2*12288);
    #pragma unroll
    for(int i=0;i<3;i++) BST128(I4,rsP,li<8? li*(9*4096)+g*16+i*4096 : OOR,0,SC1);
    if(PRE2<9) FILLD(9-PRE2,W2_,W2B);
    wso+=128u-9u*wst;
    FILLD(9,W2_,W2B);
    VMW(9); gsync(sm,C_P,4);
    #pragma unroll
    for(int ks=0;ks<9;ks++) fr[ks]=WFR(0,ks);
    gsync(sm,C_P,8); if(w4==0&&ln==0) GST(ws+W_FL+(720+6*xg+lb-8)*8,tag);
    wso+=128u-9u*wst; wld-=2*CHSZ;
    FILL(9,VW(W2B));
    int fo=W_FL+(576+((9*kq)&~1)+(ln<9? ln:9))*8;
    if(w4==0) asm volatile("global_load_dwordx2 v[112:113], %0, %1 sc1\n\ts_waitcnt vmcnt(10)" :: "v"(fo),"s"(ws) : "memory"); else VMW(9);
    gsync(sm,C_P,12);
    #pragma unroll
    for(int ks=0;ks<9;ks++) fr[9+ks]=WFR(1,ks);
    { int t2=TID; asm volatile("" : "+v"(t2)); ln=t2&63; li=ln&15; g=ln>>4; }
    const lch* hp=sm+O_TILE+(li&7)*64+g*16; int po= li<8? li*(9*4096)+g*16+4096 : OOR; PIN(hp); PIN(po);
    u32x4 rs=mkrs(ws+W_GU,2*8*F_*2);
    int of=((ln>>5)*288+18*kq+2*w4)*256+(ln&31)*16; lch* hd=sm+O_TILE+w4*512+((ln>>1)&7)*64+((ln>>4)&1)*32+(ln&1)*16+(ln>>5)*8; PIN(of); PIN(hd); asm volatile("" : "+s"(rs));
    DRAIN();
    if(w4==0){ u64 bad; asm volatile("v_cmp_ne_u64 %0, %1, v[112:113]" : "=s"(bad) : "s"(tag) : "memory");
      if(bad!=0ull) poll1<0>(ws+fo,tag); }
    gsync(sm,C_P,16);
    { u32 t0,cnt;
      #define LD_G "buffer_load_dwordx4 v[112:115], %[of], %[rs], 0 offen sc1\n\tbuffer_load_dwordx4 v[116:119], %[of], %[rs], 0 offen offset:2048 sc1\n\tbuffer_load_dwordx4 v[120:123], %[of], %[rs], %[so] offen sc1\n\t"
      #define HM1(a,b) "v_lshlrev_b32 %[t0], 16, v" #a "\n\tv_lshlrev_b32 %[t1], 16, v" #b "\n\tv_and_b32 v" #a ", %[hm], v" #a "\n\tv_and_b32 v" #b ", %[hm], v" #b "\n\tv_mul_f32 %[t0], %[t0], %[t1]\n\tv_mul_f32 v" #a ", v" #a ", v" #b "\n\tv_cvt_pk_bf16_f32 v" #a ", %[t0], v" #a "\n\t"
      #define HMUL(a,b,c,d,o) "v_permlane32_swap_b32 v" #a ", v" #c "\n\tv_permlane32_swap_b32 v" #b ", v" #d "\n\t" HM1(a,c) HM1(b,d) "ds_write_b64 %[hd], v[" #a ":" #b "] offset:" #o "\n\t"
      u32 t1;
      asm volatile(LD_G :: [of]"v"(of),[rs]"s"(rs),[so]"s"(w4==0? 4096:0) : "memory");
      fr[18]=WFR(0,0); PIN(fr[18]);
      asm volatile(POLLASM(LD_G,CHK_I0 MX2(116,117) MX2(118,119) MX2(120,121) MX2(122,123) CHK_IE,GSLP)
        HMUL(112,113,114,115,0) HMUL(116,117,118,119,2048) "s_cmp_eq_u32 %[so], 0\n\ts_cbranch_scc1 8f\n\t" HMUL(120,121,122,123,4096) "8:\n\ts_mov_b64 exec, 1\n\tds_add_u32 %[ch], %[one]\n\ts_mov_b64 exec, -1"
        : [t0]"=&v"(t0),[t1]"=&v"(t1),[cnt]"=&s"(cnt) : [of]"v"(of),[rs]"s"(rs),[so]"s"(w4==0? 4096:0),[hd]"v"(hd),[hm]"s"(0xffff0000u),[ch]"v"(C_H*4),[one]"v"(1),"v"(hp),"v"(po) : PCLOB);
      }
    { f32x4 ac[3]={{0,0,0,0},{0,0,0,0},{0,0,0,0}};
      bf16x8 hb[9];
      gwait4<512>(C_H,4,hp,hb[0],hb[1],hb[2],hb[3]);
      #pragma unroll
      for(int ks=0;ks<10;ks++){
        if(ks<9){ ac[0]=__builtin_amdgcn_mfma_f32_16x16x32_bf16(fr[ks],hb[ks],ac[0],0,0,0); ac[1]=__builtin_amdgcn_mfma_f32_16x16x32_bf16(fr[9+ks],hb[ks],ac[1],0,0,0); }
        if(ks>0) ac[2]=__builtin_amdgcn_mfma_f32_16x16x32_bf16(fr[17+ks],hb[ks-1],ac[2],0,0,0);
        SCHED();
        if(ks<4){ fr[19+2*ks]=WFR(0,1+2*ks); fr[20+2*ks]=WFR(0,2+2*ks); } if(ks>=1&&ks<=2){ hb[2*ks+2]=LD(bf16x8,hp+(2*ks+2)*512); hb[2*ks+3]=LD(bf16x8,hp+(2*ks+3)*512); } if(ks==3) hb[8]=LD(bf16x8,hp+8*512);
        SCHED(); }
      rsrc_t r0=RSRC(ws+W_PART+cgp*12288+kq*256+w4*64-4096,8*9*4096), r2=RSRC(ws+W_PART+cgp*12288+kq*256+w4*64+4096,8*9*4096-2*12288);
      BST128(nv4(BC(u32x4,ac[0])),r0,po,0,SC1); BST128(nv4(BC(u32x4,ac[1])),rsP,po,0,SC1); BST128(nv4(BC(u32x4,ac[2])),r2,po,0,SC1); }

  }
  asm volatile("s_endpgm" : KILLIN :: "memory"); }
  {
  if(__builtin_expect(q4!=0,0)){ int grp= q4==1? nch-1 : q4-2;
    chan(sm,grp,(w4+(0x48>>(2*q4)))&3,72*xg+(q4==3? 16+lb : 2*lb+(lb<8? 0:8)+2-q4),(q4+1)>>2,ws,xa,xb_,wa,wb,dq[0]);
    asm volatile("s_endpgm" : KILLIN :: "memory"); }
  u32x4 xq[3]; f32x2 rc; int xr_,xr2_;
  {
    LANE(ln); int g=ln>>4, li=ln&15, qq=li>>2, pq=li&3, j=ln&31, rl=2*w4+(ln>>5);
    lch* tile=sm+O_TILE; u32 Tr=(ln>>1)&16;
    int uo=W_U+(tb>>TL)*GST_+(tb&((1<<TL)-1))*(128>>TL)+(w4*1024<<TL)+(ln>>(3-TL))*128+(ln&(7>>TL))*16, uo1=uo+(4096<<TL), uo2=uo+(8192<<TL); PIN(uo); PIN(uo1); PIN(uo2);
    lch* tw=tile+rl*TSTR2+j*16; lch* tw2=tw+1024-j*12; u32 one=0x3f803f80u; const lch* tp=tile+qq*TSTR2+(w4*64+g*16+pq*4)*2;
    int go= ln<32? W_RG+(ln>>2)*8192+(tb*4+(ln&3))*8 : W_ST+(tb*8+(ln&7))*8; lch* kw=sm+O_RV+rl*2; const lch* kr=sm+(ln<32? O_RV+(ln&3)*4 : O_MD+(ln&7)*4); PIN(kw); PIN(kr);
    int xr=(32*(tb>>2)+8*g+(li&7))*(D_*2)+288*(lb&3)+32*w4+(ln&8)*2, xr2= w4==0? xr+256:OOR;
    rsrc_t rx=RSRC(x,L_*D_*2); u64 gm=((1ull<<(4*NRG))-1ull)|0xff00000000ull; u32 um=~UMASK, km=0x80008000u; asm volatile("" : "+s"(gm),"+s"(um),"+s"(km),"+s"(ws));
    #define D2(a,b,c) __builtin_amdgcn_fdot2_f32_bf16(BC(bf2,a),BC(bf2,b),c,false)
    asm volatile("s_waitcnt vmcnt(6)" : "+v"(wa),"+v"(wb),"+v"(wc),"+v"(Tr),"+v"(tw),"+v"(tw2),"+v"(tp),"+v"(one),"+v"(go),"+v"(xr),"+v"(xr2) :: "memory");
    u32 wq[9]={wa[0],wa[1],wa[2],wa[3],wb[0],wb[1],wb[2],wb[3],wc}; float sw;
    { float a[3];
      #pragma unroll
      for(int i=0;i<9;i++) a[i%3]=D2(wq[i],one,i<3? 0.f:a[i%3]);
      sw=add16x(sum16((a[0]+a[1])+a[2])); }
    GETEP u32 tg=(u32)ep+1u; Tr=((upat(tg,w4)>>Tr)&0xffffu)*0x10001u; u32 tgv=tg;
    float z_[9]={0,0,0,0,0,0,0,0,0};
    asm volatile("s_waitcnt vmcnt(3)" : "+v"(xa),"+v"(xb_),"+v"(xc),"+v"(Tr),"+v"(sw),"+v"(tgv),"+v"(z_[0]),"+v"(z_[1]),"+v"(z_[2]),"+v"(z_[3]),"+v"(z_[4]),"+v"(z_[5]) :: "memory");
    u32 v[9]={xa[0],xa[1],xa[2],xa[3],xb_[0],xb_[1],xb_[2],xb_[3],xc};
    { float xf[18], s1, s2, s3;
      #pragma unroll
      for(int i=0;i<9;i++){ z_[i&1]=D2(v[i],one,z_[i&1]); z_[2+(i&1)]=D2(v[i],v[i],z_[2+(i&1)]); z_[4+(i&1)]=D2(v[i],wq[i],z_[4+(i&1)]); }
      s1=z_[0]+z_[1]; s2=z_[2]+z_[3]; s3=z_[4]+z_[5];
      SCHED();
      #define RSTEP(C) s1+=dppf<C>(s1); s2+=dppf<C>(s2); s3+=dppf<C>(s3); SCHED();
      RSTEP(0xB1) RSTEP(0x4E) RSTEP(0x141) RSTEP(0x140)
      s1=add16x(s1)*(1.f/576.f); s2=add16x(s2)*(1.f/576.f); s3=add16x(s3);
      float mm=s1*s1, var=s2-mm;
      if(__builtin_expect(BALLOT(!(mm<=64.f*var))!=0ull,0)){
        float q=0.f;
        #pragma unroll
        for(int i=0;i<9;i++){ u32 t=v[i]; PIN(t); float d0=blo(t)-s1, d1=bhi(t)-s1; q=__builtin_fmaf(d0,d0,q); q=__builtin_fmaf(d1,d1,q); }
        var=add16x(sum16(q))*(1.f/576.f); }
      #define UNP(i) PIN(v[i]); xf[2*(i)]=blo(v[i]); xf[2*(i)+1]=bhi(v[i]); PIN(xf[2*(i)]); PIN(xf[2*(i)+1]); SCHED();
      u32 vb=pk(var,s1); UNP(0)
      float den=blo(vb)+1e-5f, m=bhi(vb); UNP(1)
      vb=pk(den,den); s3=__builtin_fmaf(-m,sw,s3); UNP(2)
      den=__builtin_amdgcn_sqrtf(bhi(vb)); UNP(3)
      u32 md=pk(m,den); UNP(4)
      float ri=rcpf_(bhi(md)); UNP(5)
      s3*=ri; float rs_=ri*0.0625f; UNP(6)
      u32 u=pk(s3,s3); float cs_=-m*rs_; UNP(7)
      { u32 sg=(u32)((int)u>>31); PIN(sg); u^=sg|km; } UNP(8)
      asm volatile("s_mov_b64 exec, 0x1\n\ts_mov_b32 exec_hi, 1\n\tds_write_b16 %0, %1\n\tv_add_u32 %0, %0, %0\n\tds_write_b32 %0, %2 offset:%3\n\ts_mov_b32 exec_hi, 0\n\tds_add_u32 %4, %5\n\ts_mov_b64 exec, -1" : "+v"(kw) : "v"(u),"v"(md),"i"(O_MD-2*O_RV),"v"(C_I*4),"v"(1) : "memory");
      if(w4==3){ gwait<0>(sm,C_I,4); asm volatile("ds_read_b32 v118, %4\n\tv_mov_b32 v119, %1\n\ts_mov_b64 exec, %3\n\ts_waitcnt lgkmcnt(0)\n\tglobal_store_dwordx2 %0, v[118:119], %2 sc1\n\ts_mov_b64 exec, -1" :: "v"(go),"v"(tgv),"s"(ws),"s"(gm),"v"(kr) : "memory"); }
      SCHED();
      u32 h[9];
      #define UH(i) h[i]=(pkz(__builtin_fmaf(xf[2*(i)],rs_,cs_),__builtin_fmaf(xf[2*(i)+1],rs_,cs_))&um)|Tr;
      UH(0) UH(1) UH(2) UH(3) ST(u32x4,tw)=u32x4{h[0],h[1],h[2],h[3]}; UH(4) UH(5) UH(6) UH(7) ST(u32x4,tw+512)=u32x4{h[4],h[5],h[6],h[7]};
      UH(8) ST(u32,tw2)=h[8]; }
    gsync(sm,C_L,4);
    { u32x4 uq[3];
      #pragma unroll
      for(int z=0;z<3;z++) uq[z]=BC(u32x4,trfrag(tp+z*512,tp+z*512+4*TSTR2));
      asm volatile("global_store_dwordx4 %0, %1, %2 sc1" :: "v"(uo),"v"(uq[0]),"s"(ws) : "memory");
      asm volatile("global_store_dwordx4 %0, %1, %2 sc1" :: "v"(uo1),"v"(uq[1]),"s"(ws) : "memory");
      asm volatile("s_cmp_lg_u32 %3, 0\n\ts_cbranch_scc1 1f\n\tglobal_store_dwordx4 %0, %1, %2 sc1\n1:\n\ts_nop 1" :: "v"(uo2),"v"(uq[2]),"s"(ws),"s"(w4),"v"(uq[0]),"v"(uq[1]) : "memory","scc");
      asm volatile("" : "+v"(dq[0]),"+v"(dq[1]),"+v"(dq[2]));
      xr_=xr; xr2_=xr2;
      #define XQ { rsrc_t rx_=RSRC(x,L_*D_*2); xq[0]=BLD128(rx_,xr_,0,0); xq[1]=BLD128(rx_,xr_+128,0,0); xq[2]=BLD128(rx_,xr2_,0,0); }
    }
    if(lb>=2&&lb<8){
      const u16* wm=(lb&1)? wu:wg; int co=(3-(int)((u64)wm>>7)-xg)&7, r_=576-12*PFN+(((lb>>1)-1)*4+w4)+12*j;
      asm volatile("buffer_load_dword v120, %0, %1, 0 offen" :: "v"(j<PFN&&(ln<32||co==0)? r_*(F_*2)+(9*xg+co)*128+(ln>>5)*1024 : OOR),"s"(mkrs(wm,D_*F_*2)) : "memory"); }
    if(lb>=8&&lb<14) garrive(sm,C_D);
  }
  FP fp;
  { LANE(ln); int g=ln>>4, li=ln&15; u64 tag=ep+1ull;
    const char* sp_=ws+W_ST+(32*(tb>>2)+8*g+(li&7))*8; u64 fs, ff; const char* fp_=ws+W_FL+(144*(lb&3)+16*w4+64*(g<2||w4==0? (g<3? g:2):0)+li)*8;
    if(lb==CTLB||lb>=14){
      u32 M=(u32)tag, rank,key;
      top8(sm,w4,ln,M,ws,rank,key);
      {
        int t0=__builtin_amdgcn_readlane((int)key,__builtin_ctzll(BALLOT(rank==(u32)(2*w4)))), t1=__builtin_amdgcn_readlane((int)key,__builtin_ctzll(BALLOT(rank==(u32)(2*w4+1))));
        int tsel=~((ln>>5)? t1:t0)&2047, j=ln&31; rsrc_t rx=RSRC(x,L_*D_*2);
        u32x4 a=BLD128(rx,tsel*1152+j*16,0,0), b=BLD128(rx,tsel*1152+j*16+512,0,0); u32 c=BLD32(rx,tsel*1152+j*4+1024,0,0);
        const char* rp_=ws+W_ST+tsel*8; u64 f1=GLD(rp_);
        garrive(sm,C_D);
        if(lb==CTLB&&rank<8&&!(ln&1)) GST(ws+W_IDX+xg*1024+w4*128+rank*8,(tag<<16)|(~key&2047u));
        if(w4==0){ if(rank<8) ST(u16,sm+O_IDX+(ln&1)*16+rank*2)=(u16)((ln&1)? key>>16 : ~key&2047u); garrive(sm,C_IX); }
        lch* xw=sm+O_TILE+(2*w4+(ln>>5))*XSTR+j*16;
        if(BALLOT((u32)(f1>>32)!=(u32)tag)!=0ull) f1=poll1<32>(rp_,tag<<32);
        ST(u32x4,xw)=a; ST(u32x4,xw+512)=b; ST(u32,xw+1024-j*12)=c; if(j==0) ST(u32,sm+O_RC+(2*w4+(ln>>5))*4)=(u32)f1;
        garrive(sm,C_X);
      }
    }
    PRIO(0);
    if(lb<14) gwait<8>(sm,C_U,2); else gwait<4>(sm,C_H,4);
    fs=GLD(sp_); ff=GLD(fp_); XQ
    asm volatile("buffer_load_dword v124, %0, %1, 0 offen" :: "v"(ln<18? (tb*72+w4*18+ln)*128 : OOR),"s"(mkrs(out,L_*D_*2)) : "memory");
    {
      if(BALLOT((u32)(fs>>32)!=(u32)tag)!=0ull) fs=poll1<32>(sp_,tag<<32);
      float ri=rcpf_(bhi((u32)fs)); rc=f32x2{ri,-blo((u32)fs)*ri}; }
    asm volatile("" : "+v"(rc),"+v"(xq[0]),"+v"(xq[1]),"+v"(xq[2]));
    bool ix= w4==3&&lb<14&&lb!=CTLB; u64 f; const char* p=ws+W_IDX+(xg*8+(lb&3))*128+(ln&7)*8; if(ix) f=GLD(p);
    final_pre(fp,w4,xq,dq,rc);
    if(BALLOT(ff!=tag)!=0ull) poll1<0>(fp_,tag);
    if(ix){ if(BALLOT(((f^(tag<<16))>>16)!=0ull)!=0ull) f=poll1<16>(p,tag<<16);
      if(ln<8) ST(u16,sm+O_IDX+ln*2)=(u16)f; garrive(sm,C_IX); }
  }
  final_post(sm,fp,w4,nch,out,ws);
  }
}

extern "C" void kernel_launch(void* const* d_in, const int* in_sizes, int n_in, void* d_out, int out_size, void* d_ws, size_t ws_size, hipStream_t stream) {
  const u16** I=(const u16**)d_in;
  KP kp; kp.Are=I[1]; kp.Aim=I[2]; kp.Bre=I[3]; kp.Bim=I[4]; kp.Cre=I[5]; kp.Cim=I[6]; kp.ldt=I[7]; kp.pad=I[7];
  kA<<<256,1024,0,stream>>>(I[0],(char*)d_ws,I[10],I[8],I[11],I[12],I[13],(u16*)d_out,kp);
}
```

```cpp
#include <hip/hip_runtime.h>
#include <hip/hip_bf16.h>
#include <stdint.h>
#if defined(__HIP_DEVICE_COMPILE__)
#define NOPK __attribute__((target("no-packed-fp32-ops")))
#else
#define NOPK
#endif
#define DEV __device__ NOPK __forceinline__
typedef unsigned short u16;
typedef unsigned int u32;
typedef unsigned long long u64;
typedef short bf16x8 __attribute__((ext_vector_type(8)));
typedef _Float16 h8 __attribute__((ext_vector_type(8)));
typedef _Float16 h2 __attribute__((ext_vector_type(2)));
typedef float f32x4 __attribute__((ext_vector_type(4)));
typedef float f32x2 __attribute__((ext_vector_type(2)));
typedef u32 u32x4 __attribute__((ext_vector_type(4)));
typedef u32 u32x2 __attribute__((ext_vector_type(2)));
typedef __bf16 bf2 __attribute__((ext_vector_type(2)));
typedef short v4s __attribute__((ext_vector_type(4)));
typedef __amdgpu_buffer_rsrc_t rsrc_t;
#define GAS __attribute__((address_space(1)))
#define LAS __attribute__((address_space(3)))
typedef LAS char lch;
#define L_ 2048
#define D_ 576
#define F_ 4608
#define BC(T,v) __builtin_bit_cast(T,v)
#define TID ((int)__builtin_amdgcn_workitem_id_x())
#define BID ((int)__builtin_amdgcn_workgroup_id_x())
#define RFL(x) __builtin_amdgcn_readfirstlane(x)
#define RLX_AGENT __ATOMIC_RELAXED, __HIP_MEMORY_SCOPE_AGENT
#define LD(T,p) (*(const LAS T*)(p))
#define ST(T,p) (*(LAS T*)(p))
#define SPINCAP (1<<17)
#define DRAIN() asm volatile("s_waitcnt vmcnt(0)" ::: "memory")
#define GLD(p) __hip_atomic_load((const GAS u64*)(p),RLX_AGENT)
#define GST(p,v) __hip_atomic_store((GAS u64*)(p),(u64)(v),RLX_AGENT)
#define BALLOT(c) __builtin_amdgcn_ballot_w64(c)
#define SC1 16
#define LANE(l) int l; { int t2_=TID; asm volatile("" : "+v"(t2_)); l=t2_&63; }
#define SCHED() __builtin_amdgcn_sched_barrier(0)
#define RSRC(p,n) __builtin_amdgcn_make_buffer_rsrc((void*)(p),0,(n),0x00020000)
#define BLD128 __builtin_amdgcn_raw_buffer_load_b128
#define BLD32 __builtin_amdgcn_raw_buffer_load_b32
#define BST128 __builtin_amdgcn_raw_buffer_store_b128
#define PRIO(n) __builtin_amdgcn_s_setprio(n)

DEV float b2f(u32 v){ return BC(float,v<<16); }
DEV float blo(u32 v){ return BC(float,v<<16); }
DEV float bhi(u32 v){ return BC(float,v&0xffff0000u); }
DEV u32 pk(float a,float b){ f32x2 v={a,b}; bf2 r=__builtin_convertvector(v,bf2); return BC(u32,r); }
DEV u32 pkh(float a,float b){ f32x2 v={a,b}; h2 r=__builtin_convertvector(v,h2); return BC(u32,r); }
DEV float rb(float f){ return BC(float,pk(f,f)<<16); }
DEV float rcpf_(float x){ return __builtin_amdgcn_rcpf(x); }
DEV float expf_(float x){
  const float L2E=1.4426950216293335f, L2El=1.9259629911266175e-8f; float h=x*L2E; float l=__builtin_fmaf(x,L2E,-h)+x*L2El;
  float e=__builtin_amdgcn_exp2f(h); return __builtin_fmaf(e*l,0.6931471805599453f,e); }
DEV float sigm_f(float x){ return rcpf_(1.f+__builtin_amdgcn_exp2f(-x*1.4426950408889634f)); }
DEV float silu_f(float x){ return x*sigm_f(x); }
DEV u32 umax_(u32 a,u32 b){ return a>b?a:b; }
DEV u32 umin_(u32 a,u32 b){ return a<b?a:b; }

#define WSOFF 0
#define G2 7
#define PSL 8
#define WIDE 2
#define PK 0x122
#define PS 0x300
#define DQN 0
#define RD 0
#define PD 0
#define CVP 2
#define ESL 4
#define PRN 1
#define CHN 8
#define CSL 2
#define FS 3
#define PWB (w4>>1)*(9*4096)+(w4&1)*128
#define PLO (li>>1)*(18*4096)+(li&1)*64
#define YSH 3
#define PRE1 6
#define PRE3 4
#define PRE2 5
#define W1 5
#define W3 3
#define W2_ 4
#define W1B 20
#define W2B 12
#define LAST1 4
#define RGATE 4
#define UGATE 7
#define R2D 12
#define PFN 32
#define CPF 8
#define NRG 8
#define W_EPOCH 0
#define W_RG 16384
#define W_ST (W_RG+32*8192)
#define W_U (W_ST+32768)
#define UST (D_*16+128)
#define YST (L_*2+128)
#define TL 1
#define GST_ ((D_*16<<TL)+128)
#define W_Y (W_U+256*UST)
#define W_PART (W_Y+D_*YST)
#define W_GU (W_PART+8*9*16*256)
#define W_FL 9216
#define W_IDX 1024
#define CTLB 0
struct KP { const u16 *Are,*Aim,*Bre,*Bim,*Cre,*Cim,*ldt,*pad; };

template<int CTRL> DEV float dppf(float v){ return BC(float,__builtin_amdgcn_update_dpp(0u,BC(u32,v),CTRL,0xf,0xf,false)); }
template<int CTRL> DEV u32 dppu(u32 v){ return (u32)__builtin_amdgcn_update_dpp(0u,v,CTRL,0xf,0xf,false); }
DEV float sum16(float v){ v+=dppf<0xB1>(v); v+=dppf<0x4E>(v); v+=dppf<0x141>(v); v+=dppf<0x140>(v); return v; }
DEV float add16x(float v){ auto r=__builtin_amdgcn_permlane16_swap(BC(u32,v),BC(u32,v),false,false); u32 a=r[0], b=r[1]; return BC(float,a)+BC(float,b); }
DEV u32 wmax(u32 v){
  v=umax_(v,(u32)__builtin_amdgcn_update_dpp(0u,v,0x111,0xf,0xf,false));
  v=umax_(v,(u32)__builtin_amdgcn_update_dpp(0u,v,0x112,0xf,0xf,false));
  v=umax_(v,(u32)__builtin_amdgcn_update_dpp(0u,v,0x114,0xf,0xf,false));
  v=umax_(v,(u32)__builtin_amdgcn_update_dpp(0u,v,0x118,0xf,0xf,false));
  v=umax_(v,(u32)__builtin_amdgcn_update_dpp(0u,v,0x142,0xa,0xf,false));
  v=umax_(v,(u32)__builtin_amdgcn_update_dpp(0u,v,0x143,0xc,0xf,false));
  return (u32)__builtin_amdgcn_readlane((int)v,63); }

DEV void garrive(lch* sm,int c){ u32 ad=(u32)(size_t)sm+c*4, one=1;
  asm volatile("s_mov_b64 exec, 1\n\tds_add_u32 %0, %1\n\ts_mov_b64 exec, -1" :: "v"(ad),"v"(one) : "memory"); }
template<int SLP> DEV void gwait(lch* sm,int c,u32 target){ u32 ad=(u32)(size_t)sm+c*4, tv, cnt, sv; target=RFL(target);
  asm volatile("s_mov_b32 %1, 0x20000\n"
    "1:\n\tds_read_b32 %0, %3\n\ts_waitcnt lgkmcnt(0)\n\tv_readfirstlane_b32 %2, %0\n\ts_cmp_ge_u32 %2, %4\n\ts_cbranch_scc1 2f\n\ts_sleep %5\n\ts_sub_u32 %1, %1, 1\n\ts_cbranch_scc0 1b\n"
    "2:" : "=&v"(tv),"=&s"(cnt),"=&s"(sv) : "v"(ad),"s"(target),"i"(SLP) : "memory","scc"); }
template<int ST_,class T> DEV void gwait4(int c,u32 target,const lch* p,T& a,T& b,T& d,T& e){ u32 tv,cnt,sv;
  asm volatile("s_mov_b32 %1, 0x20000\n1:\n\tds_read_b32 %0, %7\n\tds_read_b128 %3, %8\n\tds_read_b128 %4, %8 offset:%10\n\tds_read_b128 %5, %8 offset:%11\n\tds_read_b128 %6, %8 offset:%12\n\ts_waitcnt lgkmcnt(4)\n\t"
    "v_readfirstlane_b32 %2, %0\n\ts_cmp_ge_u32 %2, %9\n\ts_cbranch_scc1 2f\n\ts_sleep 1\n\ts_sub_u32 %1, %1, 1\n\ts_cbranch_scc0 1b\n2:\n\ts_waitcnt lgkmcnt(0)"
    : "=&v"(tv),"=&s"(cnt),"=&s"(sv),"=&v"(a),"=&v"(b),"=&v"(d),"=&v"(e) : "v"(c*4),"v"(p),"i"(target),"i"(ST_),"i"(2*ST_),"i"(3*ST_) : "memory","scc"); }
DEV void gsync(lch* sm,int c,u32 target){ garrive(sm,c); gwait<0>(sm,c,target); }
template<int SH> DEV u64 poll1(const char* p,u64 want){ u64 f=GLD(p);
  _Pragma("unroll 1") for(int sp=0;sp<SPINCAP;sp++){ bool bad=((f^want)>>SH)!=0ull; if(BALLOT(bad)==0ull) break; __builtin_amdgcn_s_sleep(1); if(bad) f=GLD(p); }
  __builtin_amdgcn_fence(__ATOMIC_ACQUIRE,"wavefront"); return f; }
#define STR_(x) #x
#define STR(x) STR_(x)
DEV u32x4 mkrs(const void* p,u32 n){ u64 a=(u64)p; return u32x4{(u32)RFL((int)(u32)a),(u32)RFL((int)((u32)(a>>32)&0xffffu)),n,0x00020000u}; }
#define VMW(n) asm volatile("s_waitcnt vmcnt(%0)" :: "i"(n) : "memory")
#define YSLP 1
#define GSLP 1
#define QSLP 1
#define PSLP 3
#define POLLASM(LOADS,CHECK,K) "s_mov_b32 %[cnt], 0x20000\n1:\n\ts_waitcnt vmcnt(0)\n\t" CHECK "s_cbranch_vccz 9f\n\ts_sleep " STR(K) "\n\ts_mov_b64 exec, vcc\n\t" LOADS \
  "s_mov_b64 exec, -1\n\ts_sub_u32 %[cnt], %[cnt], 1\n\ts_cbranch_scc0 1b\n9:\n\t"
#define LD_DUP2 "buffer_load_dwordx4 v[112:115], %[of], %[rs], 0 offen sc1\n\tbuffer_load_dwordx4 v[116:119], %[of], %[rs], %[so] offen sc1\n\t"
#define MX2(a,b) "v_max3_u32 %[t0], %[t0], v" #a ", v" #b "\n\t"
#define CHK_I0 "v_max_u32 %[t0], v112, v113\n\t" MX2(114,115)
#define CHK_IE "v_cmp_eq_u32 vcc, -1, %[t0]\n\t"
#define PCLOB "memory","vcc","scc"
#define UMASK 0x40074007u
DEV u32 upat(u32 tag,int k){ return ((((tag*0xea959c21u)>>(8*k))&255u)*0x04000801u)&UMASK; }
DEV u32 pkz(float a,float b){ return BC(u32,__builtin_amdgcn_cvt_pkrtz(a,b)); }
#define INV 0xffffffffu
DEV u32 nv(u32 v){ return umin_(v,0xfffeffffu); }
DEV u32x4 nv4(u32x4 v){ return u32x4{nv(v[0]),nv(v[1]),nv(v[2]),nv(v[3])}; }
#define OOR 0x40000000

#define CHSZ 36864
#define O_CH 512
#define O_CND 256
#define SLSZ 38080
#define O_R2 (O_CH+2*SLSZ)
#define O_X2 (O_R2+CHSZ)
#define O_TILE (O_X2+CHSZ)
#define XSTR 1168
#define HSTR 608
#define XAD(r,b) ((r)*HSTR+(b)+((b)>=576? 8*HSTR-576:0))
#define XS1 2
#define XG1 1
#define TSTR2 1216
#define O_ONE (O_TILE+9728+16)
#define O_CAND (O_ONE+1168+48)
#define LDS_TOTAL (O_CAND+128)
#define C_G 0
#define C_U 8
#define C_L 4
#define C_D 5
#define C_X 12
#define C_P 13
#define C_H 14
#define C_I 17
#define C_E 18
#define C_IX 19
#define C_PR 21
#define C_W 22
#define WIDA "s_mov_b64 exec, 1\n\tds_add_u32 %[cw], %[one]\n\ts_mov_b64 exec, -1\n\t"
#define WID2 WIDA
#define WID3
#define C_WIN (lb>=14? C_W:C_D)
#define O_IDX 96
#define O_EXP 144
#define O_RMX 192
#define O_RV 128
#define O_MD 400
#define O_RC (O_CAND+64)

struct C2 { float r,i; };
DEV C2 cmul(C2 a,C2 b){ C2 c; c.r=__builtin_fmaf(a.r,b.r,-(a.i*b.i)); c.i=__builtin_fmaf(a.r,b.i,a.i*b.r); return c; }
DEV h8 ldA(const lch* p){ u32x2 a=*(const volatile LAS u32x2*)p, b=*(const volatile LAS u32x2*)(p+8); return BC(h8,(u32x4{a[0],a[1],b[0],b[1]})); }
DEV bf16x8 trfrag(const lch* p0,const lch* p1){
  u32x2 b0=BC(u32x2,__builtin_amdgcn_ds_read_tr16_b64_v4i16((LAS v4s*)p0)), b1=BC(u32x2,__builtin_amdgcn_ds_read_tr16_b64_v4i16((LAS v4s*)p1));
  return BC(bf16x8,(u32x4{b0[0],b0[1],b1[0],b1[1]})); }

struct FP { u32 gg[24]; float bs[24]; int yo,y3; lch* wp; const lch* tp; };
#define D2(a,b,c) __builtin_amdgcn_fdot2_f32_bf16(BC(bf2,a),BC(bf2,b),c,false)
#define ZCH 64
DEV void final_blk(FP& f,int z,const u32x4* xq,const u32x4* dq,f32x2 rc){
  float ea=rc[0]*-1.4426950408889634f, eb=rc[1]*-1.4426950408889634f;
  #pragma unroll
  for(int e=0;e<4;e++){ u32 xw=xq[z][e], dw=dq[z][e]; int i=8*z+2*e; float a=blo(xw), b=bhi(xw);
    float pa=__builtin_fmaf(a,rc[0],rc[1]), pb=__builtin_fmaf(b,rc[0],rc[1]);
    float ga=pa*rcpf_(1.f+__builtin_amdgcn_exp2f(__builtin_fmaf(a,ea,eb))), gb=pb*rcpf_(1.f+__builtin_amdgcn_exp2f(__builtin_fmaf(b,ea,eb)));
    f.gg[i]=pk(ga,0.f); f.gg[i+1]=pk(0.f,gb);
    f.bs[i]=__builtin_fmaf(pa*blo(dw),ga,a); f.bs[i+1]=__builtin_fmaf(pb*bhi(dw),gb,b);
    asm volatile("" : "+v"(f.gg[i]),"+v"(f.gg[i+1]),"+v"(f.bs[i]),"+v"(f.bs[i+1])); }
}
DEV void final_adr(lch* sm,FP& f,int w4){
  LANE(ln); int bid=BID, xg=bid&7, lb=bid>>3, g=ln>>4, li=ln&15, qq=li>>2, pq=li&3;
  f.yo=(144*(lb&3)+16*w4+(ln>>2))*YST+(32*(8*xg+(lb>>2))+8*(ln&3))*2; f.y3= w4==0? f.yo+2*ZCH*YST:OOR;
  lch* fb=sm+O_CH+w4*3072; f.wp=fb+((ln&3)*16+(ln>>2))*16; f.tp=fb+(16*g+8*(pq>>1)+qq)*16+(pq&1)*8;
  asm volatile("" : "+v"(f.yo),"+v"(f.y3),"+v"(f.wp),"+v"(f.tp));
}
DEV void final_post(lch* sm,FP& f,int w4,int nch,int ob,int trow,const u32x4* xq,const u32x4* dq,f32x2 rc,u16* __restrict__ out,char* ws){
  int lb=BID>>3;
  u32x4 rs=mkrs(ws+W_Y,D_*YST); rsrc_t ro=RSRC(out,L_*D_*2);
  gwait<ESL>(sm,C_E,4*nch);
  if(lb>=8&&lb<14) gwait<CSL>(sm,C_H,CHN);
  PRIO(3);
  #define LD_Y "buffer_load_dwordx4 v[112:115], %[of], %[rs], 0 offen sc1\n\tbuffer_load_dwordx4 v[116:119], %[of], %[rs], %[s1] offen sc1\n\tbuffer_load_dwordx4 v[120:123], %[o3], %[rs], 0 offen sc1\n\t"
  asm volatile(LD_Y : "+v"(rc),"+v"(trow) : [of]"v"(f.yo),[rs]"s"(rs),[s1]"s"(ZCH*YST),[o3]"v"(f.y3) : "memory");
  garrive(sm,C_PR);
  if(FS<2) final_blk(f,1,xq,dq,rc);
  if(FS<3&&w4==0) final_blk(f,2,xq,dq,rc);
  { gwait<2>(sm,C_IX,1); u32x4 iq=LD(u32x4,sm+O_IDX); u32 tt=(u32)trow*0x10001u; bool routed=false;
    #pragma unroll
    for(int q=0;q<4;q++){ u32 z=iq[q]^tt; routed|=((z&0xffffu)==0u)|((z>>16)==0u); }
    ob=routed? OOR:ob; }
  { u32 t0,cnt;
    asm volatile(POLLASM(LD_Y,"ds_write_b128 %[wp], v[112:115]\n\tds_write_b128 %[wp], v[116:119] offset:1024\n\ts_cmp_lg_u32 %[w4], 0\n\ts_cbranch_scc1 8f\n\tds_write_b128 %[wp], v[120:123] offset:2048\n8:\n\t"
      CHK_I0 MX2(116,117) MX2(118,119) MX2(120,121) MX2(122,123) CHK_IE,YSLP)
      : [t0]"=&v"(t0),[cnt]"=&s"(cnt),"+v"(ob) : [of]"v"(f.yo),[rs]"s"(rs),[s1]"s"(ZCH*YST),[o3]"v"(f.y3),[wp]"v"(f.wp),[w4]"s"(w4),"s"(ro) : PCLOB); }
  u32x4 r[3];
  #pragma unroll
  for(int z=0;z<3;z++) r[z]=BC(u32x4,trfrag(f.tp+z*1024,f.tp+z*1024+64));
  #pragma unroll
  for(int z=0;z<3;z++) if(z<2||w4==0){ u32x4 ov;
    #pragma unroll
    for(int e=0;e<4;e++){ u32 rw=r[z][e]; int i=8*z+2*e;
      ov[e]=pk(D2(rw,f.gg[i],f.bs[i]),D2(rw,f.gg[i+1],f.bs[i+1]));
    }
    BST128(ov,ro,ob+z*(2*ZCH),0,0); SCHED(); }
}

#define TROW 528
#define O_TB 4640
#define O_DE 34752
#define O_DO 35968
#define O_R2K 37120
#define O_R3K 37632
#define O_DUM 37952
#define O_RED O_TB
#define MH(a,b,c) __builtin_amdgcn_mfma_f32_16x16x32_f16(a,b,c,0,0,0)
#define PIN(x) asm volatile("" : "+v"(x))
DEV h8 ld4w(const lch* p){ return BC(h8,(u32x4{LD(u32,p),LD(u32,p+4),LD(u32,p+8),LD(u32,p+12)})); }
DEV u32 __umul24_(u32 a,u32 b){ u32 r; asm("v_mul_u32_u24 %0, %1, %2" : "=v"(r) : "s"(b),"v"(a)); return r; }
DEV u32x4 band(u32 mask,int g,u32 val){ u32 by=__builtin_amdgcn_ubfe(mask,8*g,8), X=by|(by<<15), k=0x10001u; u32x4 r; asm volatile("" : "+s"(k),"+s"(val));
  #pragma unroll
  for(int d=0;d<4;d++) r[d]=__umul24_((X>>(2*d))&k,val);
  return r; }
DEV h8 zpack(f32x4 e,f32x4 o){ return BC(h8,(u32x4{pkh(e[0],o[0]),pkh(e[1],o[1]),pkh(e[2],o[2]),pkh(e[3],o[3])})); }
struct C3 { float r,i,n; };
DEV C3 c3(C2 a){ C3 c; c.r=a.r; c.i=a.i; PIN(c.i); c.n=-c.i; PIN(c.n); return c; }
DEV C2 cm(C2 a,C3 b){ C2 c; c.r=__builtin_fmaf(a.r,b.r,a.i*b.n); c.i=__builtin_fmaf(a.r,b.i,a.i*b.r); return c; }
DEV u32 ph(C2 a){ return pkh(a.r,a.i); }
#define W2(p,a,b) asm volatile("ds_write2_b32 %0, %1, %2 offset1:132" :: "v"(p),"v"(a),"v"(b) : "memory")
struct CP { const lch *x0,*x1,*bd; lch *p0,*p1,*p2,*wp; u32x4 Be,Bo,AE; int yo; };
template<int ROLE> DEV void lanes(CP& c,lch* cb,int ln){ int m=ln&15, g=ln>>4;
  const lch* ad=cb+((m&1)? O_DO+2*527 : O_DE+2*536)+2*(8*g-m);
  const lch* bd=cb+2*(256+16*m+8*g); lch* dum=cb+O_DUM;
  if(ROLE==0){ int l0=128*(g>>1)+8*m+4*(g&1); c.p0=cb+O_DE+(549-l0)*2; PIN(c.p0); c.p1=c.p0+6; PIN(c.p1); c.p2=c.p0+(O_DO-O_DE-18); c.x0=ad-640; c.bd=bd-64; c.x1=cb+O_R3K+2*(126-2*m+8*g)-192; PIN(c.x1);
    c.Be=band(0x1ffu<<(m+4),g,0x3c00u); PIN(c.Be); }
  if(ROLE==1){ lch* r=cb+O_R2K+(192-64*(g-2)-2*m)*2; c.p0= g<2? dum : r-66; c.p1= ln==0? cb+O_DE+(552-256)*2 : (ln<=32? dum : r); c.p2=cb+O_DE+(544-256-8*ln)*2; c.x0=ad-128-640; c.bd=bd-128-128; PIN(c.p0); PIN(c.p1); PIN(c.p2); }
  if(ROLE==2){ int i0=16*m+4*g; c.p0= m<8? cb+O_R3K+(125-i0)*2 : dum+2; c.p1= ln==0? cb+O_R2K+128 : (ln==8? cb+O_R3K : c.p0+6); c.x0=ad-320-640; c.bd=bd-320-128; PIN(c.p0); PIN(c.p1); }
  if(ROLE==3){ c.x0=cb+O_R2K+2*(188-4*m+8*g)-256; c.bd=cb+2*(248+8*m+8*g);
    c.Be=band(0x1fu<<(m+6),g,0x3c00u); c.Bo=band(0x7u<<(m+11),g,0x3c00u); PIN(c.Be); PIN(c.Bo); }
  PIN(c.x0); PIN(c.bd); }
template<int ROLE> DEV void lanes2(CP& c,lch* cb,int ln){ int m=ln&15, g=ln>>4;
  if(ROLE==0){ c.Bo=band(0x7fu<<(m+5),g,0x3c00u); c.AE=band(3u<<(m+12),g,0xffffu); PIN(c.Bo); PIN(c.AE); }
  c.wp=cb+O_RED+ln*16; c.yo=(g&1)*512+m*32+(g>>1)*16; PIN(c.wp); PIN(c.yo); }
template<int ROLE> DEV void chan2(lch* sm,lch* cb,const lch*& fa,const lch*& fb,int grp,int d,int one_,int emax,u64 tag,char* ws){
  CP c;
  const f32x4 Z4={0,0,0,0}; h8 A[6]; f32x4 acc[8];
  #define LD_U "buffer_load_dwordx4 v[112:115], %[of], %[rs], 0 offen sc1\n\tbuffer_load_dwordx4 v[116:119], %[of], %[rs], %[s1] offen sc1\n\tbuffer_load_dwordx4 v[120:123], %[of], %[rs], %[s2] offen sc1\n\tbuffer_load_dwordx4 v[124:127], %[of], %[rs], %[s3] offen sc1\n\t"
  #define UC(t,a,b,c) "v_bitop3_b32 " t ", v" #a ", v" #b ", %[" #c "] bitop3:0x7e\n\t"
  #define UD(t,a,c) "v_bitop3_b32 " t ", " t ", v" #a ", %[" #c "] bitop3:0xf6\n\t"
  #define CHK_U UC("%[t0]",112,116,c0) UC("%[t1]",113,117,c1) UD("%[t0]",120,c0) UD("%[t1]",121,c1) UD("%[t0]",124,c0) UD("%[t1]",125,c1) UD("%[t0]",114,c2) UD("%[t1]",115,c3) UD("%[t0]",118,c2) UD("%[t1]",119,c3) \
    UD("%[t0]",122,c2) UD("%[t1]",123,c3) UD("%[t0]",126,c2) UD("%[t1]",127,c3) "v_bitop3_b32 %[t0], %[t0], %[t1], %[mk] bitop3:0xa8\n\tv_cmp_ne_u32 vcc, 0, %[t0]\n\t"
  #define UX(a,b,c,d,o) "v_xor_b32 v" #a ", %[e0], v" #a "\n\tv_xor_b32 v" #b ", %[e1], v" #b "\n\tv_xor_b32 v" #c ", %[e2], v" #c "\n\tv_xor_b32 v" #d ", %[e3], v" #d "\n\tds_write_b128 %[up], v[" #a ":" #d "] offset:" #o "\n\t"
  #define UPOLL { u32 t0,t1,cnt; asm volatile(POLLASM(LD_U,CHK_U,PSLP) UX(112,113,114,115,544) UX(116,117,118,119,1056) UX(120,121,122,123,1568) UX(124,125,126,127,2080) \
      : [t0]"=&v"(t0),[t1]"=&v"(t1),[cnt]"=&s"(cnt) : [of]"v"(c.p0),[rs]"s"(rsU),[s1]"s"((32>>TL)*GST_),[s2]"s"((64>>TL)*GST_),[s3]"s"((96>>TL)*GST_),[c0]"s"(c0),[c1]"s"(c1),[c2]"s"(c2),[c3]"s"(c3_), \
        [e0]"s"(c0^0x40004u),[e1]"s"(c1^0x40004u),[e2]"s"(c2^0x40004u),[e3]"s"(c3_^0x40004u),[mk]"s"(UMASK),[up]"v"(c.p1) : PCLOB); garrive(sm,C_U+one_); }
  if(ROLE<3){
    h8 a[8], b[8];
    #pragma unroll
    for(int ks=0;ks<8;ks++){ a[ks]=LD(h8,fa+ks*64); b[ks]=LD(h8,fb+ks*64); }
    SCHED();
    LANE(ln); lanes<ROLE>(c,cb,ln); float fz= ln==32? 0.f : 4.76837158203125e-07f; if(ROLE==1) PIN(fz);
    SCHED();
    f32x4 k=Z4;
    #pragma unroll
    for(int ks=0;ks<8;ks++) k=MH(a[ks],b[ks],k);
    const float f=4.76837158203125e-07f;
    float k0=k[0]*f, k1=k[1]*f, k2=k[2]*f, k3=k[3]*f;
    u32 h32=pkh(k3,k2), h10=pkh(k1,k0);
    if(ROLE!=1){
      ST(u16,c.p0)=(u16)h32; ST(u32,c.p0+2)=pkh(k2,k1); ST(u16,c.p1)=(u16)(h10>>16);
      if(ROLE==0) ST(u32x2,c.p2)=u32x2{h32,h10};
    } else {
      float n0=dppf<0x130>(k[0])*fz, z1= fz==0.f? k0:k1, z2=k[2]*fz, z3=k[3]*fz; u32 h22=pkh(z2,z2);
      ST(u16,c.p0+64)=(u16)h10; ST(u16,c.p0+2)=(u16)(h32>>16); ST(u16,c.p0)=(u16)h32; ST(u16,c.p1)=(u16)(h10>>16);
      u32x4 vo={pkh(n0,z3),h22,pkh(z2,z1),pkh(k0,k0)}, ve={pkh(n0,n0),pkh(z3,z2),h22,pkh(z1,k0)};
      asm volatile("s_mov_b32 exec_hi, 1\n\tds_write_b128 %0, %1\n\tds_write_b128 %0, %2 offset:1200\n\ts_mov_b32 exec_hi, -1" :: "v"(c.p2),"v"(ve),"v"(vo) : "memory"); }
    gsync(sm,C_G+grp,7);
    u32 kv; A[0]=ld4w(c.x0+640); A[1]=ld4w(c.x0+128);
    if(ROLE==0){ A[2]=ld4w(c.x1+192); A[3]=ld4w(c.x1+128); A[4]=ld4w(c.x1+64); A[5]=ld4w(c.x1); kv=LD(u16,cb+O_R2K+128); }
    else { A[2]=ld4w(c.x0+576); A[3]=ld4w(c.x0+64); A[4]=ld4w(c.x0+512); A[5]=ld4w(c.x0); }
    SCHED(); lanes2<ROLE>(c,cb,ln); SCHED();
    if(ROLE==0){ kv|=kv<<16; c.AE=u32x4{c.AE[0]&kv,c.AE[1]&kv,c.AE[2]&kv,c.AE[3]&kv}; PIN(c.AE); }
    PRIO(2);
    gwait<1>(sm,C_U+one_,2-one_);
  } else {
    u32 tg=(u32)tag, c0=upat(tg,0), c1=upat(tg,1), c2=upat(tg,2), c3_=upat(tg,3); u32x4 rsU=mkrs(ws+W_U,256*UST);
    if(!one_) PRIO(2);
    LANE(ln); c.p0=(lch*)fa; c.p1=(lch*)fb;
    bool late= grp==0||BID>=112;
    if(!late) asm volatile(LD_U :: [of]"v"(c.p0),[rs]"s"(rsU),[s1]"s"((32>>TL)*GST_),[s2]"s"((64>>TL)*GST_),[s3]"s"((96>>TL)*GST_) : "memory");
    asm volatile("s_mov_b64 exec, 1\n\tglobal_store_dwordx2 %0, %1, %2 sc1\n\ts_mov_b64 exec, -1" :: "v"(W_FL+d*8),"v"(tag),"s"(ws) : "memory");
    lanes<3>(c,cb,ln); lanes2<3>(c,cb,ln);
    #pragma unroll
    for(int j=2;j<8;j++){ acc[j]=Z4; PIN(acc[j]); }
    if(late){ gwait<2>(sm,C_G,UGATE); if(BID>=112) gwait<1>(sm,C_D,4); asm volatile(LD_U :: [of]"v"(c.p0),[rs]"s"(rsU),[s1]"s"((32>>TL)*GST_),[s2]"s"((64>>TL)*GST_),[s3]"s"((96>>TL)*GST_) : "memory"); }
    if(!late) UPOLL
    gwait<1>(sm,C_G+grp,7);
    #pragma unroll
    for(int i=0;i<6;i++) A[i]=ldA(c.x0+320-64*i);
    if(late) UPOLL
    PRIO(2);
    gwait<1>(sm,C_U+one_,2-one_);
  }
  const lch* bd=c.bd; h8 Be=BC(h8,c.Be), Bo=BC(h8,c.Bo);
  #pragma unroll
  for(int j=0;j<(ROLE==3? 2:8);j++) acc[j]=Z4;
  #define RW(j) if((j)>=0&&((j)>>1)!=ROLE&&!(ROLE==3&&(j)<2)){ ST(f32x4,c.wp+((j)>>1)*6144+((ROLE-((j)>>1)-1)&3)*2048+((j)&1)*1024)=acc[(j)<0? 0:(j)]; SCHED(); }
  const lch* rp=c.wp+ROLE*6144; f32x4 a[3], b[3]; u32 cv;
  #define LDV(T,p) (*(const volatile LAS T*)(p))
  #define ARR { garrive(sm,C_G+grp); if(CVP>=0) PRIO(CVP<0? 0:CVP); SCHED(); }
  #define OPT { cv=LDV(u32,sm+(C_G+grp)*4); _Pragma("unroll") for(int q=0;q<(ROLE==0? 2:3);q++){ a[q]=LDV(f32x4,rp+q*2048); b[q]=LDV(f32x4,rp+q*2048+1024); } SCHED(); }
  if(ROLE==1||ROLE==2){
    h8 B[8][3];
    #define QS(s) ((2*ROLE+1+(s))&7)
    #pragma unroll
    for(int s=0;s<3;s++){
      #pragma unroll
      for(int r=0;r<3;r++) B[s][r]=LD(h8,bd+128-64*r+512*QS(s)); }
    SCHED();
    #pragma unroll
    for(int s=0;s<9;s++){ const int q=QS(s), sb_=s&7;
      if(s==8) OPT
      if(s==6){
        #pragma unroll
        for(int r=0;r<3;r++){ acc[q]=MH(A[2*r],B[s][r],acc[q]); SCHED(); if(r==1) RW(QS(s-1)) }
        #pragma unroll
        for(int r=0;r<3;r++){ acc[q+1]=MH(A[2*r+1],B[s][r],acc[q+1]); SCHED(); if(r==1){ RW(q) ARR } }
      } else {
        #pragma unroll
        for(int r=0;r<3;r++){
          if(q<7&&s<8){ acc[q+1]=MH(A[2*r+1],B[sb_][r],acc[q+1]); SCHED(); }
          if(s>0){ acc[q]=MH(A[2*r],B[sb_][r],acc[q]); SCHED(); }
          if(s+3<8){ B[s+3][r]=LD(h8,bd+128-64*r+512*QS(s+3)); SCHED(); }
          if(r==(q<7? 0:1)&&s>=2&&s<6) RW(QS(s-1)) } } } }
  if(ROLE==0){
    h8 B[8], ue[4], C[8];
    #pragma unroll
    for(int kt=0;kt<4;kt++){ ue[kt]=LD(h8,bd+48+512*kt); B[kt]=LD(h8,bd+64+512*kt); }
    f32x4 ce[2],co[2]; h8 Z[2];
    ce[0]=MH(ue[0],Be,Z4); co[0]=MH(B[0],Bo,Z4); ce[1]=MH(ue[1],Be,Z4); co[1]=MH(B[1],Bo,Z4); Z[0]=zpack(ce[0],co[0]);
    #pragma unroll
    for(int kt=0;kt<4;kt++){
      SCHED();
      if(kt+2<4){ ce[kt&1]=MH(ue[kt+2],Be,Z4); co[kt&1]=MH(B[kt+2],Bo,Z4); }
      if(kt+1<4) Z[(kt+1)&1]=zpack(ce[(kt+1)&1],co[(kt+1)&1]);
      #pragma unroll
      for(int rho=0;rho+kt<4;rho++) acc[4+kt+rho]=MH(Z[kt&1],A[2+rho],acc[4+kt+rho]);
      SCHED(); if(kt>=2) C[kt-1]=LD(h8,bd+512*(kt-1)); if(kt==0) A[5]=BC(h8,c.AE); if(kt==1) A[4]=ld4w(c.x0-384); if(kt==2) A[3]=ld4w(c.x0+64); if(kt==3) A[2]=ld4w(c.x0+576); SCHED(); }
    #pragma unroll
    for(int q=0;q<9;q++){
      if(q<4){ acc[q+4]=MH(A[5],B[q],acc[q+4]); SCHED(); } if(q<6){ acc[q+2]=MH(A[4],B[q],acc[q+2]); SCHED(); }
      if(q>=1&&q<7){ acc[q+1]=MH(A[1],B[q],acc[q+1]); SCHED(); } if(q>=2&&q<8){ acc[q]=MH(A[0],B[q],acc[q]); SCHED(); }
      if(q>=4&&q<8) RW(q-2)
      if(q<4){ B[4+q]=LD(h8,bd+64+512*(4+q)); SCHED(); }
      if(q>=3){ acc[q-1]=MH(A[2],C[q-1],acc[q-1]); SCHED(); } if(q>=2&&q<8){ acc[q]=MH(A[3],C[q-1],acc[q]); SCHED(); }
      if(q>=1&&q<7){ C[(q+2)&7]=LD(h8,bd+512*((q+2)&7)); SCHED(); } }
    acc[0]=MH(A[0],B[0],acc[0]); SCHED(); RW(6) acc[1]=MH(A[1],B[0],acc[1]); SCHED(); RW(7) ARR
    acc[0]=MH(A[2],C[0],acc[0]); SCHED(); OPT acc[1]=MH(A[0],B[1],acc[1]); acc[1]=MH(A[2],C[1],acc[1]); acc[1]=MH(A[3],C[0],acc[1]); SCHED(); }
  if(ROLE==3){
    h8 U[12];
    #pragma unroll
    for(int kt=0;kt<6;kt++) U[kt]=LD(h8,bd+256*kt);
    f32x4 ce[2],co[2]; h8 Z[2];
    ce[0]=MH(U[0],Be,Z4); co[0]=MH(U[0],Bo,Z4); ce[1]=MH(U[1],Be,Z4); co[1]=MH(U[1],Bo,Z4); Z[0]=zpack(ce[0],co[0]);
    #pragma unroll
    for(int kt=0;kt<12;kt++){
      SCHED();
      if(kt==10) OPT
      if(kt+2<12){ ce[kt&1]=MH(U[kt+2],Be,Z4); co[kt&1]=MH(U[kt+2],Bo,Z4); }
      if(kt>=2&&kt<=8&&!(kt&1)){ SCHED(); RW((kt+2)/2) if(kt==8) ARR }
      if(kt+1<12) Z[(kt+1)&1]=zpack(ce[(kt+1)&1],co[(kt+1)&1]);
      #pragma unroll
      for(int rho=-1;rho<5;rho++){ int j=(kt+4+rho)>>1; if(((kt+rho)&1)==0&&j>=2&&j<8) acc[j]=MH(Z[kt&1],A[rho+1],acc[j]); }
      if(kt<6){ SCHED(); U[kt+6]=LD(h8,bd+256*(kt+6)); } }
    SCHED();
  }
  { float sc=__builtin_amdgcn_ldexpf(1.f,emax-YSH), s0[4], s1[4]; rsrc_t ry=RSRC(ws+W_Y+d*YST,4096);
    if(RFL(cv)<11u){ u32 tv,cnt,sv;
      if(ROLE==0) asm volatile("s_mov_b32 %1, 0x20000\n1:\n\tds_read_b32 %0, %7\n\tds_read_b128 %3, %8\n\tds_read_b128 %4, %8 offset:1024\n\tds_read_b128 %5, %8 offset:2048\n\tds_read_b128 %6, %8 offset:3072\n\ts_waitcnt lgkmcnt(4)\n\t"
        "v_readfirstlane_b32 %2, %0\n\ts_cmp_ge_u32 %2, 11\n\ts_cbranch_scc1 2f\n\ts_sleep 1\n\ts_sub_u32 %1, %1, 1\n\ts_cbranch_scc0 1b\n2:\n\ts_waitcnt lgkmcnt(0)"
        : "=&v"(tv),"=&s"(cnt),"=&s"(sv),"=&v"(a[0]),"=&v"(b[0]),"=&v"(a[1]),"=&v"(b[1]) : "v"((C_G+grp)*4),"v"(rp) : "memory","scc");
      else { gwait<0>(sm,C_G+grp,11);
        #pragma unroll
        for(int q=0;q<3;q++){ a[q]=LDV(f32x4,rp+q*2048); b[q]=LDV(f32x4,rp+q*2048+1024); } } }
    f32x4 o0=acc[2*ROLE], o1=acc[2*ROLE+1];
    #pragma unroll
    for(int e=0;e<4;e++){ s0[e]=(ROLE==0? a[0][e]+a[1][e] : (a[0][e]+a[1][e])+a[2][e])*sc; s1[e]=(ROLE==0? b[0][e]+b[1][e] : (b[0][e]+b[1][e])+b[2][e])*sc; }
    #pragma unroll
    for(int e=0;e<4;e++){ s0[e]=__builtin_fmaf(o0[e],sc,s0[e]); s1[e]=__builtin_fmaf(o1[e],sc,s1[e]); }
    auto r0=__builtin_amdgcn_permlane16_swap(nv(pk(s0[0],s0[1])),nv(pk(s1[0],s1[1])),false,false); auto r1=__builtin_amdgcn_permlane16_swap(nv(pk(s0[2],s0[3])),nv(pk(s1[2],s1[3])),false,false);
    BST128(u32x4{r0[0],r1[0],r0[1],r1[1]},ry,c.yo+ROLE*1024,0,SC1); }
  garrive(sm,C_E);

  asm volatile("s_endpgm" : "=v"(fa),"=v"(fb) :: "memory");
}
#define GETEP asm volatile("s_waitcnt lgkmcnt(0)\n\ts_mov_b64 %0, s[96:97]\n\ts_mov_b32 %1, s98" : "=s"(ep),"=s"(ldw));
DEV void chan(lch* sm,int grp,int w4,int d,int one_,char* ws,u32x4 q0,u32x4 q1,u32x4 q2,u32x4 q3,u32x4 q4){
  lch* cb=sm+O_CH+grp*SLSZ; int role=w4, emax;
  #define LP(v) ((lch*)(size_t)(u32)(v))
  const lch* fa=LP(q4[0]); const lch* fb=LP(q4[1]);
  u64 tag;
  { float a2,bp,br,bi,cr,ci;
    u32 fq=q3[2], fr=q3[3], one=q4[2]; float f64=BC(float,fq), f128=BC(float,fr);
    lch *t8[4]={LP(q0[0]),LP(q0[1]),LP(q0[2]),LP(q0[3])}, *tB[4]={LP(q1[0]),LP(q1[1]),LP(q1[2]),LP(q1[3])}, *t4[2]={LP(q2[0]),LP(q2[1])}, *t2[2]={LP(q2[2]),LP(q2[3])}, *t1=LP(q3[0]), *te=LP(q3[1]);
    if(BID<64){ if(grp==0) PRIO(PK&3); if(grp==1) PRIO((PK>>4)&3); if(grp==2) PRIO((PK>>8)&3); } else
    if(grp!=0) PRIO(1);
    u64 ep; u32 ldw; GETEP tag=ep+1ull;
    float dt=rb(expf_((d&1)? bhi(ldw):blo(ldw))), ca=dt*1.4426950408889634f, cb_=dt*0.15915494309189535f;
    asm volatile("s_waitcnt vmcnt(1)\n\tv_mul_f32 %2, %8, v114\n\tv_mul_f32 %3, %8, v115\n\tv_mul_f32 %0, %6, v112\n\tv_mul_f32 %1, %7, v113\n\tv_mov_b32 %4, v116\n\tv_mov_b32 %5, v117"
      : "=&v"(a2),"=&v"(bp),"=&v"(br),"=&v"(bi),"=&v"(cr),"=&v"(ci) : "v"(ca),"v"(cb_),"v"(dt) : "memory");
    C2 W; W.r=cr*br-ci*bi; W.i=cr*bi+ci*br;
    u32 mx=BC(u32,__builtin_fmaxf(__builtin_fabsf(W.r),__builtin_fabsf(W.i)));
    #define MXS(C) mx=umax_(mx,dppu<C>(mx));
    float e8=a2*8.f, eB=__builtin_fmaf(a2,f64,14.f), p8=bp*8.f, pB=bp*f64; SCHED();
    MXS(0xB1) p8=__builtin_amdgcn_fractf(p8); pB=__builtin_amdgcn_fractf(pB); e8=__builtin_amdgcn_exp2f(e8); SCHED();
    MXS(0x4E) eB=__builtin_amdgcn_exp2f(eB); float c8=__builtin_amdgcn_cosf(p8); SCHED();
    MXS(0x141) float s8=__builtin_amdgcn_sinf(p8), cB=__builtin_amdgcn_cosf(pB); SCHED();
    float sB=__builtin_amdgcn_sinf(pB); PIN(sB); MXS(0x140) float e1=__builtin_amdgcn_exp2f(a2); PIN(e1); SCHED();
    asm volatile("s_mov_b64 exec, 0x10001\n\tds_max_u32 %0, %1\n\tds_add_u32 %0, %2 offset:4\n\ts_mov_b64 exec, -1" :: "v"(te),"v"(mx),"v"(one) : "memory");
    C3 z8; z8.r=e8*c8; z8.n=e8*s8; z8.i=-z8.n; PIN(z8.i); C2 B; B.r=eB*cB; B.i=-(eB*sB);
    float p1=__builtin_amdgcn_fractf(bp), pE=__builtin_amdgcn_fractf(bp*256.f), pV=__builtin_amdgcn_fractf(bp*f128), eE=a2*256.f, eV=a2*f128, tr[8];
    #pragma unroll
    for(int k=0;k<4;k++){ SCHED(); u32 h0=ph(B); B=cm(B,z8);
      if(k==0){ tr[0]=__builtin_amdgcn_cosf(pV); tr[1]=__builtin_amdgcn_sinf(pV); } if(k==1){ tr[2]=__builtin_amdgcn_exp2f(eV); tr[3]=__builtin_amdgcn_cosf(p1); }
      if(k==2){ tr[4]=__builtin_amdgcn_sinf(p1); tr[5]=__builtin_amdgcn_exp2f(eE); } if(k==3){ tr[6]=__builtin_amdgcn_cosf(pE); tr[7]=__builtin_amdgcn_sinf(pE); }
      PIN(tr[2*k]); PIN(tr[2*k+1]);
      u32 h1=ph(B); if(k<3) B=cm(B,z8); W2(tB[k],h0,h1); }
    asm volatile("ds_read_b64 v[118:119], %0" :: "v"(te) : "memory");
    SCHED();
    C3 z1=c3(C2{e1*tr[3],e1*tr[4]}), E=c3(C2{tr[5]*tr[6],tr[5]*tr[7]}); C2 V[8]; V[0]=cmul(W,C2{tr[2]*tr[0],tr[2]*tr[1]});
    { u32 cnt,n; PIN(V[0].r); PIN(V[0].i); PIN(z1.r); PIN(E.r);
      asm volatile("s_mov_b32 %1, 0x20000\n1:\n\ts_waitcnt lgkmcnt(0)\n\tv_readfirstlane_b32 %2, v119\n\tv_readfirstlane_b32 %0, v118\n\ts_cmp_eq_u32 %2, 8\n\ts_cbranch_scc1 2f\n\tds_read_b64 v[118:119], %3\n\ts_sub_u32 %1, %1, 1\n\ts_cbranch_scc0 1b\n2:"
        : "=&s"(emax),"=&s"(cnt),"=&s"(n) : "v"(te) : "memory","scc");
      emax=RFL(emax); emax=(emax>>23)-126; }
    V[0].r=__builtin_amdgcn_ldexpf(V[0].r,14-emax); V[0].i=__builtin_amdgcn_ldexpf(V[0].i,14-emax);
    C2 Y=cm(V[0],E), Y4; u32 y0=ph(Y);
    V[1]=cm(V[0],z1); W2(t8[0],ph(V[0]),ph(V[1])); Y=cm(Y,E); u32 y1=ph(Y);
    V[2]=cm(V[1],z1); Y=cm(Y,E); u32 y2=ph(Y);
    V[3]=cm(V[2],z1); W2(t8[1],ph(V[2]),ph(V[3])); W2(t4[0],y0,ph(cm(V[2],E)));
    V[4]=cm(V[3],z1); Y=cm(Y,E); ST(u32,t1)=ph(Y);
    V[5]=cm(V[4],z1); W2(t8[2],ph(V[4]),ph(V[5])); Y4=cm(V[4],E); u32 y4=ph(Y4);
    V[6]=cm(V[5],z1); Y=cm(Y,E); ST(u32,t1+2*TROW)=ph(Y);
    V[7]=cm(V[6],z1); W2(t8[3],ph(V[6]),ph(V[7])); Y4=cm(Y4,E); W2(t2[0],y1,ph(Y4));
    W2(t4[1],y4,ph(cm(V[6],E))); Y=cm(Y,E); ST(u32,t1+4*TROW)=ph(Y);
    Y4=cm(Y4,E); W2(t2[1],y2,ph(Y4)); Y=cm(Y,E); ST(u32,t1+6*TROW)=ph(Y);
    Y=cm(Y,E); asm volatile("s_mov_b32 exec_hi, 0\n\tds_write_b32 %0, %1 offset:4224\n\ts_mov_b32 exec_hi, -1" :: "v"(t1),"v"(ph(Y)) : "memory");
  }
  if(BID<64){ if(grp==0) PRIO(PS&3); if(grp==1) PRIO((PS>>4)&3); if(grp==2) PRIO((PS>>8)&3); }
  DRAIN();
  gsync(sm,C_G+grp,4);
  if(role==0) chan2<0>(sm,cb,fa,fb,grp,d,one_,emax,tag,ws);
  if(role==1) chan2<1>(sm,cb,fa,fb,grp,d,one_,emax,tag,ws);
  if(role==2) chan2<2>(sm,cb,fa,fb,grp,d,one_,emax,tag,ws);
  chan2<3>(sm,cb,fa,fb,grp,d,one_,emax,tag,ws);
}

DEV u32 med3u(u32 a,u32 b,u32 c){ u32 r; asm("v_med3_u32 %0, %1, %2, %3" : "=v"(r) : "v"(a),"v"(b),"v"(c)); return r; }
DEV u32 max3u(u32 a,u32 b,u32 c){ return umax_(umax_(a,b),c); }
#define GTC(r,s) "v_cmp_gt_u32 %[" #s "], v" #r ", %[c]\n\t"
#define GTA(s) "v_addc_co_u32 %[rk], vcc, 0, %[rk], %[" #s "]\n\t"
#define GT4(a,b,c,d) GTC(a,s0) GTC(b,s1) GTC(c,s2) GTC(d,s3) GTA(s0) GTA(s1) GTA(s2) GTA(s3)
#define GTS [s0]"=&s"(q0),[s1]"=&s"(q1),[s2]"=&s"(q2),[s3]"=&s"(q3)
DEV void top8(lch* sm,int w4,int ln,u32 M,char* ws,u32& rank_,u32& key_){
  u32 k[8], rc[8], hm=0xffff0000u; u64 em=0x0001000100010001ull,q0,q1,q2,q3;
  u32x4 rs=mkrs(ws+W_RG+(BID&(NRG-1))*8192,8192); int ro=w4*1024+ln*16;
  #pragma unroll
  for(int e=0;e<8;e++){ rc[e]=(u32)(0xfff-(w4*256+ln*4+(e&3)+(e>>2)*1024)); PIN(rc[e]); }
  lch *aW=sm+O_RMX+(w4*4+(ln>>4))*4, *aC=sm+O_RMX+(ln>>2)*4, *aO=sm+O_RMX+(ln&3)*16, *dC=sm+O_CND+(ln>>1)*4, *dO=sm+O_CND+(ln&1)*64; PIN(aW); PIN(aC); PIN(aO); PIN(dC); PIN(dO); PIN(ro); PIN(ln);
  asm volatile("" : "+s"(hm),"+s"(em),"+s"(rs));
  if(RGATE) gwait<2>(sm,C_G,RGATE);
  if(RD) __builtin_amdgcn_s_sleep(RD);
  { u32 cnt;
    #define KL(e,r) "v_lshl_or_b32 %[k" #e "], v" #r ", 16, %[k" #e "]\n\t"
    #define KH(e,r) "v_and_or_b32 %[k" #e "], v" #r ", %[hm], %[k" #e "]\n\t"
    #define CHK_R "v_bitop3_b32 %[t0], v113, v115, %[M] bitop3:0x7e\n\tv_bitop3_b32 %[t1], v117, v119, %[M] bitop3:0x7e\n\tv_or_b32 %[t0], %[t0], %[t1]\n\tv_cmp_ne_u32 vcc, 0, %[t0]\n\t"
    u32 t0,t1;
    asm volatile(LD_DUP2
      "s_sleep " STR(R2D) "\n\tbuffer_load_dwordx4 v[120:123], %[of], %[rs], 0 offen sc1\n\tbuffer_load_dwordx4 v[124:127], %[of], %[rs], %[so] offen sc1\n\t" WID2 "s_mov_b32 %[cnt], 0x20000\n\ts_waitcnt vmcnt(2)\n\t" CHK_R "s_cbranch_vccz 9f\n\t"
      "s_waitcnt vmcnt(0)\n\tv_mov_b64 v[112:113], v[120:121]\n\tv_mov_b64 v[114:115], v[122:123]\n\tv_mov_b64 v[116:117], v[124:125]\n\tv_mov_b64 v[118:119], v[126:127]\n\t"
      POLLASM(LD_DUP2,CHK_R,PSLP)
      WID3 KL(0,112) KH(1,112) KL(2,114) KH(3,114) KL(4,116) KH(5,116) KL(6,118) KH(7,118)
      : [k0]"+v"(rc[0]),[k1]"+v"(rc[1]),[k2]"+v"(rc[2]),[k3]"+v"(rc[3]),[k4]"+v"(rc[4]),[k5]"+v"(rc[5]),[k6]"+v"(rc[6]),[k7]"+v"(rc[7]),[cnt]"=&s"(cnt),[t0]"=&v"(t0),[t1]"=&v"(t1) : [of]"v"(ro),[rs]"s"(rs),[so]"s"(4096),[M]"s"(M),[hm]"s"(hm),[cw]"v"(C_W*4),[one]"v"(1) : PCLOB); }
  #pragma unroll
  for(int e=0;e<8;e++) k[e]=rc[e];
  u32 h=max3u(k[0],k[1],k[2]), m1=max3u(max3u(k[3],k[4],k[5]),max3u(k[6],k[7],h),h), mx=m1, l=med3u(k[0],k[1],k[2]), t=umin_(umin_(k[0],k[1]),k[2]);
  SCHED();
  #define INS(e) t=med3u(t,l,k[e]); l=med3u(h,l,k[e]); if(e<7) h=umax_(h,k[e]); PIN(t); PIN(l); SCHED();
  mx=umax_(mx,dppu<0xB1>(mx)); INS(3) mx=umax_(mx,dppu<0x4E>(mx)); INS(4) mx=umax_(mx,dppu<0x141>(mx)); INS(5) mx=umax_(mx,dppu<0x140>(mx));
  asm volatile("s_mov_b64 exec, %2\n\tds_write_b32 %0, %1\n\ts_mov_b64 exec, -1\n\tds_read_b32 v116, %3\n\tds_read_b128 v[112:115], %4" :: "v"(aW),"v"(mx),"s"(em),"v"(aC),"v"(aO) : "memory");
  INS(6) INS(7)
  u32 m2=l, m3=t, T;
  { u32 c,rk,cnt;
    asm volatile("s_mov_b32 %[cnt], 0x20000\n1:\n\ts_waitcnt lgkmcnt(0)\n\tv_cmp_eq_u32 vcc, 0, v116\n\ts_cbranch_vccz 2f\n\tds_read_b32 v116, %[aC]\n\tds_read_b128 v[112:115], %[aO]\n\ts_sub_u32 %[cnt], %[cnt], 1\n\ts_cbranch_scc0 1b\n2:\n\t"
      "v_mov_b32 %[c], v116\n\tv_mov_b32 %[rk], 0\n\t" GT4(112,113,114,115) "s_nop 1\n\tv_add_u32_dpp %[rk], %[rk], %[rk] quad_perm:[1,0,3,2] row_mask:0xf bank_mask:0xf\n\ts_nop 1\n\tv_add_u32_dpp %[rk], %[rk], %[rk] quad_perm:[2,3,0,1] row_mask:0xf bank_mask:0xf\n\t"
      "v_cmp_eq_u32 vcc, 7, %[rk]\n\ts_ff1_i32_b64 %[cnt], vcc\n\ts_nop 0\n\tv_readlane_b32 %[T], %[c], %[cnt]\n\ts_nop 1"
      : [c]"=&v"(c),[rk]"=&v"(rk),[cnt]"=&s"(cnt),[T]"=&s"(T),GTS : [aC]"v"(aC),[aO]"v"(aO) : PCLOB); T=RFL(T); }
  { u64 b1=BALLOT(m1>=T), b2=BALLOT(m2>=T), b3=BALLOT(m3>=T); int n1=__builtin_popcountll(b1), n2=n1+__builtin_popcountll(b2);
    lch* cw=sm+O_CND+w4*32;
    if(__builtin_expect(b3==0ull&&n2<=8,1)){
      int p1=(int)__builtin_amdgcn_mbcnt_hi((u32)(b1>>32),__builtin_amdgcn_mbcnt_lo((u32)b1,0u)), pf=n2+ln-p1; bool c1=m1>=T;
      if(__builtin_expect(b2!=0ull,0)){ int p2=(int)__builtin_amdgcn_mbcnt_hi((u32)(b2>>32),__builtin_amdgcn_mbcnt_lo((u32)b2,(u32)n1)); if(m2>=T) ST(u32,cw+p2*4)=m2; }
      int ps= c1? p1:pf; ps= ps<8? ps:32-8*w4; ST(u32,cw+ps*4)= c1? m1:1u;
    } else {
      u32 mine=0;
      _Pragma("unroll 1") for(int c=0;c<8;c++){ u32 lm=umax_(umax_(umax_(k[0],k[1]),umax_(k[2],k[3])),umax_(umax_(k[4],k[5]),umax_(k[6],k[7]))); u32 wm=wmax(lm); if(ln==c) mine=wm;
        #pragma unroll
        for(int e=0;e<8;e++) k[e]=(k[e]==wm)?0u:k[e]; }
      if(ln<8) ST(u32,cw+ln*4)=mine; } }
  { u32 c,rk,cnt;
    asm volatile("s_mov_b32 %[cnt], 0x20000\n\tv_mov_b32 %[rk], 0\n\ts_waitcnt vmcnt(0)\n1:\n\tds_read_b32 %[c], %[dC]\n\tds_read_b128 v[112:115], %[dO]\n\tds_read_b128 v[116:119], %[dO] offset:16\n\tds_read_b128 v[120:123], %[dO] offset:32\n\tds_read_b128 v[124:127], %[dO] offset:48\n\t"
      "s_waitcnt lgkmcnt(4)\n\tv_cmp_eq_u32 vcc, 0, %[c]\n\ts_cbranch_vccz 2f\n\ts_sub_u32 %[cnt], %[cnt], 1\n\ts_cbranch_scc0 1b\n2:\n\t"
      "s_waitcnt lgkmcnt(3)\n\t" GT4(112,113,114,115) "s_waitcnt lgkmcnt(2)\n\t" GT4(116,117,118,119) "s_waitcnt lgkmcnt(1)\n\t" GT4(120,121,122,123) "s_waitcnt lgkmcnt(0)\n\t" GT4(124,125,126,127)
      "s_nop 1\n\tv_add_u32_dpp %[rk], %[rk], %[rk] quad_perm:[1,0,3,2] row_mask:0xf bank_mask:0xf"
      : [c]"=&v"(c),[rk]"=&v"(rk),[cnt]"=&s"(cnt),GTS : [dC]"v"(dC),[dO]"v"(dO) : PCLOB);
    rank_=rk; key_=c; }
}

#define FILL_(n,WAIT) "s_mov_b32 m0, %[ld]\n\ts_mov_b32 %[c], " STR(n) "-1\n3:\n\t" WAIT "buffer_load_dwordx4 %[vo], %[rs], %[so] offen nt lds\n\ts_add_u32 m0, m0, 0x1000\n\ts_add_u32 %[so], %[so], %[st]\n\t" \
  "s_sub_u32 %[c], %[c], 1\n\ts_cbranch_scc0 3b\n\ts_mov_b32 %[ld], m0\n\t"
#define FILL(n,WAIT) do{ u32 c_; asm volatile(FILL_(n,WAIT) : [c]"=&s"(c_),[so]"+s"(wso),[ld]"+s"(wld) : [vo]"v"(wvo),[rs]"s"(wrs),[st]"s"(wst) : "memory","scc","m0"); }while(0)
#define FILLD(n,WN,WW_) do{ u32 c_,t_,s_; asm volatile(FILL_(n,"ds_read_b32 %[t], %[ad]\n\ts_waitcnt lgkmcnt(0)\n\tv_readfirstlane_b32 %[s], %[t]\n\ts_cmp_ge_u32 %[s], 4\n\ts_cbranch_scc1 4f\n\t" VW(WN) "4:\n\t" VW(WW_)) \
  : [c]"=&s"(c_),[so]"+s"(wso),[ld]"+s"(wld),[t]"=&v"(t_),[s]"=&s"(s_) : [vo]"v"(wvo),[rs]"s"(wrs),[st]"s"(wst),[ad]"v"(C_WIN*4) : "memory","scc","m0"); wso=RFL(wso); wld=RFL(wld); }while(0)
#define VW(n) "s_waitcnt vmcnt(" STR(n) ")\n\t"

__global__ NOPK __attribute__((amdgpu_num_vgpr(56),amdgpu_num_sgpr(102))) __launch_bounds__(1024,4) void kA(const u16* __restrict__ x,char* ws,const u16* __restrict__ wr,const u16* __restrict__ Dsk,const u16* __restrict__ wg,const u16* __restrict__ wu,
                                              const u16* __restrict__ wd,u16* __restrict__ out,KP kp){
  __shared__ __attribute__((aligned(16))) char smem[LDS_TOTAL];
  lch* sm=(lch*)smem;
  u32x4 xa,xb_,wa,wb,dq[3]; u32 xc,wc;
  asm volatile(
    "s_load_dwordx16 s[64:79], %[ka], 0x0\n\ts_load_dwordx16 s[80:95], %[ka], 0x40\n\t"
    "v_readfirstlane_b32 s60, %[t]\n\ts_and_b32 s61, %[wg], 7\n\ts_lshr_b32 s62, %[wg], 3\n\tv_and_b32 v118, 31, %[t]\n\ts_bfe_u32 s63, s60, 0x20006\n\ts_cmpk_ge_u32 s60, 0x100\n\ts_cbranch_scc1 1f\n\t"
    "s_setprio 3\n\ts_lshl_b32 s56, s61, 5\n\ts_add_u32 s56, s56, s62\n\ts_mulk_i32 s56, 0x2400\n\tv_lshrrev_b32 v119, 5, %[t]\n\tv_mul_u32_u24 v119, 0x480, v119\n\tv_add_u32 v119, s56, v119\n\t"
    "v_lshl_add_u32 v116, v118, 4, v119\n\tv_lshl_add_u32 v117, v118, 2, v119\n\tv_lshlrev_b32 v114, 4, v118\n\tv_lshlrev_b32 v115, 2, v118\n\t"
    "v_lshlrev_b32 v119, 1, %[t]\n\tv_mov_b32 v118, 0\n\tds_write_b16 v119, v118\n\t"
    "s_waitcnt lgkmcnt(0)\n\t"
    "s_load_dwordx2 s[96:97], s[66:67], 0x0\n\t"
    "global_load_dwordx4 %[wa], v114, s[68:69]\n\tglobal_load_dwordx4 %[wb], v114, s[68:69] offset:512\n\tglobal_load_dword %[wc], v115, s[68:69] offset:1024\n\t"
    "global_load_dwordx4 %[xa], v116, s[64:65] nt\n\tglobal_load_dwordx4 %[xb], v116, s[64:65] offset:512 nt\n\tglobal_load_dword %[xc], v117, s[64:65] offset:1024 nt\n\t"
    "s_barrier\n\t"
    "s_branch 8f\n"
    "1:\n\ts_cmp_ge_u32 s62, " STR(CPF) "\n\ts_cbranch_scc1 6f\n\ts_getpc_b64 s[56:57]\n.Lkpc:\n\ts_and_b32 s58, s62, 3\n\ts_lshl_b32 s58, s58, 13\n\ts_sub_u32 s58, s58, 0x1000\n\tv_lshl_add_u32 v119, %[t], 4, s58\n\tv_min_u32 v119, .Lkend-.Lkpc-16, v119\n\t"
    "global_load_dwordx4 v[120:123], v119, s[56:57]\n\t.subsection 1\n.Lkend:\n\t.subsection 0\n6:\n\t"
    "v_mov_b32 v124, -1\n\tv_mov_b32 v125, -1\n\tv_mov_b32 v126, -1\n\tv_mov_b32 v127, -1\n\tv_lshlrev_b32 v119, 4, %[t]\n\tv_and_b32 v119, 0xff0, v119\n\t"
    "s_lshr_b32 s59, s60, 8\n\ts_cmp_lt_u32 s62, 8\n\ts_cselect_b32 s58, 2, 1\n\ts_cbranch_scc1 2f\n\ts_cmp_eq_u32 s59, 3\n\ts_cbranch_scc1 4f\n"
    "2:\n\ts_sub_u32 s57, s59, 2\n\ts_cmp_eq_u32 s59, 1\n\ts_cselect_b32 s58, s58, s57\n\ts_cmp_lt_u32 s62, 8\n\ts_cselect_b32 s56, 0, 8\n\ts_lshl1_add_u32 s56, s62, s56\n\ts_mulk_i32 s61, 72\n\ts_add_u32 s51, s56, s61\n\ts_sub_u32 s56, 2, s59\n\ts_sub_u32 s57, 16, s62\n\ts_cmp_eq_u32 s59, 3\n\ts_cselect_b32 s50, s57, s56\n\ts_add_u32 s56, s50, s51\n\t"
    "v_mov_b32 v112, 0\n\tv_mov_b32 v113, 0\n\tv_mov_b32 v114, 0\n\tv_mov_b32 v115, 0\n\tv_mov_b32 v116, 0\n\tv_mov_b32 v117, 0\n\ts_setprio 2\n\t"
    "s_lshl_b32 s57, s56, 2\n\ts_add_u32 s57, s57, s63\n\ts_lshl_b32 s57, s57, 6\n\tv_lshl_add_u32 v22, v118, 1, s57\n\ts_mul_i32 s57, s56, %[yst]\n\ts_add_u32 s57, s57, %[wy]\n\tv_add_u32 v23, s57, v119\n\ts_lshl_b32 s56, s56, 1\n\ts_and_b32 s56, s56, -4\n\t"
    "s_waitcnt lgkmcnt(0)\n\t"
    "global_load_short_d16_hi v112, v22, s[80:81] nt\n\tglobal_load_short_d16_hi v113, v22, s[82:83] nt\n\tglobal_load_short_d16_hi v114, v22, s[84:85] nt\n\t"
    "global_load_short_d16_hi v115, v22, s[86:87] nt\n\tglobal_load_short_d16_hi v116, v22, s[88:89] nt\n\tglobal_load_short_d16_hi v117, v22, s[90:91] nt\n\t"
    "s_load_dwordx2 s[96:97], s[66:67], 0x0\n\ts_load_dword s98, s[92:93], s56\n\t"
    "s_barrier\n\t"
    "global_store_dwordx4 v23, v[124:127], s[66:67] sc1\n\t"
    "s_mul_i32 s52, s58, %[sl]\n\ts_add_u32 s52, s52, %[tb]\n\ts_lshl_b32 s53, s63, 7\n\ts_add_u32 s53, s53, s52\n\tv_lshl_add_u32 v30, v118, 2, s53\n\tv_bfe_u32 v31, %[t], 5, 1\n\ts_movk_i32 s54, 528\n\tv_mul_u32_u24 v29, s54, v31\n\t"
    "v_lshl_add_u32 v2, v29, 3, v30\n\ts_movk_i32 s53, 1056\n\tv_add_u32 v3, s53, v2\n\tv_add_u32 v4, s53, v3\n\tv_add_u32 v5, s53, v4\n\tv_add_u32 v6, 41*528, v2\n\tv_add_u32 v7, s53, v6\n\tv_add_u32 v8, s53, v7\n\tv_add_u32 v9, s53, v8\n\t"
    "v_lshl_add_u32 v10, v29, 2, v30\n\tv_add_u32 v10, 16*528, v10\n\tv_add_u32 v11, s53, v10\n\tv_lshl_add_u32 v12, v29, 1, v30\n\tv_add_u32 v12, 24*528, v12\n\tv_add_u32 v13, 2112, v12\n\tv_add_u32 v14, v29, v30\n\tv_add_u32 v14, 32*528, v14\n\t"
    "s_lshl_b32 s53, s58, 4\n\ts_add_u32 s53, s53, %[ex]\n\tv_mov_b32 v15, s53\n\tv_cvt_f32_ubyte0 v31, v31\n\tv_mul_f32 v16, 0x42800000, v31\n\tv_add_f32 v17, v16, v16\n\tv_mov_b32 v20, 1\n\t"
    "v_and_b32 v29, 15, %[t]\n\tv_and_b32 v30, 48, %[t]\n\tv_mad_u32_u24 v30, v29, s54, v30\n\ts_lshl_b32 s55, s59, 1\n\ts_lshr_b32 s55, 0x48, s55\n\ts_add_u32 s55, s55, s63\n\ts_and_b32 s55, s55, 3\n\ts_mul_i32 s53, s55, 16*528\n\ts_movk_i32 s57, 41*528\n\ts_cmp_eq_u32 s55, 2\n\ts_cselect_b32 s53, s57, s53\n\ts_cselect_b32 s57, 32*528, s57\n\t"
    "s_add_u32 s53, s53, s52\n\ts_add_u32 s57, s57, s52\n\tv_add_u32 v18, s53, v30\n\tv_add_u32 v19, s57, v30\n\t"
    "v_min_u32 v29, 237*16, v119\n\tv_subrev_u32 v30, 204*16, v29\n\tv_add_u32 v29, %[de], v29\n\tv_min_u32 v29, v29, v30\n\ts_sub_u32 s53, s52, 4640\n\tv_add_u32 v29, s53, v29\n\tv_mov_b32 v24, 0\n\tv_mov_b32 v25, 0\n\tv_mov_b32 v26, 0\n\tv_mov_b32 v27, 0\n\tds_write_b128 v29, v[24:27]\n\t"
    "s_cmp_lg_u32 s55, 3\n\ts_cbranch_scc1 9f\n\t"
    "v_bfe_u32 v18, %[t], 1, 5\n\tv_and_b32 v19, 1, %[t]\n\ts_cmp_lt_u32 s62, 8\n\ts_cselect_b32 s53, 2, 1\n\ts_mul_i32 s53, s53, %[sl]\n\tv_mul_u32_u24 v21, s53, v19\n\ts_sub_u32 s55, 2, s59\n\ts_lshl_b32 s55, s55, 7\n\tv_add_u32 v18, s55, v18\n\ts_cmp_lg_u32 s59, 3\n\ts_cbranch_scc1 7f\n\t"
    "v_bfe_u32 v18, %[t], 5, 1\n\tv_lshl_add_u32 v18, v18, 7, v118\n\tv_mov_b32 v19, s50\n\tv_mov_b32 v21, %[sl]\n"
    "7:\n\tv_add_u32 v19, s51, v19\n\tv_lshl_add_u32 v21, v18, 4, v21\n\tv_lshrrev_b32 v24, 3-" STR(TL) ", v19\n\tv_and_b32 v19, 7>>" STR(TL) ", v19\n\tv_lshlrev_b32 v19, 4, v19\n\tv_lshl_add_u32 v19, v24, 7, v19\n\t"
    "v_and_b32 v24, (1<<" STR(TL) ")-1, v18\n\tv_lshl_add_u32 v19, v24, 7-" STR(TL) ", v19\n\tv_lshrrev_b32 v18, " STR(TL) ", v18\n\tv_mul_u32_u24 v18, %[ust], v18\n\tv_add_u32 v18, v18, v19\n\tv_add_u32 v19, 512, v21\n\t"
    "s_branch 9f\n"
    "4:\n\t"
    "v_bfe_u32 v118, %[t], 3, 5\n\tv_lshrrev_b32 v117, 1, v118\n\tv_and_b32 v117, 4, v117\n\tv_and_b32 v116, 2, v118\n\tv_and_b32 v115, 7, %[t]\n\tv_or_b32 v116, v116, v117\n\tv_xor_b32 v115, v115, v116\n\tv_lshlrev_b32 v115, 4, v115\n\t"
    "s_mul_i32 s54, s61, 9\n\ts_sub_u32 s55, s62, 14\n\ts_lshr_b32 s55, s55, 1\n\ts_add_u32 s54, s54, s55\n\ts_lshl_b32 s54, s54, 7\n\t"
    "s_mul_i32 s52, s61, 0xa2000\n\ts_mul_i32 s53, s62, 0x180\n\ts_add_u32 s52, s52, s53\n\ts_cmp_ge_u32 s62, 11\n\ts_cselect_b32 s53, 0x51000-0x480, 0\n\ts_add_u32 s52, s52, s53\n\ts_sub_u32 s52, s52, 0xc00\n\t"
    "s_movk_i32 s55, 0x480\n\ts_cmp_ge_u32 s62, 14\n\ts_cselect_b32 s54, s54, s52\n\ts_cselect_b32 s55, 0x2400, s55\n\tv_mad_u32_u24 v118, v118, s55, v115\n\ts_lshl_b32 s55, s55, 5\n\t"
    "s_lshl_b32 s52, s63, 10\n\ts_add_u32 m0, s52, %[r2]\n\ts_mov_b32 s58, 0x510000\n\ts_mov_b32 s59, 0x20000\n\ts_cmp_ge_u32 s62, 14\n\ts_cselect_b32 s53, " STR(PRE1) "-1, " STR(PRE2) "-1\n\ts_setprio 3\n\t"
    "s_waitcnt lgkmcnt(0)\n\t"
    "s_bitcmp1_b32 %[wg], 3\n\ts_cselect_b64 s[56:57], s[74:75], s[72:73]\n\ts_cmp_ge_u32 s62, 14\n\ts_cselect_b64 s[56:57], s[56:57], s[76:77]\n\ts_cselect_b32 s52, 0x180, -1\n\ts_and_b32 s57, s57, 0xffff\n\ts_add_u32 s60, s56, s54\n\ts_and_b32 s60, s60, 0x380\n\ts_cmp_eq_u32 s60, s52\n\ts_cselect_b32 s53, " STR(PRE3) "-1, s53\n\t"
    "s_load_dwordx2 s[96:97], s[66:67], 0x0\n\t" "s_barrier\n\t"
    "\n3:\n\t" "buffer_load_dwordx4 v118, s[56:59], s54 offen nt lds\n\ts_add_u32 m0, m0, 0x1000\n\ts_add_u32 s54, s54, s55\n\ts_sub_u32 s53, s53, 1\n\ts_cbranch_scc0 3b\n"
    "9:\n8:"
    : [xa]"=&{v[2:5]}"(xa),[xb]"=&{v[6:9]}"(xb_),[xc]"=&{v30}"(xc),[wa]"=&{v[10:13]}"(wa),[wb]"=&{v[14:17]}"(wb),[wc]"=&{v31}"(wc),[d0]"=&{v[18:21]}"(dq[0]),[d1]"=&{v[22:25]}"(dq[1]),[d2]"=&{v[26:29]}"(dq[2])
    : [ka]"s"(__builtin_amdgcn_kernarg_segment_ptr()),[wg]"s"(BID),[t]"v"(TID),[yst]"i"(YST),[wy]"i"(W_Y),[r2]"i"(O_R2),[sl]"i"(SLSZ),[tb]"i"(O_CH+O_TB),[ex]"i"(O_EXP),[de]"i"(O_DE),[ust]"i"(GST_)
    : "memory","scc","m0","v127","s50","s51","s52","s53","s54","s55","s56","s57","s58","s59","s60","s61","s62","s63","s64","s65","s66","s67","s68","s69","s70","s71","s72","s73","s74","s75","s76","s77","s78","s79",
      "s80","s81","s82","s83","s84","s85","s86","s87","s88","s89","s90","s91","s92","s93","s94","s95","s99");
  int wv=RFL(TID>>6), bid=BID, q4=wv>>2, w4=wv&3;
  int xg=bid&7, lb=bid>>3, tb=32*xg+lb, nch=2+(lb<8);
  u64 ep; u32 ldw;
  {
  }
  #define KILLIN "=v"(xa),"=v"(xb_),"=v"(xc),"=v"(wa),"=v"(wb),"=v"(wc),"=v"(dq[0]),"=v"(dq[1]),"=v"(dq[2])
  if(__builtin_expect(q4==3&&lb>=8,0)){
    int fu=18*xg+lb-14, kq=2*xg+(lb>=11), cgp=lb-8-3*(lb>=11);
  u32 wst= lb>=14? F_*64:D_*64;
  u32 wso=(lb>=14? (fu>>1)*128 : kq*(288*D_*2)+384*cgp)+(lb>=14? PRE1:PRE2)*wst, wld=(u32)(size_t)sm+O_R2+w4*1024+(lb>=14? PRE1:PRE2)*4096; u32x4 wrs=mkrs(lb>=14? ((fu&1)? wu:wg):wd,D_*F_*2);
  GETEP
  #define FLANE(RS) LANE(ln); int g=ln>>4, li=ln&15, qq=li>>2, pq=li&3; u32 wvo=(u32)(8*w4+(ln>>3))*(RS)+(u32)((ln&7)^(((ln>>3)&2)|((w4&1)<<2)))*16u; \
    const lch* tp=sm+O_R2+(8*g+qq)*128+((w4^((qq>>1)|((g&1)<<1)))*32)+pq*8;
  #define WFR(img,ks) trfrag(tp+(img)*CHSZ+(ks)*4096,tp+(img)*CHSZ+(ks)*4096+512)
  u64 tag=ep+1ull; const u32x4 I4={INV,INV,INV,INV};
  if(lb>=14){
    FLANE(F_*2)
    int mt=fu&1;
    const lch* xp=sm+O_TILE+li*HSTR+g*16; int of=(mt*288+(fu>>1)*4+w4)*256+(li&7)*32+g*8+(li>>3)*4;
    const lch* mp=sm+O_RC+(li&7)*4;
    asm volatile("" : "+v"(xp),"+v"(of),"+v"(mp));
    rsrc_t rsG=RSRC(ws+W_GU,2*8*F_*2);
    if(w4==0) BST128(I4,rsG,(mt*288+(fu>>1)*4)*256+ln*16,0,SC1);
    bool slow=(((u32)wrs[0]+wso)&0x380u)==0x180u;
    if(slow){ wso-=(PRE1-PRE3)*wst; wld-=(PRE1-PRE3)*4096; FILL(5+PRE1-PRE3,VW(W3)); } else FILLD(5,W1,W1B);
    if(w4==0){ VMW(5); if(ln==0) GST(ws+W_FL+(576+fu)*8,tag); }
    if(slow) FILLD(13-PRE1,W3,W1B); else FILLD(13-PRE1,W1,W1B);
    bf16x8 fB[18];
    VMW(LAST1);
    gsync(sm,C_P,4);
    #pragma unroll
    for(int ks=0;ks<18-LAST1;ks++) fB[ks]=WFR(0,ks);
    f32x4 cs={0,0,0,0}; const bf16x8 on=BC(bf16x8,(u32x4{0x3f803f80u,0x3f803f80u,0x3f803f80u,0x3f803f80u}));
    SCHED();
    #pragma unroll
    for(int ks=0;ks<18-LAST1;ks++) cs=__builtin_amdgcn_mfma_f32_16x16x32_bf16(fB[ks],on,cs,0,0,0);
    PIN(cs);
    #pragma unroll
    for(int ks=0;ks<18-LAST1;ks++) asm volatile("" : "+v"(fB[ks]));
    DRAIN();
    gsync(sm,C_P,8);
    #pragma unroll
    for(int ks=18-LAST1;ks<18;ks++) fB[ks]=WFR(0,ks);
    SCHED();
    #pragma unroll
    for(int ks=18-LAST1;ks<18;ks++) cs=__builtin_amdgcn_mfma_f32_16x16x32_bf16(fB[ks],on,cs,0,0,0);
    SCHED(); float cA,cB;
    float ri,ce,p0,p1,q0,q1; u32 mdw;
    { bf16x8 xb[9];
      gwait<XS1>(sm,C_X,XG1);
      gwait4<64>(C_X,4,xp,xb[0],xb[1],xb[2],xb[3]);
      mdw=LD(u32,mp); xb[4]=LD(bf16x8,xp+256); xb[5]=LD(bf16x8,xp+320);
      SCHED();
      #pragma unroll
      for(int ks=0;ks<8;ks++){
        if(ks==0) asm volatile("v_mfma_f32_16x16x32_bf16 v[112:115], %0, %2, 0\n\tv_mfma_f32_16x16x32_bf16 v[116:119], %1, %2, 0" :: "v"(fB[0]),"v"(fB[9]),"v"(xb[0]));
        else asm volatile("v_mfma_f32_16x16x32_bf16 v[112:115], %0, %2, v[112:115]\n\tv_mfma_f32_16x16x32_bf16 v[116:119], %1, %2, v[116:119]" :: "v"(fB[ks]),"v"(fB[ks+9]),"v"(xb[ks]));
        SCHED();
        if(ks<3) xb[ks+6]=LD(bf16x8,xp+(ks+6)*64);
        if(ks==2){ PIN(cs); cA= li<8? cs[0]:cs[2]; cB= li<8? cs[1]:cs[3]; PIN(cA); PIN(cB); }
        if(ks==3){ PIN(mdw); ri=rcpf_(bhi(mdw)); float nm=-blo(mdw); cA*=nm; cB*=nm; PIN(cA); PIN(cB); } if(ks==4){ ce=ri*-1.4426950408889634f; p0=cA*ri; p1=cB*ri; PIN(p0); PIN(p1); } if(ks==5){ q0=cA*ce; q1=cB*ce; PIN(q0); PIN(q1); }
        SCHED(); }
      #define RR8 " row_ror:8 row_mask:0xf bank_mask:"
      float v0,v1,s0,s1;
      asm volatile("v_mfma_f32_16x16x32_bf16 v[112:115], %4, %6, v[112:115]\n\tv_mfma_f32_16x16x32_bf16 v[116:119], %5, %6, v[116:119]\n\ts_nop 7\n\tv_add_f32_dpp %0, v116, v112" RR8 "0x3\n\tv_add_f32_dpp %1, v117, v113" RR8 "0x3\n\tv_add_f32_dpp %0, v114, v118" RR8 "0xc\n\tv_add_f32_dpp %1, v115, v119" RR8 "0xc\n\t"
        "v_fmac_f32 %2, %0, %8\n\tv_fmac_f32 %3, %1, %8\n\tv_fma_f32 %0, %0, %7, %9\n\tv_fma_f32 %1, %1, %7, %10"
        : "=&v"(v0),"=&v"(v1),"+v"(q0),"+v"(q1) : "v"(fB[8]),"v"(fB[17]),"v"(xb[8]),"v"(ri),"v"(ce),"v"(p0),"v"(p1));
      s0=q0; s1=q1;
      if(mt==0){ v0*=rcpf_(1.f+__builtin_amdgcn_exp2f(s0)); v1*=rcpf_(1.f+__builtin_amdgcn_exp2f(s1)); }
      __builtin_amdgcn_raw_buffer_store_b32(nv(pk(v0,v1)),rsG,of,0,SC1); garrive(sm,C_H); }
    if(mt==0&&w4==0){
    LANE(l2); ln=l2; li=ln&15; g=ln>>4;
    int c9=(lb-14)>>1, tk=(xg&6)+((li>>2)&1), t=(int)LD(u16,sm+O_IDX+tk*2), c=64*c9+32*(xg&1)+16*(li>>3)+4*(li&3)+g; f32x4 pr;
    { u32 u=LD(u16,sm+O_IDX+16+tk*2)^0x8000u, xb=LD(u16,sm+O_TILE+XAD(tk,c*2)), sv=LD(u32,sm+O_RC+tk*4); float ds=b2f(Dsk[c]); u^=(u32)(((int)(u<<16))>>31)&0x7fffu;
      poll1<0>(ws+W_FL+(64*c9+ln)*8,tag);
 poll1<0>(ws+W_FL+(720+(ln<48? ln:47))*8,tag);
      float xn=(b2f(xb)-blo(sv))*rcpf_(bhi(sv)); pr=f32x4{b2f(xb),xn,xn*ds,sigm_f(b2f(u))}; }
    gwait<2>(sm,C_PR,PRN);
    { u32x4 rs=mkrs(ws+W_PART+(xg*9+c9)*4096,4096), ry=mkrs(ws+W_Y,D_*YST);
      int yo=c*YST+(t&~1)*2, po=ln*16; u32 a[4], t0,cnt,yb; u16* op=out+t*D_+c; u32 ysl=0x3f80u<<((t&1)*16); PIN(ysl); f32x2 pe={pr[1]*-1.4426950408889634f,pr[3]*-1.4426950408889634f};
      #define LD_P "buffer_load_dwordx4 v[112:115], %[of], %[rs], 0 offen sc1\n\tbuffer_load_dwordx4 v[116:119], %[of], %[rs], 0 offen offset:1024 sc1\n\tbuffer_load_dwordx4 v[120:123], %[of], %[rs], 0 offen offset:2048 sc1\n\tbuffer_load_dwordx4 v[124:127], %[of], %[rs], 0 offen offset:3072 sc1\n\t"
      #define LD_YB "buffer_load_dword %[yb], %[yo], %[ry], 0 offen sc1\n\t"
      #define AD4(o,a,b,c,d) "v_add_f32 %[t0], v" #a ", v" #b "\n\tv_add_f32 %[" #o "], v" #c ", v" #d "\n\tv_add_f32 %[" #o "], %[t0], %[" #o "]\n\t"
      u64 ym;
      #define PGAP "s_sleep " STR(PSL) "\n\t"
      u64 nm;
      asm volatile(LD_YB PGAP LD_P "s_mov_b32 %[cnt], 0x20000\n1:\n\ts_waitcnt vmcnt(0)\n\t" AD4(a0,112,116,120,124) AD4(a1,113,117,121,125) "v_cmp_eq_u32 %[ym], -1, %[yb]\n\t" AD4(a2,114,118,122,126) "v_cmp_u_f32 %[nm], %[a0], %[a1]\n\t" AD4(a3,115,119,123,127)
        "s_or_b64 %[nm], %[nm], %[ym]\n\tv_cmp_u_f32 vcc, %[a2], %[a3]\n\ts_or_b64 vcc, vcc, %[nm]\n\ts_cbranch_scc0 9f\n\t"
        CHK_I0 MX2(116,117) MX2(118,119) MX2(120,121) MX2(122,123) MX2(124,125) MX2(126,127) CHK_IE
        "s_or_b64 exec, vcc, %[ym]\n\ts_cbranch_scc0 8f\n\ts_sleep " STR(QSLP) "\n\ts_mov_b64 exec, %[ym]\n\ts_cbranch_execz 2f\n\t" LD_YB "2:\n\ts_mov_b64 exec, vcc\n\ts_cbranch_execz 3f\n\t" LD_P
        "3:\n\ts_mov_b64 exec, -1\n\ts_sub_u32 %[cnt], %[cnt], 1\n\ts_cbranch_scc0 1b\n8:\n\ts_mov_b64 exec, -1\n9:"
        : [t0]"=&v"(t0),[cnt]"=&s"(cnt),[a0]"=&v"(a[0]),[a1]"=&v"(a[1]),[a2]"=&v"(a[2]),[a3]"=&v"(a[3]),[yb]"=&v"(yb),[ym]"=&s"(ym),[nm]"=&s"(nm) : [of]"v"(po),[rs]"s"(rs),[yo]"v"(yo),[ry]"s"(ry),"v"(op),"v"(pr),"v"(pe) : PCLOB);
      auto s0=__builtin_amdgcn_permlane32_swap(a[0],a[2],false,false); auto s1=__builtin_amdgcn_permlane32_swap(a[1],a[3],false,false);
      u32 x0=s0[0],x1=s0[1],y0=s1[0],y1=s1[1]; u32 p0=BC(u32,BC(float,x0)+BC(float,x1)), p1=BC(u32,BC(float,y0)+BC(float,y1));
      auto s2=__builtin_amdgcn_permlane16_swap(p0,p1,false,false); u32 z0=s2[0], z1=s2[1]; float s=BC(float,z0)+BC(float,z1);
      float gq=__builtin_fmaf(s,pr[3],pr[1])*rcpf_(1.f+__builtin_amdgcn_exp2f(__builtin_fmaf(s,pe[1],pe[0])));
      float sy=D2(yb,ysl,pr[2]);
      float o=__builtin_fmaf(sy,gq,pr[0]);
      *op=(u16)(pk(o,o)&0xffffu);
      if(bid==112&&ln==0) GST(ws+W_EPOCH,tag); }
    }
  } else
  {
    FLANE(D_*2)
    bf16x8 fr[27];
    rsrc_t rsP=RSRC(ws+W_PART+cgp*12288+kq*256+PWB,8*9*4096-2*12288);
    #pragma unroll
    for(int i=0;i<3;i++) BST128(I4,rsP,li<8? PLO+g*16+i*4096 : OOR,0,SC1);
    if(PRE2<9) FILLD(9-PRE2,W2_,W2B);
    wso+=128u-9u*wst;
    FILLD(9,W2_,W2B);
    VMW(9); gsync(sm,C_P,4);
    #pragma unroll
    for(int ks=0;ks<9;ks++) fr[ks]=WFR(0,ks);
    gsync(sm,C_P,8);
    if(w4==0&&ln==0) GST(ws+W_FL+(720+6*xg+lb-8)*8,tag);
    wso+=128u-9u*wst; wld-=2*CHSZ;
    FILL(9,VW(W2B));
    int fo=W_FL+(576+((9*kq)&~1)+(ln<9? ln:9))*8;
    if(w4==0) asm volatile("global_load_dwordx2 v[112:113], %0, %1 sc1\n\ts_waitcnt vmcnt(10)" :: "v"(fo),"s"(ws) : "memory"); else VMW(9);
    gsync(sm,C_P,12);
    #pragma unroll
    for(int ks=0;ks<9;ks++) fr[9+ks]=WFR(1,ks);
    { int t2=TID; asm volatile("" : "+v"(t2)); ln=t2&63; li=ln&15; g=ln>>4; }
    const lch* hp=sm+O_TILE+(li&7)*64+g*16; int po= li<8? PLO+g*16+4096 : OOR; PIN(hp); PIN(po);
    u32x4 rs=mkrs(ws+W_GU,2*8*F_*2);
    int of=((ln>>5)*288+18*kq+2*w4)*256+(ln&31)*16; lch* hd=sm+O_TILE+w4*512+((ln>>1)&7)*64+((ln>>4)&1)*32+(ln&1)*16+(ln>>5)*8; PIN(of); PIN(hd); asm volatile("" : "+s"(rs));
    u32x4 rp=mkrs(ws+W_PART+cgp*12288+kq*256+PWB-4096,8*9*4096); u32 ch=C_H*4, one=1; int o3= w4==0? of+4096:OOR; PIN(ch); PIN(one); PIN(o3); asm volatile("" : "+s"(rp));
    DRAIN();
    if(w4==0){ u64 bad; asm volatile("v_cmp_ne_u64 %0, %1, v[112:113]" : "=s"(bad) : "s"(tag) : "memory");
      if(bad!=0ull) poll1<0>(ws+fo,tag); }
    __builtin_amdgcn_s_waitcnt(0x0f70);
    gsync(sm,C_P,16);
    { u32 t0,t1,cnt;
      #define LD_G "buffer_load_dwordx4 v[112:115], %[of], %[rs], 0 offen sc1\n\tbuffer_load_dwordx4 v[116:119], %[of], %[rs], 0 offen offset:2048 sc1\n\tbuffer_load_dwordx4 v[120:123], %[o3], %[rs], 0 offen sc1\n\t"
      #define HM1(a,b) "v_and_b32 %[t0], %[lm], v" #b "\n\tv_and_b32 v" #b ", %[hm], v" #b "\n\tv_dot2_f32_bf16 %[t0], v" #a ", %[t0], 0\n\tv_dot2_f32_bf16 v" #a ", v" #a ", v" #b ", 0\n\t"
      #define HM2(a,b) "v_and_b32 %[t1], %[lm], v" #b "\n\tv_and_b32 v" #b ", %[hm], v" #b "\n\tv_dot2_f32_bf16 %[t1], v" #a ", %[t1], 0\n\tv_dot2_f32_bf16 v" #a ", v" #a ", v" #b ", 0\n\t"
      #define HMUL(a,b,c,d,o) "v_permlane32_swap_b32 v" #a ", v" #c "\n\tv_permlane32_swap_b32 v" #b ", v" #d "\n\t" HM1(a,c) HM2(b,d) "v_cvt_pk_bf16_f32 v" #a ", %[t0], v" #a "\n\ts_nop 1\n\tv_cvt_pk_bf16_f32 v" #b ", %[t1], v" #b "\n\tds_write_b64 %[hd], v[" #a ":" #b "] offset:" #o "\n\t"
      asm volatile(LD_G :: [of]"v"(of),[rs]"s"(rs),[o3]"v"(o3) : "memory");
      #pragma unroll
      for(int ks=0;ks<G2;ks++) fr[18+ks]=WFR(0,ks);
      #pragma unroll
      for(int ks=0;ks<G2;ks++) PIN(fr[18+ks]);
      asm volatile(POLLASM(LD_G,CHK_I0 MX2(116,117) MX2(118,119) MX2(120,121) MX2(122,123) CHK_IE,GSLP)
        HMUL(112,113,114,115,0) HMUL(116,117,118,119,2048) "s_cmp_lg_u32 %[w4], 0\n\ts_cbranch_scc1 8f\n\t" HMUL(120,121,122,123,4096) "8:\n\ts_mov_b64 exec, 1\n\tds_add_u32 %[ch], %[one]\n\ts_mov_b64 exec, -1\n\t"
        "s_mov_b32 %[cnt], 0x20000\n6:\n\tds_read_b32 %[t0], %[ch]\n\tds_read_b128 v[112:115], %[hp]\n\tds_read_b128 v[116:119], %[hp] offset:512\n\tds_read_b128 v[120:123], %[hp] offset:1024\n\tds_read_b128 v[124:127], %[hp] offset:1536\n\ts_waitcnt lgkmcnt(4)\n\t"
        "v_cmp_gt_u32 vcc, 4, %[t0]\n\ts_cbranch_vccz 7f\n\ts_sleep 1\n\ts_sub_u32 %[cnt], %[cnt], 1\n\ts_cbranch_scc0 6b\n7:"
        : [t0]"=&v"(t0),[t1]"=&v"(t1),[cnt]"=&s"(cnt) : [of]"v"(of),[rs]"s"(rs),[o3]"v"(o3),[w4]"s"(w4),[hd]"v"(hd),[hm]"s"(0xffff0000u),[lm]"s"(0xffffu),[ch]"v"(ch),[one]"v"(one),[hp]"v"(hp),"v"(po) : PCLOB); }
    { bf16x8 hb[9];
      #define MF "v_mfma_f32_16x16x32_bf16 "
      #define MF3(h) MF "%0, %3, " h ", %0\n\t" MF "%1, %4, " h ", %1\n\t" MF "%2, %5, " h ", %2"
      #define ACC "+v"(fr[0]),"+v"(fr[9]),"+v"(fr[18])
      #define FRS(k) "v"(fr[k]),"v"(fr[9+(k)]),"v"(fr[18+(k)])
      asm volatile("s_waitcnt lgkmcnt(3)\n\t" MF "%0, %0, v[112:115], 0\n\t" MF "%1, %1, v[112:115], 0\n\t" MF "%2, %2, v[112:115], 0\n\ts_waitcnt lgkmcnt(0)\n\t" MF3("v[116:119]") : ACC : FRS(1));
      hb[4]=LD(bf16x8,hp+4*512); hb[5]=LD(bf16x8,hp+5*512); hb[6]=LD(bf16x8,hp+6*512); SCHED();
      asm volatile(MF3("v[120:123]") : ACC : FRS(2));
      #pragma unroll
      for(int ks=G2;ks<9;ks++) fr[18+ks]=WFR(0,ks);
      SCHED();
      asm volatile(MF3("v[124:127]") : ACC : FRS(3));
      hb[7]=LD(bf16x8,hp+7*512); hb[8]=LD(bf16x8,hp+8*512); SCHED();
      #pragma unroll
      for(int ks=4;ks<8;ks++){ asm volatile(MF3("%6") : ACC : FRS(ks),"v"(hb[ks])); SCHED(); }
      asm volatile(MF3("%6") "\n\ts_nop 5\n\tbuffer_store_dwordx4 %0, %7, %8, 0 offen sc1\n\tbuffer_store_dwordx4 %1, %7, %8, %9 offen sc1\n\tbuffer_store_dwordx4 %2, %7, %8, %10 offen sc1\n\ts_mov_b64 exec, 1\n\tds_add_u32 %11, %12\n\ts_mov_b64 exec, -1"
        : ACC : FRS(8),"v"(hb[8]),"v"(po),"s"(rp),"s"(4096),"s"(8192),"v"(ch),"v"(one) : "memory"); }

  }
  asm volatile("s_endpgm" : KILLIN :: "memory"); }
  {
  if(__builtin_expect(q4!=0,0)){ int grp= q4==1? nch-1 : q4-2;
    chan(sm,grp,(w4+(0x48>>(2*q4)))&3,72*xg+(q4==3? 16+lb : 2*lb+(lb<8? 0:8)+2-q4),(q4+1)>>2,ws,xa,xb_,wa,wb,dq[0]);
    asm volatile("s_endpgm" : KILLIN :: "memory"); }
  u32x4 xq[3]; f32x2 rc; int xr_,xr2_;
  {
    LANE(ln); int g=ln>>4, li=ln&15, qq=li>>2, pq=li&3, j=ln&31, rl=2*w4+(ln>>5);
    lch* tile=sm+O_TILE; u32 Tr=(ln>>1)&16;
    int uo=W_U+(tb>>TL)*GST_+(tb&((1<<TL)-1))*(128>>TL)+(w4*1024<<TL)+(ln>>(3-TL))*128+(ln&(7>>TL))*16, uo1=uo+(4096<<TL), uo2=uo+(8192<<TL); PIN(uo); PIN(uo1); PIN(uo2);
    lch* tw=tile+rl*TSTR2+j*16; lch* tw2=tw+1024-j*12; u32 one=0x3f803f80u; const lch* tp=tile+qq*TSTR2+(w4*64+g*16+pq*4)*2;
    int go= ln<32? W_RG+(ln>>2)*8192+(tb*4+(ln&3))*8 : W_ST+(tb*8+(ln&7))*8; lch* kw=sm+O_RV+rl*2; const lch* kr=sm+(ln<32? O_RV+(ln&3)*4 : O_MD+(ln&7)*4); PIN(kw); PIN(kr);
    int xr=(32*(tb>>2)+8*g+(li&7))*(D_*2)+288*(lb&3)+32*w4+(ln&8)*2, xr2= w4==0? xr+256:OOR;
    rsrc_t rx=RSRC(x,L_*D_*2); u64 gm=((1ull<<(4*NRG))-1ull)|0xff00000000ull; u32 um=~UMASK, km=0x80008000u; asm volatile("" : "+s"(gm),"+s"(um),"+s"(km),"+s"(ws));
    asm volatile("s_waitcnt vmcnt(3+" STR(DQN) ")" : "+v"(wa),"+v"(wb),"+v"(wc),"+v"(Tr),"+v"(tw),"+v"(tw2),"+v"(tp),"+v"(one),"+v"(go),"+v"(xr),"+v"(xr2) :: "memory");
    u32 wq[9]={wa[0],wa[1],wa[2],wa[3],wb[0],wb[1],wb[2],wb[3],wc}; float sw;
    { float a[3];
      #pragma unroll
      for(int i=0;i<9;i++) a[i%3]=D2(wq[i],one,i<3? 0.f:a[i%3]);
      sw=add16x(sum16((a[0]+a[1])+a[2])); }
    GETEP u32 tg=(u32)ep+1u; Tr=((upat(tg,w4)>>Tr)&0xffffu)*0x10001u; u32 tgv=tg;
    float z_[9]={0,0,0,0,0,0,0,0,0};
    asm volatile("s_waitcnt vmcnt(" STR(DQN) ")" : "+v"(xa),"+v"(xb_),"+v"(xc),"+v"(Tr),"+v"(sw),"+v"(tgv),"+v"(z_[0]),"+v"(z_[1]),"+v"(z_[2]),"+v"(z_[3]),"+v"(z_[4]),"+v"(z_[5]) :: "memory");
    u32 v[9]={xa[0],xa[1],xa[2],xa[3],xb_[0],xb_[1],xb_[2],xb_[3],xc};
    { float xf[18], s1, s2, s3;
      #pragma unroll
      for(int i=0;i<9;i++){ z_[i&1]=D2(v[i],one,z_[i&1]); z_[2+(i&1)]=D2(v[i],v[i],z_[2+(i&1)]); z_[4+(i&1)]=D2(v[i],wq[i],z_[4+(i&1)]); }
      s1=z_[0]+z_[1]; s2=z_[2]+z_[3]; s3=z_[4]+z_[5];
      SCHED();
      #define RSTEP(C) s1+=dppf<C>(s1); s2+=dppf<C>(s2); s3+=dppf<C>(s3); SCHED();
      RSTEP(0xB1) RSTEP(0x4E) RSTEP(0x141) RSTEP(0x140)
      s1=add16x(s1)*(1.f/576.f); s2=add16x(s2)*(1.f/576.f); s3=add16x(s3);
      float mm=s1*s1, var=s2-mm;
      if(__builtin_expect(BALLOT(!(mm<=64.f*var))!=0ull,0)){
        float q=0.f;
        #pragma unroll
        for(int i=0;i<9;i++){ u32 t=v[i]; PIN(t); float d0=blo(t)-s1, d1=bhi(t)-s1; q=__builtin_fmaf(d0,d0,q); q=__builtin_fmaf(d1,d1,q); }
        var=add16x(sum16(q))*(1.f/576.f); }
      #define UNP(i) PIN(v[i]); xf[2*(i)]=blo(v[i]); xf[2*(i)+1]=bhi(v[i]); PIN(xf[2*(i)]); PIN(xf[2*(i)+1]); SCHED();
      u32 vb=pk(var,s1); UNP(0)
      float den=blo(vb)+1e-5f, m=bhi(vb); UNP(1)
      vb=pk(den,den); s3=__builtin_fmaf(-m,sw,s3); UNP(2)
      den=__builtin_amdgcn_sqrtf(bhi(vb)); UNP(3)
      u32 md=pk(m,den); UNP(4)
      float ri=rcpf_(bhi(md)); UNP(5)
      s3*=ri; float rs_=ri*0.0625f; UNP(6)
      u32 u=pk(s3,s3); float cs_=-m*rs_; UNP(7)
      { u32 sg=(u32)((int)u>>31); PIN(sg); u^=sg|km; } UNP(8)
      asm volatile("s_mov_b64 exec, 0x1\n\ts_mov_b32 exec_hi, 1\n\tds_write_b16 %0, %1\n\tv_add_u32 %0, %0, %0\n\tds_write_b32 %0, %2 offset:%3\n\ts_mov_b32 exec_hi, 0\n\tds_add_u32 %4, %5\n\ts_mov_b64 exec, -1" : "+v"(kw) : "v"(u),"v"(md),"i"(O_MD-2*O_RV),"v"(C_I*4),"v"(1) : "memory");
      if(w4==3){ gwait<0>(sm,C_I,4); asm volatile("ds_read_b32 v118, %4\n\tv_mov_b32 v119, %1\n\ts_mov_b64 exec, %3\n\ts_waitcnt lgkmcnt(0)\n\tglobal_store_dwordx2 %0, v[118:119], %2 sc1\n\ts_mov_b64 exec, -1" :: "v"(go),"v"(tgv),"s"(ws),"s"(gm),"v"(kr) : "memory"); }
      SCHED();
      u32 h[9];
      #define UH(i) h[i]=(pkz(__builtin_fmaf(xf[2*(i)],rs_,cs_),__builtin_fmaf(xf[2*(i)+1],rs_,cs_))&um)|Tr;
      UH(0) UH(1) UH(2) UH(3) ST(u32x4,tw)=u32x4{h[0],h[1],h[2],h[3]}; UH(4) UH(5) UH(6) UH(7) ST(u32x4,tw+512)=u32x4{h[4],h[5],h[6],h[7]};
      UH(8) ST(u32,tw2)=h[8]; }
    gsync(sm,C_L,4);
    { u32x4 uq[3];
      #pragma unroll
      for(int z=0;z<3;z++) uq[z]=BC(u32x4,trfrag(tp+z*512,tp+z*512+4*TSTR2));
      asm volatile("global_store_dwordx4 %0, %1, %2 sc1" :: "v"(uo),"v"(uq[0]),"s"(ws) : "memory");
      asm volatile("global_store_dwordx4 %0, %1, %2 sc1" :: "v"(uo1),"v"(uq[1]),"s"(ws) : "memory");
      asm volatile("s_cmp_lg_u32 %3, 0\n\ts_cbranch_scc1 1f\n\tglobal_store_dwordx4 %0, %1, %2 sc1\n1:\n\ts_nop 1" :: "v"(uo2),"v"(uq[2]),"s"(ws),"s"(w4),"v"(uq[0]),"v"(uq[1]) : "memory","scc");
      xr_=xr; xr2_=xr2;
      #define XQ { rsrc_t rx_=RSRC(x,L_*D_*2); xq[0]=BLD128(rx_,xr_,0,0); xq[1]=BLD128(rx_,xr_+128,0,0); xq[2]=BLD128(rx_,xr2_,0,0); }
    }
    if(lb>=2&&lb<8){
      const u16* wm=(lb&1)? wu:wg; int co=(3-(int)((u64)wm>>7)-xg)&7, r_=576-12*PFN+(((lb>>1)-1)*4+w4)+12*j;
      asm volatile("buffer_load_dword v120, %0, %1, 0 offen" :: "v"(j<PFN&&(ln<32||co==0)? r_*(F_*2)+(9*xg+co)*128+(ln>>5)*1024 : OOR),"s"(mkrs(wm,D_*F_*2)) : "memory"); }
    if(lb>=8&&lb<14) garrive(sm,C_D);
  }
  FP fp; int trow;
  { LANE(ln); int g=ln>>4, li=ln&15; u64 tag=ep+1ull;
    const char* sp_=ws+W_ST+(32*(tb>>2)+8*g+(li&7))*8; u64 fs, ff; const char* fp_=ws+W_FL+(144*(lb&3)+16*w4+64*(g<2||w4==0? (g<3? g:2):0)+li)*8;
    if(lb==CTLB||lb>=14){
      u32 M=(u32)tag, rank,key;
      top8(sm,w4,ln,M,ws,rank,key);
      {
        int t0=__builtin_amdgcn_readlane((int)key,__builtin_ctzll(BALLOT(rank==(u32)(2*w4)))), t1=__builtin_amdgcn_readlane((int)key,__builtin_ctzll(BALLOT(rank==(u32)(2*w4+1))));
        int tsel=~((ln>>5)? t1:t0)&2047, j=ln&31; rsrc_t rx=RSRC(x,L_*D_*2);
        u32x4 a=BLD128(rx,tsel*1152+j*16,0,0), b=BLD128(rx,tsel*1152+j*16+512,0,0); u32 c=BLD32(rx,tsel*1152+j*4+1024,0,0);
        const char* rp_=ws+W_ST+tsel*8; u64 f1=GLD(rp_);
        garrive(sm,C_D);
        if(lb==CTLB&&rank<8&&!(ln&1)) GST(ws+W_IDX+xg*1024+w4*128+rank*8,(tag<<16)|(~key&2047u));
        if(w4==0){ if(rank<8) ST(u16,sm+O_IDX+(ln&1)*16+rank*2)=(u16)((ln&1)? key>>16 : ~key&2047u); garrive(sm,C_IX); }
        int rr=2*w4+(ln>>5); lch *xw=sm+O_TILE+XAD(rr,j*16), *xw1=sm+O_TILE+XAD(rr,j*16+512), *xw2=sm+O_TILE+XAD(rr,j*4+1024);
        if(BALLOT((u32)(f1>>32)!=(u32)tag)!=0ull) f1=poll1<32>(rp_,tag<<32);
        ST(u32x4,xw)=a; ST(u32x4,xw1)=b; ST(u32,xw2)=c; if(j==0) ST(u32,sm+O_RC+(2*w4+(ln>>5))*4)=(u32)f1;
        garrive(sm,C_X);
      }
    }
    PRIO(0);
    if(lb<14) gwait<8>(sm,C_U,2); else gwait<4>(sm,C_H,4);
    fs=GLD(sp_); ff=GLD(fp_); XQ
    { rsrc_t rd=RSRC(Dsk,D_*2); int do_=288*(lb&3)+32*w4+(ln&8)*2; dq[0]=BLD128(rd,do_,0,0); dq[1]=BLD128(rd,do_+128,0,0); dq[2]=BLD128(rd,do_+256,0,0); }
    final_adr(sm,fp,w4); trow=32*(tb>>2)+8*g+(li&7); PIN(trow);
    {
      if(BALLOT((u32)(fs>>32)!=(u32)tag)!=0ull) fs=poll1<32>(sp_,tag<<32);
      float ri=rcpf_(bhi((u32)fs)); rc=f32x2{ri,-blo((u32)fs)*ri}; }
    asm volatile("" : "+v"(rc),"+v"(xq[0]),"+v"(xq[1]),"+v"(xq[2]));
    bool ix= w4==3&&lb<14&&lb!=CTLB; u64 f; const char* p=ws+W_IDX+(xg*8+(lb&3))*128+(ln&7)*8; if(ix) f=GLD(p);
    final_blk(fp,0,xq,dq,rc); if(FS>=2) final_blk(fp,1,xq,dq,rc); if(FS>=3&&w4==0) final_blk(fp,2,xq,dq,rc);
    if(BALLOT(ff!=tag)!=0ull) poll1<0>(fp_,tag);
    if(ix){ if(BALLOT(((f^(tag<<16))>>16)!=0ull)!=0ull) f=poll1<16>(p,tag<<16);
      if(ln<8) ST(u16,sm+O_IDX+ln*2)=(u16)f; garrive(sm,C_IX); }
  }
  final_post(sm,fp,w4,nch,xr_,trow,xq,dq,rc,out,ws);
  }
}

extern "C" void kernel_launch(void* const* d_in, const int* in_sizes, int n_in, void* d_out, int out_size, void* d_ws, size_t ws_size, hipStream_t stream) {
  const u16** I=(const u16**)d_in;
  KP kp; kp.Are=I[1]; kp.Aim=I[2]; kp.Bre=I[3]; kp.Bim=I[4]; kp.Cre=I[5]; kp.Cim=I[6]; kp.ldt=I[7]; kp.pad=I[7];
  kA<<<256,1024,0,stream>>>(I[0],(char*)d_ws+WSOFF,I[10],I[8],I[11],I[12],I[13],(u16*)d_out,kp);
}
```

```cpp
#define XD 1
#include <hip/hip_runtime.h>
#include <hip/hip_bf16.h>
#include <stdint.h>
#if defined(__HIP_DEVICE_COMPILE__)
#define NOPK __attribute__((target("no-packed-fp32-ops")))
#else
#define NOPK
#endif
#define DEV __device__ NOPK __forceinline__
typedef unsigned short u16;
typedef unsigned int u32;
typedef unsigned long long u64;
typedef short bf16x8 __attribute__((ext_vector_type(8)));
typedef _Float16 h8 __attribute__((ext_vector_type(8)));
typedef _Float16 h2 __attribute__((ext_vector_type(2)));
typedef float f32x4 __attribute__((ext_vector_type(4)));
typedef float f32x2 __attribute__((ext_vector_type(2)));
typedef u32 u32x4 __attribute__((ext_vector_type(4)));
typedef u32 u32x2 __attribute__((ext_vector_type(2)));
typedef __bf16 bf2 __attribute__((ext_vector_type(2)));
typedef short v4s __attribute__((ext_vector_type(4)));
typedef __amdgpu_buffer_rsrc_t rsrc_t;
#define GAS __attribute__((address_space(1)))
#define LAS __attribute__((address_space(3)))
typedef LAS char lch;
#define L_ 2048
#define D_ 576
#define F_ 4608
#define BC(T,v) __builtin_bit_cast(T,v)
#define TID ((int)__builtin_amdgcn_workitem_id_x())
#define BID ((int)__builtin_amdgcn_workgroup_id_x())
#define RFL(x) __builtin_amdgcn_readfirstlane(x)
#define RLX_AGENT __ATOMIC_RELAXED, __HIP_MEMORY_SCOPE_AGENT
#define LD(T,p) (*(const LAS T*)(p))
#define ST(T,p) (*(LAS T*)(p))
#define SPINCAP (1<<17)
#define DRAIN() asm volatile("s_waitcnt vmcnt(0)" ::: "memory")
#define GLD(p) __hip_atomic_load((const GAS u64*)(p),RLX_AGENT)
#define GST(p,v) __hip_atomic_store((GAS u64*)(p),(u64)(v),RLX_AGENT)
#define BALLOT(c) __builtin_amdgcn_ballot_w64(c)
#define SC1 16
#define LANE(l) int l; { int t2_=TID; asm volatile("" : "+v"(t2_)); l=t2_&63; }
#define SCHED() __builtin_amdgcn_sched_barrier(0)
#define RSRC(p,n) __builtin_amdgcn_make_buffer_rsrc((void*)(p),0,(n),0x00020000)
#define BLD128 __builtin_amdgcn_raw_buffer_load_b128
#define BLD32 __builtin_amdgcn_raw_buffer_load_b32
#define BST128 __builtin_amdgcn_raw_buffer_store_b128
#define PRIO(n) __builtin_amdgcn_s_setprio(n)

DEV float b2f(u32 v){ return BC(float,v<<16); }
DEV float blo(u32 v){ return BC(float,v<<16); }
DEV float bhi(u32 v){ return BC(float,v&0xffff0000u); }
DEV u32 pk(float a,float b){ f32x2 v={a,b}; bf2 r=__builtin_convertvector(v,bf2); return BC(u32,r); }
DEV u32 pkh(float a,float b){ f32x2 v={a,b}; h2 r=__builtin_convertvector(v,h2); return BC(u32,r); }
DEV float rb(float f){ return BC(float,pk(f,f)<<16); }
DEV float rcpf_(float x){ return __builtin_amdgcn_rcpf(x); }
DEV float expf_(float x){
  const float L2E=1.4426950216293335f, L2El=1.9259629911266175e-8f; float h=x*L2E; float l=__builtin_fmaf(x,L2E,-h)+x*L2El;
  float e=__builtin_amdgcn_exp2f(h); return __builtin_fmaf(e*l,0.6931471805599453f,e); }
DEV float sigm_f(float x){ return rcpf_(1.f+__builtin_amdgcn_exp2f(-x*1.4426950408889634f)); }
DEV float silu_f(float x){ return x*sigm_f(x); }
DEV u32 umax_(u32 a,u32 b){ return a>b?a:b; }
DEV u32 umin_(u32 a,u32 b){ return a<b?a:b; }

#define WSOFF 0
#define G2 7
#define PSL 8
#define NA 5
#define NB 11
#define IVS "global_store_dwordx4 v23, v[124:127], s[66:67] sc1\n\t"
#define EB0 "s_barrier\n\t"
#define EB1
#define IV0 IVS
#define IV1
#define GD 3
#define HG -1
#define UD_ -1
#define UDB 64
#define PA -1
#define AD 14
#define BD 0
#define PX 0
#define XAUX 0
#define OAUX 0
#define HSL 4
#define HN 4
#define PPOL ""
#define XPOL ""
#define XEP " nt"
#define XE 0
#define QPOL " nt"
#define PK 0x112
#define PS 0x030
#define DQN 0
#define RD 0
#define PD 0
#define P12 64
#define ESL 4
#define PRN 1
#define CHN 8
#define CSL 2
#define FS 3
#define PWB (w4>>1)*(9*4096)+(w4&1)*128
#define PLO (li>>1)*(18*4096)+(li&1)*64
#define YSH 3
#define PRE1 6
#define PRE3 4
#define PRE2 5
#define W1 5
#define W3 3
#define W2_ 4
#define W1B 20
#define W2B 12
#define LAST1 3
#define RGATE 4
#define UGATE 7
#define R2D 12
#define PFN 32
#define CPF 8
#define NRG 8
#define W_EPOCH 0
#define W_RG 16384
#define W_ST (W_RG+32*8192)
#define W_U (W_ST+32768)
#define UST (D_*16+128)
#define YST (L_*2+128)
#define TL 1
#define GST_ ((D_*16<<TL)+128)
#define W_Y (W_U+256*UST)
#define W_PART (W_Y+D_*YST)
#define W_GU (W_PART+8*9*16*256)
#define W_FL 9216
#define W_IDX 1024
#define CTLB 0
struct KP { const u16 *Are,*Aim,*Bre,*Bim,*Cre,*Cim,*ldt,*pad; };

template<int CTRL> DEV float dppf(float v){ return BC(float,__builtin_amdgcn_update_dpp(0u,BC(u32,v),CTRL,0xf,0xf,false)); }
template<int CTRL> DEV u32 dppu(u32 v){ return (u32)__builtin_amdgcn_update_dpp(0u,v,CTRL,0xf,0xf,false); }
DEV float sum16(float v){ v+=dppf<0xB1>(v); v+=dppf<0x4E>(v); v+=dppf<0x141>(v); v+=dppf<0x140>(v); return v; }
DEV float add16x(float v){ auto r=__builtin_amdgcn_permlane16_swap(BC(u32,v),BC(u32,v),false,false); u32 a=r[0], b=r[1]; return BC(float,a)+BC(float,b); }
DEV u32 wmax(u32 v){
  v=umax_(v,(u32)__builtin_amdgcn_update_dpp(0u,v,0x111,0xf,0xf,false));
  v=umax_(v,(u32)__builtin_amdgcn_update_dpp(0u,v,0x112,0xf,0xf,false));
  v=umax_(v,(u32)__builtin_amdgcn_update_dpp(0u,v,0x114,0xf,0xf,false));
  v=umax_(v,(u32)__builtin_amdgcn_update_dpp(0u,v,0x118,0xf,0xf,false));
  v=umax_(v,(u32)__builtin_amdgcn_update_dpp(0u,v,0x142,0xa,0xf,false));
  v=umax_(v,(u32)__builtin_amdgcn_update_dpp(0u,v,0x143,0xc,0xf,false));
  return (u32)__builtin_amdgcn_readlane((int)v,63); }

DEV void garrive(lch* sm,int c){ u32 ad=(u32)(size_t)sm+c*4, one=1;
  asm volatile("s_mov_b64 exec, 1\n\tds_add_u32 %0, %1\n\ts_mov_b64 exec, -1" :: "v"(ad),"v"(one) : "memory"); }
template<int SLP> DEV void gwait(lch* sm,int c,u32 target){ u32 ad=(u32)(size_t)sm+c*4, tv, cnt, sv; target=RFL(target);
  asm volatile("s_mov_b32 %1, 0x20000\n"
    "1:\n\tds_read_b32 %0, %3\n\ts_waitcnt lgkmcnt(0)\n\tv_readfirstlane_b32 %2, %0\n\ts_cmp_ge_u32 %2, %4\n\ts_cbranch_scc1 2f\n\ts_sleep %5\n\ts_sub_u32 %1, %1, 1\n\ts_cbranch_scc0 1b\n"
    "2:" : "=&v"(tv),"=&s"(cnt),"=&s"(sv) : "v"(ad),"s"(target),"i"(SLP) : "memory","scc"); }
template<int ST_,class T> DEV void gwait4(int c,u32 target,const lch* p,T& a,T& b,T& d,T& e){ u32 tv,cnt,sv;
  asm volatile("s_mov_b32 %1, 0x20000\n1:\n\tds_read_b32 %0, %7\n\tds_read_b128 %3, %8\n\tds_read_b128 %4, %8 offset:%10\n\tds_read_b128 %5, %8 offset:%11\n\tds_read_b128 %6, %8 offset:%12\n\ts_waitcnt lgkmcnt(4)\n\t"
    "v_readfirstlane_b32 %2, %0\n\ts_cmp_ge_u32 %2, %9\n\ts_cbranch_scc1 2f\n\ts_sleep 1\n\ts_sub_u32 %1, %1, 1\n\ts_cbranch_scc0 1b\n2:\n\ts_waitcnt lgkmcnt(0)"
    : "=&v"(tv),"=&s"(cnt),"=&s"(sv),"=&v"(a),"=&v"(b),"=&v"(d),"=&v"(e) : "v"(c*4),"v"(p),"i"(target),"i"(ST_),"i"(2*ST_),"i"(3*ST_) : "memory","scc"); }
DEV void gsync(lch* sm,int c,u32 target){ garrive(sm,c); gwait<0>(sm,c,target); }
template<int SH> DEV u64 poll1(const char* p,u64 want){ u64 f=GLD(p);
  _Pragma("unroll 1") for(int sp=0;sp<SPINCAP;sp++){ bool bad=((f^want)>>SH)!=0ull; if(BALLOT(bad)==0ull) break; __builtin_amdgcn_s_sleep(1); if(bad) f=GLD(p); }
  __builtin_amdgcn_fence(__ATOMIC_ACQUIRE,"wavefront"); return f; }
#define STR_(x) #x
#define STR(x) STR_(x)
DEV u32x4 mkrs(const void* p,u32 n){ u64 a=(u64)p; return u32x4{(u32)RFL((int)(u32)a),(u32)RFL((int)((u32)(a>>32)&0xffffu)),n,0x00020000u}; }
#define VMW(n) asm volatile("s_waitcnt vmcnt(%0)" :: "i"(n) : "memory")
#define VW(n) "s_waitcnt vmcnt(" STR(n) ")\n\t"
#define YSLP 1
#define GSLP 1
#define QSLP 1
#define PSLP 3
#define POLLASM(LOADS,CHECK,K) "s_mov_b32 %[cnt], 0x20000\n1:\n\ts_waitcnt vmcnt(0)\n\t" CHECK "s_cbranch_vccz 9f\n\ts_sleep " STR(K) "\n\ts_mov_b64 exec, vcc\n\t" LOADS \
  "s_mov_b64 exec, -1\n\ts_sub_u32 %[cnt], %[cnt], 1\n\ts_cbranch_scc0 1b\n9:\n\t"
#define LD_DUP2 "buffer_load_dwordx4 v[112:115], %[of], %[rs], 0 offen sc1\n\tbuffer_load_dwordx4 v[116:119], %[of], %[rs], %[so] offen sc1\n\t"
#define MX2(a,b) "v_max3_u32 %[t0], %[t0], v" #a ", v" #b "\n\t"
#define CHK_I0 "v_max_u32 %[t0], v112, v113\n\t" MX2(114,115)
#define CHK_IE "v_cmp_eq_u32 vcc, -1, %[t0]\n\t"
#define PCLOB "memory","vcc","scc"
#define UMASK 0x40074007u
DEV u32 upat(u32 tag,int k){ return ((((tag*0xea959c21u)>>(8*k))&255u)*0x04000801u)&UMASK; }
DEV u32 pkz(float a,float b){ return BC(u32,__builtin_amdgcn_cvt_pkrtz(a,b)); }
#define INV 0xffffffffu
DEV u32 nv(u32 v){ return umin_(v,0xfffeffffu); }
DEV u32x4 nv4(u32x4 v){ return u32x4{nv(v[0]),nv(v[1]),nv(v[2]),nv(v[3])}; }
#define OOR 0x40000000

#define CHSZ 36864
#define O_CH 512
#define O_CND 256
#define SLSZ 38976
#define O_R2 (O_CH+2*SLSZ)
#define O_X2 (O_R2+CHSZ)
#define O_TILE (O_X2+CHSZ)
#define XSTR 1168
#define HSTR 608
#define HT 96
#define XAD(r,b) ((r)*HSTR+(b)+((b)>=576? 8*HSTR-576:0))
#define XS1 1
#define XG1 1
#define TSTR2 1216
#define O_CAND (O_TILE+9728+1024)
#define LDS_TOTAL (O_CAND+128)
#define C_G 0
#define C_U 8
#define C_L 4
#define C_D 5
#define C_X 12
#define C_P 13
#define C_H 14
#define C_I 17
#define C_E 18
#define C_IX 19
#define C_PR 21
#define C_W 22
#define C_WIN (lb>=14? C_W:C_D)
#define O_IDX 96
#define O_EXP 144
#define O_RMX 192
#define O_RV 128
#define O_MD 400
#define O_RC (O_CAND+64)

struct C2 { float r,i; };
DEV C2 cmul(C2 a,C2 b){ C2 c; c.r=__builtin_fmaf(a.r,b.r,-(a.i*b.i)); c.i=__builtin_fmaf(a.r,b.i,a.i*b.r); return c; }
DEV h8 ldA(const lch* p){ u32x2 a=*(const volatile LAS u32x2*)p, b=*(const volatile LAS u32x2*)(p+8); return BC(h8,(u32x4{a[0],a[1],b[0],b[1]})); }
DEV bf16x8 trfrag(const lch* p0,const lch* p1){
  u32x2 b0=BC(u32x2,__builtin_amdgcn_ds_read_tr16_b64_v4i16((LAS v4s*)p0)), b1=BC(u32x2,__builtin_amdgcn_ds_read_tr16_b64_v4i16((LAS v4s*)p1));
  return BC(bf16x8,(u32x4{b0[0],b0[1],b1[0],b1[1]})); }

struct FP { u32 gg[24]; float bs[24]; int yo,y3; lch* wp; const lch* tp; };
#define D2(a,b,c) __builtin_amdgcn_fdot2_f32_bf16(BC(bf2,a),BC(bf2,b),c,false)
#define ZCH 64
#define YZ 1280
#define YG1 320
#define YG2 672
DEV void final_blk(FP& f,int z,const u32x4* xq,const u32x4* dq,f32x2 rc){
  float ea=rc[0]*-1.4426950408889634f, eb=rc[1]*-1.4426950408889634f;
  #pragma unroll
  for(int e=0;e<4;e++){ u32 xw=xq[z][e], dw=dq[z][e]; int i=8*z+2*e; float a=blo(xw), b=bhi(xw);
    float pa=__builtin_fmaf(a,rc[0],rc[1]), pb=__builtin_fmaf(b,rc[0],rc[1]);
    float ga=pa*rcpf_(1.f+__builtin_amdgcn_exp2f(__builtin_fmaf(a,ea,eb))), gb=pb*rcpf_(1.f+__builtin_amdgcn_exp2f(__builtin_fmaf(b,ea,eb)));
    f.gg[i]=pk(ga,0.f); f.gg[i+1]=pk(0.f,gb);
    f.bs[i]=__builtin_fmaf(pa*blo(dw),ga,a); f.bs[i+1]=__builtin_fmaf(pb*bhi(dw),gb,b);
    asm volatile("" : "+v"(f.gg[i]),"+v"(f.gg[i+1]),"+v"(f.bs[i]),"+v"(f.bs[i+1])); }
}
DEV void final_adr(lch* sm,FP& f,int w4){
  LANE(ln); int bid=BID, xg=bid&7, lb=bid>>3, g=ln>>4, li=ln&15, qq=li>>2, pq=li&3;
  f.yo=(144*(lb&3)+16*w4+(ln>>2))*YST+(32*(8*xg+(lb>>2))+8*(ln&3))*2; f.y3= w4==0? f.yo+2*ZCH*YST:OOR;
  lch* fb=sm+O_CH+w4*(3*YZ); f.wp=fb+(ln&1)*YG1+(ln&2)*(YG2/2)+(ln>>2)*16; f.tp=fb+(g&1)*YG1+(g&2)*(YG2/2)+(8*(pq>>1)+qq)*16+(pq&1)*8;
  asm volatile("" : "+v"(f.yo),"+v"(f.y3),"+v"(f.wp),"+v"(f.tp));
}
DEV void final_post(lch* sm,FP& f,int w4,int nch,int ob,int trow,const u32x4* xq,const u32x4* dq,f32x2 rc,u16* __restrict__ out,char* ws){
  int lb=BID>>3;
  u32x4 rs=mkrs(ws+W_Y,D_*YST); rsrc_t ro=RSRC(out,L_*D_*2);
  gwait<ESL>(sm,C_E,4*nch);
  if(AD&&lb<8) __builtin_amdgcn_s_sleep(AD);
  if(BD&&lb>=14) __builtin_amdgcn_s_sleep(BD);
  if(lb>=8&&lb<14) gwait<CSL>(sm,C_H,CHN);
  PRIO(3);
  #define LD_Y "buffer_load_dwordx4 v[112:115], %[of], %[rs], 0 offen sc1\n\tbuffer_load_dwordx4 v[116:119], %[of], %[rs], %[s1] offen sc1\n\tbuffer_load_dwordx4 v[120:123], %[o3], %[rs], 0 offen sc1\n\t"
  asm volatile(LD_Y : "+v"(rc),"+v"(trow) : [of]"v"(f.yo),[rs]"s"(rs),[s1]"s"(ZCH*YST),[o3]"v"(f.y3) : "memory");
  garrive(sm,C_PR);
  if(FS<2) final_blk(f,1,xq,dq,rc);
  if(FS<3&&w4==0) final_blk(f,2,xq,dq,rc);
  { gwait<2>(sm,C_IX,1); u32x4 iq=LD(u32x4,sm+O_IDX); u32 tt=(u32)trow*0x10001u; bool routed=false;
    #pragma unroll
    for(int q=0;q<4;q++){ u32 z=iq[q]^tt; routed|=((z&0xffffu)==0u)|((z>>16)==0u); }
    ob=routed? OOR:ob; }
  { u32 t0,cnt;
    asm volatile(POLLASM(LD_Y,"ds_write_b128 %[wp], v[112:115]\n\tds_write_b128 %[wp], v[116:119] offset:" STR(YZ) "\n\ts_cmp_lg_u32 %[w4], 0\n\ts_cbranch_scc1 8f\n\tds_write_b128 %[wp], v[120:123] offset:2*" STR(YZ) "\n8:\n\t"
      CHK_I0 MX2(116,117) MX2(118,119) MX2(120,121) MX2(122,123) CHK_IE,YSLP)
      : [t0]"=&v"(t0),[cnt]"=&s"(cnt),"+v"(ob) : [of]"v"(f.yo),[rs]"s"(rs),[s1]"s"(ZCH*YST),[o3]"v"(f.y3),[wp]"v"(f.wp),[w4]"s"(w4),"s"(ro) : PCLOB); }
  u32x4 r[3];
  #pragma unroll
  for(int z=0;z<3;z++) r[z]=BC(u32x4,trfrag(f.tp+z*YZ,f.tp+z*YZ+64));
  #pragma unroll
  for(int z=0;z<3;z++) if(z<2||w4==0){ u32x4 ov;
    #pragma unroll
    for(int e=0;e<4;e++){ u32 rw=r[z][e]; int i=8*z+2*e;
      ov[e]=pk(D2(rw,f.gg[i],f.bs[i]),D2(rw,f.gg[i+1],f.bs[i+1]));
    }
    BST128(ov,ro,ob+z*(2*ZCH),0,OAUX); SCHED(); }
}

#define TROW 544
#define O_TB 4640
#define O_DE 35664
#define O_DO (O_DE+1232)
#define O_R2K (O_DO+1152)
#define O_R3K (O_R2K+512)
#define O_DUM (O_R3K+320)
#define O_RED O_TB
#define MH(a,b,c) __builtin_amdgcn_mfma_f32_16x16x32_f16(a,b,c,0,0,0)
#define PIN(x) asm volatile("" : "+v"(x))
DEV h8 ld4w(const lch* p){ return BC(h8,(u32x4{LD(u32,p),LD(u32,p+4),LD(u32,p+8),LD(u32,p+12)})); }
DEV u32 __umul24_(u32 a,u32 b){ u32 r; asm("v_mul_u32_u24 %0, %1, %2" : "=v"(r) : "s"(b),"v"(a)); return r; }
DEV u32x4 band(u32 mask,int g,u32 val){ u32 by=__builtin_amdgcn_ubfe(mask,8*g,8), X=by|(by<<15), k=0x10001u; u32x4 r; asm volatile("" : "+s"(k),"+s"(val));
  #pragma unroll
  for(int d=0;d<4;d++) r[d]=__umul24_((X>>(2*d))&k,val);
  return r; }
DEV h8 zpack(f32x4 e,f32x4 o){ return BC(h8,(u32x4{pkh(e[0],o[0]),pkh(e[1],o[1]),pkh(e[2],o[2]),pkh(e[3],o[3])})); }
struct C3 { float r,i,n; };
DEV C3 c3(C2 a){ C3 c; c.r=a.r; c.i=a.i; PIN(c.i); c.n=-c.i; PIN(c.n); return c; }
DEV C2 cm(C2 a,C3 b){ C2 c; c.r=__builtin_fmaf(a.r,b.r,a.i*b.n); c.i=__builtin_fmaf(a.r,b.i,a.i*b.r); return c; }
DEV u32 ph(C2 a){ return pkh(a.r,a.i); }
#define W2(p,a,b) asm volatile("ds_write2_b32 %0, %1, %2 offset1:136" :: "v"(p),"v"(a),"v"(b) : "memory")
struct CP { const lch *x0,*x1,*bd; lch *p0,*p1,*p2,*wp; u32x4 Be,Bo,AE; int yo; };
template<int ROLE> DEV void lanes(CP& c,lch* cb,int ln){ int m=ln&15, g=ln>>4;
  const lch* ad=cb+((m&1)? O_DO+2*527 : O_DE+2*536)+2*(8*g-m);
  const lch* bd=cb+2*(256+16*m+8*g); lch* dum=cb+O_DUM;
  if(ROLE==0){ int l0=128*(g>>1)+8*m+4*(g&1); c.p0=cb+O_DE+(549-l0)*2; PIN(c.p0); c.p1=c.p0+6; PIN(c.p1); c.p2=c.p0+(O_DO-O_DE-18); c.x0=ad-640; c.bd=bd-64; c.x1=cb+O_R3K+2*(126-2*m+8*g)-192; PIN(c.x1);
    c.Be=band(0x1ffu<<(m+4),g,0x3c00u); PIN(c.Be); }
  if(ROLE==1){ lch* r=cb+O_R2K+(192-64*(g-2)-2*m)*2; c.p0= g<2? dum : r-66; c.p1= ln==0? cb+O_DE+(552-256)*2 : (ln<=32? dum : r); c.p2=cb+O_DE+(544-256-8*ln)*2; c.x0=ad-960; c.bd=bd-448; PIN(c.p0); PIN(c.p1); PIN(c.p2); }
  if(ROLE==2){ int i0=16*m+4*g; c.p0= m<8? cb+O_R3K+(125-i0)*2 : dum+2; c.p1= ln==0? cb+O_R2K+128 : (ln==8? cb+O_R3K : c.p0+6); c.x0=ad-960; c.bd=bd-448; PIN(c.p0); PIN(c.p1); }
  if(ROLE==3){ c.x0=cb+O_R2K+2*(188-4*m+8*g)-256; c.bd=cb+2*(248+8*m+8*g);
    c.Be=band(0x1fu<<(m+6),g,0x3c00u); c.Bo=band(0x7u<<(m+11),g,0x3c00u); PIN(c.Be); PIN(c.Bo); }
  PIN(c.x0); PIN(c.bd); }
template<int ROLE> DEV void lanes2(CP& c,lch* cb,int ln){ int m=ln&15, g=ln>>4;
  if(ROLE==0){ c.Bo=band(0x7fu<<(m+5),g,0x3c00u); c.AE=band(3u<<(m+12),g,0xffffu); PIN(c.Bo); PIN(c.AE); }
  c.wp=cb+O_RED+ln*16; c.yo=(g&1)*512+m*32+(g>>1)*16; PIN(c.wp); PIN(c.yo); }
template<int ROLE> DEV void chan2(lch* sm,lch* cb,const lch*& fa,const lch*& fb,int grp,int d,int one_,int emax,u64 tag,char* ws){
  CP c;
  const f32x4 Z4={0,0,0,0}; h8 A[12]; f32x4 acc[8]; float sc; u32x4 ry; u32 cga;
  #define YSET sc=__builtin_amdgcn_ldexpf(1.f,emax-YSH); ry=mkrs(ws+W_Y+d*YST,4096); cga=(C_G+grp)*4; PIN(sc); PIN(cga); asm volatile("" : "+s"(ry));
  #define LD_U "buffer_load_dwordx4 v[112:115], %[of], %[rs], 0 offen sc1\n\tbuffer_load_dwordx4 v[116:119], %[of], %[rs], %[s1] offen sc1\n\tbuffer_load_dwordx4 v[120:123], %[of], %[rs], %[s2] offen sc1\n\tbuffer_load_dwordx4 v[124:127], %[of], %[rs], %[s3] offen sc1\n\t"
  #define UC(t,a,b,c) "v_bitop3_b32 " t ", v" #a ", v" #b ", %[" #c "] bitop3:0x7e\n\t"
  #define UD(t,a,c) "v_bitop3_b32 " t ", " t ", v" #a ", %[" #c "] bitop3:0xf6\n\t"
  #define CHK_U UC("%[t0]",112,116,c0) UC("%[t1]",113,117,c1) UD("%[t0]",120,c0) UD("%[t1]",121,c1) UD("%[t0]",124,c0) UD("%[t1]",125,c1) UD("%[t0]",114,c2) UD("%[t1]",115,c3) UD("%[t0]",118,c2) UD("%[t1]",119,c3) \
    UD("%[t0]",122,c2) UD("%[t1]",123,c3) UD("%[t0]",126,c2) UD("%[t1]",127,c3) "v_bitop3_b32 %[t0], %[t0], %[t1], %[mk] bitop3:0xa8\n\tv_cmp_ne_u32 vcc, 0, %[t0]\n\t"
  #define UX(a,b,c,d,o) "v_xor_b32 v" #a ", %[e0], v" #a "\n\tv_xor_b32 v" #b ", %[e1], v" #b "\n\tv_xor_b32 v" #c ", %[e2], v" #c "\n\tv_xor_b32 v" #d ", %[e3], v" #d "\n\tds_write_b128 %[up], v[" #a ":" #d "] offset:" #o "\n\t"
  #define UK(n,a,b,c,d,o,T) VW(n) UX(a,b,c,d,o) UC("%[t1]",a,b,k4) UC("%[t2]",c,d,k4) T
  #define OR3 "v_or3_b32 %[t0], %[t0], %[t1], %[t2]\n\t"
  #define UPOLL { u32 t0,t1,t2,cnt; asm volatile("s_mov_b32 %[cnt], 0x20000\n1:\n\t" UK(3,112,113,114,115,544,"v_or_b32 %[t0], %[t1], %[t2]\n\t") UK(2,116,117,118,119,1056,OR3) UK(1,120,121,122,123,1568,OR3) UK(0,124,125,126,127,2080,OR3) \
      "v_and_b32 %[t0], %[mk], %[t0]\n\tv_cmp_ne_u32 vcc, 0, %[t0]\n\ts_cbranch_vccz 9f\n\ts_sleep " STR(PSLP) "\n\ts_mov_b64 exec, vcc\n\t" LD_U "s_sub_u32 %[cnt], %[cnt], 1\n\ts_cbranch_scc0 1b\n9:\n\ts_mov_b64 exec, 1\n\tds_add_u32 %[cu], %[one]\n\ts_mov_b64 exec, -1" \
      : [t0]"=&v"(t0),[t1]"=&v"(t1),[t2]"=&v"(t2),[cnt]"=&s"(cnt) : [of]"v"(c.p0),[rs]"s"(rsU),[s1]"s"((32>>TL)*GST_),[s2]"s"((64>>TL)*GST_),[s3]"s"((96>>TL)*GST_),[k4]"s"(0x40004u), \
        [e0]"s"(c0^0x40004u),[e1]"s"(c1^0x40004u),[e2]"s"(c2^0x40004u),[e3]"s"(c3_^0x40004u),[mk]"s"(UMASK),[up]"v"(c.p1),[cu]"v"((C_U+one_)*4),[one]"v"(4) : PCLOB); }
  if(ROLE<3){
    h8 a[8], b[8];
    #pragma unroll
    for(int ks=0;ks<8;ks++){ a[ks]=LD(h8,fa+ks*64); b[ks]=LD(h8,fb+ks*64); }
    SCHED();
    LANE(ln); lanes<ROLE>(c,cb,ln); float fz= ln==32? 0.f : 4.76837158203125e-07f; if(ROLE==1) PIN(fz);
    SCHED();
    f32x4 k=Z4;
    #pragma unroll
    for(int ks=0;ks<8;ks++) k=MH(a[ks],b[ks],k);
    const float f=4.76837158203125e-07f;
    float k0=k[0]*f, k1=k[1]*f, k2=k[2]*f, k3=k[3]*f;
    u32 h32=pkh(k3,k2), h10=pkh(k1,k0);
    if(ROLE!=1){
      ST(u16,c.p0)=(u16)h32; ST(u32,c.p0+2)=pkh(k2,k1); ST(u16,c.p1)=(u16)(h10>>16);
      if(ROLE==0) ST(u32x2,c.p2)=u32x2{h32,h10};
    } else {
      float n0=dppf<0x130>(k[0])*fz, z1= fz==0.f? k0:k1, z2=k[2]*fz, z3=k[3]*fz; u32 h22=pkh(z2,z2);
      ST(u16,c.p0+64)=(u16)h10; ST(u16,c.p0+2)=(u16)(h32>>16); ST(u16,c.p0)=(u16)h32; ST(u16,c.p1)=(u16)(h10>>16);
      u32x4 vo={pkh(n0,z3),h22,pkh(z2,z1),pkh(k0,k0)}, ve={pkh(n0,n0),pkh(z3,z2),h22,pkh(z1,k0)};
      asm volatile("s_mov_b32 exec_hi, 1\n\tds_write_b128 %0, %1\n\tds_write_b128 %0, %2 offset:%3\n\ts_mov_b32 exec_hi, -1" :: "v"(c.p2),"v"(ve),"v"(vo),"i"(O_DO-O_DE-16) : "memory"); }
    if(PA>=0) PRIO(PA<0? 0:PA);
    gsync(sm,C_G+grp,7);
    u32 kv;
    if(ROLE==0){ A[0]=ld4w(c.x0+640); A[1]=ld4w(c.x0+128); A[2]=ld4w(c.x1+192); A[3]=ld4w(c.x1+128); A[4]=ld4w(c.x1+64); A[5]=ld4w(c.x1); kv=LD(u16,cb+O_R2K+128); }
    else {
      #pragma unroll
      for(int r=0;r<6;r++){ A[2*r]=ld4w(c.x0+832-64*r); A[2*r+1]=ld4w(c.x0+320-64*r); } }
    SCHED(); lanes2<ROLE>(c,cb,ln); YSET SCHED();
    if(ROLE==0){ kv|=kv<<16; c.AE=u32x4{c.AE[0]&kv,c.AE[1]&kv,c.AE[2]&kv,c.AE[3]&kv}; PIN(c.AE); }
    PRIO(2); if(ROLE!=0&&BID>=P12) PRIO(1);
    gwait<1>(sm,C_U+one_,HG>=0&&BID>=112? 12:8-4*one_);
  } else {
    u32 tg=(u32)tag, c0=upat(tg,0), c1=upat(tg,1), c2=upat(tg,2), c3_=upat(tg,3); u32x4 rsU=mkrs(ws+W_U,256*UST);
    if(!one_) PRIO(2);
    LANE(ln); c.p0=(lch*)fa; c.p1=(lch*)fb;
    bool late= grp==0||BID>=112;
    if(!late) asm volatile(LD_U :: [of]"v"(c.p0),[rs]"s"(rsU),[s1]"s"((32>>TL)*GST_),[s2]"s"((64>>TL)*GST_),[s3]"s"((96>>TL)*GST_) : "memory");
    asm volatile("s_mov_b64 exec, 1\n\tglobal_store_dwordx2 %0, %1, %2 sc1\n\ts_mov_b64 exec, -1" :: "v"(W_FL+d*8),"v"(tag),"s"(ws) : "memory");
    lanes<3>(c,cb,ln); lanes2<3>(c,cb,ln); YSET
    #pragma unroll
    for(int j=2;j<8;j++){ acc[j]=Z4; PIN(acc[j]); }
    if(late){ if(UD_>=0&&BID<UDB) __builtin_amdgcn_s_sleep(UD_<0? 0:UD_); else gwait<2>(sm,C_G,UGATE); if(BID>=112) gwait<1>(sm,C_D,4); asm volatile(LD_U :: [of]"v"(c.p0),[rs]"s"(rsU),[s1]"s"((32>>TL)*GST_),[s2]"s"((64>>TL)*GST_),[s3]"s"((96>>TL)*GST_) : "memory"); }
    if(!late) UPOLL
    gwait<1>(sm,C_G+grp,7);
    #pragma unroll
    for(int i=0;i<6;i++) A[i]=ldA(c.x0+320-64*i);
    if(late) UPOLL
    PRIO(2);
    gwait<1>(sm,C_U+one_,HG>=0&&BID>=112? 12:8-4*one_);
  }
  const lch* bd=c.bd; h8 Be=BC(h8,c.Be), Bo=BC(h8,c.Bo);
  #pragma unroll
  for(int j=0;j<(ROLE==3? 2:8);j++) acc[j]=Z4;
  constexpr int OWN= ROLE==1? 2 : ROLE==2? 1 : ROLE, NS= ROLE==0? 1:2;
  #define RW(j) { ST(f32x4,c.wp+((j)>>1)*4096+(ROLE&1)*2048+((j)&1)*1024)=acc[j]; SCHED(); }
  const lch* rp=c.wp+OWN*4096; f32x4 a[2], b[2]; u32 cv;
  #define LDV(T,p) (*(const volatile LAS T*)(p))
  #define ARR { garrive(sm,C_G+grp); SCHED(); }
  #define OPT { cv=LDV(u32,(lch*)(size_t)cga); _Pragma("unroll") for(int q=0;q<NS;q++){ a[q]=LDV(f32x4,rp+q*2048); b[q]=LDV(f32x4,rp+q*2048+1024); } SCHED(); }
  #define SUM(v) { _Pragma("unroll") for(int e=0;e<4;e++) v[0][e]=(NS==1? v[0][e] : v[0][e]+v[1][e])*sc; PIN(v[0]); SCHED(); }
  if(ROLE==1||ROLE==2){
    constexpr int NQ= ROLE==1? 5:4, LA=9; h8 B[30];
    #define QS(s) (ROLE==2? (s) : (s)<3? 5+(s) : (s))
    #define LB(i) B[i]=LD(h8,bd+320-64*((i)%6)+512*QS((i)/6));
    #pragma unroll
    for(int i=0;i<LA;i++) LB(i)
    SCHED();
    #pragma unroll
    for(int i=0;i<6*NQ;i++){ const int q=QS(i/6), r=i%6;
      if(i==6*NQ-(ROLE==1? 4:7)){ PRIO(2); OPT } if(i==6*NQ-(ROLE==1? 2:3)) SUM(a) if(i==6*NQ-1) SUM(b)
      if(!(ROLE==1&&q==3)){ acc[q]=MH(A[2*r],B[i],acc[q]); SCHED(); }
      if(q!=7&&!(ROLE==2&&q==3)){ acc[q+1]=MH(A[2*r+1],B[i],acc[q+1]); SCHED(); }
      if(i+LA<6*NQ){ LB(i+LA) SCHED(); }
      if(ROLE==2&&i==6) RW(0) if(ROLE==2&&i==12){ RW(1) ARR } if(ROLE==1&&i==13) RW(6) if(ROLE==1&&i==19){ RW(7) ARR } } }
  if(ROLE==0){
    h8 B[8], ue[4], C[8];
    #pragma unroll
    for(int kt=0;kt<4;kt++){ ue[kt]=LD(h8,bd+48+512*kt); B[kt]=LD(h8,bd+64+512*kt); }
    f32x4 ce[2],co[2]; h8 Z[2];
    ce[0]=MH(ue[0],Be,Z4); co[0]=MH(B[0],Bo,Z4); ce[1]=MH(ue[1],Be,Z4); co[1]=MH(B[1],Bo,Z4); Z[0]=zpack(ce[0],co[0]);
    #pragma unroll
    for(int kt=0;kt<4;kt++){
      SCHED();
      if(kt+2<4){ ce[kt&1]=MH(ue[kt+2],Be,Z4); co[kt&1]=MH(B[kt+2],Bo,Z4); }
      if(kt+1<4) Z[(kt+1)&1]=zpack(ce[(kt+1)&1],co[(kt+1)&1]);
      #pragma unroll
      for(int rho=0;rho+kt<4;rho++) acc[4+kt+rho]=MH(Z[kt&1],A[2+rho],acc[4+kt+rho]);
      SCHED(); if(kt>=2) C[kt-1]=LD(h8,bd+512*(kt-1)); if(kt==0) A[5]=BC(h8,c.AE); if(kt==1) A[4]=ld4w(c.x0-384); if(kt==2) A[3]=ld4w(c.x0+64); if(kt==3) A[2]=ld4w(c.x0+576); SCHED(); }
    #pragma unroll
    for(int q=0;q<9;q++){
      if(q<4){ acc[q+4]=MH(A[5],B[q],acc[q+4]); SCHED(); } if(q<6){ acc[q+2]=MH(A[4],B[q],acc[q+2]); SCHED(); }
      if(q>=1&&q<7){ acc[q+1]=MH(A[1],B[q],acc[q+1]); SCHED(); } if(q>=2&&q<8){ acc[q]=MH(A[0],B[q],acc[q]); SCHED(); }
      if(q>=4&&q<8) RW(q-2)
      if(q<4){ B[4+q]=LD(h8,bd+64+512*(4+q)); SCHED(); }
      if(q>=3){ acc[q-1]=MH(A[2],C[q-1],acc[q-1]); SCHED(); } if(q>=2&&q<8){ acc[q]=MH(A[3],C[q-1],acc[q]); SCHED(); }
      if(q>=1&&q<7){ C[(q+2)&7]=LD(h8,bd+512*((q+2)&7)); SCHED(); } }
    acc[0]=MH(A[0],B[0],acc[0]); SCHED(); RW(6) acc[1]=MH(A[1],B[0],acc[1]); SCHED(); RW(7) ARR
    OPT acc[0]=MH(A[2],C[0],acc[0]); acc[1]=MH(A[0],B[1],acc[1]); acc[1]=MH(A[2],C[1],acc[1]); acc[1]=MH(A[3],C[0],acc[1]); SCHED(); SUM(a) SUM(b) }
  if(ROLE==3){
    h8 U[12];
    #pragma unroll
    for(int kt=0;kt<6;kt++) U[kt]=LD(h8,bd+256*kt);
    f32x4 ce[2],co[2]; h8 Z[2];
    ce[0]=MH(U[0],Be,Z4); co[0]=MH(U[0],Bo,Z4); ce[1]=MH(U[1],Be,Z4); co[1]=MH(U[1],Bo,Z4); Z[0]=zpack(ce[0],co[0]);
    #pragma unroll
    for(int kt=0;kt<12;kt++){
      SCHED();
      if(kt==10) OPT if(kt==11) SUM(a)
      if(kt+2<12){ ce[kt&1]=MH(U[kt+2],Be,Z4); co[kt&1]=MH(U[kt+2],Bo,Z4); }
      if(kt>=2&&kt<=8&&!(kt&1)){ SCHED(); RW((kt+2)/2) if(kt==8) ARR }
      if(kt+1<12) Z[(kt+1)&1]=zpack(ce[(kt+1)&1],co[(kt+1)&1]);
      #pragma unroll
      for(int rho=-1;rho<5;rho++){ int j=(kt+4+rho)>>1; if(((kt+rho)&1)==0&&j>=2&&j<8) acc[j]=MH(Z[kt&1],A[rho+1],acc[j]); }
      if(kt<6){ SCHED(); U[kt+6]=LD(h8,bd+256*(kt+6)); } }
    SCHED(); SUM(b)
  }
  { float s0[4], s1[4];
    if(RFL(cv)<11u){ u32 tv,cnt;
      asm volatile("s_mov_b32 %1, 0x20000\n1:\n\tds_read_b32 %0, %6\n\tds_read_b128 %2, %7\n\tds_read_b128 %4, %7 offset:2048\n\tds_read_b128 %3, %7 offset:1024\n\tds_read_b128 %5, %7 offset:3072\n\ts_waitcnt lgkmcnt(4)\n\t"
        "v_cmp_gt_u32 vcc, 11, %0\n\ts_cbranch_vccz 2f\n\ts_sleep 1\n\ts_sub_u32 %1, %1, 1\n\ts_cbranch_scc0 1b\n2:\n\ts_waitcnt lgkmcnt(0)"
        : "=&v"(tv),"=&s"(cnt),"=&v"(a[0]),"=&v"(b[0]),"=&v"(a[1]),"=&v"(b[1]) : "v"(cga),"v"(rp) : "memory","scc","vcc");
      SUM(a) SUM(b) }
    f32x4 o0=acc[2*OWN], o1=acc[2*OWN+1];
    #pragma unroll
    for(int e=0;e<4;e++){ s0[e]=__builtin_fmaf(o0[e],sc,a[0][e]); s1[e]=__builtin_fmaf(o1[e],sc,b[0][e]); }
    auto r0=__builtin_amdgcn_permlane16_swap(nv(pk(s0[0],s0[1])),nv(pk(s1[0],s1[1])),false,false); auto r1=__builtin_amdgcn_permlane16_swap(nv(pk(s0[2],s0[3])),nv(pk(s1[2],s1[3])),false,false);
    asm volatile("buffer_store_dwordx4 %0, %1, %2, 0 offen offset:%3 sc1\n\ts_nop 1" :: "v"(u32x4{r0[0],r1[0],r0[1],r1[1]}),"v"(c.yo),"s"(ry),"i"(OWN*1024) : "memory"); }
  garrive(sm,C_E);

  asm volatile("s_endpgm" : "=v"(fa),"=v"(fb) :: "memory");
}
#define GETEP asm volatile("s_waitcnt lgkmcnt(0)\n\ts_mov_b64 %0, s[96:97]\n\ts_mov_b32 %1, s98" : "=s"(ep),"=s"(ldw));
DEV void chan(lch* sm,int grp,int w4,int d,int one_,char* ws,u32x4 q0,u32x4 q1,u32x4 q2,u32x4 q3,u32x4 q4){
  lch* cb=sm+O_CH+grp*SLSZ; int role=w4, emax;
  #define LP(v) ((lch*)(size_t)(u32)(v))
  const lch* fa=LP(q4[0]); const lch* fb=LP(q4[1]);
  u64 tag;
  { float a2,bp,br,bi,cr,ci;
    u32 fq=q3[2], fr=q3[3], one=q4[2]; float f64=BC(float,fq), f128=BC(float,fr);
    lch *t8[4]={LP(q0[0]),LP(q0[1]),LP(q0[2]),LP(q0[3])}, *tB[4]={LP(q1[0]),LP(q1[1]),LP(q1[2]),LP(q1[3])}, *t4[2]={LP(q2[0]),LP(q2[1])}, *t2[2]={LP(q2[2]),LP(q2[3])}, *t1=LP(q3[0]), *te=LP(q3[1]);
    if(BID<64){ if(grp==0) PRIO(PK&3); if(grp==1) PRIO((PK>>4)&3); if(grp==2) PRIO((PK>>8)&3); } else
    if(grp!=0) PRIO(1);
    u64 ep; u32 ldw; GETEP tag=ep+1ull;
    float dt=rb(expf_((d&1)? bhi(ldw):blo(ldw))), ca=dt*1.4426950408889634f, cb_=dt*0.15915494309189535f;
    asm volatile("s_waitcnt vmcnt(1)\n\tv_mul_f32 %2, %8, v114\n\tv_mul_f32 %3, %8, v115\n\tv_mul_f32 %0, %6, v112\n\tv_mul_f32 %1, %7, v113\n\tv_mov_b32 %4, v116\n\tv_mov_b32 %5, v117"
      : "=&v"(a2),"=&v"(bp),"=&v"(br),"=&v"(bi),"=&v"(cr),"=&v"(ci) : "v"(ca),"v"(cb_),"v"(dt) : "memory");
    C2 W; W.r=cr*br-ci*bi; W.i=cr*bi+ci*br;
    u32 mx=BC(u32,__builtin_fmaxf(__builtin_fabsf(W.r),__builtin_fabsf(W.i)));
    #define MXS(C) mx=umax_(mx,dppu<C>(mx));
    float e8=a2*8.f, eB=__builtin_fmaf(a2,f64,14.f), p8=bp*8.f, pB=bp*f64; SCHED();
    MXS(0xB1) p8=__builtin_amdgcn_fractf(p8); pB=__builtin_amdgcn_fractf(pB); e8=__builtin_amdgcn_exp2f(e8); SCHED();
    MXS(0x4E) eB=__builtin_amdgcn_exp2f(eB); float c8=__builtin_amdgcn_cosf(p8); SCHED();
    MXS(0x141) float s8=__builtin_amdgcn_sinf(p8), cB=__builtin_amdgcn_cosf(pB); SCHED();
    float sB=__builtin_amdgcn_sinf(pB); PIN(sB); MXS(0x140) float e1=__builtin_amdgcn_exp2f(a2); PIN(e1); SCHED();
    asm volatile("s_mov_b64 exec, 0x10001\n\tds_max_u32 %0, %1\n\tds_add_u32 %0, %2 offset:4\n\ts_mov_b64 exec, -1" :: "v"(te),"v"(mx),"v"(one) : "memory");
    C3 z8; z8.r=e8*c8; z8.n=e8*s8; z8.i=-z8.n; PIN(z8.i); C2 B; B.r=eB*cB; B.i=-(eB*sB);
    float p1=__builtin_amdgcn_fractf(bp), pE=__builtin_amdgcn_fractf(bp*256.f), pV=__builtin_amdgcn_fractf(bp*f128), eE=a2*256.f, eV=a2*f128, tr[8];
    #pragma unroll
    for(int k=0;k<4;k++){ SCHED(); u32 h0=ph(B); B=cm(B,z8);
      if(k==0){ tr[0]=__builtin_amdgcn_cosf(pV); tr[1]=__builtin_amdgcn_sinf(pV); } if(k==1){ tr[2]=__builtin_amdgcn_exp2f(eV); tr[3]=__builtin_amdgcn_cosf(p1); }
      if(k==2){ tr[4]=__builtin_amdgcn_sinf(p1); tr[5]=__builtin_amdgcn_exp2f(eE); } if(k==3){ tr[6]=__builtin_amdgcn_cosf(pE); tr[7]=__builtin_amdgcn_sinf(pE); }
      PIN(tr[2*k]); PIN(tr[2*k+1]);
      u32 h1=ph(B); if(k<3) B=cm(B,z8); W2(tB[k],h0,h1); }
    asm volatile("ds_read_b64 v[118:119], %0" :: "v"(te) : "memory");
    SCHED();
    C3 z1=c3(C2{e1*tr[3],e1*tr[4]}), E=c3(C2{tr[5]*tr[6],tr[5]*tr[7]}); C2 V[8]; V[0]=cmul(W,C2{tr[2]*tr[0],tr[2]*tr[1]});
    { u32 cnt,n; PIN(V[0].r); PIN(V[0].i); PIN(z1.r); PIN(E.r);
      asm volatile("s_mov_b32 %1, 0x20000\n1:\n\ts_waitcnt lgkmcnt(0)\n\tv_readfirstlane_b32 %2, v119\n\tv_readfirstlane_b32 %0, v118\n\ts_cmp_eq_u32 %2, 8\n\ts_cbranch_scc1 2f\n\tds_read_b64 v[118:119], %3\n\ts_sub_u32 %1, %1, 1\n\ts_cbranch_scc0 1b\n2:"
        : "=&s"(emax),"=&s"(cnt),"=&s"(n) : "v"(te) : "memory","scc");
      emax=RFL(emax); emax=(emax>>23)-126; }
    V[0].r=__builtin_amdgcn_ldexpf(V[0].r,14-emax); V[0].i=__builtin_amdgcn_ldexpf(V[0].i,14-emax);
    C2 Y=cm(V[0],E), Y4; u32 y0=ph(Y);
    V[1]=cm(V[0],z1); W2(t8[0],ph(V[0]),ph(V[1])); Y=cm(Y,E); u32 y1=ph(Y);
    V[2]=cm(V[1],z1); Y=cm(Y,E); u32 y2=ph(Y);
    V[3]=cm(V[2],z1); W2(t8[1],ph(V[2]),ph(V[3])); W2(t4[0],y0,ph(cm(V[2],E)));
    V[4]=cm(V[3],z1); Y=cm(Y,E); ST(u32,t1)=ph(Y);
    V[5]=cm(V[4],z1); W2(t8[2],ph(V[4]),ph(V[5])); Y4=cm(V[4],E); u32 y4=ph(Y4);
    V[6]=cm(V[5],z1); Y=cm(Y,E); ST(u32,t1+2*TROW)=ph(Y);
    V[7]=cm(V[6],z1); W2(t8[3],ph(V[6]),ph(V[7])); Y4=cm(Y4,E); W2(t2[0],y1,ph(Y4));
    W2(t4[1],y4,ph(cm(V[6],E))); Y=cm(Y,E); ST(u32,t1+4*TROW)=ph(Y);
    Y4=cm(Y4,E); W2(t2[1],y2,ph(Y4)); Y=cm(Y,E); ST(u32,t1+6*TROW)=ph(Y);
    Y=cm(Y,E); asm volatile("s_mov_b32 exec_hi, 0\n\tds_write_b32 %0, %1 offset:4352\n\ts_mov_b32 exec_hi, -1" :: "v"(t1),"v"(ph(Y)) : "memory");
  }
  if(BID<64){ if(grp==0) PRIO(PS&3); if(grp==1) PRIO((PS>>4)&3); if(grp==2) PRIO((PS>>8)&3); }
  DRAIN();
  gsync(sm,C_G+grp,4);
  if(role==0) chan2<0>(sm,cb,fa,fb,grp,d,one_,emax,tag,ws);
  if(role==1) chan2<1>(sm,cb,fa,fb,grp,d,one_,emax,tag,ws);
  if(role==2) chan2<2>(sm,cb,fa,fb,grp,d,one_,emax,tag,ws);
  chan2<3>(sm,cb,fa,fb,grp,d,one_,emax,tag,ws);
}

#define GTC(r,s,c) "v_cmp_gt_u32 %[" #s "], v" #r ", " c "\n\t"
#define GTA(s) "v_addc_co_u32 %[rk], vcc, 0, %[rk], %[" #s "]\n\t"
#define GT0(s) "v_cndmask_b32 %[rk], 0, 1, %[" #s "]\n\t"
#define GT4(a,b,c,d,k,F) GTC(a,s0,k) GTC(b,s1,k) GTC(c,s2,k) GTC(d,s3,k) F(s0) GTA(s1) GTA(s2) GTA(s3)
#define GTS [s0]"=&s"(q0),[s1]"=&s"(q1),[s2]"=&s"(q2),[s3]"=&s"(q3)
DEV void top8(lch* sm,int w4,int ln,u32 M,char* ws,u32& rank_,u32& key_){
  u32 k[8], hm=0xffff0000u, mx=1+w4*4+(ln>>4), one=1, cw=O_CND+w4*32; u64 em=0x0001000100010001ull,q0,q1,q2,q3;
  u32x4 rs=mkrs(ws+W_RG+(BID&(NRG-1))*8192,8192); int ro=w4*1024+ln*16;
  #pragma unroll
  for(int e=0;e<8;e++){ k[e]=(u32)(0xfff-(w4*256+ln*4+(e&3)+(e>>2)*1024)); PIN(k[e]); }
  lch *aW=sm+O_RMX+(w4*4+(ln>>4))*4, *aC=sm+O_RMX+(ln>>2)*4, *aO=sm+O_RMX+(ln&3)*16, *dC=sm+O_CND+(ln>>1)*4, *dO=sm+O_CND+(ln&1)*64, *cW=sm+C_W*4; PIN(aW); PIN(aC); PIN(aO); PIN(dC); PIN(dO); PIN(ro); PIN(ln); PIN(mx); PIN(one); PIN(cW);
  asm volatile("" : "+s"(hm),"+s"(em),"+s"(rs),"+s"(cw));
  gwait<2>(sm,C_G,RGATE);
  if(RD) __builtin_amdgcn_s_sleep(RD);
  { u32 cnt,t0,t1;
    #define KL(e,r) "v_lshl_or_b32 %[k" #e "], v" #r ", 16, %[k" #e "]\n\t"
    #define KH(e,r) "v_and_or_b32 %[k" #e "], v" #r ", %[hm], %[k" #e "]\n\t"
    #define CHK_R "v_bitop3_b32 %[t0], v113, v115, %[M] bitop3:0x7e\n\tv_bitop3_b32 %[t1], v117, v119, %[M] bitop3:0x7e\n\tv_or_b32 %[t0], %[t0], %[t1]\n\tv_cmp_ne_u32 vcc, 0, %[t0]\n\t"
    #define MXD(c) "v_max_u32_dpp %[mx], %[mx], %[mx] " c " row_mask:0xf bank_mask:0xf\n\t"
    asm volatile(LD_DUP2
      "s_sleep " STR(R2D) "\n\tbuffer_load_dwordx4 v[120:123], %[of], %[rs], 0 offen sc1\n\tbuffer_load_dwordx4 v[124:127], %[of], %[rs], %[so] offen sc1\n\t"
      "s_mov_b64 exec, 1\n\tds_add_u32 %[cw], %[one]\n\ts_mov_b64 exec, -1\n\ts_mov_b32 %[cnt], 0x20000\n\ts_waitcnt vmcnt(2)\n\t" CHK_R "s_cbranch_vccz 9f\n\t"
      "s_waitcnt vmcnt(0)\n\tv_mov_b64 v[112:113], v[120:121]\n\tv_mov_b64 v[114:115], v[122:123]\n\tv_mov_b64 v[116:117], v[124:125]\n\tv_mov_b64 v[118:119], v[126:127]\n\t"
      POLLASM(LD_DUP2,CHK_R,PSLP)
      "v_pk_max_u16 %[t0], v112, v114\n\tv_pk_max_u16 %[t1], v116, v118\n\tv_pk_max_u16 %[t0], %[t0], %[t1]\n\t"
      "v_max_u16_sdwa %[mx], %[t0], %[t0] dst_sel:WORD_1 dst_unused:UNUSED_PRESERVE src0_sel:WORD_0 src1_sel:WORD_1\n\t"
      KL(0,112) KH(1,112) MXD("quad_perm:[1,0,3,2]") KL(2,114) KH(3,114) MXD("quad_perm:[2,3,0,1]") KL(4,116) KH(5,116) MXD("row_half_mirror") KL(6,118) KH(7,118) MXD("row_mirror")
      "s_mov_b64 exec, %[em]\n\tds_write_b32 %[aW], %[mx]\n\ts_mov_b64 exec, -1\n\tds_read_b32 v116, %[aC]\n\tds_read_b128 v[112:115], %[aO]"
      : [k0]"+v"(k[0]),[k1]"+v"(k[1]),[k2]"+v"(k[2]),[k3]"+v"(k[3]),[k4]"+v"(k[4]),[k5]"+v"(k[5]),[k6]"+v"(k[6]),[k7]"+v"(k[7]),[mx]"+v"(mx),[cnt]"=&s"(cnt),[t0]"=&v"(t0),[t1]"=&v"(t1)
      : [of]"v"(ro),[rs]"s"(rs),[so]"s"(4096),[M]"s"(M),[hm]"s"(hm),[cw]"v"(cW),[one]"v"(one),[em]"s"(em),[aW]"v"(aW),[aC]"v"(aC),[aO]"v"(aO) : PCLOB); }
  u32 slow;
  { u32 rk,cnt,T,h,l,t,n;
    #define INS(e) "v_med3_u32 %[t], %[t], %[l], %[k" #e "]\n\tv_med3_u32 %[l], %[h], %[l], %[k" #e "]\n\tv_max_u32 %[h], %[h], %[k" #e "]\n\t"
    asm volatile("s_branch 7f\n"
      "3:\n\tv_cmp_le_u32 %[s2], %[T], %[t]\n\ts_add_u32 %[cnt], %[cnt], %[n]\n\ts_cmp_gt_u32 %[cnt], 8\n\ts_cbranch_scc1 5f\n\ts_cmp_lg_u64 %[s2], 0\n\ts_cbranch_scc1 5f\n\t"
      "s_mov_b64 exec, %[s1]\n\tv_mov_b32 %[t], %[n]\n\tv_mbcnt_lo_u32_b32 %[t], exec_lo, %[t]\n\tv_mbcnt_hi_u32_b32 %[t], exec_hi, %[t]\n\tv_lshl_add_u32 %[t], %[t], 2, %[cw]\n\tds_write_b32 %[t], %[l]\n\ts_mov_b64 exec, -1\n\ts_branch 4f\n"
      "5:\n\ts_mov_b32 %[sl], 1\n\ts_branch 6f\n7:\n\t"
      "v_max3_u32 %[h], %[k0], %[k1], %[k2]\n\tv_med3_u32 %[l], %[k0], %[k1], %[k2]\n\tv_min3_u32 %[t], %[k0], %[k1], %[k2]\n\t" INS(3) INS(4) INS(5) INS(6) INS(7)
      "s_mov_b32 %[cnt], 0x20000\n1:\n\ts_waitcnt lgkmcnt(0)\n\tv_cmp_eq_u32 vcc, 0, v116\n\ts_cbranch_vccz 2f\n\tds_read_b32 v116, %[aC]\n\tds_read_b128 v[112:115], %[aO]\n\ts_sub_u32 %[cnt], %[cnt], 1\n\ts_cbranch_scc0 1b\n2:\n\t"
      GT4(112,113,114,115,"v116",GT0) "v_and_b32 v116, %[hm], v116\n\ts_nop 0\n\tv_add_u32_dpp %[rk], %[rk], %[rk] quad_perm:[1,0,3,2] row_mask:0xf bank_mask:0xf\n\ts_mov_b32 %[sl], 0\n\ts_nop 0\n\tv_add_u32_dpp %[rk], %[rk], %[rk] quad_perm:[2,3,0,1] row_mask:0xf bank_mask:0xf\n\t"
      "v_cmp_eq_u32 vcc, 7, %[rk]\n\ts_ff1_i32_b64 %[cnt], vcc\n\ts_nop 0\n\tv_readlane_b32 %[T], v116, %[cnt]\n\ts_nop 1\n\t"
      "v_cmp_le_u32 vcc, %[T], %[h]\n\tv_cmp_le_u32 %[s1], %[T], %[l]\n\ts_bcnt1_i32_b64 %[n], vcc\n\tv_mbcnt_lo_u32_b32 %[rk], vcc_lo, 0\n\tv_mbcnt_hi_u32_b32 %[rk], vcc_hi, %[rk]\n\ts_bcnt1_i32_b64 %[cnt], %[s1]\n\ts_cbranch_scc1 3b\n\t"
      "s_cmp_gt_u32 %[n], 8\n\ts_cbranch_scc1 5b\n"
      "4:\n\tv_lshl_add_u32 %[rk], %[rk], 2, %[cw]\n\ts_mov_b64 exec, vcc\n\tds_write_b32 %[rk], %[h]\n\ts_mov_b64 exec, 1\n\tds_add_u32 %[cW], %[one] offset:4\n\ts_mov_b64 exec, -1\n6:"
      : [rk]"=&v"(rk),[cnt]"=&s"(cnt),[T]"=&s"(T),[h]"=&v"(h),[l]"=&v"(l),[t]"=&v"(t),[n]"=&s"(n),[sl]"=&s"(slow),GTS
      : [k0]"v"(k[0]),[k1]"v"(k[1]),[k2]"v"(k[2]),[k3]"v"(k[3]),[k4]"v"(k[4]),[k5]"v"(k[5]),[k6]"v"(k[6]),[k7]"v"(k[7]),[aC]"v"(aC),[aO]"v"(aO),[hm]"s"(hm),[cw]"s"(cw),[cW]"v"(cW),[one]"v"(one) : PCLOB); }
  if(__builtin_expect(RFL(slow)!=0,0)){
    u32 mine=0;
    _Pragma("unroll 1") for(int c=0;c<8;c++){ u32 lm=umax_(umax_(umax_(k[0],k[1]),umax_(k[2],k[3])),umax_(umax_(k[4],k[5]),umax_(k[6],k[7]))); u32 wm=wmax(lm); if(ln==c) mine=wm;
      #pragma unroll
      for(int e=0;e<8;e++) k[e]=(k[e]==wm)?0u:k[e]; }
    if(ln<8) ST(u32,sm+cw+ln*4)=mine;
    garrive(sm,C_W+1); }
  { u32 c,rk,cnt;
    asm volatile("s_mov_b32 %[cnt], 0x20000\n1:\n\tds_read_b32 %[rk], %[cW] offset:4\n\tds_read_b32 %[c], %[dC]\n\tds_read_b128 v[96:99], %[dO]\n\tds_read_b128 v[100:103], %[dO] offset:16\n\tds_read_b128 v[104:107], %[dO] offset:32\n\tds_read_b128 v[108:111], %[dO] offset:48\n\t"
      "s_waitcnt lgkmcnt(5)\n\tv_cmp_gt_u32 vcc, 4, %[rk]\n\ts_cbranch_vccz 2f\n\ts_sub_u32 %[cnt], %[cnt], 1\n\ts_cbranch_scc0 1b\n2:\n\t"
      "s_waitcnt lgkmcnt(3)\n\t" GT4(96,97,98,99,"%[c]",GT0) "s_waitcnt lgkmcnt(2)\n\t" GT4(100,101,102,103,"%[c]",GTA) "s_waitcnt lgkmcnt(1)\n\t" GT4(104,105,106,107,"%[c]",GTA) "s_waitcnt lgkmcnt(0)\n\t" GT4(108,109,110,111,"%[c]",GTA)
      "s_nop 1\n\tv_add_u32_dpp %[rk], %[rk], %[rk] quad_perm:[1,0,3,2] row_mask:0xf bank_mask:0xf"
      : [c]"=&v"(c),[rk]"=&v"(rk),[cnt]"=&s"(cnt),GTS : [dC]"v"(dC),[dO]"v"(dO),[cW]"v"(cW)
      : PCLOB,"v96","v97","v98","v99","v100","v101","v102","v103","v104","v105","v106","v107","v108","v109","v110","v111");
    rank_=rk; key_=c; }
}

#define FILL_(n,WAIT) "s_mov_b32 m0, %[ld]\n\ts_mov_b32 %[c], " STR(n) "-1\n3:\n\t" WAIT "buffer_load_dwordx4 %[vo], %[rs], %[so] offen nt lds\n\ts_add_u32 m0, m0, 0x1000\n\ts_add_u32 %[so], %[so], %[st]\n\t" \
  "s_sub_u32 %[c], %[c], 1\n\ts_cbranch_scc0 3b\n\ts_mov_b32 %[ld], m0\n\t"
#define FILL(n,WAIT) do{ u32 c_; asm volatile(FILL_(n,WAIT) : [c]"=&s"(c_),[so]"+s"(wso),[ld]"+s"(wld) : [vo]"v"(wvo),[rs]"s"(wrs),[st]"s"(wst) : "memory","scc","m0"); }while(0)
#define FILLD(n,WN,WW_,STK) do{ u32 c_,t_,s_=0; asm volatile(FILL_(n,STK "ds_read_b32 %[t], %[ad]\n\ts_waitcnt lgkmcnt(0)\n\tv_readfirstlane_b32 %[s], %[t]\n\ts_cmp_ge_u32 %[s], 4\n\ts_cbranch_scc1 4f\n\t" VW(WN) "4:\n\t" VW(WW_)) \
  : [c]"=&s"(c_),[so]"+s"(wso),[ld]"+s"(wld),[t]"=&v"(t_),[s]"+s"(s_) : [vo]"v"(wvo),[rs]"s"(wrs),[st]"s"(wst),[ad]"v"(C_WIN*4) : "memory","scc","m0"); wso=RFL(wso); wld=RFL(wld); }while(0)
#define STKB "s_cmp_ge_u32 %[s], 4\n\ts_cbranch_scc1 4f\n\t"

__global__ NOPK __attribute__((amdgpu_num_vgpr(56),amdgpu_num_sgpr(102))) __launch_bounds__(1024,4) void kA(const u16* __restrict__ x,char* ws,const u16* __restrict__ wr,const u16* __restrict__ Dsk,const u16* __restrict__ wg,const u16* __restrict__ wu,
                                              const u16* __restrict__ wd,u16* __restrict__ out,KP kp){
  __shared__ __attribute__((aligned(16))) char smem[LDS_TOTAL];
  lch* sm=(lch*)smem;
  u32x4 xa,xb_,wa,wb,dq[3]; u32 xc,wc;
  asm volatile(
    "s_load_dwordx16 s[64:79], %[ka], 0x0\n\ts_load_dwordx16 s[80:95], %[ka], 0x40\n\t"
    "v_readfirstlane_b32 s60, %[t]\n\ts_and_b32 s61, %[wg], 7\n\ts_lshr_b32 s62, %[wg], 3\n\tv_and_b32 v118, 31, %[t]\n\ts_bfe_u32 s63, s60, 0x20006\n\ts_cmpk_ge_u32 s60, 0x100\n\ts_cbranch_scc1 1f\n\t"
    "s_setprio 3\n\ts_lshl_b32 s56, s61, 5\n\ts_add_u32 s56, s56, s62\n\ts_mulk_i32 s56, 0x2400\n\tv_lshrrev_b32 v119, 5, %[t]\n\tv_mul_u32_u24 v119, 0x480, v119\n\tv_add_u32 v119, s56, v119\n\t"
    "v_lshl_add_u32 v116, v118, 4, v119\n\tv_lshl_add_u32 v117, v118, 2, v119\n\tv_lshlrev_b32 v114, 4, v118\n\tv_lshlrev_b32 v115, 2, v118\n\t"
    "v_lshlrev_b32 v119, 1, %[t]\n\tv_mov_b32 v118, 0\n\tds_write_b16 v119, v118\n\t"
    "s_waitcnt lgkmcnt(0)\n\t"
    "s_load_dwordx2 s[96:97], s[66:67], 0x0\n\t"
    "global_load_dwordx4 %[wa], v114, s[68:69]\n\tglobal_load_dwordx4 %[wb], v114, s[68:69] offset:512\n\tglobal_load_dword %[wc], v115, s[68:69] offset:1024\n\t"
    "global_load_dwordx4 %[xa], v116, s[64:65]" XPOL "\n\tglobal_load_dwordx4 %[xb], v116, s[64:65] offset:512" XPOL "\n\tglobal_load_dword %[xc], v117, s[64:65] offset:1024" XPOL "\n\t"
    "s_barrier\n\t"
    "s_branch 8f\n"
    "1:\n\ts_cmp_ge_u32 s62, " STR(CPF) "\n\ts_cbranch_scc1 6f\n\ts_getpc_b64 s[56:57]\n.Lkpc:\n\ts_and_b32 s58, s62, 3\n\ts_lshl_b32 s58, s58, 13\n\ts_sub_u32 s58, s58, 0x1000\n\tv_lshl_add_u32 v119, %[t], 4, s58\n\tv_min_u32 v119, .Lkend-.Lkpc-16, v119\n\t"
    "global_load_dwordx4 v[120:123], v119, s[56:57]\n\t.subsection 1\n.Lkend:\n\t.subsection 0\n6:\n\t"
    "v_mov_b32 v124, -1\n\tv_mov_b32 v125, -1\n\tv_mov_b32 v126, -1\n\tv_mov_b32 v127, -1\n\tv_lshlrev_b32 v119, 4, %[t]\n\tv_and_b32 v119, 0xff0, v119\n\t"
    "s_lshr_b32 s59, s60, 8\n\ts_cmp_lt_u32 s62, 8\n\ts_cselect_b32 s58, 2, 1\n\ts_cbranch_scc1 2f\n\ts_cmp_eq_u32 s59, 3\n\ts_cbranch_scc1 4f\n"
    "2:\n\ts_sub_u32 s57, s59, 2\n\ts_cmp_eq_u32 s59, 1\n\ts_cselect_b32 s58, s58, s57\n\ts_cmp_lt_u32 s62, 8\n\ts_cselect_b32 s56, 0, 8\n\ts_lshl1_add_u32 s56, s62, s56\n\ts_mulk_i32 s61, 72\n\ts_add_u32 s51, s56, s61\n\ts_sub_u32 s56, 2, s59\n\ts_sub_u32 s57, 16, s62\n\ts_cmp_eq_u32 s59, 3\n\ts_cselect_b32 s50, s57, s56\n\ts_add_u32 s56, s50, s51\n\t"
    "v_mov_b32 v112, 0\n\tv_mov_b32 v113, 0\n\tv_mov_b32 v114, 0\n\tv_mov_b32 v115, 0\n\tv_mov_b32 v116, 0\n\tv_mov_b32 v117, 0\n\ts_setprio 2\n\t"
    "s_lshl_b32 s57, s56, 2\n\ts_add_u32 s57, s57, s63\n\ts_lshl_b32 s57, s57, 6\n\tv_lshl_add_u32 v22, v118, 1, s57\n\ts_mul_i32 s57, s56, %[yst]\n\ts_add_u32 s57, s57, %[wy]\n\tv_add_u32 v23, s57, v119\n\ts_lshl_b32 s56, s56, 1\n\ts_and_b32 s56, s56, -4\n\t"
    EB1 "s_waitcnt lgkmcnt(0)\n\t"
    "global_load_short_d16_hi v112, v22, s[80:81]" QPOL "\n\tglobal_load_short_d16_hi v113, v22, s[82:83]" QPOL "\n\tglobal_load_short_d16_hi v114, v22, s[84:85]" QPOL "\n\t"
    "global_load_short_d16_hi v115, v22, s[86:87]" QPOL "\n\tglobal_load_short_d16_hi v116, v22, s[88:89]" QPOL "\n\tglobal_load_short_d16_hi v117, v22, s[90:91]" QPOL "\n\t"
    "s_load_dwordx2 s[96:97], s[66:67], 0x0\n\ts_load_dword s98, s[92:93], s56\n\t"
    EB0 IV0
    "s_mul_i32 s52, s58, %[sl]\n\ts_add_u32 s52, s52, %[tb]\n\ts_lshl_b32 s53, s63, 7\n\ts_add_u32 s53, s53, s52\n\tv_lshl_add_u32 v30, v118, 2, s53\n\tv_bfe_u32 v31, %[t], 5, 1\n\ts_movk_i32 s54, 544\n\tv_mul_u32_u24 v29, s54, v31\n\t"
    "v_lshl_add_u32 v2, v29, 3, v30\n\ts_movk_i32 s53, 1088\n\tv_add_u32 v3, s53, v2\n\tv_add_u32 v4, s53, v3\n\tv_add_u32 v5, s53, v4\n\tv_add_u32 v6, 41*544, v2\n\tv_add_u32 v7, s53, v6\n\tv_add_u32 v8, s53, v7\n\tv_add_u32 v9, s53, v8\n\t"
    "v_lshl_add_u32 v10, v29, 2, v30\n\tv_add_u32 v10, 16*544, v10\n\tv_add_u32 v11, s53, v10\n\tv_lshl_add_u32 v12, v29, 1, v30\n\tv_add_u32 v12, 24*544, v12\n\tv_add_u32 v13, 2176, v12\n\tv_add_u32 v14, v29, v30\n\tv_add_u32 v14, 32*544, v14\n\t"
    "s_lshl_b32 s53, s58, 4\n\ts_add_u32 s53, s53, %[ex]\n\tv_mov_b32 v15, s53\n\tv_cvt_f32_ubyte0 v31, v31\n\tv_mul_f32 v16, 0x42800000, v31\n\tv_add_f32 v17, v16, v16\n\tv_mov_b32 v20, 1\n\t"
    "v_and_b32 v29, 15, %[t]\n\tv_and_b32 v30, 48, %[t]\n\tv_mad_u32_u24 v30, v29, s54, v30\n\ts_lshl_b32 s55, s59, 1\n\ts_lshr_b32 s55, 0x48, s55\n\ts_add_u32 s55, s55, s63\n\ts_and_b32 s55, s55, 3\n\ts_mul_i32 s53, s55, 16*544\n\ts_movk_i32 s57, 41*544\n\ts_cmp_eq_u32 s55, 2\n\ts_cselect_b32 s53, s57, s53\n\ts_cselect_b32 s57, 32*544, s57\n\t"
    "s_add_u32 s53, s53, s52\n\ts_add_u32 s57, s57, s52\n\tv_add_u32 v18, s53, v30\n\tv_add_u32 v19, s57, v30\n\t"
    "v_min_u32 v29, 237*16, v119\n\tv_subrev_u32 v30, 204*16, v29\n\tv_add_u32 v29, %[de], v29\n\tv_min_u32 v29, v29, v30\n\ts_sub_u32 s53, s52, 4640\n\tv_add_u32 v29, s53, v29\n\tv_mov_b32 v24, 0\n\tv_mov_b32 v25, 0\n\tv_mov_b32 v26, 0\n\tv_mov_b32 v27, 0\n\tds_write_b128 v29, v[24:27]\n\t"
    IV1
    "s_cmp_lg_u32 s55, 3\n\ts_cbranch_scc1 9f\n\t"
    "v_bfe_u32 v18, %[t], 1, 5\n\tv_and_b32 v19, 1, %[t]\n\ts_cmp_lt_u32 s62, 8\n\ts_cselect_b32 s53, 2, 1\n\ts_mul_i32 s53, s53, %[sl]\n\tv_mul_u32_u24 v21, s53, v19\n\ts_sub_u32 s55, 2, s59\n\ts_lshl_b32 s55, s55, 7\n\tv_add_u32 v18, s55, v18\n\ts_cmp_lg_u32 s59, 3\n\ts_cbranch_scc1 7f\n\t"
    "v_bfe_u32 v18, %[t], 5, 1\n\tv_lshl_add_u32 v18, v18, 7, v118\n\tv_mov_b32 v19, s50\n\tv_mov_b32 v21, %[sl]\n"
    "7:\n\tv_add_u32 v19, s51, v19\n\tv_lshl_add_u32 v21, v18, 4, v21\n\tv_lshrrev_b32 v24, 3-" STR(TL) ", v19\n\tv_and_b32 v19, 7>>" STR(TL) ", v19\n\tv_lshlrev_b32 v19, 4, v19\n\tv_lshl_add_u32 v19, v24, 7, v19\n\t"
    "v_and_b32 v24, (1<<" STR(TL) ")-1, v18\n\tv_lshl_add_u32 v19, v24, 7-" STR(TL) ", v19\n\tv_lshrrev_b32 v18, " STR(TL) ", v18\n\tv_mul_u32_u24 v18, %[ust], v18\n\tv_add_u32 v18, v18, v19\n\tv_add_u32 v19, 512, v21\n\t"
    "s_branch 9f\n"
    "4:\n\t" EB1
    "v_bfe_u32 v118, %[t], 3, 5\n\tv_lshrrev_b32 v117, 1, v118\n\tv_and_b32 v117, 4, v117\n\tv_and_b32 v116, 2, v118\n\tv_and_b32 v115, 7, %[t]\n\tv_or_b32 v116, v116, v117\n\tv_xor_b32 v115, v115, v116\n\tv_lshlrev_b32 v115, 4, v115\n\t"
    "s_mul_i32 s54, s61, 9\n\ts_sub_u32 s55, s62, 14\n\ts_lshr_b32 s55, s55, 1\n\ts_add_u32 s54, s54, s55\n\ts_lshl_b32 s54, s54, 7\n\t"
    "s_mul_i32 s52, s61, 0xa2000\n\ts_mul_i32 s53, s62, 0x180\n\ts_add_u32 s52, s52, s53\n\ts_cmp_ge_u32 s62, 11\n\ts_cselect_b32 s53, 0x51000-0x480, 0\n\ts_add_u32 s52, s52, s53\n\ts_sub_u32 s52, s52, 0xc00\n\t"
    "s_movk_i32 s55, 0x480\n\ts_cmp_ge_u32 s62, 14\n\ts_cselect_b32 s54, s54, s52\n\ts_cselect_b32 s55, 0x2400, s55\n\tv_mad_u32_u24 v118, v118, s55, v115\n\ts_lshl_b32 s55, s55, 5\n\t"
    "s_lshl_b32 s52, s63, 10\n\ts_add_u32 m0, s52, %[r2]\n\ts_mov_b32 s58, 0x510000\n\ts_mov_b32 s59, 0x20000\n\ts_cmp_ge_u32 s62, 14\n\ts_cselect_b32 s53, " STR(PRE1) "-1, " STR(PRE2) "-1\n\ts_setprio 3\n\t"
    "s_waitcnt lgkmcnt(0)\n\t"
    "s_bitcmp1_b32 %[wg], 3\n\ts_cselect_b64 s[56:57], s[74:75], s[72:73]\n\ts_cmp_ge_u32 s62, 14\n\ts_cselect_b64 s[56:57], s[56:57], s[76:77]\n\ts_cselect_b32 s52, 0x180, -1\n\ts_and_b32 s57, s57, 0xffff\n\ts_add_u32 s60, s56, s54\n\ts_and_b32 s60, s60, 0x380\n\ts_cmp_eq_u32 s60, s52\n\ts_cselect_b32 s53, " STR(PRE3) "-1, s53\n\t"
    "s_load_dwordx2 s[96:97], s[66:67], 0x0\n\t" EB0
    "\n3:\n\t" "buffer_load_dwordx4 v118, s[56:59], s54 offen nt lds\n\ts_add_u32 m0, m0, 0x1000\n\ts_add_u32 s54, s54, s55\n\ts_sub_u32 s53, s53, 1\n\ts_cbranch_scc0 3b\n"
    "9:\n8:"
    : [xa]"=&{v[2:5]}"(xa),[xb]"=&{v[6:9]}"(xb_),[xc]"=&{v30}"(xc),[wa]"=&{v[10:13]}"(wa),[wb]"=&{v[14:17]}"(wb),[wc]"=&{v31}"(wc),[d0]"=&{v[18:21]}"(dq[0]),[d1]"=&{v[22:25]}"(dq[1]),[d2]"=&{v[26:29]}"(dq[2])
    : [ka]"s"(__builtin_amdgcn_kernarg_segment_ptr()),[wg]"s"(BID),[t]"v"(TID),[yst]"i"(YST),[wy]"i"(W_Y),[r2]"i"(O_R2),[sl]"i"(SLSZ),[tb]"i"(O_CH+O_TB),[ex]"i"(O_EXP),[de]"i"(O_DE),[ust]"i"(GST_)
    : "memory","scc","m0","v127","s50","s51","s52","s53","s54","s55","s56","s57","s58","s59","s60","s61","s62","s63","s64","s65","s66","s67","s68","s69","s70","s71","s72","s73","s74","s75","s76","s77","s78","s79",
      "s80","s81","s82","s83","s84","s85","s86","s87","s88","s89","s90","s91","s92","s93","s94","s95","s99");
  int wv=RFL(TID>>6), bid=BID, q4=wv>>2, w4=wv&3;
  int xg=bid&7, lb=bid>>3, tb=32*xg+lb, nch=2+(lb<8);
  u64 ep; u32 ldw;
  {
  }
  #define KILLIN "=v"(xa),"=v"(xb_),"=v"(xc),"=v"(wa),"=v"(wb),"=v"(wc),"=v"(dq[0]),"=v"(dq[1]),"=v"(dq[2])
  if(__builtin_expect(q4==3&&lb>=8,0)){
    int fu=18*xg+lb-14, kq=2*xg+(lb>=11), cgp=lb-8-3*(lb>=11);
  u32 wst= lb>=14? F_*64:D_*64;
  u32 wso=(lb>=14? (fu>>1)*128 : kq*(288*D_*2)+384*cgp)+(lb>=14? PRE1:PRE2)*wst, wld=(u32)(size_t)sm+O_R2+w4*1024+(lb>=14? PRE1:PRE2)*4096; u32x4 wrs=mkrs(lb>=14? ((fu&1)? wu:wg):wd,D_*F_*2);
  GETEP
  #define FLANE(RS) LANE(ln); int g=ln>>4, li=ln&15, qq=li>>2, pq=li&3; u32 wvo=(u32)(8*w4+(ln>>3))*(RS)+(u32)((ln&7)^(((ln>>3)&2)|((w4&1)<<2)))*16u; \
    const lch* tp=sm+O_R2+(8*g+qq)*128+((w4^((qq>>1)|((g&1)<<1)))*32)+pq*8;
  #define WFR(img,ks) trfrag(tp+(img)*CHSZ+(ks)*4096,tp+(img)*CHSZ+(ks)*4096+512)
  u64 tag=ep+1ull; const u32x4 I4={INV,INV,INV,INV};
  if(lb>=14){
    FLANE(F_*2)
    int mt=fu&1;
    const lch* xp=sm+O_TILE+li*HSTR+g*16; int of=(mt*288+(fu>>1)*4+w4)*256+(li&7)*32+g*8+(li>>3)*4;
    const lch* mp=sm+O_RC+(li&7)*4;
    asm volatile("" : "+v"(xp),"+v"(of),"+v"(mp));
    rsrc_t rsG=RSRC(ws+W_GU,2*8*F_*2);
    if(w4==0) BST128(I4,rsG,(mt*288+(fu>>1)*4)*256+ln*16,0,SC1);
    bool slow=(((u32)wrs[0]+wso)&0x380u)==0x180u;
    if(slow){ wso-=(PRE1-PRE3)*wst; wld-=(PRE1-PRE3)*4096; FILL(5+PRE1-PRE3,VW(W3)); } else FILLD(5,W1,W1B,"");
    if(NA){ VMW(11-NA); garrive(sm,C_P); }
    if(w4==0){ VMW(5); if(ln==0) GST(ws+W_FL+(576+fu)*8,tag); }
    if(slow) FILLD(13-PRE1,W3,W1B,STKB); else FILLD(13-PRE1,W1,W1B,STKB);
    bf16x8 fB[18]; f32x4 cs={0,0,0,0}; const bf16x8 on=BC(bf16x8,(u32x4{0x3f803f80u,0x3f803f80u,0x3f803f80u,0x3f803f80u}));
    const lch* tq=tp+CHSZ; PIN(tq);
    #define RDF(a,b) _Pragma("unroll") for(int ks=a;ks<b;ks++) fB[ks]= ks<9? WFR(0,ks) : trfrag(tq+(ks-9)*4096,tq+(ks-9)*4096+512);
    #define CSF(a,b) SCHED(); _Pragma("unroll") for(int ks=a;ks<b;ks++) cs=__builtin_amdgcn_mfma_f32_16x16x32_bf16(fB[ks],on,cs,0,0,0); SCHED();
    #define PNF(a,b) PIN(cs); _Pragma("unroll") for(int ks=a;ks<b;ks++) PIN(fB[ks]);
    if(NA){ gwait<0>(sm,C_P,4); RDF(0,NA) CSF(0,NA) PNF(0,NA) }
    if(NB){ VMW(18-NB); gsync(sm,C_P,NA? 8:4); RDF(NA,NB) CSF(NA,NB) PNF(NA,NB) }
    #define NC (NB? NB:NA)
    #define CP0 ((NA? 4:0)+(NB? 4:0))
    VMW(LAST1);
    gsync(sm,C_P,CP0+4);
    RDF(NC,18-LAST1) CSF(NC,18-LAST1) PNF(NC,18-LAST1)
    DRAIN();
    gsync(sm,C_P,CP0+8);
    float cA,cB,ri,ce,p0,p1,q0,q1; u32 mdw;
    { bf16x8 xb[9];
      RDF(18-LAST1,18) SCHED();
      u32 cx=*(const volatile LAS u32*)(sm+C_X*4);
      #pragma unroll
      for(int ks=0;ks<4;ks++) xb[ks]=*(const volatile LAS bf16x8*)(xp+ks*64);
      CSF(18-LAST1,18)
      if(RFL(cx)<4u){ gwait<XS1>(sm,C_X,XG1); gwait4<64>(C_X,4,xp,xb[0],xb[1],xb[2],xb[3]); }
      mdw=LD(u32,mp); xb[4]=LD(bf16x8,xp+256); xb[5]=LD(bf16x8,xp+320);
      SCHED();
      #pragma unroll
      for(int ks=0;ks<8;ks++){
        if(ks==0) asm volatile("v_mfma_f32_16x16x32_bf16 v[112:115], %0, %2, 0\n\tv_mfma_f32_16x16x32_bf16 v[116:119], %1, %2, 0" :: "v"(fB[0]),"v"(fB[9]),"v"(xb[0]));
        else asm volatile("v_mfma_f32_16x16x32_bf16 v[112:115], %0, %2, v[112:115]\n\tv_mfma_f32_16x16x32_bf16 v[116:119], %1, %2, v[116:119]" :: "v"(fB[ks]),"v"(fB[ks+9]),"v"(xb[ks]));
        SCHED();
        if(ks<3) xb[ks+6]=LD(bf16x8,xp+(ks+6)*64); if(ks==HG) garrive(sm,C_U);
        if(ks==2){ PIN(cs); cA= li<8? cs[0]:cs[2]; cB= li<8? cs[1]:cs[3]; PIN(cA); PIN(cB); }
        if(ks==3){ PIN(mdw); ri=rcpf_(bhi(mdw)); float nm=-blo(mdw); cA*=nm; cB*=nm; PIN(cA); PIN(cB); } if(ks==4){ ce=ri*-1.4426950408889634f; p0=cA*ri; p1=cB*ri; PIN(p0); PIN(p1); } if(ks==5){ q0=cA*ce; q1=cB*ce; PIN(q0); PIN(q1); }
        SCHED(); }
      #define RR8 " row_ror:8 row_mask:0xf bank_mask:"
      float v0,v1,s0,s1;
      asm volatile("v_mfma_f32_16x16x32_bf16 v[112:115], %4, %6, v[112:115]\n\tv_mfma_f32_16x16x32_bf16 v[116:119], %5, %6, v[116:119]\n\ts_nop 7\n\tv_add_f32_dpp %0, v116, v112" RR8 "0x3\n\tv_add_f32_dpp %1, v117, v113" RR8 "0x3\n\tv_add_f32_dpp %0, v114, v118" RR8 "0xc\n\tv_add_f32_dpp %1, v115, v119" RR8 "0xc\n\t"
        "v_fmac_f32 %2, %0, %8\n\tv_fmac_f32 %3, %1, %8\n\tv_fma_f32 %0, %0, %7, %9\n\tv_fma_f32 %1, %1, %7, %10"
        : "=&v"(v0),"=&v"(v1),"+v"(q0),"+v"(q1) : "v"(fB[8]),"v"(fB[17]),"v"(xb[8]),"v"(ri),"v"(ce),"v"(p0),"v"(p1));
      s0=q0; s1=q1;
      if(mt==0){ v0*=rcpf_(1.f+__builtin_amdgcn_exp2f(s0)); v1*=rcpf_(1.f+__builtin_amdgcn_exp2f(s1)); }
      __builtin_amdgcn_raw_buffer_store_b32(nv(pk(v0,v1)),rsG,of,0,SC1); garrive(sm,C_H); }
    if(mt==0&&w4==0){
    LANE(l2); ln=l2; li=ln&15; g=ln>>4;
    int c9=(lb-14)>>1, tk=(xg&6)+((li>>2)&1), t=(int)LD(u16,sm+O_IDX+tk*2), c=64*c9+32*(xg&1)+16*(li>>3)+4*(li&3)+g; f32x4 pr;
    { u32 u=LD(u16,sm+O_IDX+16+tk*2)^0x8000u, xb=LD(u16,sm+O_TILE+XAD(tk,c*2)), sv=LD(u32,sm+O_RC+tk*4); float ds=b2f(Dsk[c]); u^=(u32)(((int)(u<<16))>>31)&0x7fffu;
      poll1<0>(ws+W_FL+(64*c9+ln)*8,tag);
 poll1<0>(ws+W_FL+(720+(ln<48? ln:47))*8,tag);
      float xn=(b2f(xb)-blo(sv))*rcpf_(bhi(sv)); pr=f32x4{b2f(xb),xn,xn*ds,sigm_f(b2f(u))}; }
    gwait<2>(sm,C_PR,PRN);
    { u32x4 rs=mkrs(ws+W_PART+(xg*9+c9)*4096,4096), ry=mkrs(ws+W_Y,D_*YST);
      int yo=c*YST+(t&~1)*2, po=ln*16; u32 a[4], t0,cnt,yb; u16* op=out+t*D_+c; u32 ysl=0x3f80u<<((t&1)*16); PIN(ysl); f32x2 pe={pr[1]*-1.4426950408889634f,pr[3]*-1.4426950408889634f};
      #define LD_P "buffer_load_dwordx4 v[112:115], %[of], %[rs], 0 offen sc1\n\tbuffer_load_dwordx4 v[116:119], %[of], %[rs], 0 offen offset:1024 sc1\n\tbuffer_load_dwordx4 v[120:123], %[of], %[rs], 0 offen offset:2048 sc1\n\tbuffer_load_dwordx4 v[124:127], %[of], %[rs], 0 offen offset:3072 sc1\n\t"
      #define LD_YB "buffer_load_dword %[yb], %[yo], %[ry], 0 offen sc1\n\t"
      #define AD4(o,a,b,c,d) "v_add_f32 %[t0], v" #a ", v" #b "\n\tv_add_f32 %[" #o "], v" #c ", v" #d "\n\tv_add_f32 %[" #o "], %[t0], %[" #o "]\n\t"
      u64 ym;
      #define PGAP "s_sleep " STR(PSL) "\n\t"
      u64 nm;
      asm volatile(LD_YB PGAP LD_P "s_mov_b32 %[cnt], 0x20000\n1:\n\ts_waitcnt vmcnt(0)\n\t" AD4(a0,112,116,120,124) AD4(a1,113,117,121,125) "v_cmp_eq_u32 %[ym], -1, %[yb]\n\t" AD4(a2,114,118,122,126) "v_cmp_u_f32 %[nm], %[a0], %[a1]\n\t" AD4(a3,115,119,123,127)
        "s_or_b64 %[nm], %[nm], %[ym]\n\tv_cmp_u_f32 vcc, %[a2], %[a3]\n\ts_or_b64 vcc, vcc, %[nm]\n\ts_cbranch_scc0 9f\n\t"
        CHK_I0 MX2(116,117) MX2(118,119) MX2(120,121) MX2(122,123) MX2(124,125) MX2(126,127) CHK_IE
        "s_or_b64 exec, vcc, %[ym]\n\ts_cbranch_scc0 8f\n\ts_sleep " STR(QSLP) "\n\ts_mov_b64 exec, %[ym]\n\ts_cbranch_execz 2f\n\t" LD_YB "2:\n\ts_mov_b64 exec, vcc\n\ts_cbranch_execz 3f\n\t" LD_P
        "3:\n\ts_mov_b64 exec, -1\n\ts_sub_u32 %[cnt], %[cnt], 1\n\ts_cbranch_scc0 1b\n8:\n\ts_mov_b64 exec, -1\n9:"
        : [t0]"=&v"(t0),[cnt]"=&s"(cnt),[a0]"=&v"(a[0]),[a1]"=&v"(a[1]),[a2]"=&v"(a[2]),[a3]"=&v"(a[3]),[yb]"=&v"(yb),[ym]"=&s"(ym),[nm]"=&s"(nm) : [of]"v"(po),[rs]"s"(rs),[yo]"v"(yo),[ry]"s"(ry),"v"(op),"v"(pr),"v"(pe) : PCLOB);
      auto s0=__builtin_amdgcn_permlane32_swap(a[0],a[2],false,false); auto s1=__builtin_amdgcn_permlane32_swap(a[1],a[3],false,false);
      u32 x0=s0[0],x1=s0[1],y0=s1[0],y1=s1[1]; u32 p0=BC(u32,BC(float,x0)+BC(float,x1)), p1=BC(u32,BC(float,y0)+BC(float,y1));
      auto s2=__builtin_amdgcn_permlane16_swap(p0,p1,false,false); u32 z0=s2[0], z1=s2[1]; float s=BC(float,z0)+BC(float,z1);
      float gq=__builtin_fmaf(s,pr[3],pr[1])*rcpf_(1.f+__builtin_amdgcn_exp2f(__builtin_fmaf(s,pe[1],pe[0])));
      float sy=D2(yb,ysl,pr[2]);
      float o=__builtin_fmaf(sy,gq,pr[0]);
      *op=(u16)(pk(o,o)&0xffffu);
      if(bid==112&&ln==0) GST(ws+W_EPOCH,tag); }
    }
  } else
  {
    FLANE(D_*2)
    bf16x8 fr[27];
    rsrc_t rsP=RSRC(ws+W_PART+cgp*12288+kq*256+PWB,8*9*4096-2*12288);
    #pragma unroll
    for(int i=0;i<3;i++) BST128(I4,rsP,li<8? PLO+g*16+i*4096 : OOR,0,SC1);
    if(PRE2<9) FILLD(9-PRE2,W2_,W2B,"");
    wso+=128u-9u*wst;
    FILLD(9,W2_,W2B,"");
    VMW(9); gsync(sm,C_P,4);
    #pragma unroll
    for(int ks=0;ks<9;ks++) fr[ks]=WFR(0,ks);
    gsync(sm,C_P,8);
    if(w4==0&&ln==0) GST(ws+W_FL+(720+6*xg+lb-8)*8,tag);
    wso+=128u-9u*wst; wld-=2*CHSZ;
    FILL(9,VW(W2B));
    int fo=W_FL+(576+((9*kq)&~1)+(ln<9? ln:9))*8;
    if(w4==0) asm volatile("global_load_dwordx2 v[112:113], %0, %1 sc1\n\ts_waitcnt vmcnt(10)" :: "v"(fo),"s"(ws) : "memory"); else VMW(9);
    gsync(sm,C_P,12);
    #pragma unroll
    for(int ks=0;ks<9;ks++) fr[9+ks]=WFR(1,ks);
    { int t2=TID; asm volatile("" : "+v"(t2)); ln=t2&63; li=ln&15; g=ln>>4; }
    const lch* hp=sm+O_TILE+(li&7)*HT+g*16; int po= li<8? PLO+g*16+4096 : OOR; PIN(hp); PIN(po);
    u32x4 rs=mkrs(ws+W_GU,2*8*F_*2);
    int of=((ln>>5)*288+18*kq+2*w4)*256+(ln&31)*16; lch* hd=sm+O_TILE+w4*(8*HT)+((ln>>1)&7)*HT+((ln>>4)&1)*32+(ln&1)*16+(ln>>5)*8; PIN(of); PIN(hd); asm volatile("" : "+s"(rs));
    u32x4 rp=mkrs(ws+W_PART+cgp*12288+kq*256+PWB-4096,8*9*4096); u32 ch=C_H*4, one=1; int o3= w4==0? of+4096:OOR; PIN(ch); PIN(one); PIN(o3); asm volatile("" : "+s"(rp));
    DRAIN();
    if(w4==0){ u64 bad; asm volatile("v_cmp_ne_u64 %0, %1, v[112:113]" : "=s"(bad) : "s"(tag) : "memory");
      if(bad!=0ull) poll1<0>(ws+fo,tag); }
    __builtin_amdgcn_s_waitcnt(0x0f70);
    gsync(sm,C_P,16);
    { u32 t0,t1,cnt;
      #define LD_G "buffer_load_dwordx4 v[112:115], %[of], %[rs], 0 offen sc1\n\tbuffer_load_dwordx4 v[116:119], %[of], %[rs], 0 offen offset:2048 sc1\n\tbuffer_load_dwordx4 v[120:123], %[o3], %[rs], 0 offen sc1\n\t"
      #define HM1(a,b) "v_and_b32 %[t0], %[lm], v" #b "\n\tv_and_b32 v" #b ", %[hm], v" #b "\n\tv_dot2_f32_bf16 %[t0], v" #a ", %[t0], 0\n\tv_dot2_f32_bf16 v" #a ", v" #a ", v" #b ", 0\n\t"
      #define HM2(a,b) "v_and_b32 %[t1], %[lm], v" #b "\n\tv_and_b32 v" #b ", %[hm], v" #b "\n\tv_dot2_f32_bf16 %[t1], v" #a ", %[t1], 0\n\tv_dot2_f32_bf16 v" #a ", v" #a ", v" #b ", 0\n\t"
      #define HMUL(a,b,c,d,o) "v_permlane32_swap_b32 v" #a ", v" #c "\n\tv_permlane32_swap_b32 v" #b ", v" #d "\n\t" HM1(a,c) HM2(b,d) "v_cvt_pk_bf16_f32 v" #a ", %[t0], v" #a "\n\ts_nop 1\n\tv_cvt_pk_bf16_f32 v" #b ", %[t1], v" #b "\n\tds_write_b64 %[hd], v[" #a ":" #b "] offset:" STR(o) "\n\t"
      if(GD) __builtin_amdgcn_s_sleep(GD);
      asm volatile(LD_G :: [of]"v"(of),[rs]"s"(rs),[o3]"v"(o3) : "memory");
      #pragma unroll
      for(int ks=0;ks<G2;ks++) fr[18+ks]=WFR(0,ks);
      #pragma unroll
      for(int ks=0;ks<G2;ks++) PIN(fr[18+ks]);
      asm volatile(POLLASM(LD_G,CHK_I0 MX2(116,117) MX2(118,119) MX2(120,121) MX2(122,123) CHK_IE,GSLP)
        HMUL(112,113,114,115,0) HMUL(116,117,118,119,32*HT) "s_cmp_lg_u32 %[w4], 0\n\ts_cbranch_scc1 8f\n\t" HMUL(120,121,122,123,64*HT) "8:\n\ts_mov_b64 exec, 1\n\tds_add_u32 %[ch], %[one]\n\ts_mov_b64 exec, -1\n\t"
        "s_mov_b32 %[cnt], 0x20000\n6:\n\tds_read_b32 %[t0], %[ch]\n\tds_read_b128 v[112:115], %[hp]\n\tds_read_b128 v[116:119], %[hp] offset:8*" STR(HT) "\n\tds_read_b128 v[120:123], %[hp] offset:16*" STR(HT) "\n\tds_read_b128 v[124:127], %[hp] offset:24*" STR(HT) "\n\ts_waitcnt lgkmcnt(4)\n\t"
        "v_cmp_gt_u32 vcc, 4, %[t0]\n\ts_cbranch_vccz 7f\n\ts_sleep 1\n\ts_sub_u32 %[cnt], %[cnt], 1\n\ts_cbranch_scc0 6b\n7:"
        : [t0]"=&v"(t0),[t1]"=&v"(t1),[cnt]"=&s"(cnt) : [of]"v"(of),[rs]"s"(rs),[o3]"v"(o3),[w4]"s"(w4),[hd]"v"(hd),[hm]"s"(0xffff0000u),[lm]"s"(0xffffu),[ch]"v"(ch),[one]"v"(one),[hp]"v"(hp),"v"(po) : PCLOB); }
    { bf16x8 hb[9];
      #define MF "v_mfma_f32_16x16x32_bf16 "
      #define MF3(h) MF "%0, %3, " h ", %0\n\t" MF "%1, %4, " h ", %1\n\t" MF "%2, %5, " h ", %2"
      #define ACC "+v"(fr[0]),"+v"(fr[9]),"+v"(fr[18])
      #define FRS(k) "v"(fr[k]),"v"(fr[9+(k)]),"v"(fr[18+(k)])
      asm volatile("s_waitcnt lgkmcnt(3)\n\t" MF "%0, %0, v[112:115], 0\n\t" MF "%1, %1, v[112:115], 0\n\t" MF "%2, %2, v[112:115], 0\n\ts_waitcnt lgkmcnt(0)\n\t" MF3("v[116:119]") : ACC : FRS(1));
      hb[4]=LD(bf16x8,hp+4*8*HT); hb[5]=LD(bf16x8,hp+5*8*HT); hb[6]=LD(bf16x8,hp+6*8*HT); SCHED();
      asm volatile(MF3("v[120:123]") : ACC : FRS(2));
      #pragma unroll
      for(int ks=G2;ks<9;ks++) fr[18+ks]=WFR(0,ks);
      SCHED();
      asm volatile(MF3("v[124:127]") : ACC : FRS(3));
      hb[7]=LD(bf16x8,hp+7*8*HT); hb[8]=LD(bf16x8,hp+8*8*HT); SCHED();
      #pragma unroll
      for(int ks=4;ks<8;ks++){ asm volatile(MF3("%6") : ACC : FRS(ks),"v"(hb[ks])); SCHED(); }
      asm volatile(MF3("%6") "\n\ts_nop 5\n\tbuffer_store_dwordx4 %0, %7, %8, 0 offen sc1\n\tbuffer_store_dwordx4 %1, %7, %8, %9 offen sc1\n\tbuffer_store_dwordx4 %2, %7, %8, %10 offen sc1\n\ts_mov_b64 exec, 1\n\tds_add_u32 %11, %12\n\ts_mov_b64 exec, -1"
        : ACC : FRS(8),"v"(hb[8]),"v"(po),"s"(rp),"s"(4096),"s"(8192),"v"(ch),"v"(one) : "memory"); }

  }
  asm volatile("s_endpgm" : KILLIN :: "memory"); }
  {
  if(__builtin_expect(q4!=0,0)){ int grp= q4==1? nch-1 : q4-2;
    chan(sm,grp,(w4+(0x48>>(2*q4)))&3,72*xg+(q4==3? 16+lb : 2*lb+(lb<8? 0:8)+2-q4),(q4+1)>>2,ws,xa,xb_,wa,wb,dq[0]);
    asm volatile("s_endpgm" : KILLIN :: "memory"); }
  u32x4 xq[3]; f32x2 rc; int xr_,xr2_;
  {
    LANE(ln); int g=ln>>4, li=ln&15, qq=li>>2, pq=li&3, j=ln&31, rl=2*w4+(ln>>5);
    lch* tile=sm+O_TILE; u32 Tr=(ln>>1)&16;
    int uo=W_U+(tb>>TL)*GST_+(tb&((1<<TL)-1))*(128>>TL)+(w4*1024<<TL)+(ln>>(3-TL))*128+(ln&(7>>TL))*16, uo1=uo+(4096<<TL), uo2=uo+(8192<<TL); PIN(uo); PIN(uo1); PIN(uo2);
    lch* tw=tile+rl*TSTR2+j*16; lch* tw2=tw+1024-j*12; u32 one=0x3f803f80u; const lch* tp=tile+qq*TSTR2+(w4*64+g*16+pq*4)*2;
    int go= ln<32? W_RG+(ln>>2)*8192+(tb*4+(ln&3))*8 : W_ST+(tb*8+(ln&7))*8; lch* kw=sm+O_RV+rl*2; const lch* kr=sm+(ln<32? O_RV+(ln&3)*4 : O_MD+(ln&7)*4); PIN(kw); PIN(kr);
    int xr=(32*(tb>>2)+8*g+(li&7))*(D_*2)+288*(lb&3)+32*w4+(ln&8)*2, xr2= w4==0? xr+256:OOR;
    rsrc_t rx=RSRC(x,L_*D_*2); u64 gm=((1ull<<(4*NRG))-1ull)|0xff00000000ull; u32 um=~UMASK, km=0x80008000u; asm volatile("" : "+s"(gm),"+s"(um),"+s"(km),"+s"(ws));
    #define XN 0
    asm volatile("s_waitcnt vmcnt(3+" STR(XN) ")" : "+v"(wa),"+v"(wb),"+v"(wc),"+v"(Tr),"+v"(tw),"+v"(tw2),"+v"(tp),"+v"(one),"+v"(go),"+v"(xr),"+v"(xr2) :: "memory");
    u32 wq[9]={wa[0],wa[1],wa[2],wa[3],wb[0],wb[1],wb[2],wb[3],wc}; float sw;
    { float a[3];
      #pragma unroll
      for(int i=0;i<9;i++) a[i%3]=D2(wq[i],one,i<3? 0.f:a[i%3]);
      sw=add16x(sum16((a[0]+a[1])+a[2])); }
    GETEP u32 tg=(u32)ep+1u; Tr=((upat(tg,w4)>>Tr)&0xffffu)*0x10001u; u32 tgv=tg;
    float z_[9]={0,0,0,0,0,0,0,0,0};
    asm volatile("s_waitcnt vmcnt(" STR(XN) ")" : "+v"(xa),"+v"(xb_),"+v"(xc),"+v"(Tr),"+v"(sw),"+v"(tgv),"+v"(z_[0]),"+v"(z_[1]),"+v"(z_[2]),"+v"(z_[3]),"+v"(z_[4]),"+v"(z_[5]) :: "memory");
    u32 v[9]={xa[0],xa[1],xa[2],xa[3],xb_[0],xb_[1],xb_[2],xb_[3],xc};
    { float xf[18], s1, s2, s3;
      #pragma unroll
      for(int i=0;i<9;i++){ z_[i&1]=D2(v[i],one,z_[i&1]); z_[2+(i&1)]=D2(v[i],v[i],z_[2+(i&1)]); z_[4+(i&1)]=D2(v[i],wq[i],z_[4+(i&1)]); }
      s1=z_[0]+z_[1]; s2=z_[2]+z_[3]; s3=z_[4]+z_[5];
      SCHED();
      #define RSTEP(C) s1+=dppf<C>(s1); s2+=dppf<C>(s2); s3+=dppf<C>(s3); SCHED();
      RSTEP(0xB1) RSTEP(0x4E) RSTEP(0x141) RSTEP(0x140)
      s1=add16x(s1)*(1.f/576.f); s2=add16x(s2)*(1.f/576.f); s3=add16x(s3);
      float mm=s1*s1, var=s2-mm;
      if(__builtin_expect(BALLOT(!(mm<=64.f*var))!=0ull,0)){
        float q=0.f;
        #pragma unroll
        for(int i=0;i<9;i++){ u32 t=v[i]; PIN(t); float d0=blo(t)-s1, d1=bhi(t)-s1; q=__builtin_fmaf(d0,d0,q); q=__builtin_fmaf(d1,d1,q); }
        var=add16x(sum16(q))*(1.f/576.f); }
      #define UNP(i) PIN(v[i]); xf[2*(i)]=blo(v[i]); xf[2*(i)+1]=bhi(v[i]); PIN(xf[2*(i)]); PIN(xf[2*(i)+1]); SCHED();
      u32 vb=pk(var,s1); UNP(0)
      float den=blo(vb)+1e-5f, m=bhi(vb); UNP(1)
      vb=pk(den,den); s3=__builtin_fmaf(-m,sw,s3); UNP(2)
      den=__builtin_amdgcn_sqrtf(bhi(vb)); UNP(3)
      u32 md=pk(m,den); UNP(4)
      float ri=rcpf_(bhi(md)); UNP(5)
      s3*=ri; float rs_=ri*0.0625f; UNP(6)
      u32 u=pk(s3,s3); float cs_=-m*rs_; UNP(7)
      { u32 sg=(u32)((int)u>>31); PIN(sg); u^=sg|km; } UNP(8)
      asm volatile("s_mov_b64 exec, 0x1\n\ts_mov_b32 exec_hi, 1\n\tds_write_b16 %0, %1\n\tv_add_u32 %0, %0, %0\n\tds_write_b32 %0, %2 offset:%3\n\ts_mov_b32 exec_hi, 0\n\tds_add_u32 %4, %5\n\ts_mov_b64 exec, -1" : "+v"(kw) : "v"(u),"v"(md),"i"(O_MD-2*O_RV),"v"(C_I*4),"v"(1) : "memory");
      if(w4==3){ gwait<0>(sm,C_I,4); asm volatile("ds_read_b32 v118, %4\n\tv_mov_b32 v119, %1\n\ts_mov_b64 exec, %3\n\ts_waitcnt lgkmcnt(0)\n\tglobal_store_dwordx2 %0, v[118:119], %2 sc1\n\ts_mov_b64 exec, -1" :: "v"(go),"v"(tgv),"s"(ws),"s"(gm),"v"(kr) : "memory"); }
      SCHED();
      u32 h[9];
      #define UH(i) h[i]=(pkz(__builtin_fmaf(xf[2*(i)],rs_,cs_),__builtin_fmaf(xf[2*(i)+1],rs_,cs_))&um)|Tr;
      UH(0) UH(1) UH(2) UH(3) ST(u32x4,tw)=u32x4{h[0],h[1],h[2],h[3]}; UH(4) UH(5) UH(6) UH(7) ST(u32x4,tw+512)=u32x4{h[4],h[5],h[6],h[7]};
      UH(8) ST(u32,tw2)=h[8]; }
    gsync(sm,C_L,4);
    { u32x4 uq[3];
      #pragma unroll
      for(int z=0;z<3;z++) uq[z]=BC(u32x4,trfrag(tp+z*512,tp+z*512+4*TSTR2));
      asm volatile("global_store_dwordx4 %0, %1, %2 sc1" :: "v"(uo),"v"(uq[0]),"s"(ws) : "memory");
      asm volatile("global_store_dwordx4 %0, %1, %2 sc1" :: "v"(uo1),"v"(uq[1]),"s"(ws) : "memory");
      asm volatile("s_cmp_lg_u32 %3, 0\n\ts_cbranch_scc1 1f\n\tglobal_store_dwordx4 %0, %1, %2 sc1\n1:\n\ts_nop 1" :: "v"(uo2),"v"(uq[2]),"s"(ws),"s"(w4),"v"(uq[0]),"v"(uq[1]) : "memory","scc");
      xr_=xr; xr2_=xr2;
      #define XQ { rsrc_t rx_=RSRC(x,L_*D_*2); xq[0]=BLD128(rx_,xr_,0,XAUX); xq[1]=BLD128(rx_,xr_+128,0,XAUX); xq[2]=BLD128(rx_,xr2_,0,XAUX); }
    }
    if(lb>=2&&lb<8){
      const u16* wm=(lb&1)? wu:wg; int co=(3-(int)((u64)wm>>7)-xg)&7, wi=((lb>>1)-1)*4+w4, r_=576-12*PFN+wi+12*j;
      int po= j<PFN? r_*(F_*2)+(9*xg+co)*128+(ln>>5)*1024 : OOR;
      if(ln>=32&&co!=0){ int c8=j&7; po= PX? (575-wi*4-(j>>3))*(F_*2)+(9*xg+c8+(c8>=co))*128 : OOR; }
      asm volatile("buffer_load_dword v120, %0, %1, 0 offen" PPOL :: "v"(po),"s"(mkrs(wm,D_*F_*2)) : "memory");
      if(PX>1){ int c8=ln&7; asm volatile("buffer_load_dword v120, %0, %1, 0 offen" PPOL :: "v"(ln<PX? (527-wi-12*(ln>>3))*(F_*2)+(9*xg+c8+(c8>=co))*128 : OOR),"s"(mkrs(wm,D_*F_*2)) : "memory"); } }
    if(lb>=8&&lb<14) garrive(sm,C_D);
  }
  FP fp; int trow;
  { LANE(ln); int g=ln>>4, li=ln&15; u64 tag=ep+1ull;
    const char* sp_=ws+W_ST+(32*(tb>>2)+8*g+(li&7))*8; u64 fs, ff; const char* fp_=ws+W_FL+(144*(lb&3)+16*w4+64*(g<2||w4==0? (g<3? g:2):0)+li)*8;
    if(lb==CTLB||lb>=14){
      u32 M=(u32)tag, rank,key; u32x4 rx=mkrs(x,L_*D_*2);
      int j=ln&31, rr=2*w4+(ln>>5), o16=2047*1152+j*16, o4=2047*1152+j*4, so=W_ST+2047*8, sh=(ln>>5)*16, m9=-1152, r0=2*w4, r1=2*w4+1;
      lch *xw=sm+O_TILE+XAD(rr,j*16), *xw1=sm+O_TILE+XAD(rr,j*16+512), *xw2=sm+O_TILE+XAD(rr,j*4+1024), *rw=sm+O_RC+rr*4; u32 zero=0, one=1;
      PIN(o16); PIN(o4); PIN(so); PIN(sh); PIN(xw); PIN(xw1); PIN(xw2); PIN(rw); PIN(zero); PIN(one); asm volatile("" : "+s"(rx),"+s"(m9),"+s"(r0),"+s"(r1));
      top8(sm,w4,ln,M,ws,rank,key);
      {
        u64 q; u32 a,b;
        asm volatile("v_cmp_eq_u32 vcc, %[r0], %[rk]\n\tv_cmp_eq_u32 %[q], %[r1], %[rk]\n\ts_ff1_i32_b64 %[a], vcc\n\ts_ff1_i32_b64 %[b], %[q]\n\tv_readlane_b32 %[a], %[key], %[a]\n\tv_readlane_b32 %[b], %[key], %[b]\n\ts_pack_ll_b32_b16 %[a], %[a], %[b]\n\t"
          "v_bfe_u32 %[sh], %[a], %[sh], 11\n\tv_mad_i32_i24 %[o16], %[sh], %[m9], %[o16]\n\tv_mad_i32_i24 %[o4], %[sh], %[m9], %[o4]\n\t"
          "buffer_load_dwordx4 v[112:115], %[o16], %[rx], 0 offen\n\tv_mad_i32_i24 %[so], %[sh], -8, %[so]\n\tbuffer_load_dwordx4 v[116:119], %[o16], %[rx], 0 offen offset:512\n\tbuffer_load_dword v120, %[o4], %[rx], 0 offen offset:1024\n\tglobal_load_dwordx2 v[122:123], %[so], %[ws] sc1\n\t"
          "s_mov_b64 exec, 1\n\tds_add_u32 %[z], %[one] offset:%[cd]\n\ts_mov_b64 exec, -1"
          : [q]"=&s"(q),[a]"=&s"(a),[b]"=&s"(b),[sh]"+v"(sh),[o16]"+v"(o16),[o4]"+v"(o4),[so]"+v"(so) : [r0]"s"(r0),[r1]"s"(r1),[rk]"v"(rank),[key]"v"(key),[m9]"s"(m9),[rx]"s"(rx),[ws]"s"(ws),[z]"v"(zero),[one]"v"(one),[cd]"i"(C_D*4) : PCLOB);
        if(lb==CTLB&&rank<8&&!(ln&1)) GST(ws+W_IDX+xg*1024+w4*128+rank*8,(tag<<16)|(~key&2047u));
        if(w4==0){ if(rank<8) ST(u16,sm+O_IDX+(ln&1)*16+rank*2)=(u16)((ln&1)? key>>16 : ~key&2047u); garrive(sm,C_IX); }
        u64 bad; asm volatile("s_waitcnt vmcnt(2)\n\tds_write_b128 %2, v[112:115]\n\ts_waitcnt vmcnt(0)\n\tds_write_b128 %3, v[116:119]\n\tv_cmp_ne_u32 %0, %1, v123\n\tds_write_b32 %4, v120" : "=s"(bad) : "s"(M),"v"(xw),"v"(xw1),"v"(xw2) : "memory");
        if(__builtin_expect(bad!=0ull,0)){ u32 f1=(u32)poll1<32>(ws+so,tag<<32); asm volatile("v_mov_b32 v122, %0" :: "v"(f1) : "memory"); }
        asm volatile("ds_write_b32 %0, v122\n\ts_mov_b64 exec, 1\n\tds_add_u32 %1, %2 offset:%3\n\ts_mov_b64 exec, -1" :: "v"(rw),"v"(zero),"v"(one),"i"(C_X*4) : "memory");
      }
    }
    PRIO(0);
    if(lb<14) gwait<8>(sm,C_U,8); else gwait<HSL>(sm,C_H,HN);
    fs=GLD(sp_); ff=GLD(fp_); if(XE<2) XQ
    { rsrc_t rd=RSRC(Dsk,D_*2); int do_=288*(lb&3)+32*w4+(ln&8)*2; dq[0]=BLD128(rd,do_,0,0); dq[1]=BLD128(rd,do_+128,0,0); dq[2]=BLD128(rd,do_+256,0,0); }
    final_adr(sm,fp,w4); trow=32*(tb>>2)+8*g+(li&7); PIN(trow);
    {
      if(BALLOT((u32)(fs>>32)!=(u32)tag)!=0ull) fs=poll1<32>(sp_,tag<<32);
      float ri=rcpf_(bhi((u32)fs)); rc=f32x2{ri,-blo((u32)fs)*ri}; }
    asm volatile("" : "+v"(rc),"+v"(xq[0]),"+v"(xq[1]),"+v"(xq[2]));
    bool ix= w4==3&&lb<14&&lb!=CTLB; u64 f; const char* p=ws+W_IDX+(xg*8+(lb&3))*128+(ln&7)*8; if(ix) f=GLD(p);
    final_blk(fp,0,xq,dq,rc); if(FS>=2) final_blk(fp,1,xq,dq,rc); if(FS>=3&&w4==0) final_blk(fp,2,xq,dq,rc);
    if(BALLOT(ff!=tag)!=0ull) poll1<0>(fp_,tag);
    if(ix){ if(BALLOT(((f^(tag<<16))>>16)!=0ull)!=0ull) f=poll1<16>(p,tag<<16);
      if(ln<8) ST(u16,sm+O_IDX+ln*2)=(u16)f; garrive(sm,C_IX); }
  }
  final_post(sm,fp,w4,nch,xr_,trow,xq,dq,rc,out,ws);
  }
}

extern "C" void kernel_launch(void* const* d_in, const int* in_sizes, int n_in, void* d_out, int out_size, void* d_ws, size_t ws_size, hipStream_t stream) {
  const u16** I=(const u16**)d_in;
  KP kp; kp.Are=I[1]; kp.Aim=I[2]; kp.Bre=I[3]; kp.Bim=I[4]; kp.Cre=I[5]; kp.Cim=I[6]; kp.ldt=I[7]; kp.pad=I[7];
  kA<<<256,1024,0,stream>>>(I[0],(char*)d_ws+WSOFF,I[10],I[8],I[11],I[12],I[13],(u16*)d_out,kp);
}
```

```cpp
#include <hip/hip_runtime.h>
#include <hip/hip_bf16.h>
#include <stdint.h>
#if defined(__HIP_DEVICE_COMPILE__)
#define NOPK __attribute__((target("no-packed-fp32-ops")))
#else
#define NOPK
#endif
#define DEV __device__ NOPK __forceinline__
typedef unsigned short u16;
typedef unsigned int u32;
typedef unsigned long long u64;
typedef short bf16x8 __attribute__((ext_vector_type(8)));
typedef _Float16 h8 __attribute__((ext_vector_type(8)));
typedef _Float16 h2 __attribute__((ext_vector_type(2)));
typedef float f32x4 __attribute__((ext_vector_type(4)));
typedef float f32x2 __attribute__((ext_vector_type(2)));
typedef u32 u32x4 __attribute__((ext_vector_type(4)));
typedef u32 u32x2 __attribute__((ext_vector_type(2)));
typedef __bf16 bf2 __attribute__((ext_vector_type(2)));
typedef short v4s __attribute__((ext_vector_type(4)));
typedef __amdgpu_buffer_rsrc_t rsrc_t;
#define GAS __attribute__((address_space(1)))
#define LAS __attribute__((address_space(3)))
typedef LAS char lch;
#define L_ 2048
#define D_ 576
#define F_ 4608
#define BC(T,v) __builtin_bit_cast(T,v)
#define TID ((int)__builtin_amdgcn_workitem_id_x())
#define BID ((int)__builtin_amdgcn_workgroup_id_x())
#define RFL(x) __builtin_amdgcn_readfirstlane(x)
#define RLX_AGENT __ATOMIC_RELAXED, __HIP_MEMORY_SCOPE_AGENT
#define LD(T,p) (*(const LAS T*)(p))
#define ST(T,p) (*(LAS T*)(p))
#define SPINCAP (1<<17)
#define DRAIN() asm volatile("s_waitcnt vmcnt(0)" ::: "memory")
#define GLD(p) __hip_atomic_load((const GAS u64*)(p),RLX_AGENT)
#define GST(p,v) __hip_atomic_store((GAS u64*)(p),(u64)(v),RLX_AGENT)
#define BALLOT(c) __builtin_amdgcn_ballot_w64(c)
#define SC1 16
#define LANE(l) int l; { int t2_=TID; asm volatile("" : "+v"(t2_)); l=t2_&63; }
#define SCHED() __builtin_amdgcn_sched_barrier(0)
#define RSRC(p,n) __builtin_amdgcn_make_buffer_rsrc((void*)(p),0,(n),0x00020000)
#define BLD128 __builtin_amdgcn_raw_buffer_load_b128
#define BLD32 __builtin_amdgcn_raw_buffer_load_b32
#define BST128 __builtin_amdgcn_raw_buffer_store_b128
#define PRIO(n) __builtin_amdgcn_s_setprio(n)

DEV float b2f(u32 v){ return BC(float,v<<16); }
DEV float blo(u32 v){ return BC(float,v<<16); }
DEV float bhi(u32 v){ return BC(float,v&0xffff0000u); }
DEV u32 pk(float a,float b){ f32x2 v={a,b}; bf2 r=__builtin_convertvector(v,bf2); return BC(u32,r); }
DEV u32 pkh(float a,float b){ f32x2 v={a,b}; h2 r=__builtin_convertvector(v,h2); return BC(u32,r); }
DEV float rb(float f){ return BC(float,pk(f,f)<<16); }
DEV float rcpf_(float x){ return __builtin_amdgcn_rcpf(x); }
DEV float expf_(float x){
  const float L2E=1.4426950216293335f, L2El=1.9259629911266175e-8f; float h=x*L2E; float l=__builtin_fmaf(x,L2E,-h)+x*L2El;
  float e=__builtin_amdgcn_exp2f(h); return __builtin_fmaf(e*l,0.6931471805599453f,e); }
DEV float sigm_f(float x){ return rcpf_(1.f+__builtin_amdgcn_exp2f(-x*1.4426950408889634f)); }
DEV float silu_f(float x){ return x*sigm_f(x); }
DEV u32 umax_(u32 a,u32 b){ return a>b?a:b; }
DEV u32 umin_(u32 a,u32 b){ return a<b?a:b; }

#define WSOFF 0
#define G2 7
#define PSL 8
#define NA 5
#define NB 11
#define IVS "global_store_dwordx4 v23, v[124:127], s[66:67] sc1\n\t"
#define EB0 "s_barrier\n\t"
#define EB1
#define IV0 IVS
#define IV1
#define GD 3
#define HG -1
#define UD_ -1
#define UDB 64
#define PA -1
#define AD 14
#define BD 0
#define PX 0
#define XAUX 0
#define OAUX 0
#define HSL 4
#define HN 4
#define PPOL ""
#define WPOL " nt"
#define QPOL " nt"
#define PK 0x112
#define PS 0x030
#define RD 0
#define PD 0
#define P12 64
#define ESL 4
#define PRN 1
#define CHN 8
#define CSL 2
#define FS 3
#define PWB (w4>>1)*(9*4096)+(w4&1)*128
#define PLO (li>>1)*(18*4096)+(li&1)*64
#define YSH 3
#define PRE1 6
#define PRE3 4
#define PRE2 5
#define W1 5
#define W3 3
#define W2_ 4
#define W1B 20
#define W2B 12
#define LAST1 3
#define RGATE 4
#define UGATE 7
#define R2D 12
#define PFN 32
#define CPF 8
#define NRG 8
#define W_EPOCH 0
#define W_RG 16384
#define W_ST (W_RG+32*8192)
#define W_U (W_ST+32768)
#define UST (D_*16+128)
#define YST (L_*2+128)
#define TL 1
#define GST_ ((D_*16<<TL)+128)
#define W_Y (W_U+256*UST)
#define W_PART (W_Y+D_*YST)
#define W_GU (W_PART+8*9*16*256)
#define W_FL 9216
#define W_IDX 1024
#define CTLB 0
struct KP { const u16 *Are,*Aim,*Bre,*Bim,*Cre,*Cim,*ldt,*pad; };

template<int CTRL> DEV float dppf(float v){ return BC(float,__builtin_amdgcn_update_dpp(0u,BC(u32,v),CTRL,0xf,0xf,false)); }
template<int CTRL> DEV u32 dppu(u32 v){ return (u32)__builtin_amdgcn_update_dpp(0u,v,CTRL,0xf,0xf,false); }
DEV float sum16(float v){ v+=dppf<0xB1>(v); v+=dppf<0x4E>(v); v+=dppf<0x141>(v); v+=dppf<0x140>(v); return v; }
DEV float add16x(float v){ auto r=__builtin_amdgcn_permlane16_swap(BC(u32,v),BC(u32,v),false,false); u32 a=r[0], b=r[1]; return BC(float,a)+BC(float,b); }
DEV u32 wmax(u32 v){
  v=umax_(v,(u32)__builtin_amdgcn_update_dpp(0u,v,0x111,0xf,0xf,false));
  v=umax_(v,(u32)__builtin_amdgcn_update_dpp(0u,v,0x112,0xf,0xf,false));
  v=umax_(v,(u32)__builtin_amdgcn_update_dpp(0u,v,0x114,0xf,0xf,false));
  v=umax_(v,(u32)__builtin_amdgcn_update_dpp(0u,v,0x118,0xf,0xf,false));
  v=umax_(v,(u32)__builtin_amdgcn_update_dpp(0u,v,0x142,0xa,0xf,false));
  v=umax_(v,(u32)__builtin_amdgcn_update_dpp(0u,v,0x143,0xc,0xf,false));
  return (u32)__builtin_amdgcn_readlane((int)v,63); }

DEV void garrive(lch* sm,int c){ u32 ad=(u32)(size_t)sm+c*4, one=1;
  asm volatile("s_mov_b64 exec, 1\n\tds_add_u32 %0, %1\n\ts_mov_b64 exec, -1" :: "v"(ad),"v"(one) : "memory"); }
template<int SLP> DEV void gwait(lch* sm,int c,u32 target){ u32 ad=(u32)(size_t)sm+c*4, tv, cnt, sv; target=RFL(target);
  asm volatile("s_mov_b32 %1, 0x20000\n"
    "1:\n\tds_read_b32 %0, %3\n\ts_waitcnt lgkmcnt(0)\n\tv_readfirstlane_b32 %2, %0\n\ts_cmp_ge_u32 %2, %4\n\ts_cbranch_scc1 2f\n\ts_sleep %5\n\ts_sub_u32 %1, %1, 1\n\ts_cbranch_scc0 1b\n"
    "2:" : "=&v"(tv),"=&s"(cnt),"=&s"(sv) : "v"(ad),"s"(target),"i"(SLP) : "memory","scc"); }
template<int ST_,class T> DEV void gwait4(int c,u32 target,const lch* p,T& a,T& b,T& d,T& e){ u32 tv,cnt,sv;
  asm volatile("s_mov_b32 %1, 0x20000\n1:\n\tds_read_b32 %0, %7\n\tds_read_b128 %3, %8\n\tds_read_b128 %4, %8 offset:%10\n\tds_read_b128 %5, %8 offset:%11\n\tds_read_b128 %6, %8 offset:%12\n\ts_waitcnt lgkmcnt(4)\n\t"
    "v_readfirstlane_b32 %2, %0\n\ts_cmp_ge_u32 %2, %9\n\ts_cbranch_scc1 2f\n\ts_sleep 1\n\ts_sub_u32 %1, %1, 1\n\ts_cbranch_scc0 1b\n2:\n\ts_waitcnt lgkmcnt(0)"
    : "=&v"(tv),"=&s"(cnt),"=&s"(sv),"=&v"(a),"=&v"(b),"=&v"(d),"=&v"(e) : "v"(c*4),"v"(p),"i"(target),"i"(ST_),"i"(2*ST_),"i"(3*ST_) : "memory","scc"); }
DEV void gsync(lch* sm,int c,u32 target){ garrive(sm,c); gwait<0>(sm,c,target); }
template<int SH> DEV u64 poll1(const char* p,u64 want){ u64 f=GLD(p);
  _Pragma("unroll 1") for(int sp=0;sp<SPINCAP;sp++){ bool bad=((f^want)>>SH)!=0ull; if(BALLOT(bad)==0ull) break; __builtin_amdgcn_s_sleep(1); if(bad) f=GLD(p); }
  __builtin_amdgcn_fence(__ATOMIC_ACQUIRE,"wavefront"); return f; }
#define STR_(x) #x
#define STR(x) STR_(x)
DEV u32x4 mkrs(const void* p,u32 n){ u64 a=(u64)p; return u32x4{(u32)RFL((int)(u32)a),(u32)RFL((int)((u32)(a>>32)&0xffffu)),n,0x00020000u}; }
#define VMW(n) asm volatile("s_waitcnt vmcnt(%0)" :: "i"(n) : "memory")
#define VW(n) "s_waitcnt vmcnt(" STR(n) ")\n\t"
#define YSLP 1
#define GSLP 1
#define QSLP 1
#define PSLP 3
#define POLLASM(LOADS,CHECK,K) "s_mov_b32 %[cnt], 0x20000\n1:\n\ts_waitcnt vmcnt(0)\n\t" CHECK "s_cbranch_vccz 9f\n\ts_sleep " STR(K) "\n\ts_mov_b64 exec, vcc\n\t" LOADS \
  "s_mov_b64 exec, -1\n\ts_sub_u32 %[cnt], %[cnt], 1\n\ts_cbranch_scc0 1b\n9:\n\t"
#define LD_DUP2 "buffer_load_dwordx4 v[112:115], %[of], %[rs], 0 offen sc1\n\tbuffer_load_dwordx4 v[116:119], %[of], %[rs], %[so] offen sc1\n\t"
#define MX2(a,b) "v_max3_u32 %[t0], %[t0], v" #a ", v" #b "\n\t"
#define CHK_I0 "v_max_u32 %[t0], v112, v113\n\t" MX2(114,115)
#define CHK_IE "v_cmp_eq_u32 vcc, -1, %[t0]\n\t"
#define PCLOB "memory","vcc","scc"
#define UMASK 0x40074007u
DEV u32 upat(u32 tag,int k){ return ((((tag*0xea959c21u)>>(8*k))&255u)*0x04000801u)&UMASK; }
DEV u32 pkz(float a,float b){ return BC(u32,__builtin_amdgcn_cvt_pkrtz(a,b)); }
#define INV 0xffffffffu
DEV u32 nv(u32 v){ return umin_(v,0xfffeffffu); }
DEV u32x4 nv4(u32x4 v){ return u32x4{nv(v[0]),nv(v[1]),nv(v[2]),nv(v[3])}; }
#define OOR 0x40000000

#define CHSZ 36864
#define O_CH 512
#define O_CND 256
#define SLSZ 38976
#define O_R2 (O_CH+2*SLSZ)
#define O_X2 (O_R2+CHSZ)
#define O_TILE (O_X2+CHSZ)
#define XSTR 1168
#define HSTR 608
#define HT 96
#define XAD(r,b) ((r)*HSTR+(b)+((b)>=576? 8*HSTR-576:0))
#define XS1 1
#define XG1 1
#define TSTR2 1216
#define O_CAND (O_TILE+9728+1024)
#define LDS_TOTAL (O_CAND+128)
#define C_G 0
#define C_U 8
#define C_L 4
#define C_D 5
#define C_X 12
#define C_P 13
#define C_H 14
#define C_I 17
#define C_E 18
#define C_IX 19
#define C_PR 21
#define C_W 22
#define C_WIN (lb>=14? C_W:C_D)
#define O_IDX 96
#define O_EXP 144
#define O_RMX 192
#define O_RV 128
#define O_MD 400
#define O_RC (O_CAND+64)

struct C2 { float r,i; };
DEV C2 cmul(C2 a,C2 b){ C2 c; c.r=__builtin_fmaf(a.r,b.r,-(a.i*b.i)); c.i=__builtin_fmaf(a.r,b.i,a.i*b.r); return c; }
DEV h8 ldA(const lch* p){ u32x2 a=*(const volatile LAS u32x2*)p, b=*(const volatile LAS u32x2*)(p+8); return BC(h8,(u32x4{a[0],a[1],b[0],b[1]})); }
DEV bf16x8 trfrag(const lch* p0,const lch* p1){
  u32x2 b0=BC(u32x2,__builtin_amdgcn_ds_read_tr16_b64_v4i16((LAS v4s*)p0)), b1=BC(u32x2,__builtin_amdgcn_ds_read_tr16_b64_v4i16((LAS v4s*)p1));
  return BC(bf16x8,(u32x4{b0[0],b0[1],b1[0],b1[1]})); }

struct FP { u32 gg[24]; float bs[24]; int yo,y3; lch* wp; const lch* tp; };
#define D2(a,b,c) __builtin_amdgcn_fdot2_f32_bf16(BC(bf2,a),BC(bf2,b),c,false)
#define ZCH 64
#define YZ 1280
#define YG1 320
#define YG2 672
DEV void final_blk(FP& f,int z,const u32x4* xq,const u32x4* dq,f32x2 rc){
  float ea=rc[0]*-1.4426950408889634f, eb=rc[1]*-1.4426950408889634f;
  #pragma unroll
  for(int e=0;e<4;e++){ u32 xw=xq[z][e], dw=dq[z][e]; int i=8*z+2*e; float a=blo(xw), b=bhi(xw);
    float pa=__builtin_fmaf(a,rc[0],rc[1]), pb=__builtin_fmaf(b,rc[0],rc[1]);
    float ga=pa*rcpf_(1.f+__builtin_amdgcn_exp2f(__builtin_fmaf(a,ea,eb))), gb=pb*rcpf_(1.f+__builtin_amdgcn_exp2f(__builtin_fmaf(b,ea,eb)));
    f.gg[i]=pk(ga,0.f); f.gg[i+1]=pk(0.f,gb);
    f.bs[i]=__builtin_fmaf(pa*blo(dw),ga,a); f.bs[i+1]=__builtin_fmaf(pb*bhi(dw),gb,b);
    asm volatile("" : "+v"(f.gg[i]),"+v"(f.gg[i+1]),"+v"(f.bs[i]),"+v"(f.bs[i+1])); }
}
DEV void final_adr(lch* sm,FP& f,int w4){
  LANE(ln); int bid=BID, xg=bid&7, lb=bid>>3, g=ln>>4, li=ln&15, qq=li>>2, pq=li&3;
  f.yo=(144*(lb&3)+16*w4+(ln>>2))*YST+(32*(8*xg+(lb>>2))+8*(ln&3))*2; f.y3= w4==0? f.yo+2*ZCH*YST:OOR;
  lch* fb=sm+O_CH+w4*(3*YZ); f.wp=fb+(ln&1)*YG1+(ln&2)*(YG2/2)+(ln>>2)*16; f.tp=fb+(g&1)*YG1+(g&2)*(YG2/2)+(8*(pq>>1)+qq)*16+(pq&1)*8;
  asm volatile("" : "+v"(f.yo),"+v"(f.y3),"+v"(f.wp),"+v"(f.tp));
}
DEV void final_post(lch* sm,FP& f,int w4,int nch,int ob,int trow,const u32x4* xq,const u32x4* dq,f32x2 rc,u16* __restrict__ out,char* ws){
  int lb=BID>>3;
  u32x4 rs=mkrs(ws+W_Y,D_*YST); rsrc_t ro=RSRC(out,L_*D_*2);
  gwait<ESL>(sm,C_E,4*nch);
  if(AD&&lb<8) __builtin_amdgcn_s_sleep(AD);
  if(BD&&lb>=14) __builtin_amdgcn_s_sleep(BD);
  if(lb>=8&&lb<14) gwait<CSL>(sm,C_H,CHN);
  PRIO(3);
  #define LD_Y "buffer_load_dwordx4 v[112:115], %[of], %[rs], 0 offen sc1\n\tbuffer_load_dwordx4 v[116:119], %[of], %[rs], %[s1] offen sc1\n\tbuffer_load_dwordx4 v[120:123], %[o3], %[rs], 0 offen sc1\n\t"
  asm volatile(LD_Y : "+v"(rc),"+v"(trow) : [of]"v"(f.yo),[rs]"s"(rs),[s1]"s"(ZCH*YST),[o3]"v"(f.y3) : "memory");
  garrive(sm,C_PR);
  if(FS<2) final_blk(f,1,xq,dq,rc);
  if(FS<3&&w4==0) final_blk(f,2,xq,dq,rc);
  { gwait<2>(sm,C_IX,1); u32x4 iq=LD(u32x4,sm+O_IDX); u32 tt=(u32)trow*0x10001u; bool routed=false;
    #pragma unroll
    for(int q=0;q<4;q++){ u32 z=iq[q]^tt; routed|=((z&0xffffu)==0u)|((z>>16)==0u); }
    ob=routed? OOR:ob; }
  { u32 t0,cnt;
    asm volatile(POLLASM(LD_Y,"ds_write_b128 %[wp], v[112:115]\n\tds_write_b128 %[wp], v[116:119] offset:" STR(YZ) "\n\ts_cmp_lg_u32 %[w4], 0\n\ts_cbranch_scc1 8f\n\tds_write_b128 %[wp], v[120:123] offset:2*" STR(YZ) "\n8:\n\t"
      CHK_I0 MX2(116,117) MX2(118,119) MX2(120,121) MX2(122,123) CHK_IE,YSLP)
      : [t0]"=&v"(t0),[cnt]"=&s"(cnt),"+v"(ob) : [of]"v"(f.yo),[rs]"s"(rs),[s1]"s"(ZCH*YST),[o3]"v"(f.y3),[wp]"v"(f.wp),[w4]"s"(w4),"s"(ro) : PCLOB); }
  u32x4 r[3];
  #pragma unroll
  for(int z=0;z<3;z++) r[z]=BC(u32x4,trfrag(f.tp+z*YZ,f.tp+z*YZ+64));
  #pragma unroll
  for(int z=0;z<3;z++) if(z<2||w4==0){ u32x4 ov;
    #pragma unroll
    for(int e=0;e<4;e++){ u32 rw=r[z][e]; int i=8*z+2*e;
      ov[e]=pk(D2(rw,f.gg[i],f.bs[i]),D2(rw,f.gg[i+1],f.bs[i+1]));
    }
    BST128(ov,ro,ob+z*(2*ZCH),0,OAUX); SCHED(); }
}

#define TROW 544
#define O_TB 4640
#define O_DE 35664
#define O_DO (O_DE+1232)
#define O_R2K (O_DO+1152)
#define O_R3K (O_R2K+512)
#define O_DUM (O_R3K+320)
#define O_RED O_TB
#define MH(a,b,c) __builtin_amdgcn_mfma_f32_16x16x32_f16(a,b,c,0,0,0)
#define PIN(x) asm volatile("" : "+v"(x))
DEV h8 ld4w(const lch* p){ return BC(h8,(u32x4{LD(u32,p),LD(u32,p+4),LD(u32,p+8),LD(u32,p+12)})); }
DEV u32 __umul24_(u32 a,u32 b){ u32 r; asm("v_mul_u32_u24 %0, %1, %2" : "=v"(r) : "s"(b),"v"(a)); return r; }
DEV u32x4 band(u32 mask,int g,u32 val){ u32 by=__builtin_amdgcn_ubfe(mask,8*g,8), X=by|(by<<15), k=0x10001u; u32x4 r; asm volatile("" : "+s"(k),"+s"(val));
  #pragma unroll
  for(int d=0;d<4;d++) r[d]=__umul24_((X>>(2*d))&k,val);
  return r; }
DEV h8 zpack(f32x4 e,f32x4 o){ return BC(h8,(u32x4{pkh(e[0],o[0]),pkh(e[1],o[1]),pkh(e[2],o[2]),pkh(e[3],o[3])})); }
struct C3 { float r,i,n; };
DEV C3 c3(C2 a){ C3 c; c.r=a.r; c.i=a.i; PIN(c.i); c.n=-c.i; PIN(c.n); return c; }
DEV C2 cm(C2 a,C3 b){ C2 c; c.r=__builtin_fmaf(a.r,b.r,a.i*b.n); c.i=__builtin_fmaf(a.r,b.i,a.i*b.r); return c; }
DEV u32 ph(C2 a){ return pkh(a.r,a.i); }
#define W2(p,a,b) asm volatile("ds_write2_b32 %0, %1, %2 offset1:136" :: "v"(p),"v"(a),"v"(b) : "memory")
struct CP { const lch *x0,*x1,*bd; lch *p0,*p1,*p2,*wp; u32x4 Be,Bo,AE; int yo; };
template<int ROLE> DEV void lanes(CP& c,lch* cb,int ln){ int m=ln&15, g=ln>>4;
  const lch* ad=cb+((m&1)? O_DO+2*527 : O_DE+2*536)+2*(8*g-m);
  const lch* bd=cb+2*(256+16*m+8*g); lch* dum=cb+O_DUM;
  if(ROLE==0){ int l0=128*(g>>1)+8*m+4*(g&1); c.p0=cb+O_DE+(549-l0)*2; PIN(c.p0); c.p1=c.p0+6; PIN(c.p1); c.p2=c.p0+(O_DO-O_DE-18); c.x0=ad-640; c.bd=bd-64; c.x1=cb+O_R3K+2*(126-2*m+8*g)-192; PIN(c.x1);
    c.Be=band(0x1ffu<<(m+4),g,0x3c00u); PIN(c.Be); }
  if(ROLE==1){ lch* r=cb+O_R2K+(192-64*(g-2)-2*m)*2; c.p0= g<2? dum : r-66; c.p1= ln==0? cb+O_DE+(552-256)*2 : (ln<=32? dum : r); c.p2=cb+O_DE+(544-256-8*ln)*2; c.x0=ad-960; c.bd=bd-448; PIN(c.p0); PIN(c.p1); PIN(c.p2); }
  if(ROLE==2){ int i0=16*m+4*g; c.p0= m<8? cb+O_R3K+(125-i0)*2 : dum+2; c.p1= ln==0? cb+O_R2K+128 : (ln==8? cb+O_R3K : c.p0+6); c.x0=ad-960; c.bd=bd-448; PIN(c.p0); PIN(c.p1); }
  if(ROLE==3){ c.x0=cb+O_R2K+2*(188-4*m+8*g)-256; c.bd=cb+2*(248+8*m+8*g);
    c.Be=band(0x1fu<<(m+6),g,0x3c00u); c.Bo=band(0x7u<<(m+11),g,0x3c00u); PIN(c.Be); PIN(c.Bo); }
  PIN(c.x0); PIN(c.bd); }
template<int ROLE> DEV void lanes2(CP& c,lch* cb,int ln){ int m=ln&15, g=ln>>4;
  if(ROLE==0){ c.Bo=band(0x7fu<<(m+5),g,0x3c00u); c.AE=band(3u<<(m+12),g,0xffffu); PIN(c.Bo); PIN(c.AE); }
  c.wp=cb+O_RED+ln*16; c.yo=(g&1)*512+m*32+(g>>1)*16; PIN(c.wp); PIN(c.yo); }
template<int ROLE> DEV void chan2(lch* sm,lch* cb,const lch*& fa,const lch*& fb,int grp,int d,int one_,int emax,u64 tag,char* ws){
  CP c;
  const f32x4 Z4={0,0,0,0}; h8 A[12]; f32x4 acc[8]; float sc; u32x4 ry; u32 cga;
  #define YSET sc=__builtin_amdgcn_ldexpf(1.f,emax-YSH); ry=mkrs(ws+W_Y+d*YST,4096); cga=(C_G+grp)*4; PIN(sc); PIN(cga); asm volatile("" : "+s"(ry));
  #define LD_U "buffer_load_dwordx4 v[112:115], %[of], %[rs], 0 offen sc1\n\tbuffer_load_dwordx4 v[116:119], %[of], %[rs], %[s1] offen sc1\n\tbuffer_load_dwordx4 v[120:123], %[of], %[rs], %[s2] offen sc1\n\tbuffer_load_dwordx4 v[124:127], %[of], %[rs], %[s3] offen sc1\n\t"
  #define UC(t,a,b,c) "v_bitop3_b32 " t ", v" #a ", v" #b ", %[" #c "] bitop3:0x7e\n\t"
  #define UD(t,a,c) "v_bitop3_b32 " t ", " t ", v" #a ", %[" #c "] bitop3:0xf6\n\t"
  #define CHK_U UC("%[t0]",112,116,c0) UC("%[t1]",113,117,c1) UD("%[t0]",120,c0) UD("%[t1]",121,c1) UD("%[t0]",124,c0) UD("%[t1]",125,c1) UD("%[t0]",114,c2) UD("%[t1]",115,c3) UD("%[t0]",118,c2) UD("%[t1]",119,c3) \
    UD("%[t0]",122,c2) UD("%[t1]",123,c3) UD("%[t0]",126,c2) UD("%[t1]",127,c3) "v_bitop3_b32 %[t0], %[t0], %[t1], %[mk] bitop3:0xa8\n\tv_cmp_ne_u32 vcc, 0, %[t0]\n\t"
  #define UX(a,b,c,d,o) "v_xor_b32 v" #a ", %[e0], v" #a "\n\tv_xor_b32 v" #b ", %[e1], v" #b "\n\tv_xor_b32 v" #c ", %[e2], v" #c "\n\tv_xor_b32 v" #d ", %[e3], v" #d "\n\tds_write_b128 %[up], v[" #a ":" #d "] offset:" #o "\n\t"
  #define UK(n,a,b,c,d,o,T) VW(n) UX(a,b,c,d,o) UC("%[t1]",a,b,k4) UC("%[t2]",c,d,k4) T
  #define OR3 "v_or3_b32 %[t0], %[t0], %[t1], %[t2]\n\t"
  #define UPOLL { u32 t0,t1,t2,cnt; asm volatile("s_mov_b32 %[cnt], 0x20000\n1:\n\t" UK(3,112,113,114,115,544,"v_or_b32 %[t0], %[t1], %[t2]\n\t") UK(2,116,117,118,119,1056,OR3) UK(1,120,121,122,123,1568,OR3) UK(0,124,125,126,127,2080,OR3) \
      "v_and_b32 %[t0], %[mk], %[t0]\n\tv_cmp_ne_u32 vcc, 0, %[t0]\n\ts_cbranch_vccz 9f\n\ts_sleep " STR(PSLP) "\n\ts_mov_b64 exec, vcc\n\t" LD_U "s_sub_u32 %[cnt], %[cnt], 1\n\ts_cbranch_scc0 1b\n9:\n\ts_mov_b64 exec, 1\n\tds_add_u32 %[cu], %[one]\n\ts_mov_b64 exec, -1" \
      : [t0]"=&v"(t0),[t1]"=&v"(t1),[t2]"=&v"(t2),[cnt]"=&s"(cnt) : [of]"v"(c.p0),[rs]"s"(rsU),[s1]"s"((32>>TL)*GST_),[s2]"s"((64>>TL)*GST_),[s3]"s"((96>>TL)*GST_),[k4]"s"(0x40004u), \
        [e0]"s"(c0^0x40004u),[e1]"s"(c1^0x40004u),[e2]"s"(c2^0x40004u),[e3]"s"(c3_^0x40004u),[mk]"s"(UMASK),[up]"v"(c.p1),[cu]"v"((C_U+one_)*4),[one]"v"(4) : PCLOB); }
  if(ROLE<3){
    h8 a[8], b[8];
    #pragma unroll
    for(int ks=0;ks<8;ks++){ a[ks]=LD(h8,fa+ks*64); b[ks]=LD(h8,fb+ks*64); }
    SCHED();
    LANE(ln); lanes<ROLE>(c,cb,ln); float fz= ln==32? 0.f : 4.76837158203125e-07f; if(ROLE==1) PIN(fz);
    SCHED();
    f32x4 k=Z4;
    #pragma unroll
    for(int ks=0;ks<8;ks++) k=MH(a[ks],b[ks],k);
    const float f=4.76837158203125e-07f;
    float k0=k[0]*f, k1=k[1]*f, k2=k[2]*f, k3=k[3]*f;
    u32 h32=pkh(k3,k2), h10=pkh(k1,k0);
    if(ROLE!=1){
      ST(u16,c.p0)=(u16)h32; ST(u32,c.p0+2)=pkh(k2,k1); ST(u16,c.p1)=(u16)(h10>>16);
      if(ROLE==0) ST(u32x2,c.p2)=u32x2{h32,h10};
    } else {
      float n0=dppf<0x130>(k[0])*fz, z1= fz==0.f? k0:k1, z2=k[2]*fz, z3=k[3]*fz; u32 h22=pkh(z2,z2);
      ST(u16,c.p0+64)=(u16)h10; ST(u16,c.p0+2)=(u16)(h32>>16); ST(u16,c.p0)=(u16)h32; ST(u16,c.p1)=(u16)(h10>>16);
      u32x4 vo={pkh(n0,z3),h22,pkh(z2,z1),pkh(k0,k0)}, ve={pkh(n0,n0),pkh(z3,z2),h22,pkh(z1,k0)};
      asm volatile("s_mov_b32 exec_hi, 1\n\tds_write_b128 %0, %1\n\tds_write_b128 %0, %2 offset:%3\n\ts_mov_b32 exec_hi, -1" :: "v"(c.p2),"v"(ve),"v"(vo),"i"(O_DO-O_DE-16) : "memory"); }
    if(PA>=0) PRIO(PA<0? 0:PA);
    gsync(sm,C_G+grp,7);
    u32 kv;
    if(ROLE==0){ A[0]=ld4w(c.x0+640); A[1]=ld4w(c.x0+128); A[2]=ld4w(c.x1+192); A[3]=ld4w(c.x1+128); A[4]=ld4w(c.x1+64); A[5]=ld4w(c.x1); kv=LD(u16,cb+O_R2K+128); }
    else {
      #pragma unroll
      for(int r=0;r<6;r++){ A[2*r]=ld4w(c.x0+832-64*r); A[2*r+1]=ld4w(c.x0+320-64*r); } }
    SCHED(); lanes2<ROLE>(c,cb,ln); YSET SCHED();
    if(ROLE==0){ kv|=kv<<16; c.AE=u32x4{c.AE[0]&kv,c.AE[1]&kv,c.AE[2]&kv,c.AE[3]&kv}; PIN(c.AE); }
    PRIO(2); if(ROLE!=0&&BID>=P12) PRIO(1);
    gwait<1>(sm,C_U+one_,HG>=0&&BID>=112? 12:8-4*one_);
  } else {
    u32 tg=(u32)tag, c0=upat(tg,0), c1=upat(tg,1), c2=upat(tg,2), c3_=upat(tg,3); u32x4 rsU=mkrs(ws+W_U,256*UST);
    if(!one_) PRIO(2);
    LANE(ln); c.p0=(lch*)fa; c.p1=(lch*)fb;
    bool late= grp==0||BID>=112;
    if(!late) asm volatile(LD_U :: [of]"v"(c.p0),[rs]"s"(rsU),[s1]"s"((32>>TL)*GST_),[s2]"s"((64>>TL)*GST_),[s3]"s"((96>>TL)*GST_) : "memory");
    asm volatile("s_mov_b64 exec, 1\n\tglobal_store_dwordx2 %0, %1, %2 sc1\n\ts_mov_b64 exec, -1" :: "v"(W_FL+d*8),"v"(tag),"s"(ws) : "memory");
    lanes<3>(c,cb,ln); lanes2<3>(c,cb,ln); YSET
    #pragma unroll
    for(int j=2;j<8;j++){ acc[j]=Z4; PIN(acc[j]); }
    if(late){ if(UD_>=0&&BID<UDB) __builtin_amdgcn_s_sleep(UD_<0? 0:UD_); else gwait<2>(sm,C_G,UGATE); if(BID>=112) gwait<1>(sm,C_D,4); asm volatile(LD_U :: [of]"v"(c.p0),[rs]"s"(rsU),[s1]"s"((32>>TL)*GST_),[s2]"s"((64>>TL)*GST_),[s3]"s"((96>>TL)*GST_) : "memory"); }
    if(!late) UPOLL
    gwait<1>(sm,C_G+grp,7);
    #pragma unroll
    for(int i=0;i<6;i++) A[i]=ldA(c.x0+320-64*i);
    if(late) UPOLL
    PRIO(2);
    gwait<1>(sm,C_U+one_,HG>=0&&BID>=112? 12:8-4*one_);
  }
  const lch* bd=c.bd; h8 Be=BC(h8,c.Be), Bo=BC(h8,c.Bo);
  #pragma unroll
  for(int j=0;j<(ROLE==3? 2:8);j++) acc[j]=Z4;
  constexpr int OWN= ROLE==1? 2 : ROLE==2? 1 : ROLE, NS= ROLE==0? 1:2;
  #define RW(j) { ST(f32x4,c.wp+((j)>>1)*4096+(ROLE&1)*2048+((j)&1)*1024)=acc[j]; SCHED(); }
  const lch* rp=c.wp+OWN*4096; f32x4 a[2], b[2]; u32 cv;
  #define LDV(T,p) (*(const volatile LAS T*)(p))
  #define ARR { garrive(sm,C_G+grp); SCHED(); }
  #define OPT { cv=LDV(u32,(lch*)(size_t)cga); _Pragma("unroll") for(int q=0;q<NS;q++){ a[q]=LDV(f32x4,rp+q*2048); b[q]=LDV(f32x4,rp+q*2048+1024); } SCHED(); }
  #define SUM(v) { _Pragma("unroll") for(int e=0;e<4;e++) v[0][e]=(NS==1? v[0][e] : v[0][e]+v[1][e])*sc; PIN(v[0]); SCHED(); }
  if(ROLE==1||ROLE==2){
    constexpr int NQ= ROLE==1? 5:4, LA=9; h8 B[30];
    #define QS(s) (ROLE==2? (s) : (s)<3? 5+(s) : (s))
    #define LB(i) B[i]=LD(h8,bd+320-64*((i)%6)+512*QS((i)/6));
    #pragma unroll
    for(int i=0;i<LA;i++) LB(i)
    SCHED();
    #pragma unroll
    for(int i=0;i<6*NQ;i++){ const int q=QS(i/6), r=i%6;
      if(i==6*NQ-(ROLE==1? 4:7)){ PRIO(2); OPT } if(i==6*NQ-(ROLE==1? 2:3)) SUM(a) if(i==6*NQ-1) SUM(b)
      if(!(ROLE==1&&q==3)){ acc[q]=MH(A[2*r],B[i],acc[q]); SCHED(); }
      if(q!=7&&!(ROLE==2&&q==3)){ acc[q+1]=MH(A[2*r+1],B[i],acc[q+1]); SCHED(); }
      if(i+LA<6*NQ){ LB(i+LA) SCHED(); }
      if(ROLE==2&&i==6) RW(0) if(ROLE==2&&i==12){ RW(1) ARR } if(ROLE==1&&i==13) RW(6) if(ROLE==1&&i==19){ RW(7) ARR } } }
  if(ROLE==0){
    h8 B[8], ue[4], C[8];
    #pragma unroll
    for(int kt=0;kt<4;kt++){ ue[kt]=LD(h8,bd+48+512*kt); B[kt]=LD(h8,bd+64+512*kt); }
    f32x4 ce[2],co[2]; h8 Z[2];
    ce[0]=MH(ue[0],Be,Z4); co[0]=MH(B[0],Bo,Z4); ce[1]=MH(ue[1],Be,Z4); co[1]=MH(B[1],Bo,Z4); Z[0]=zpack(ce[0],co[0]);
    #pragma unroll
    for(int kt=0;kt<4;kt++){
      SCHED();
      if(kt+2<4){ ce[kt&1]=MH(ue[kt+2],Be,Z4); co[kt&1]=MH(B[kt+2],Bo,Z4); }
      if(kt+1<4) Z[(kt+1)&1]=zpack(ce[(kt+1)&1],co[(kt+1)&1]);
      #pragma unroll
      for(int rho=0;rho+kt<4;rho++) acc[4+kt+rho]=MH(Z[kt&1],A[2+rho],acc[4+kt+rho]);
      SCHED(); if(kt>=2) C[kt-1]=LD(h8,bd+512*(kt-1)); if(kt==0) A[5]=BC(h8,c.AE); if(kt==1) A[4]=ld4w(c.x0-384); if(kt==2) A[3]=ld4w(c.x0+64); if(kt==3) A[2]=ld4w(c.x0+576); SCHED(); }
    #pragma unroll
    for(int q=0;q<9;q++){
      if(q<4){ acc[q+4]=MH(A[5],B[q],acc[q+4]); SCHED(); } if(q<6){ acc[q+2]=MH(A[4],B[q],acc[q+2]); SCHED(); }
      if(q>=1&&q<7){ acc[q+1]=MH(A[1],B[q],acc[q+1]); SCHED(); } if(q>=2&&q<8){ acc[q]=MH(A[0],B[q],acc[q]); SCHED(); }
      if(q>=4&&q<8) RW(q-2)
      if(q<4){ B[4+q]=LD(h8,bd+64+512*(4+q)); SCHED(); }
      if(q>=3){ acc[q-1]=MH(A[2],C[q-1],acc[q-1]); SCHED(); } if(q>=2&&q<8){ acc[q]=MH(A[3],C[q-1],acc[q]); SCHED(); }
      if(q>=1&&q<7){ C[(q+2)&7]=LD(h8,bd+512*((q+2)&7)); SCHED(); } }
    acc[0]=MH(A[0],B[0],acc[0]); SCHED(); RW(6) acc[1]=MH(A[1],B[0],acc[1]); SCHED(); RW(7) ARR
    OPT acc[0]=MH(A[2],C[0],acc[0]); acc[1]=MH(A[0],B[1],acc[1]); acc[1]=MH(A[2],C[1],acc[1]); acc[1]=MH(A[3],C[0],acc[1]); SCHED(); SUM(a) SUM(b) }
  if(ROLE==3){
    h8 U[12];
    #pragma unroll
    for(int kt=0;kt<6;kt++) U[kt]=LD(h8,bd+256*kt);
    f32x4 ce[2],co[2]; h8 Z[2];
    ce[0]=MH(U[0],Be,Z4); co[0]=MH(U[0],Bo,Z4); ce[1]=MH(U[1],Be,Z4); co[1]=MH(U[1],Bo,Z4); Z[0]=zpack(ce[0],co[0]);
    #pragma unroll
    for(int kt=0;kt<12;kt++){
      SCHED();
      if(kt==10) OPT if(kt==11) SUM(a)
      if(kt+2<12){ ce[kt&1]=MH(U[kt+2],Be,Z4); co[kt&1]=MH(U[kt+2],Bo,Z4); }
      if(kt>=2&&kt<=8&&!(kt&1)){ SCHED(); RW((kt+2)/2) if(kt==8) ARR }
      if(kt+1<12) Z[(kt+1)&1]=zpack(ce[(kt+1)&1],co[(kt+1)&1]);
      #pragma unroll
      for(int rho=-1;rho<5;rho++){ int j=(kt+4+rho)>>1; if(((kt+rho)&1)==0&&j>=2&&j<8) acc[j]=MH(Z[kt&1],A[rho+1],acc[j]); }
      if(kt<6){ SCHED(); U[kt+6]=LD(h8,bd+256*(kt+6)); } }
    SCHED(); SUM(b)
  }
  { float s0[4], s1[4];
    if(RFL(cv)<11u){ u32 tv,cnt;
      asm volatile("s_mov_b32 %1, 0x20000\n1:\n\tds_read_b32 %0, %6\n\tds_read_b128 %2, %7\n\tds_read_b128 %4, %7 offset:2048\n\tds_read_b128 %3, %7 offset:1024\n\tds_read_b128 %5, %7 offset:3072\n\ts_waitcnt lgkmcnt(4)\n\t"
        "v_cmp_gt_u32 vcc, 11, %0\n\ts_cbranch_vccz 2f\n\ts_sleep 1\n\ts_sub_u32 %1, %1, 1\n\ts_cbranch_scc0 1b\n2:\n\ts_waitcnt lgkmcnt(0)"
        : "=&v"(tv),"=&s"(cnt),"=&v"(a[0]),"=&v"(b[0]),"=&v"(a[1]),"=&v"(b[1]) : "v"(cga),"v"(rp) : "memory","scc","vcc");
      SUM(a) SUM(b) }
    f32x4 o0=acc[2*OWN], o1=acc[2*OWN+1];
    #pragma unroll
    for(int e=0;e<4;e++){ s0[e]=__builtin_fmaf(o0[e],sc,a[0][e]); s1[e]=__builtin_fmaf(o1[e],sc,b[0][e]); }
    auto r0=__builtin_amdgcn_permlane16_swap(nv(pk(s0[0],s0[1])),nv(pk(s1[0],s1[1])),false,false); auto r1=__builtin_amdgcn_permlane16_swap(nv(pk(s0[2],s0[3])),nv(pk(s1[2],s1[3])),false,false);
    asm volatile("buffer_store_dwordx4 %0, %1, %2, 0 offen offset:%3 sc1\n\ts_nop 1" :: "v"(u32x4{r0[0],r1[0],r0[1],r1[1]}),"v"(c.yo),"s"(ry),"i"(OWN*1024) : "memory"); }
  garrive(sm,C_E);

  asm volatile("s_endpgm" : "=v"(fa),"=v"(fb) :: "memory");
}
#define GETEP asm volatile("s_waitcnt lgkmcnt(0)\n\ts_mov_b64 %0, s[96:97]\n\ts_mov_b32 %1, s98" : "=s"(ep),"=s"(ldw));
DEV void chan(lch* sm,int grp,int w4,int d,int one_,char* ws,u32x4 q0,u32x4 q1,u32x4 q2,u32x4 q3,u32x4 q4){
  lch* cb=sm+O_CH+grp*SLSZ; int role=w4, emax;
  #define LP(v) ((lch*)(size_t)(u32)(v))
  const lch* fa=LP(q4[0]); const lch* fb=LP(q4[1]);
  u64 tag;
  { float a2,bp,br,bi,cr,ci;
    u32 fq=q3[2], fr=q3[3], one=q4[2]; float f64=BC(float,fq), f128=BC(float,fr);
    lch *t8[4]={LP(q0[0]),LP(q0[1]),LP(q0[2]),LP(q0[3])}, *tB[4]={LP(q1[0]),LP(q1[1]),LP(q1[2]),LP(q1[3])}, *t4[2]={LP(q2[0]),LP(q2[1])}, *t2[2]={LP(q2[2]),LP(q2[3])}, *t1=LP(q3[0]), *te=LP(q3[1]);
    if(BID<64){ if(grp==0) PRIO(PK&3); if(grp==1) PRIO((PK>>4)&3); if(grp==2) PRIO((PK>>8)&3); } else
    if(grp!=0) PRIO(1);
    u64 ep; u32 ldw; GETEP tag=ep+1ull;
    float dt=rb(expf_((d&1)? bhi(ldw):blo(ldw))), ca=dt*1.4426950408889634f, cb_=dt*0.15915494309189535f;
    asm volatile("s_waitcnt vmcnt(1)\n\tv_mul_f32 %2, %8, v114\n\tv_mul_f32 %3, %8, v115\n\tv_mul_f32 %0, %6, v112\n\tv_mul_f32 %1, %7, v113\n\tv_mov_b32 %4, v116\n\tv_mov_b32 %5, v117"
      : "=&v"(a2),"=&v"(bp),"=&v"(br),"=&v"(bi),"=&v"(cr),"=&v"(ci) : "v"(ca),"v"(cb_),"v"(dt) : "memory");
    C2 W; W.r=cr*br-ci*bi; W.i=cr*bi+ci*br;
    u32 mx=BC(u32,__builtin_fmaxf(__builtin_fabsf(W.r),__builtin_fabsf(W.i)));
    #define MXS(C) mx=umax_(mx,dppu<C>(mx));
    float e8=a2*8.f, eB=__builtin_fmaf(a2,f64,14.f), p8=bp*8.f, pB=bp*f64; SCHED();
    MXS(0xB1) p8=__builtin_amdgcn_fractf(p8); pB=__builtin_amdgcn_fractf(pB); e8=__builtin_amdgcn_exp2f(e8); SCHED();
    MXS(0x4E) eB=__builtin_amdgcn_exp2f(eB); float c8=__builtin_amdgcn_cosf(p8); SCHED();
    MXS(0x141) float s8=__builtin_amdgcn_sinf(p8), cB=__builtin_amdgcn_cosf(pB); SCHED();
    float sB=__builtin_amdgcn_sinf(pB); PIN(sB); MXS(0x140) float e1=__builtin_amdgcn_exp2f(a2); PIN(e1); SCHED();
    asm volatile("s_mov_b64 exec, 0x10001\n\tds_max_u32 %0, %1\n\tds_add_u32 %0, %2 offset:4\n\ts_mov_b64 exec, -1" :: "v"(te),"v"(mx),"v"(one) : "memory");
    C3 z8; z8.r=e8*c8; z8.n=e8*s8; z8.i=-z8.n; PIN(z8.i); C2 B; B.r=eB*cB; B.i=-(eB*sB);
    float p1=__builtin_amdgcn_fractf(bp), pE=__builtin_amdgcn_fractf(bp*256.f), pV=__builtin_amdgcn_fractf(bp*f128), eE=a2*256.f, eV=a2*f128, tr[8];
    #pragma unroll
    for(int k=0;k<4;k++){ SCHED(); u32 h0=ph(B); B=cm(B,z8);
      if(k==0){ tr[0]=__builtin_amdgcn_cosf(pV); tr[1]=__builtin_amdgcn_sinf(pV); } if(k==1){ tr[2]=__builtin_amdgcn_exp2f(eV); tr[3]=__builtin_amdgcn_cosf(p1); }
      if(k==2){ tr[4]=__builtin_amdgcn_sinf(p1); tr[5]=__builtin_amdgcn_exp2f(eE); } if(k==3){ tr[6]=__builtin_amdgcn_cosf(pE); tr[7]=__builtin_amdgcn_sinf(pE); }
      PIN(tr[2*k]); PIN(tr[2*k+1]);
      u32 h1=ph(B); if(k<3) B=cm(B,z8); W2(tB[k],h0,h1); }
    asm volatile("ds_read_b64 v[118:119], %0" :: "v"(te) : "memory");
    SCHED();
    C3 z1=c3(C2{e1*tr[3],e1*tr[4]}), E=c3(C2{tr[5]*tr[6],tr[5]*tr[7]}); C2 V[8]; V[0]=cmul(W,C2{tr[2]*tr[0],tr[2]*tr[1]});
    { u32 cnt,n; PIN(V[0].r); PIN(V[0].i); PIN(z1.r); PIN(E.r);
      asm volatile("s_mov_b32 %1, 0x20000\n1:\n\ts_waitcnt lgkmcnt(0)\n\tv_readfirstlane_b32 %2, v119\n\tv_readfirstlane_b32 %0, v118\n\ts_cmp_eq_u32 %2, 8\n\ts_cbranch_scc1 2f\n\tds_read_b64 v[118:119], %3\n\ts_sub_u32 %1, %1, 1\n\ts_cbranch_scc0 1b\n2:"
        : "=&s"(emax),"=&s"(cnt),"=&s"(n) : "v"(te) : "memory","scc");
      emax=RFL(emax); emax=(emax>>23)-126; }
    V[0].r=__builtin_amdgcn_ldexpf(V[0].r,14-emax); V[0].i=__builtin_amdgcn_ldexpf(V[0].i,14-emax);
    C2 Y=cm(V[0],E), Y4; u32 y0=ph(Y);
    V[1]=cm(V[0],z1); W2(t8[0],ph(V[0]),ph(V[1])); Y=cm(Y,E); u32 y1=ph(Y);
    V[2]=cm(V[1],z1); Y=cm(Y,E); u32 y2=ph(Y);
    V[3]=cm(V[2],z1); W2(t8[1],ph(V[2]),ph(V[3])); W2(t4[0],y0,ph(cm(V[2],E)));
    V[4]=cm(V[3],z1); Y=cm(Y,E); ST(u32,t1)=ph(Y);
    V[5]=cm(V[4],z1); W2(t8[2],ph(V[4]),ph(V[5])); Y4=cm(V[4],E); u32 y4=ph(Y4);
    V[6]=cm(V[5],z1); Y=cm(Y,E); ST(u32,t1+2*TROW)=ph(Y);
    V[7]=cm(V[6],z1); W2(t8[3],ph(V[6]),ph(V[7])); Y4=cm(Y4,E); W2(t2[0],y1,ph(Y4));
    W2(t4[1],y4,ph(cm(V[6],E))); Y=cm(Y,E); ST(u32,t1+4*TROW)=ph(Y);
    Y4=cm(Y4,E); W2(t2[1],y2,ph(Y4)); Y=cm(Y,E); ST(u32,t1+6*TROW)=ph(Y);
    Y=cm(Y,E); asm volatile("s_mov_b32 exec_hi, 0\n\tds_write_b32 %0, %1 offset:4352\n\ts_mov_b32 exec_hi, -1" :: "v"(t1),"v"(ph(Y)) : "memory");
  }
  if(BID<64){ if(grp==0) PRIO(PS&3); if(grp==1) PRIO((PS>>4)&3); if(grp==2) PRIO((PS>>8)&3); }
  DRAIN();
  gsync(sm,C_G+grp,4);
  if(role==0) chan2<0>(sm,cb,fa,fb,grp,d,one_,emax,tag,ws);
  if(role==1) chan2<1>(sm,cb,fa,fb,grp,d,one_,emax,tag,ws);
  if(role==2) chan2<2>(sm,cb,fa,fb,grp,d,one_,emax,tag,ws);
  chan2<3>(sm,cb,fa,fb,grp,d,one_,emax,tag,ws);
}

#define GTC(r,s,c) "v_cmp_gt_u32 %[" #s "], v" #r ", " c "\n\t"
#define GTA(s) "v_addc_co_u32 %[rk], vcc, 0, %[rk], %[" #s "]\n\t"
#define GT0(s) "v_cndmask_b32 %[rk], 0, 1, %[" #s "]\n\t"
#define GT4(a,b,c,d,k,F) GTC(a,s0,k) GTC(b,s1,k) GTC(c,s2,k) GTC(d,s3,k) F(s0) GTA(s1) GTA(s2) GTA(s3)
#define GTS [s0]"=&s"(q0),[s1]"=&s"(q1),[s2]"=&s"(q2),[s3]"=&s"(q3)
DEV void top8(lch* sm,int w4,int ln,u32 M,char* ws,u32& rank_,u32& key_){
  u32 k[8], hm=0xffff0000u, mx=1+w4*4+(ln>>4), one=1, cw=O_CND+w4*32; u64 em=0x0001000100010001ull,q0,q1,q2,q3;
  u32x4 rs=mkrs(ws+W_RG+(BID&(NRG-1))*8192,8192); int ro=w4*1024+ln*16;
  #pragma unroll
  for(int e=0;e<8;e++){ k[e]=(u32)(0xfff-(w4*256+ln*4+(e&3)+(e>>2)*1024)); PIN(k[e]); }
  lch *aW=sm+O_RMX+(w4*4+(ln>>4))*4, *aC=sm+O_RMX+(ln>>2)*4, *aO=sm+O_RMX+(ln&3)*16, *dC=sm+O_CND+(ln>>1)*4, *dO=sm+O_CND+(ln&1)*64, *cW=sm+C_W*4; PIN(aW); PIN(aC); PIN(aO); PIN(dC); PIN(dO); PIN(ro); PIN(ln); PIN(mx); PIN(one); PIN(cW);
  asm volatile("" : "+s"(hm),"+s"(em),"+s"(rs),"+s"(cw));
  gwait<2>(sm,C_G,RGATE);
  if(RD) __builtin_amdgcn_s_sleep(RD);
  { u32 cnt,t0,t1;
    #define KL(e,r) "v_lshl_or_b32 %[k" #e "], v" #r ", 16, %[k" #e "]\n\t"
    #define KH(e,r) "v_and_or_b32 %[k" #e "], v" #r ", %[hm], %[k" #e "]\n\t"
    #define CHK_R "v_bitop3_b32 %[t0], v113, v115, %[M] bitop3:0x7e\n\tv_bitop3_b32 %[t1], v117, v119, %[M] bitop3:0x7e\n\tv_or_b32 %[t0], %[t0], %[t1]\n\tv_cmp_ne_u32 vcc, 0, %[t0]\n\t"
    #define MXD(c) "v_max_u32_dpp %[mx], %[mx], %[mx] " c " row_mask:0xf bank_mask:0xf\n\t"
    asm volatile(LD_DUP2
      "s_sleep " STR(R2D) "\n\tbuffer_load_dwordx4 v[120:123], %[of], %[rs], 0 offen sc1\n\tbuffer_load_dwordx4 v[124:127], %[of], %[rs], %[so] offen sc1\n\t"
      "s_mov_b64 exec, 1\n\tds_add_u32 %[cw], %[one]\n\ts_mov_b64 exec, -1\n\ts_mov_b32 %[cnt], 0x20000\n\ts_waitcnt vmcnt(2)\n\t" CHK_R "s_cbranch_vccz 9f\n\t"
      "s_waitcnt vmcnt(0)\n\tv_mov_b64 v[112:113], v[120:121]\n\tv_mov_b64 v[114:115], v[122:123]\n\tv_mov_b64 v[116:117], v[124:125]\n\tv_mov_b64 v[118:119], v[126:127]\n\t"
      POLLASM(LD_DUP2,CHK_R,PSLP)
      "v_pk_max_u16 %[t0], v112, v114\n\tv_pk_max_u16 %[t1], v116, v118\n\tv_pk_max_u16 %[t0], %[t0], %[t1]\n\t"
      "v_max_u16_sdwa %[mx], %[t0], %[t0] dst_sel:WORD_1 dst_unused:UNUSED_PRESERVE src0_sel:WORD_0 src1_sel:WORD_1\n\t"
      KL(0,112) KH(1,112) MXD("quad_perm:[1,0,3,2]") KL(2,114) KH(3,114) MXD("quad_perm:[2,3,0,1]") KL(4,116) KH(5,116) MXD("row_half_mirror") KL(6,118) KH(7,118) MXD("row_mirror")
      "s_mov_b64 exec, %[em]\n\tds_write_b32 %[aW], %[mx]\n\ts_mov_b64 exec, -1\n\tds_read_b32 v116, %[aC]\n\tds_read_b128 v[112:115], %[aO]"
      : [k0]"+v"(k[0]),[k1]"+v"(k[1]),[k2]"+v"(k[2]),[k3]"+v"(k[3]),[k4]"+v"(k[4]),[k5]"+v"(k[5]),[k6]"+v"(k[6]),[k7]"+v"(k[7]),[mx]"+v"(mx),[cnt]"=&s"(cnt),[t0]"=&v"(t0),[t1]"=&v"(t1)
      : [of]"v"(ro),[rs]"s"(rs),[so]"s"(4096),[M]"s"(M),[hm]"s"(hm),[cw]"v"(cW),[one]"v"(one),[em]"s"(em),[aW]"v"(aW),[aC]"v"(aC),[aO]"v"(aO) : PCLOB); }
  u32 slow;
  { u32 rk,cnt,T,h,l,t,n;
    #define INS(e) "v_med3_u32 %[t], %[t], %[l], %[k" #e "]\n\tv_med3_u32 %[l], %[h], %[l], %[k" #e "]\n\tv_max_u32 %[h], %[h], %[k" #e "]\n\t"
    asm volatile("s_branch 7f\n"
      "3:\n\tv_cmp_le_u32 %[s2], %[T], %[t]\n\ts_add_u32 %[cnt], %[cnt], %[n]\n\ts_cmp_gt_u32 %[cnt], 8\n\ts_cbranch_scc1 5f\n\ts_cmp_lg_u64 %[s2], 0\n\ts_cbranch_scc1 5f\n\t"
      "s_mov_b64 exec, %[s1]\n\tv_mov_b32 %[t], %[n]\n\tv_mbcnt_lo_u32_b32 %[t], exec_lo, %[t]\n\tv_mbcnt_hi_u32_b32 %[t], exec_hi, %[t]\n\tv_lshl_add_u32 %[t], %[t], 2, %[cw]\n\tds_write_b32 %[t], %[l]\n\ts_mov_b64 exec, -1\n\ts_branch 4f\n"
      "5:\n\ts_mov_b32 %[sl], 1\n\ts_branch 6f\n7:\n\t"
      "v_max3_u32 %[h], %[k0], %[k1], %[k2]\n\tv_med3_u32 %[l], %[k0], %[k1], %[k2]\n\tv_min3_u32 %[t], %[k0], %[k1], %[k2]\n\t" INS(3) INS(4) INS(5) INS(6) INS(7)
      "s_mov_b32 %[cnt], 0x20000\n1:\n\ts_waitcnt lgkmcnt(0)\n\tv_cmp_eq_u32 vcc, 0, v116\n\ts_cbranch_vccz 2f\n\tds_read_b32 v116, %[aC]\n\tds_read_b128 v[112:115], %[aO]\n\ts_sub_u32 %[cnt], %[cnt], 1\n\ts_cbranch_scc0 1b\n2:\n\t"
      GT4(112,113,114,115,"v116",GT0) "v_and_b32 v116, %[hm], v116\n\ts_nop 0\n\tv_add_u32_dpp %[rk], %[rk], %[rk] quad_perm:[1,0,3,2] row_mask:0xf bank_mask:0xf\n\ts_mov_b32 %[sl], 0\n\ts_nop 0\n\tv_add_u32_dpp %[rk], %[rk], %[rk] quad_perm:[2,3,0,1] row_mask:0xf bank_mask:0xf\n\t"
      "v_cmp_eq_u32 vcc, 7, %[rk]\n\ts_ff1_i32_b64 %[cnt], vcc\n\ts_nop 0\n\tv_readlane_b32 %[T], v116, %[cnt]\n\ts_nop 1\n\t"
      "v_cmp_le_u32 vcc, %[T], %[h]\n\tv_cmp_le_u32 %[s1], %[T], %[l]\n\ts_bcnt1_i32_b64 %[n], vcc\n\tv_mbcnt_lo_u32_b32 %[rk], vcc_lo, 0\n\tv_mbcnt_hi_u32_b32 %[rk], vcc_hi, %[rk]\n\ts_bcnt1_i32_b64 %[cnt], %[s1]\n\ts_cbranch_scc1 3b\n\t"
      "s_cmp_gt_u32 %[n], 8\n\ts_cbranch_scc1 5b\n"
      "4:\n\tv_lshl_add_u32 %[rk], %[rk], 2, %[cw]\n\ts_mov_b64 exec, vcc\n\tds_write_b32 %[rk], %[h]\n\ts_mov_b64 exec, 1\n\tds_add_u32 %[cW], %[one] offset:4\n\ts_mov_b64 exec, -1\n6:"
      : [rk]"=&v"(rk),[cnt]"=&s"(cnt),[T]"=&s"(T),[h]"=&v"(h),[l]"=&v"(l),[t]"=&v"(t),[n]"=&s"(n),[sl]"=&s"(slow),GTS
      : [k0]"v"(k[0]),[k1]"v"(k[1]),[k2]"v"(k[2]),[k3]"v"(k[3]),[k4]"v"(k[4]),[k5]"v"(k[5]),[k6]"v"(k[6]),[k7]"v"(k[7]),[aC]"v"(aC),[aO]"v"(aO),[hm]"s"(hm),[cw]"s"(cw),[cW]"v"(cW),[one]"v"(one) : PCLOB); }
  if(__builtin_expect(RFL(slow)!=0,0)){
    u32 mine=0;
    _Pragma("unroll 1") for(int c=0;c<8;c++){ u32 lm=umax_(umax_(umax_(k[0],k[1]),umax_(k[2],k[3])),umax_(umax_(k[4],k[5]),umax_(k[6],k[7]))); u32 wm=wmax(lm); if(ln==c) mine=wm;
      #pragma unroll
      for(int e=0;e<8;e++) k[e]=(k[e]==wm)?0u:k[e]; }
    if(ln<8) ST(u32,sm+cw+ln*4)=mine;
    garrive(sm,C_W+1); }
  { u32 c,rk,cnt;
    asm volatile("s_mov_b32 %[cnt], 0x20000\n1:\n\tds_read_b32 %[rk], %[cW] offset:4\n\tds_read_b32 %[c], %[dC]\n\tds_read_b128 v[96:99], %[dO]\n\tds_read_b128 v[100:103], %[dO] offset:16\n\tds_read_b128 v[104:107], %[dO] offset:32\n\tds_read_b128 v[108:111], %[dO] offset:48\n\t"
      "s_waitcnt lgkmcnt(5)\n\tv_cmp_gt_u32 vcc, 4, %[rk]\n\ts_cbranch_vccz 2f\n\ts_sub_u32 %[cnt], %[cnt], 1\n\ts_cbranch_scc0 1b\n2:\n\t"
      "s_waitcnt lgkmcnt(3)\n\t" GT4(96,97,98,99,"%[c]",GT0) "s_waitcnt lgkmcnt(2)\n\t" GT4(100,101,102,103,"%[c]",GTA) "s_waitcnt lgkmcnt(1)\n\t" GT4(104,105,106,107,"%[c]",GTA) "s_waitcnt lgkmcnt(0)\n\t" GT4(108,109,110,111,"%[c]",GTA)
      "s_nop 1\n\tv_add_u32_dpp %[rk], %[rk], %[rk] quad_perm:[1,0,3,2] row_mask:0xf bank_mask:0xf"
      : [c]"=&v"(c),[rk]"=&v"(rk),[cnt]"=&s"(cnt),GTS : [dC]"v"(dC),[dO]"v"(dO),[cW]"v"(cW)
      : PCLOB,"v96","v97","v98","v99","v100","v101","v102","v103","v104","v105","v106","v107","v108","v109","v110","v111");
    rank_=rk; key_=c; }
}

#define FILL_(n,WAIT) "s_mov_b32 m0, %[ld]\n\ts_mov_b32 %[c], " STR(n) "-1\n3:\n\t" WAIT "buffer_load_dwordx4 %[vo], %[rs], %[so] offen" WPOL " lds\n\ts_add_u32 m0, m0, 0x1000\n\ts_add_u32 %[so], %[so], %[st]\n\t" \
  "s_sub_u32 %[c], %[c], 1\n\ts_cbranch_scc0 3b\n\ts_mov_b32 %[ld], m0\n\t"
#define FILL(n,WAIT) do{ u32 c_; asm volatile(FILL_(n,WAIT) : [c]"=&s"(c_),[so]"+s"(wso),[ld]"+s"(wld) : [vo]"v"(wvo),[rs]"s"(wrs),[st]"s"(wst) : "memory","scc","m0"); }while(0)
#define FILLD(n,WN,WW_,STK) do{ u32 c_,t_,s_=0; asm volatile(FILL_(n,STK "ds_read_b32 %[t], %[ad]\n\ts_waitcnt lgkmcnt(0)\n\tv_readfirstlane_b32 %[s], %[t]\n\ts_cmp_ge_u32 %[s], 4\n\ts_cbranch_scc1 4f\n\t" VW(WN) "4:\n\t" VW(WW_)) \
  : [c]"=&s"(c_),[so]"+s"(wso),[ld]"+s"(wld),[t]"=&v"(t_),[s]"+s"(s_) : [vo]"v"(wvo),[rs]"s"(wrs),[st]"s"(wst),[ad]"v"(C_WIN*4) : "memory","scc","m0"); wso=RFL(wso); wld=RFL(wld); }while(0)
#define STKB "s_cmp_ge_u32 %[s], 4\n\ts_cbranch_scc1 4f\n\t"

__global__ NOPK __attribute__((amdgpu_num_vgpr(56),amdgpu_num_sgpr(102))) __launch_bounds__(1024,4) void kA(const u16* __restrict__ x,char* ws,const u16* __restrict__ wr,const u16* __restrict__ Dsk,const u16* __restrict__ wg,const u16* __restrict__ wu,
                                              const u16* __restrict__ wd,u16* __restrict__ out,KP kp){
  __shared__ __attribute__((aligned(16))) char smem[LDS_TOTAL];
  lch* sm=(lch*)smem;
  u32x4 xa,xb_,wa,wb,dq[3]; u32 xc,wc;
  asm volatile(
    "s_load_dwordx16 s[64:79], %[ka], 0x0\n\ts_load_dwordx16 s[80:95], %[ka], 0x40\n\t"
    "v_readfirstlane_b32 s60, %[t]\n\ts_and_b32 s61, %[wg], 7\n\ts_lshr_b32 s62, %[wg], 3\n\tv_and_b32 v118, 31, %[t]\n\ts_bfe_u32 s63, s60, 0x20006\n\ts_cmpk_ge_u32 s60, 0x100\n\ts_cbranch_scc1 1f\n\t"
    "s_setprio 3\n\ts_lshl_b32 s56, s61, 5\n\ts_add_u32 s56, s56, s62\n\ts_mulk_i32 s56, 0x2400\n\tv_lshrrev_b32 v119, 5, %[t]\n\tv_mul_u32_u24 v119, 0x480, v119\n\tv_add_u32 v119, s56, v119\n\t"
    "v_lshl_add_u32 v116, v118, 4, v119\n\tv_lshl_add_u32 v117, v118, 2, v119\n\tv_lshlrev_b32 v114, 4, v118\n\tv_lshlrev_b32 v115, 2, v118\n\t"
    "v_lshlrev_b32 v119, 1, %[t]\n\tv_mov_b32 v118, 0\n\tds_write_b16 v119, v118\n\t"
    "s_waitcnt lgkmcnt(0)\n\t"
    "s_load_dwordx2 s[96:97], s[66:67], 0x0\n\t"
    "global_load_dwordx4 %[wa], v114, s[68:69]\n\tglobal_load_dwordx4 %[wb], v114, s[68:69] offset:512\n\tglobal_load_dword %[wc], v115, s[68:69] offset:1024\n\t"
    "global_load_dwordx4 %[xa], v116, s[64:65]\n\tglobal_load_dwordx4 %[xb], v116, s[64:65] offset:512\n\tglobal_load_dword %[xc], v117, s[64:65] offset:1024\n\t"
    "s_barrier\n\t"
    "s_branch 8f\n"
    "1:\n\ts_cmp_ge_u32 s62, " STR(CPF) "\n\ts_cbranch_scc1 6f\n\ts_getpc_b64 s[56:57]\n.Lkpc:\n\ts_and_b32 s58, s62, 3\n\ts_lshl_b32 s58, s58, 13\n\ts_sub_u32 s58, s58, 0x1000\n\tv_lshl_add_u32 v119, %[t], 4, s58\n\tv_min_u32 v119, .Lkend-.Lkpc-16, v119\n\t"
    "global_load_dwordx4 v[120:123], v119, s[56:57]\n\t.subsection 1\n.Lkend:\n\t.subsection 0\n6:\n\t"
    "v_mov_b32 v124, -1\n\tv_mov_b32 v125, -1\n\tv_mov_b32 v126, -1\n\tv_mov_b32 v127, -1\n\tv_lshlrev_b32 v119, 4, %[t]\n\tv_and_b32 v119, 0xff0, v119\n\t"
    "s_lshr_b32 s59, s60, 8\n\ts_cmp_lt_u32 s62, 8\n\ts_cselect_b32 s58, 2, 1\n\ts_cbranch_scc1 2f\n\ts_cmp_eq_u32 s59, 3\n\ts_cbranch_scc1 4f\n"
    "2:\n\ts_sub_u32 s57, s59, 2\n\ts_cmp_eq_u32 s59, 1\n\ts_cselect_b32 s58, s58, s57\n\ts_cmp_lt_u32 s62, 8\n\ts_cselect_b32 s56, 0, 8\n\ts_lshl1_add_u32 s56, s62, s56\n\ts_mulk_i32 s61, 72\n\ts_add_u32 s51, s56, s61\n\ts_sub_u32 s56, 2, s59\n\ts_sub_u32 s57, 16, s62\n\ts_cmp_eq_u32 s59, 3\n\ts_cselect_b32 s50, s57, s56\n\ts_add_u32 s56, s50, s51\n\t"
    "v_mov_b32 v112, 0\n\tv_mov_b32 v113, 0\n\tv_mov_b32 v114, 0\n\tv_mov_b32 v115, 0\n\tv_mov_b32 v116, 0\n\tv_mov_b32 v117, 0\n\ts_setprio 2\n\t"
    "s_lshl_b32 s57, s56, 2\n\ts_add_u32 s57, s57, s63\n\ts_lshl_b32 s57, s57, 6\n\tv_lshl_add_u32 v22, v118, 1, s57\n\ts_mul_i32 s57, s56, %[yst]\n\ts_add_u32 s57, s57, %[wy]\n\tv_add_u32 v23, s57, v119\n\ts_lshl_b32 s56, s56, 1\n\ts_and_b32 s56, s56, -4\n\t"
    EB1 "s_waitcnt lgkmcnt(0)\n\t"
    "global_load_short_d16_hi v112, v22, s[80:81]" QPOL "\n\tglobal_load_short_d16_hi v113, v22, s[82:83]" QPOL "\n\tglobal_load_short_d16_hi v114, v22, s[84:85]" QPOL "\n\t"
    "global_load_short_d16_hi v115, v22, s[86:87]" QPOL "\n\tglobal_load_short_d16_hi v116, v22, s[88:89]" QPOL "\n\tglobal_load_short_d16_hi v117, v22, s[90:91]" QPOL "\n\t"
    "s_load_dwordx2 s[96:97], s[66:67], 0x0\n\ts_load_dword s98, s[92:93], s56\n\t"
    EB0 IV0
    "s_mul_i32 s52, s58, %[sl]\n\ts_add_u32 s52, s52, %[tb]\n\ts_lshl_b32 s53, s63, 7\n\ts_add_u32 s53, s53, s52\n\tv_lshl_add_u32 v30, v118, 2, s53\n\tv_bfe_u32 v31, %[t], 5, 1\n\ts_movk_i32 s54, 544\n\tv_mul_u32_u24 v29, s54, v31\n\t"
    "v_lshl_add_u32 v2, v29, 3, v30\n\ts_movk_i32 s53, 1088\n\tv_add_u32 v3, s53, v2\n\tv_add_u32 v4, s53, v3\n\tv_add_u32 v5, s53, v4\n\tv_add_u32 v6, 41*544, v2\n\tv_add_u32 v7, s53, v6\n\tv_add_u32 v8, s53, v7\n\tv_add_u32 v9, s53, v8\n\t"
    "v_lshl_add_u32 v10, v29, 2, v30\n\tv_add_u32 v10, 16*544, v10\n\tv_add_u32 v11, s53, v10\n\tv_lshl_add_u32 v12, v29, 1, v30\n\tv_add_u32 v12, 24*544, v12\n\tv_add_u32 v13, 2176, v12\n\tv_add_u32 v14, v29, v30\n\tv_add_u32 v14, 32*544, v14\n\t"
    "s_lshl_b32 s53, s58, 4\n\ts_add_u32 s53, s53, %[ex]\n\tv_mov_b32 v15, s53\n\tv_cvt_f32_ubyte0 v31, v31\n\tv_mul_f32 v16, 0x42800000, v31\n\tv_add_f32 v17, v16, v16\n\tv_mov_b32 v20, 1\n\t"
    "v_and_b32 v29, 15, %[t]\n\tv_and_b32 v30, 48, %[t]\n\tv_mad_u32_u24 v30, v29, s54, v30\n\ts_lshl_b32 s55, s59, 1\n\ts_lshr_b32 s55, 0x48, s55\n\ts_add_u32 s55, s55, s63\n\ts_and_b32 s55, s55, 3\n\ts_mul_i32 s53, s55, 16*544\n\ts_movk_i32 s57, 41*544\n\ts_cmp_eq_u32 s55, 2\n\ts_cselect_b32 s53, s57, s53\n\ts_cselect_b32 s57, 32*544, s57\n\t"
    "s_add_u32 s53, s53, s52\n\ts_add_u32 s57, s57, s52\n\tv_add_u32 v18, s53, v30\n\tv_add_u32 v19, s57, v30\n\t"
    "v_min_u32 v29, 237*16, v119\n\tv_subrev_u32 v30, 204*16, v29\n\tv_add_u32 v29, %[de], v29\n\tv_min_u32 v29, v29, v30\n\ts_sub_u32 s53, s52, 4640\n\tv_add_u32 v29, s53, v29\n\tv_mov_b32 v24, 0\n\tv_mov_b32 v25, 0\n\tv_mov_b32 v26, 0\n\tv_mov_b32 v27, 0\n\tds_write_b128 v29, v[24:27]\n\t"
    IV1
    "s_cmp_lg_u32 s55, 3\n\ts_cbranch_scc1 9f\n\t"
    "v_bfe_u32 v18, %[t], 1, 5\n\tv_and_b32 v19, 1, %[t]\n\ts_cmp_lt_u32 s62, 8\n\ts_cselect_b32 s53, 2, 1\n\ts_mul_i32 s53, s53, %[sl]\n\tv_mul_u32_u24 v21, s53, v19\n\ts_sub_u32 s55, 2, s59\n\ts_lshl_b32 s55, s55, 7\n\tv_add_u32 v18, s55, v18\n\ts_cmp_lg_u32 s59, 3\n\ts_cbranch_scc1 7f\n\t"
    "v_bfe_u32 v18, %[t], 5, 1\n\tv_lshl_add_u32 v18, v18, 7, v118\n\tv_mov_b32 v19, s50\n\tv_mov_b32 v21, %[sl]\n"
    "7:\n\tv_add_u32 v19, s51, v19\n\tv_lshl_add_u32 v21, v18, 4, v21\n\tv_lshrrev_b32 v24, 3-" STR(TL) ", v19\n\tv_and_b32 v19, 7>>" STR(TL) ", v19\n\tv_lshlrev_b32 v19, 4, v19\n\tv_lshl_add_u32 v19, v24, 7, v19\n\t"
    "v_and_b32 v24, (1<<" STR(TL) ")-1, v18\n\tv_lshl_add_u32 v19, v24, 7-" STR(TL) ", v19\n\tv_lshrrev_b32 v18, " STR(TL) ", v18\n\tv_mul_u32_u24 v18, %[ust], v18\n\tv_add_u32 v18, v18, v19\n\tv_add_u32 v19, 512, v21\n\t"
    "s_branch 9f\n"
    "4:\n\t" EB1
    "v_bfe_u32 v118, %[t], 3, 5\n\tv_lshrrev_b32 v117, 1, v118\n\tv_and_b32 v117, 4, v117\n\tv_and_b32 v116, 2, v118\n\tv_and_b32 v115, 7, %[t]\n\tv_or_b32 v116, v116, v117\n\tv_xor_b32 v115, v115, v116\n\tv_lshlrev_b32 v115, 4, v115\n\t"
    "s_mul_i32 s54, s61, 9\n\ts_sub_u32 s55, s62, 14\n\ts_lshr_b32 s55, s55, 1\n\ts_add_u32 s54, s54, s55\n\ts_lshl_b32 s54, s54, 7\n\t"
    "s_mul_i32 s52, s61, 0xa2000\n\ts_mul_i32 s53, s62, 0x180\n\ts_add_u32 s52, s52, s53\n\ts_cmp_ge_u32 s62, 11\n\ts_cselect_b32 s53, 0x51000-0x480, 0\n\ts_add_u32 s52, s52, s53\n\ts_sub_u32 s52, s52, 0xc00\n\t"
    "s_movk_i32 s55, 0x480\n\ts_cmp_ge_u32 s62, 14\n\ts_cselect_b32 s54, s54, s52\n\ts_cselect_b32 s55, 0x2400, s55\n\tv_mad_u32_u24 v118, v118, s55, v115\n\ts_lshl_b32 s55, s55, 5\n\t"
    "s_lshl_b32 s52, s63, 10\n\ts_add_u32 m0, s52, %[r2]\n\ts_mov_b32 s58, 0x510000\n\ts_mov_b32 s59, 0x20000\n\ts_cmp_ge_u32 s62, 14\n\ts_cselect_b32 s53, " STR(PRE1) "-1, " STR(PRE2) "-1\n\ts_setprio 3\n\t"
    "s_waitcnt lgkmcnt(0)\n\t"
    "s_bitcmp1_b32 %[wg], 3\n\ts_cselect_b64 s[56:57], s[74:75], s[72:73]\n\ts_cmp_ge_u32 s62, 14\n\ts_cselect_b64 s[56:57], s[56:57], s[76:77]\n\ts_cselect_b32 s52, 0x180, -1\n\ts_and_b32 s57, s57, 0xffff\n\ts_add_u32 s60, s56, s54\n\ts_and_b32 s60, s60, 0x380\n\ts_cmp_eq_u32 s60, s52\n\ts_cselect_b32 s53, " STR(PRE3) "-1, s53\n\t"
    "s_load_dwordx2 s[96:97], s[66:67], 0x0\n\t" EB0
    "\n3:\n\t" "buffer_load_dwordx4 v118, s[56:59], s54 offen" WPOL " lds\n\ts_add_u32 m0, m0, 0x1000\n\ts_add_u32 s54, s54, s55\n\ts_sub_u32 s53, s53, 1\n\ts_cbranch_scc0 3b\n"
    "9:\n8:"
    : [xa]"=&{v[2:5]}"(xa),[xb]"=&{v[6:9]}"(xb_),[xc]"=&{v30}"(xc),[wa]"=&{v[10:13]}"(wa),[wb]"=&{v[14:17]}"(wb),[wc]"=&{v31}"(wc),[d0]"=&{v[18:21]}"(dq[0]),[d1]"=&{v[22:25]}"(dq[1]),[d2]"=&{v[26:29]}"(dq[2])
    : [ka]"s"(__builtin_amdgcn_kernarg_segment_ptr()),[wg]"s"(BID),[t]"v"(TID),[yst]"i"(YST),[wy]"i"(W_Y),[r2]"i"(O_R2),[sl]"i"(SLSZ),[tb]"i"(O_CH+O_TB),[ex]"i"(O_EXP),[de]"i"(O_DE),[ust]"i"(GST_)
    : "memory","scc","m0","v127","s50","s51","s52","s53","s54","s55","s56","s57","s58","s59","s60","s61","s62","s63","s64","s65","s66","s67","s68","s69","s70","s71","s72","s73","s74","s75","s76","s77","s78","s79",
      "s80","s81","s82","s83","s84","s85","s86","s87","s88","s89","s90","s91","s92","s93","s94","s95","s99");
  int wv=RFL(TID>>6), bid=BID, q4=wv>>2, w4=wv&3;
  int xg=bid&7, lb=bid>>3, tb=32*xg+lb, nch=2+(lb<8);
  u64 ep; u32 ldw;
  {
  }
  #define KILLIN "=v"(xa),"=v"(xb_),"=v"(xc),"=v"(wa),"=v"(wb),"=v"(wc),"=v"(dq[0]),"=v"(dq[1]),"=v"(dq[2])
  if(__builtin_expect(q4==3&&lb>=8,0)){
    int fu=18*xg+lb-14, kq=2*xg+(lb>=11), cgp=lb-8-3*(lb>=11);
  u32 wst= lb>=14? F_*64:D_*64;
  u32 wso=(lb>=14? (fu>>1)*128 : kq*(288*D_*2)+384*cgp)+(lb>=14? PRE1:PRE2)*wst, wld=(u32)(size_t)sm+O_R2+w4*1024+(lb>=14? PRE1:PRE2)*4096; u32x4 wrs=mkrs(lb>=14? ((fu&1)? wu:wg):wd,D_*F_*2);
  GETEP
  #define FLANE(RS) LANE(ln); int g=ln>>4, li=ln&15, qq=li>>2, pq=li&3; u32 wvo=(u32)(8*w4+(ln>>3))*(RS)+(u32)((ln&7)^(((ln>>3)&2)|((w4&1)<<2)))*16u; \
    const lch* tp=sm+O_R2+(8*g+qq)*128+((w4^((qq>>1)|((g&1)<<1)))*32)+pq*8;
  #define WFR(img,ks) trfrag(tp+(img)*CHSZ+(ks)*4096,tp+(img)*CHSZ+(ks)*4096+512)
  u64 tag=ep+1ull; const u32x4 I4={INV,INV,INV,INV};
  if(lb>=14){
    FLANE(F_*2)
    int mt=fu&1;
    const lch* xp=sm+O_TILE+li*HSTR+g*16; int of=(mt*288+(fu>>1)*4+w4)*256+(li&7)*32+g*8+(li>>3)*4;
    const lch* mp=sm+O_RC+(li&7)*4;
    asm volatile("" : "+v"(xp),"+v"(of),"+v"(mp));
    rsrc_t rsG=RSRC(ws+W_GU,2*8*F_*2);
    if(w4==0) BST128(I4,rsG,(mt*288+(fu>>1)*4)*256+ln*16,0,SC1);
    bool slow=(((u32)wrs[0]+wso)&0x380u)==0x180u;
    if(slow){ wso-=(PRE1-PRE3)*wst; wld-=(PRE1-PRE3)*4096; FILL(5+PRE1-PRE3,VW(W3)); } else FILLD(5,W1,W1B,"");
    if(NA){ VMW(11-NA); garrive(sm,C_P); }
    if(w4==0){ VMW(5); if(ln==0) GST(ws+W_FL+(576+fu)*8,tag); }
    if(slow) FILLD(13-PRE1,W3,W1B,STKB); else FILLD(13-PRE1,W1,W1B,STKB);
    bf16x8 fB[18]; f32x4 cs={0,0,0,0}; const bf16x8 on=BC(bf16x8,(u32x4{0x3f803f80u,0x3f803f80u,0x3f803f80u,0x3f803f80u}));
    const lch* tq=tp+CHSZ; PIN(tq);
    #define RDF(a,b) _Pragma("unroll") for(int ks=a;ks<b;ks++) fB[ks]= ks<9? WFR(0,ks) : trfrag(tq+(ks-9)*4096,tq+(ks-9)*4096+512);
    #define CSF(a,b) SCHED(); _Pragma("unroll") for(int ks=a;ks<b;ks++) cs=__builtin_amdgcn_mfma_f32_16x16x32_bf16(fB[ks],on,cs,0,0,0); SCHED();
    #define PNF(a,b) PIN(cs); _Pragma("unroll") for(int ks=a;ks<b;ks++) PIN(fB[ks]);
    if(NA){ gwait<0>(sm,C_P,4); RDF(0,NA) CSF(0,NA) PNF(0,NA) }
    if(NB){ VMW(18-NB); gsync(sm,C_P,NA? 8:4); RDF(NA,NB) CSF(NA,NB) PNF(NA,NB) }
    #define NC (NB? NB:NA)
    #define CP0 ((NA? 4:0)+(NB? 4:0))
    VMW(LAST1);
    gsync(sm,C_P,CP0+4);
    RDF(NC,18-LAST1) CSF(NC,18-LAST1) PNF(NC,18-LAST1)
    DRAIN();
    gsync(sm,C_P,CP0+8);
    float cA,cB,ri,ce,p0,p1,q0,q1; u32 mdw;
    { bf16x8 xb[9];
      RDF(18-LAST1,18) SCHED();
      u32 cx=*(const volatile LAS u32*)(sm+C_X*4);
      #pragma unroll
      for(int ks=0;ks<4;ks++) xb[ks]=*(const volatile LAS bf16x8*)(xp+ks*64);
      CSF(18-LAST1,18)
      if(RFL(cx)<4u){ gwait<XS1>(sm,C_X,XG1); gwait4<64>(C_X,4,xp,xb[0],xb[1],xb[2],xb[3]); }
      mdw=LD(u32,mp); xb[4]=LD(bf16x8,xp+256); xb[5]=LD(bf16x8,xp+320);
      SCHED();
      #pragma unroll
      for(int ks=0;ks<8;ks++){
        if(ks==0) asm volatile("v_mfma_f32_16x16x32_bf16 v[112:115], %0, %2, 0\n\tv_mfma_f32_16x16x32_bf16 v[116:119], %1, %2, 0" :: "v"(fB[0]),"v"(fB[9]),"v"(xb[0]));
        else asm volatile("v_mfma_f32_16x16x32_bf16 v[112:115], %0, %2, v[112:115]\n\tv_mfma_f32_16x16x32_bf16 v[116:119], %1, %2, v[116:119]" :: "v"(fB[ks]),"v"(fB[ks+9]),"v"(xb[ks]));
        SCHED();
        if(ks<3) xb[ks+6]=LD(bf16x8,xp+(ks+6)*64); if(ks==HG) garrive(sm,C_U);
        if(ks==2){ PIN(cs); cA= li<8? cs[0]:cs[2]; cB= li<8? cs[1]:cs[3]; PIN(cA); PIN(cB); }
        if(ks==3){ PIN(mdw); ri=rcpf_(bhi(mdw)); float nm=-blo(mdw); cA*=nm; cB*=nm; PIN(cA); PIN(cB); } if(ks==4){ ce=ri*-1.4426950408889634f; p0=cA*ri; p1=cB*ri; PIN(p0); PIN(p1); } if(ks==5){ q0=cA*ce; q1=cB*ce; PIN(q0); PIN(q1); }
        SCHED(); }
      #define RR8 " row_ror:8 row_mask:0xf bank_mask:"
      float v0,v1,s0,s1;
      asm volatile("v_mfma_f32_16x16x32_bf16 v[112:115], %4, %6, v[112:115]\n\tv_mfma_f32_16x16x32_bf16 v[116:119], %5, %6, v[116:119]\n\ts_nop 7\n\tv_add_f32_dpp %0, v116, v112" RR8 "0x3\n\tv_add_f32_dpp %1, v117, v113" RR8 "0x3\n\tv_add_f32_dpp %0, v114, v118" RR8 "0xc\n\tv_add_f32_dpp %1, v115, v119" RR8 "0xc\n\t"
        "v_fmac_f32 %2, %0, %8\n\tv_fmac_f32 %3, %1, %8\n\tv_fma_f32 %0, %0, %7, %9\n\tv_fma_f32 %1, %1, %7, %10"
        : "=&v"(v0),"=&v"(v1),"+v"(q0),"+v"(q1) : "v"(fB[8]),"v"(fB[17]),"v"(xb[8]),"v"(ri),"v"(ce),"v"(p0),"v"(p1));
      s0=q0; s1=q1;
      if(mt==0){ v0*=rcpf_(1.f+__builtin_amdgcn_exp2f(s0)); v1*=rcpf_(1.f+__builtin_amdgcn_exp2f(s1)); }
      __builtin_amdgcn_raw_buffer_store_b32(nv(pk(v0,v1)),rsG,of,0,SC1); garrive(sm,C_H); }
    if(mt==0&&w4==0){
    LANE(l2); ln=l2; li=ln&15; g=ln>>4;
    int c9=(lb-14)>>1, tk=(xg&6)+((li>>2)&1), t=(int)LD(u16,sm+O_IDX+tk*2), c=64*c9+32*(xg&1)+16*(li>>3)+4*(li&3)+g; f32x4 pr;
    { u32 u=LD(u16,sm+O_IDX+16+tk*2)^0x8000u, xb=LD(u16,sm+O_TILE+XAD(tk,c*2)), sv=LD(u32,sm+O_RC+tk*4); float ds=b2f(Dsk[c]); u^=(u32)(((int)(u<<16))>>31)&0x7fffu;
      poll1<0>(ws+W_FL+(64*c9+ln)*8,tag);
 poll1<0>(ws+W_FL+(720+(ln<48? ln:47))*8,tag);
      float xn=(b2f(xb)-blo(sv))*rcpf_(bhi(sv)); pr=f32x4{b2f(xb),xn,xn*ds,sigm_f(b2f(u))}; }
    gwait<2>(sm,C_PR,PRN);
    { u32x4 rs=mkrs(ws+W_PART+(xg*9+c9)*4096,4096), ry=mkrs(ws+W_Y,D_*YST);
      int yo=c*YST+(t&~1)*2, po=ln*16; u32 a[4], t0,cnt,yb; u16* op=out+t*D_+c; u32 ysl=0x3f80u<<((t&1)*16); PIN(ysl); f32x2 pe={pr[1]*-1.4426950408889634f,pr[3]*-1.4426950408889634f};
      #define LD_P "buffer_load_dwordx4 v[112:115], %[of], %[rs], 0 offen sc1\n\tbuffer_load_dwordx4 v[116:119], %[of], %[rs], 0 offen offset:1024 sc1\n\tbuffer_load_dwordx4 v[120:123], %[of], %[rs], 0 offen offset:2048 sc1\n\tbuffer_load_dwordx4 v[124:127], %[of], %[rs], 0 offen offset:3072 sc1\n\t"
      #define LD_YB "buffer_load_dword %[yb], %[yo], %[ry], 0 offen sc1\n\t"
      #define AD4(o,a,b,c,d) "v_add_f32 %[t0], v" #a ", v" #b "\n\tv_add_f32 %[" #o "], v" #c ", v" #d "\n\tv_add_f32 %[" #o "], %[t0], %[" #o "]\n\t"
      u64 ym;
      #define PGAP "s_sleep " STR(PSL) "\n\t"
      u64 nm;
      asm volatile(LD_YB PGAP LD_P "s_mov_b32 %[cnt], 0x20000\n1:\n\ts_waitcnt vmcnt(0)\n\t" AD4(a0,112,116,120,124) AD4(a1,113,117,121,125) "v_cmp_eq_u32 %[ym], -1, %[yb]\n\t" AD4(a2,114,118,122,126) "v_cmp_u_f32 %[nm], %[a0], %[a1]\n\t" AD4(a3,115,119,123,127)
        "s_or_b64 %[nm], %[nm], %[ym]\n\tv_cmp_u_f32 vcc, %[a2], %[a3]\n\ts_or_b64 vcc, vcc, %[nm]\n\ts_cbranch_scc0 9f\n\t"
        CHK_I0 MX2(116,117) MX2(118,119) MX2(120,121) MX2(122,123) MX2(124,125) MX2(126,127) CHK_IE
        "s_or_b64 exec, vcc, %[ym]\n\ts_cbranch_scc0 8f\n\ts_sleep " STR(QSLP) "\n\ts_mov_b64 exec, %[ym]\n\ts_cbranch_execz 2f\n\t" LD_YB "2:\n\ts_mov_b64 exec, vcc\n\ts_cbranch_execz 3f\n\t" LD_P
        "3:\n\ts_mov_b64 exec, -1\n\ts_sub_u32 %[cnt], %[cnt], 1\n\ts_cbranch_scc0 1b\n8:\n\ts_mov_b64 exec, -1\n9:"
        : [t0]"=&v"(t0),[cnt]"=&s"(cnt),[a0]"=&v"(a[0]),[a1]"=&v"(a[1]),[a2]"=&v"(a[2]),[a3]"=&v"(a[3]),[yb]"=&v"(yb),[ym]"=&s"(ym),[nm]"=&s"(nm) : [of]"v"(po),[rs]"s"(rs),[yo]"v"(yo),[ry]"s"(ry),"v"(op),"v"(pr),"v"(pe) : PCLOB);
      auto s0=__builtin_amdgcn_permlane32_swap(a[0],a[2],false,false); auto s1=__builtin_amdgcn_permlane32_swap(a[1],a[3],false,false);
      u32 x0=s0[0],x1=s0[1],y0=s1[0],y1=s1[1]; u32 p0=BC(u32,BC(float,x0)+BC(float,x1)), p1=BC(u32,BC(float,y0)+BC(float,y1));
      auto s2=__builtin_amdgcn_permlane16_swap(p0,p1,false,false); u32 z0=s2[0], z1=s2[1]; float s=BC(float,z0)+BC(float,z1);
      float gq=__builtin_fmaf(s,pr[3],pr[1])*rcpf_(1.f+__builtin_amdgcn_exp2f(__builtin_fmaf(s,pe[1],pe[0])));
      float sy=D2(yb,ysl,pr[2]);
      float o=__builtin_fmaf(sy,gq,pr[0]);
      *op=(u16)(pk(o,o)&0xffffu);
      if(bid==112&&ln==0) GST(ws+W_EPOCH,tag); }
    }
  } else
  {
    FLANE(D_*2)
    bf16x8 fr[27];
    rsrc_t rsP=RSRC(ws+W_PART+cgp*12288+kq*256+PWB,8*9*4096-2*12288);
    #pragma unroll
    for(int i=0;i<3;i++) BST128(I4,rsP,li<8? PLO+g*16+i*4096 : OOR,0,SC1);
    if(PRE2<9) FILLD(9-PRE2,W2_,W2B,"");
    wso+=128u-9u*wst;
    FILLD(9,W2_,W2B,"");
    VMW(9); gsync(sm,C_P,4);
    #pragma unroll
    for(int ks=0;ks<9;ks++) fr[ks]=WFR(0,ks);
    gsync(sm,C_P,8);
    if(w4==0&&ln==0) GST(ws+W_FL+(720+6*xg+lb-8)*8,tag);
    wso+=128u-9u*wst; wld-=2*CHSZ;
    FILL(9,VW(W2B));
    int fo=W_FL+(576+((9*kq)&~1)+(ln<9? ln:9))*8;
    if(w4==0) asm volatile("global_load_dwordx2 v[112:113], %0, %1 sc1\n\ts_waitcnt vmcnt(10)" :: "v"(fo),"s"(ws) : "memory"); else VMW(9);
    gsync(sm,C_P,12);
    #pragma unroll
    for(int ks=0;ks<9;ks++) fr[9+ks]=WFR(1,ks);
    { int t2=TID; asm volatile("" : "+v"(t2)); ln=t2&63; li=ln&15; g=ln>>4; }
    const lch* hp=sm+O_TILE+(li&7)*HT+g*16; int po= li<8? PLO+g*16+4096 : OOR; PIN(hp); PIN(po);
    u32x4 rs=mkrs(ws+W_GU,2*8*F_*2);
    int of=((ln>>5)*288+18*kq+2*w4)*256+(ln&31)*16; lch* hd=sm+O_TILE+w4*(8*HT)+((ln>>1)&7)*HT+((ln>>4)&1)*32+(ln&1)*16+(ln>>5)*8; PIN(of); PIN(hd); asm volatile("" : "+s"(rs));
    u32x4 rp=mkrs(ws+W_PART+cgp*12288+kq*256+PWB-4096,8*9*4096); u32 ch=C_H*4, one=1; int o3= w4==0? of+4096:OOR; PIN(ch); PIN(one); PIN(o3); asm volatile("" : "+s"(rp));
    DRAIN();
    if(w4==0){ u64 bad; asm volatile("v_cmp_ne_u64 %0, %1, v[112:113]" : "=s"(bad) : "s"(tag) : "memory");
      if(bad!=0ull) poll1<0>(ws+fo,tag); }
    __builtin_amdgcn_s_waitcnt(0x0f70);
    gsync(sm,C_P,16);
    { u32 t0,t1,cnt;
      #define LD_G "buffer_load_dwordx4 v[112:115], %[of], %[rs], 0 offen sc1\n\tbuffer_load_dwordx4 v[116:119], %[of], %[rs], 0 offen offset:2048 sc1\n\tbuffer_load_dwordx4 v[120:123], %[o3], %[rs], 0 offen sc1\n\t"
      #define HM1(a,b) "v_and_b32 %[t0], %[lm], v" #b "\n\tv_and_b32 v" #b ", %[hm], v" #b "\n\tv_dot2_f32_bf16 %[t0], v" #a ", %[t0], 0\n\tv_dot2_f32_bf16 v" #a ", v" #a ", v" #b ", 0\n\t"
      #define HM2(a,b) "v_and_b32 %[t1], %[lm], v" #b "\n\tv_and_b32 v" #b ", %[hm], v" #b "\n\tv_dot2_f32_bf16 %[t1], v" #a ", %[t1], 0\n\tv_dot2_f32_bf16 v" #a ", v" #a ", v" #b ", 0\n\t"
      #define HMUL(a,b,c,d,o) "v_permlane32_swap_b32 v" #a ", v" #c "\n\tv_permlane32_swap_b32 v" #b ", v" #d "\n\t" HM1(a,c) HM2(b,d) "v_cvt_pk_bf16_f32 v" #a ", %[t0], v" #a "\n\ts_nop 1\n\tv_cvt_pk_bf16_f32 v" #b ", %[t1], v" #b "\n\tds_write_b64 %[hd], v[" #a ":" #b "] offset:" STR(o) "\n\t"
      if(GD) __builtin_amdgcn_s_sleep(GD);
      asm volatile(LD_G :: [of]"v"(of),[rs]"s"(rs),[o3]"v"(o3) : "memory");
      #pragma unroll
      for(int ks=0;ks<G2;ks++) fr[18+ks]=WFR(0,ks);
      #pragma unroll
      for(int ks=0;ks<G2;ks++) PIN(fr[18+ks]);
      asm volatile(POLLASM(LD_G,CHK_I0 MX2(116,117) MX2(118,119) MX2(120,121) MX2(122,123) CHK_IE,GSLP)
        HMUL(112,113,114,115,0) HMUL(116,117,118,119,32*HT) "s_cmp_lg_u32 %[w4], 0\n\ts_cbranch_scc1 8f\n\t" HMUL(120,121,122,123,64*HT) "8:\n\ts_mov_b64 exec, 1\n\tds_add_u32 %[ch], %[one]\n\ts_mov_b64 exec, -1\n\t"
        "s_mov_b32 %[cnt], 0x20000\n6:\n\tds_read_b32 %[t0], %[ch]\n\tds_read_b128 v[112:115], %[hp]\n\tds_read_b128 v[116:119], %[hp] offset:8*" STR(HT) "\n\tds_read_b128 v[120:123], %[hp] offset:16*" STR(HT) "\n\tds_read_b128 v[124:127], %[hp] offset:24*" STR(HT) "\n\ts_waitcnt lgkmcnt(4)\n\t"
        "v_cmp_gt_u32 vcc, 4, %[t0]\n\ts_cbranch_vccz 7f\n\ts_sleep 1\n\ts_sub_u32 %[cnt], %[cnt], 1\n\ts_cbranch_scc0 6b\n7:"
        : [t0]"=&v"(t0),[t1]"=&v"(t1),[cnt]"=&s"(cnt) : [of]"v"(of),[rs]"s"(rs),[o3]"v"(o3),[w4]"s"(w4),[hd]"v"(hd),[hm]"s"(0xffff0000u),[lm]"s"(0xffffu),[ch]"v"(ch),[one]"v"(one),[hp]"v"(hp),"v"(po) : PCLOB); }
    { bf16x8 hb[9];
      #define MF "v_mfma_f32_16x16x32_bf16 "
      #define MF3(h) MF "%0, %3, " h ", %0\n\t" MF "%1, %4, " h ", %1\n\t" MF "%2, %5, " h ", %2"
      #define ACC "+v"(fr[0]),"+v"(fr[9]),"+v"(fr[18])
      #define FRS(k) "v"(fr[k]),"v"(fr[9+(k)]),"v"(fr[18+(k)])
      asm volatile("s_waitcnt lgkmcnt(3)\n\t" MF "%0, %0, v[112:115], 0\n\t" MF "%1, %1, v[112:115], 0\n\t" MF "%2, %2, v[112:115], 0\n\ts_waitcnt lgkmcnt(0)\n\t" MF3("v[116:119]") : ACC : FRS(1));
      hb[4]=LD(bf16x8,hp+4*8*HT); hb[5]=LD(bf16x8,hp+5*8*HT); hb[6]=LD(bf16x8,hp+6*8*HT); SCHED();
      asm volatile(MF3("v[120:123]") : ACC : FRS(2));
      #pragma unroll
      for(int ks=G2;ks<9;ks++) fr[18+ks]=WFR(0,ks);
      SCHED();
      asm volatile(MF3("v[124:127]") : ACC : FRS(3));
      hb[7]=LD(bf16x8,hp+7*8*HT); hb[8]=LD(bf16x8,hp+8*8*HT); SCHED();
      #pragma unroll
      for(int ks=4;ks<8;ks++){ asm volatile(MF3("%6") : ACC : FRS(ks),"v"(hb[ks])); SCHED(); }
      asm volatile(MF3("%6") "\n\ts_nop 5\n\tbuffer_store_dwordx4 %0, %7, %8, 0 offen sc1\n\tbuffer_store_dwordx4 %1, %7, %8, %9 offen sc1\n\tbuffer_store_dwordx4 %2, %7, %8, %10 offen sc1\n\ts_mov_b64 exec, 1\n\tds_add_u32 %11, %12\n\ts_mov_b64 exec, -1"
        : ACC : FRS(8),"v"(hb[8]),"v"(po),"s"(rp),"s"(4096),"s"(8192),"v"(ch),"v"(one) : "memory"); }

  }
  asm volatile("s_endpgm" : KILLIN :: "memory"); }
  {
  if(__builtin_expect(q4!=0,0)){ int grp= q4==1? nch-1 : q4-2;
    chan(sm,grp,(w4+(0x48>>(2*q4)))&3,72*xg+(q4==3? 16+lb : 2*lb+(lb<8? 0:8)+2-q4),(q4+1)>>2,ws,xa,xb_,wa,wb,dq[0]);
    asm volatile("s_endpgm" : KILLIN :: "memory"); }
  u32x4 xq[3]; f32x2 rc; int xr_,xr2_;
  {
    LANE(ln); int g=ln>>4, li=ln&15, qq=li>>2, pq=li&3, j=ln&31, rl=2*w4+(ln>>5);
    lch* tile=sm+O_TILE; u32 Tr=(ln>>1)&16;
    int uo=W_U+(tb>>TL)*GST_+(tb&((1<<TL)-1))*(128>>TL)+(w4*1024<<TL)+(ln>>(3-TL))*128+(ln&(7>>TL))*16, uo1=uo+(4096<<TL), uo2=uo+(8192<<TL); PIN(uo); PIN(uo1); PIN(uo2);
    lch* tw=tile+rl*TSTR2+j*16; lch* tw2=tw+1024-j*12; u32 one=0x3f803f80u; const lch* tp=tile+qq*TSTR2+(w4*64+g*16+pq*4)*2;
    int go= ln<32? W_RG+(ln>>2)*8192+(tb*4+(ln&3))*8 : W_ST+(tb*8+(ln&7))*8; lch* kw=sm+O_RV+rl*2; const lch* kr=sm+(ln<32? O_RV+(ln&3)*4 : O_MD+(ln&7)*4); PIN(kw); PIN(kr);
    int xr=(32*(tb>>2)+8*g+(li&7))*(D_*2)+288*(lb&3)+32*w4+(ln&8)*2, xr2= w4==0? xr+256:OOR;
    rsrc_t rx=RSRC(x,L_*D_*2); u64 gm=((1ull<<(4*NRG))-1ull)|0xff00000000ull; u32 um=~UMASK, km=0x80008000u; asm volatile("" : "+s"(gm),"+s"(um),"+s"(km),"+s"(ws));
    asm volatile("s_waitcnt vmcnt(3)" : "+v"(wa),"+v"(wb),"+v"(wc),"+v"(Tr),"+v"(tw),"+v"(tw2),"+v"(tp),"+v"(one),"+v"(go),"+v"(xr),"+v"(xr2) :: "memory");
    u32 wq[9]={wa[0],wa[1],wa[2],wa[3],wb[0],wb[1],wb[2],wb[3],wc}; float sw;
    { float a[3];
      #pragma unroll
      for(int i=0;i<9;i++) a[i%3]=D2(wq[i],one,i<3? 0.f:a[i%3]);
      sw=add16x(sum16((a[0]+a[1])+a[2])); }
    GETEP u32 tg=(u32)ep+1u; Tr=((upat(tg,w4)>>Tr)&0xffffu)*0x10001u; u32 tgv=tg;
    float z_[9]={0,0,0,0,0,0,0,0,0};
    asm volatile("s_waitcnt vmcnt(0)" : "+v"(xa),"+v"(xb_),"+v"(xc),"+v"(Tr),"+v"(sw),"+v"(tgv),"+v"(z_[0]),"+v"(z_[1]),"+v"(z_[2]),"+v"(z_[3]),"+v"(z_[4]),"+v"(z_[5]) :: "memory");
    u32 v[9]={xa[0],xa[1],xa[2],xa[3],xb_[0],xb_[1],xb_[2],xb_[3],xc};
    { float xf[18], s1, s2, s3;
      #pragma unroll
      for(int i=0;i<9;i++){ z_[i&1]=D2(v[i],one,z_[i&1]); z_[2+(i&1)]=D2(v[i],v[i],z_[2+(i&1)]); z_[4+(i&1)]=D2(v[i],wq[i],z_[4+(i&1)]); }
      s1=z_[0]+z_[1]; s2=z_[2]+z_[3]; s3=z_[4]+z_[5];
      SCHED();
      #define RSTEP(C) s1+=dppf<C>(s1); s2+=dppf<C>(s2); s3+=dppf<C>(s3); SCHED();
      RSTEP(0xB1) RSTEP(0x4E) RSTEP(0x141) RSTEP(0x140)
      s1=add16x(s1)*(1.f/576.f); s2=add16x(s2)*(1.f/576.f); s3=add16x(s3);
      float mm=s1*s1, var=s2-mm;
      if(__builtin_expect(BALLOT(!(mm<=64.f*var))!=0ull,0)){
        float q=0.f;
        #pragma unroll
        for(int i=0;i<9;i++){ u32 t=v[i]; PIN(t); float d0=blo(t)-s1, d1=bhi(t)-s1; q=__builtin_fmaf(d0,d0,q); q=__builtin_fmaf(d1,d1,q); }
        var=add16x(sum16(q))*(1.f/576.f); }
      #define UNP(i) PIN(v[i]); xf[2*(i)]=blo(v[i]); xf[2*(i)+1]=bhi(v[i]); PIN(xf[2*(i)]); PIN(xf[2*(i)+1]); SCHED();
      u32 vb=pk(var,s1); UNP(0)
      float den=blo(vb)+1e-5f, m=bhi(vb); UNP(1)
      vb=pk(den,den); s3=__builtin_fmaf(-m,sw,s3); UNP(2)
      den=__builtin_amdgcn_sqrtf(bhi(vb)); UNP(3)
      u32 md=pk(m,den); UNP(4)
      float ri=rcpf_(bhi(md)); UNP(5)
      s3*=ri; float rs_=ri*0.0625f; UNP(6)
      u32 u=pk(s3,s3); float cs_=-m*rs_; UNP(7)
      { u32 sg=(u32)((int)u>>31); PIN(sg); u^=sg|km; } UNP(8)
      asm volatile("s_mov_b64 exec, 0x1\n\ts_mov_b32 exec_hi, 1\n\tds_write_b16 %0, %1\n\tv_add_u32 %0, %0, %0\n\tds_write_b32 %0, %2 offset:%3\n\ts_mov_b32 exec_hi, 0\n\tds_add_u32 %4, %5\n\ts_mov_b64 exec, -1" : "+v"(kw) : "v"(u),"v"(md),"i"(O_MD-2*O_RV),"v"(C_I*4),"v"(1) : "memory");
      if(w4==3){ gwait<0>(sm,C_I,4); asm volatile("ds_read_b32 v118, %4\n\tv_mov_b32 v119, %1\n\ts_mov_b64 exec, %3\n\ts_waitcnt lgkmcnt(0)\n\tglobal_store_dwordx2 %0, v[118:119], %2 sc1\n\ts_mov_b64 exec, -1" :: "v"(go),"v"(tgv),"s"(ws),"s"(gm),"v"(kr) : "memory"); }
      SCHED();
      u32 h[9];
      #define UH(i) h[i]=(pkz(__builtin_fmaf(xf[2*(i)],rs_,cs_),__builtin_fmaf(xf[2*(i)+1],rs_,cs_))&um)|Tr;
      UH(0) UH(1) UH(2) UH(3) ST(u32x4,tw)=u32x4{h[0],h[1],h[2],h[3]}; UH(4) UH(5) UH(6) UH(7) ST(u32x4,tw+512)=u32x4{h[4],h[5],h[6],h[7]};
      UH(8) ST(u32,tw2)=h[8]; }
    gsync(sm,C_L,4);
    { u32x4 uq[3];
      #pragma unroll
      for(int z=0;z<3;z++) uq[z]=BC(u32x4,trfrag(tp+z*512,tp+z*512+4*TSTR2));
      asm volatile("global_store_dwordx4 %0, %1, %2 sc1" :: "v"(uo),"v"(uq[0]),"s"(ws) : "memory");
      asm volatile("global_store_dwordx4 %0, %1, %2 sc1" :: "v"(uo1),"v"(uq[1]),"s"(ws) : "memory");
      asm volatile("s_cmp_lg_u32 %3, 0\n\ts_cbranch_scc1 1f\n\tglobal_store_dwordx4 %0, %1, %2 sc1\n1:\n\ts_nop 1" :: "v"(uo2),"v"(uq[2]),"s"(ws),"s"(w4),"v"(uq[0]),"v"(uq[1]) : "memory","scc");
      xr_=xr; xr2_=xr2;
      #define XQ { rsrc_t rx_=RSRC(x,L_*D_*2); xq[0]=BLD128(rx_,xr_,0,XAUX); xq[1]=BLD128(rx_,xr_+128,0,XAUX); xq[2]=BLD128(rx_,xr2_,0,XAUX); }
    }
    if(lb>=2&&lb<8){
      const u16* wm=(lb&1)? wu:wg; int co=(3-(int)((u64)wm>>7)-xg)&7, wi=((lb>>1)-1)*4+w4, r_=576-12*PFN+wi+12*j;
      int po= j<PFN? r_*(F_*2)+(9*xg+co)*128+(ln>>5)*1024 : OOR;
      if(ln>=32&&co!=0){ int c8=j&7; po= PX? (575-wi*4-(j>>3))*(F_*2)+(9*xg+c8+(c8>=co))*128 : OOR; }
      asm volatile("buffer_load_dword v120, %0, %1, 0 offen" PPOL :: "v"(po),"s"(mkrs(wm,D_*F_*2)) : "memory");
      if(PX>1){ int c8=ln&7; asm volatile("buffer_load_dword v120, %0, %1, 0 offen" PPOL :: "v"(ln<PX? (527-wi-12*(ln>>3))*(F_*2)+(9*xg+c8+(c8>=co))*128 : OOR),"s"(mkrs(wm,D_*F_*2)) : "memory"); } }
    if(lb>=8&&lb<14) garrive(sm,C_D);
  }
  FP fp; int trow;
  { LANE(ln); int g=ln>>4, li=ln&15; u64 tag=ep+1ull;
    const char* sp_=ws+W_ST+(32*(tb>>2)+8*g+(li&7))*8; u64 fs, ff; const char* fp_=ws+W_FL+(144*(lb&3)+16*w4+64*(g<2||w4==0? (g<3? g:2):0)+li)*8;
    if(lb==CTLB||lb>=14){
      u32 M=(u32)tag, rank,key; u32x4 rx=mkrs(x,L_*D_*2);
      int j=ln&31, rr=2*w4+(ln>>5), o16=2047*1152+j*16, o4=2047*1152+j*4, so=W_ST+2047*8, sh=(ln>>5)*16, m9=-1152, r0=2*w4, r1=2*w4+1;
      lch *xw=sm+O_TILE+XAD(rr,j*16), *xw1=sm+O_TILE+XAD(rr,j*16+512), *xw2=sm+O_TILE+XAD(rr,j*4+1024), *rw=sm+O_RC+rr*4; u32 zero=0, one=1;
      PIN(o16); PIN(o4); PIN(so); PIN(sh); PIN(xw); PIN(xw1); PIN(xw2); PIN(rw); PIN(zero); PIN(one); asm volatile("" : "+s"(rx),"+s"(m9),"+s"(r0),"+s"(r1));
      top8(sm,w4,ln,M,ws,rank,key);
      {
        u64 q; u32 a,b;
        asm volatile("v_cmp_eq_u32 vcc, %[r0], %[rk]\n\tv_cmp_eq_u32 %[q], %[r1], %[rk]\n\ts_ff1_i32_b64 %[a], vcc\n\ts_ff1_i32_b64 %[b], %[q]\n\tv_readlane_b32 %[a], %[key], %[a]\n\tv_readlane_b32 %[b], %[key], %[b]\n\ts_pack_ll_b32_b16 %[a], %[a], %[b]\n\t"
          "v_bfe_u32 %[sh], %[a], %[sh], 11\n\tv_mad_i32_i24 %[o16], %[sh], %[m9], %[o16]\n\tv_mad_i32_i24 %[o4], %[sh], %[m9], %[o4]\n\t"
          "buffer_load_dwordx4 v[112:115], %[o16], %[rx], 0 offen\n\tv_mad_i32_i24 %[so], %[sh], -8, %[so]\n\tbuffer_load_dwordx4 v[116:119], %[o16], %[rx], 0 offen offset:512\n\tbuffer_load_dword v120, %[o4], %[rx], 0 offen offset:1024\n\tglobal_load_dwordx2 v[122:123], %[so], %[ws] sc1\n\t"
          "s_mov_b64 exec, 1\n\tds_add_u32 %[z], %[one] offset:%[cd]\n\ts_mov_b64 exec, -1"
          : [q]"=&s"(q),[a]"=&s"(a),[b]"=&s"(b),[sh]"+v"(sh),[o16]"+v"(o16),[o4]"+v"(o4),[so]"+v"(so) : [r0]"s"(r0),[r1]"s"(r1),[rk]"v"(rank),[key]"v"(key),[m9]"s"(m9),[rx]"s"(rx),[ws]"s"(ws),[z]"v"(zero),[one]"v"(one),[cd]"i"(C_D*4) : PCLOB);
        if(lb==CTLB&&rank<8&&!(ln&1)) GST(ws+W_IDX+xg*1024+w4*128+rank*8,(tag<<16)|(~key&2047u));
        if(w4==0){ if(rank<8) ST(u16,sm+O_IDX+(ln&1)*16+rank*2)=(u16)((ln&1)? key>>16 : ~key&2047u); garrive(sm,C_IX); }
        u64 bad; asm volatile("s_waitcnt vmcnt(2)\n\tds_write_b128 %2, v[112:115]\n\ts_waitcnt vmcnt(0)\n\tds_write_b128 %3, v[116:119]\n\tv_cmp_ne_u32 %0, %1, v123\n\tds_write_b32 %4, v120" : "=s"(bad) : "s"(M),"v"(xw),"v"(xw1),"v"(xw2) : "memory");
        if(__builtin_expect(bad!=0ull,0)){ u32 f1=(u32)poll1<32>(ws+so,tag<<32); asm volatile("v_mov_b32 v122, %0" :: "v"(f1) : "memory"); }
        asm volatile("ds_write_b32 %0, v122\n\ts_mov_b64 exec, 1\n\tds_add_u32 %1, %2 offset:%3\n\ts_mov_b64 exec, -1" :: "v"(rw),"v"(zero),"v"(one),"i"(C_X*4) : "memory");
      }
    }
    PRIO(0);
    if(lb<14) gwait<8>(sm,C_U,8); else gwait<HSL>(sm,C_H,HN);
    fs=GLD(sp_); ff=GLD(fp_); XQ
    { rsrc_t rd=RSRC(Dsk,D_*2); int do_=288*(lb&3)+32*w4+(ln&8)*2; dq[0]=BLD128(rd,do_,0,0); dq[1]=BLD128(rd,do_+128,0,0); dq[2]=BLD128(rd,do_+256,0,0); }
    final_adr(sm,fp,w4); trow=32*(tb>>2)+8*g+(li&7); PIN(trow);
    {
      if(BALLOT((u32)(fs>>32)!=(u32)tag)!=0ull) fs=poll1<32>(sp_,tag<<32);
      float ri=rcpf_(bhi((u32)fs)); rc=f32x2{ri,-blo((u32)fs)*ri}; }
    asm volatile("" : "+v"(rc),"+v"(xq[0]),"+v"(xq[1]),"+v"(xq[2]),"+v"(dq[0]),"+v"(dq[1]),"+v"(dq[2]));
    bool ix= w4==3&&lb<14&&lb!=CTLB; u64 f; const char* p=ws+W_IDX+(xg*8+(lb&3))*128+(ln&7)*8; if(ix) f=GLD(p);
    final_blk(fp,0,xq,dq,rc); if(FS>=2) final_blk(fp,1,xq,dq,rc); if(FS>=3&&w4==0) final_blk(fp,2,xq,dq,rc);
    if(BALLOT(ff!=tag)!=0ull) poll1<0>(fp_,tag);
    if(ix){ if(BALLOT(((f^(tag<<16))>>16)!=0ull)!=0ull) f=poll1<16>(p,tag<<16);
      if(ln<8) ST(u16,sm+O_IDX+ln*2)=(u16)f; garrive(sm,C_IX); }
  }
  final_post(sm,fp,w4,nch,xr_,trow,xq,dq,rc,out,ws);
  }
}

extern "C" void kernel_launch(void* const* d_in, const int* in_sizes, int n_in, void* d_out, int out_size, void* d_ws, size_t ws_size, hipStream_t stream) {
  const u16** I=(const u16**)d_in;
  KP kp; kp.Are=I[1]; kp.Aim=I[2]; kp.Bre=I[3]; kp.Bim=I[4]; kp.Cre=I[5]; kp.Cim=I[6]; kp.ldt=I[7]; kp.pad=I[7];
  kA<<<256,1024,0,stream>>>(I[0],(char*)d_ws+WSOFF,I[10],I[8],I[11],I[12],I[13],(u16*)d_out,kp);
}
```

```cpp
#include <hip/hip_runtime.h>
#include <hip/hip_bf16.h>
#include <stdint.h>
#if defined(__HIP_DEVICE_COMPILE__)
#define NOPK __attribute__((target("no-packed-fp32-ops")))
#else
#define NOPK
#endif
#define DEV __device__ NOPK __forceinline__
typedef unsigned short u16;
typedef unsigned int u32;
typedef unsigned long long u64;
typedef short bf16x8 __attribute__((ext_vector_type(8)));
typedef _Float16 h8 __attribute__((ext_vector_type(8)));
typedef _Float16 h2 __attribute__((ext_vector_type(2)));
typedef float f32x4 __attribute__((ext_vector_type(4)));
typedef float f32x2 __attribute__((ext_vector_type(2)));
typedef u32 u32x4 __attribute__((ext_vector_type(4)));
typedef u32 u32x2 __attribute__((ext_vector_type(2)));
typedef __bf16 bf2 __attribute__((ext_vector_type(2)));
typedef short v4s __attribute__((ext_vector_type(4)));
typedef __amdgpu_buffer_rsrc_t rsrc_t;
#define GAS __attribute__((address_space(1)))
#define LAS __attribute__((address_space(3)))
typedef LAS char lch;
#define L_ 2048
#define D_ 576
#define F_ 4608
#define BC(T,v) __builtin_bit_cast(T,v)
#define TID ((int)__builtin_amdgcn_workitem_id_x())
#define BID ((int)__builtin_amdgcn_workgroup_id_x())
#define RFL(x) __builtin_amdgcn_readfirstlane(x)
#define RLX_AGENT __ATOMIC_RELAXED, __HIP_MEMORY_SCOPE_AGENT
#define LD(T,p) (*(const LAS T*)(p))
#define ST(T,p) (*(LAS T*)(p))
#define SPINCAP (1<<17)
#define DRAIN() asm volatile("s_waitcnt vmcnt(0)" ::: "memory")
#define GLD(p) __hip_atomic_load((const GAS u64*)(p),RLX_AGENT)
#define GST(p,v) __hip_atomic_store((GAS u64*)(p),(u64)(v),RLX_AGENT)
#define BALLOT(c) __builtin_amdgcn_ballot_w64(c)
#define SC1 16
#define LANE(l) int l; { int t2_=TID; asm volatile("" : "+v"(t2_)); l=t2_&63; }
#define SCHED() __builtin_amdgcn_sched_barrier(0)
#define RSRC(p,n) __builtin_amdgcn_make_buffer_rsrc((void*)(p),0,(n),0x00020000)
#define BLD128 __builtin_amdgcn_raw_buffer_load_b128
#define BLD32 __builtin_amdgcn_raw_buffer_load_b32
#define BST128 __builtin_amdgcn_raw_buffer_store_b128
#define PRIO(n) __builtin_amdgcn_s_setprio(n)

DEV float b2f(u32 v){ return BC(float,v<<16); }
DEV float blo(u32 v){ return BC(float,v<<16); }
DEV float bhi(u32 v){ return BC(float,v&0xffff0000u); }
DEV u32 pk(float a,float b){ f32x2 v={a,b}; bf2 r=__builtin_convertvector(v,bf2); return BC(u32,r); }
DEV u32 pkh(float a,float b){ f32x2 v={a,b}; h2 r=__builtin_convertvector(v,h2); return BC(u32,r); }
DEV float rb(float f){ return BC(float,pk(f,f)<<16); }
DEV float rcpf_(float x){ return __builtin_amdgcn_rcpf(x); }
DEV float expf_(float x){
  const float L2E=1.4426950216293335f, L2El=1.9259629911266175e-8f; float h=x*L2E; float l=__builtin_fmaf(x,L2E,-h)+x*L2El;
  float e=__builtin_amdgcn_exp2f(h); return __builtin_fmaf(e*l,0.6931471805599453f,e); }
DEV float sigm_f(float x){ return rcpf_(1.f+__builtin_amdgcn_exp2f(-x*1.4426950408889634f)); }
DEV float silu_f(float x){ return x*sigm_f(x); }
DEV u32 umax_(u32 a,u32 b){ return a>b?a:b; }
DEV u32 umin_(u32 a,u32 b){ return a<b?a:b; }

#define WSOFF 0
#define G2 7
#define PSL 8
#define NA 5
#define NB 11
#define IVS "global_store_dwordx4 v23, v[124:127], s[66:67] sc1\n\t"
#define EB0 "s_barrier\n\t"
#define EB1
#define IV0 IVS
#define IV1
#define GD 3
#define HG -1
#define UD_ -1
#define UDB 64
#define PA -1
#define AD 14
#define BD 0
#define PX 0
#define XAUX 0
#define OAUX 0
#define HSL 4
#define HN 4
#define PPOL ""
#define WPOL " nt"
#define QPOL " nt"
#define UE 0
#define DN 1
#define RPH 1
#define PK 0x112
#define PS 0x030
#define RD 0
#define PD 0
#define P12 64
#define ESL 4
#define PRN 1
#define CHN 8
#define CSL 2
#define FS 3
#define PWB (w4>>1)*(9*4096)+(w4&1)*128
#define PLO (li>>1)*(18*4096)+(li&1)*64
#define YSH 3
#define PRE1 6
#define PRE3 4
#define PRE2 5
#define W1 5
#define W3 3
#define W2_ 4
#define W1B 20
#define W2B 12
#define LAST1 3
#define RGATE 1
#define UGATE 7
#define R2D 12
#define PFN 32
#define CPF 8
#define NRG 8
#define W_EPOCH 0
#define W_RG 16384
#define W_ST (W_RG+32*8192)
#define W_U (W_ST+32768)
#define UST (D_*16+128)
#define YST (L_*2+128)
#define TL 1
#define GST_ ((D_*16<<TL)+128)
#define W_Y (W_U+256*UST)
#define W_PART (W_Y+D_*YST)
#define W_GU (W_PART+8*9*16*256)
#define W_FL 9216
#define W_IDX 1024
#define CTLB 0
struct KP { const u16 *Are,*Aim,*Bre,*Bim,*Cre,*Cim,*ldt,*pad; };

template<int CTRL> DEV float dppf(float v){ return BC(float,__builtin_amdgcn_update_dpp(0u,BC(u32,v),CTRL,0xf,0xf,false)); }
template<int CTRL> DEV u32 dppu(u32 v){ return (u32)__builtin_amdgcn_update_dpp(0u,v,CTRL,0xf,0xf,false); }
DEV float sum16(float v){ v+=dppf<0xB1>(v); v+=dppf<0x4E>(v); v+=dppf<0x141>(v); v+=dppf<0x140>(v); return v; }
DEV float add16x(float v){ auto r=__builtin_amdgcn_permlane16_swap(BC(u32,v),BC(u32,v),false,false); u32 a=r[0], b=r[1]; return BC(float,a)+BC(float,b); }
DEV u32 wmax(u32 v){
  v=umax_(v,(u32)__builtin_amdgcn_update_dpp(0u,v,0x111,0xf,0xf,false));
  v=umax_(v,(u32)__builtin_amdgcn_update_dpp(0u,v,0x112,0xf,0xf,false));
  v=umax_(v,(u32)__builtin_amdgcn_update_dpp(0u,v,0x114,0xf,0xf,false));
  v=umax_(v,(u32)__builtin_amdgcn_update_dpp(0u,v,0x118,0xf,0xf,false));
  v=umax_(v,(u32)__builtin_amdgcn_update_dpp(0u,v,0x142,0xa,0xf,false));
  v=umax_(v,(u32)__builtin_amdgcn_update_dpp(0u,v,0x143,0xc,0xf,false));
  return (u32)__builtin_amdgcn_readlane((int)v,63); }

DEV void garrive(lch* sm,int c){ u32 ad=(u32)(size_t)sm+c*4, one=1;
  asm volatile("s_mov_b64 exec, 1\n\tds_add_u32 %0, %1\n\ts_mov_b64 exec, -1" :: "v"(ad),"v"(one) : "memory"); }
template<int SLP> DEV void gwait(lch* sm,int c,u32 target){ u32 ad=(u32)(size_t)sm+c*4, tv, cnt, sv; target=RFL(target);
  asm volatile("s_mov_b32 %1, 0x20000\n"
    "1:\n\tds_read_b32 %0, %3\n\ts_waitcnt lgkmcnt(0)\n\tv_readfirstlane_b32 %2, %0\n\ts_cmp_ge_u32 %2, %4\n\ts_cbranch_scc1 2f\n\ts_sleep %5\n\ts_sub_u32 %1, %1, 1\n\ts_cbranch_scc0 1b\n"
    "2:" : "=&v"(tv),"=&s"(cnt),"=&s"(sv) : "v"(ad),"s"(target),"i"(SLP) : "memory","scc"); }
template<int ST_,class T> DEV void gwait4(int c,u32 target,const lch* p,T& a,T& b,T& d,T& e){ u32 tv,cnt,sv;
  asm volatile("s_mov_b32 %1, 0x20000\n1:\n\tds_read_b32 %0, %7\n\tds_read_b128 %3, %8\n\tds_read_b128 %4, %8 offset:%10\n\tds_read_b128 %5, %8 offset:%11\n\tds_read_b128 %6, %8 offset:%12\n\ts_waitcnt lgkmcnt(4)\n\t"
    "v_readfirstlane_b32 %2, %0\n\ts_cmp_ge_u32 %2, %9\n\ts_cbranch_scc1 2f\n\ts_sleep 1\n\ts_sub_u32 %1, %1, 1\n\ts_cbranch_scc0 1b\n2:\n\ts_waitcnt lgkmcnt(0)"
    : "=&v"(tv),"=&s"(cnt),"=&s"(sv),"=&v"(a),"=&v"(b),"=&v"(d),"=&v"(e) : "v"(c*4),"v"(p),"i"(target),"i"(ST_),"i"(2*ST_),"i"(3*ST_) : "memory","scc"); }
DEV void gsync(lch* sm,int c,u32 target){ garrive(sm,c); gwait<0>(sm,c,target); }
template<int SH> DEV u64 poll1(const char* p,u64 want){ u64 f=GLD(p);
  _Pragma("unroll 1") for(int sp=0;sp<SPINCAP;sp++){ bool bad=((f^want)>>SH)!=0ull; if(BALLOT(bad)==0ull) break; __builtin_amdgcn_s_sleep(1); if(bad) f=GLD(p); }
  __builtin_amdgcn_fence(__ATOMIC_ACQUIRE,"wavefront"); return f; }
#define STR_(x) #x
#define STR(x) STR_(x)
DEV u32x4 mkrs(const void* p,u32 n){ u64 a=(u64)p; return u32x4{(u32)RFL((int)(u32)a),(u32)RFL((int)((u32)(a>>32)&0xffffu)),n,0x00020000u}; }
#define VMW(n) asm volatile("s_waitcnt vmcnt(%0)" :: "i"(n) : "memory")
#define VW(n) "s_waitcnt vmcnt(" STR(n) ")\n\t"
#define YSLP 1
#define GSLP 1
#define QSLP 1
#define PSLP 3
#define POLLASM(LOADS,CHECK,K) "s_mov_b32 %[cnt], 0x20000\n1:\n\ts_waitcnt vmcnt(0)\n\t" CHECK "s_cbranch_vccz 9f\n\ts_sleep " STR(K) "\n\ts_mov_b64 exec, vcc\n\t" LOADS \
  "s_mov_b64 exec, -1\n\ts_sub_u32 %[cnt], %[cnt], 1\n\ts_cbranch_scc0 1b\n9:\n\t"
#define LD_DUP2 "buffer_load_dwordx4 v[112:115], %[of], %[rs], 0 offen sc1\n\tbuffer_load_dwordx4 v[116:119], %[of], %[rs], %[so] offen sc1\n\t"
#define MX2(a,b) "v_max3_u32 %[t0], %[t0], v" #a ", v" #b "\n\t"
#define CHK_I0 "v_max_u32 %[t0], v112, v113\n\t" MX2(114,115)
#define CHK_IE "v_cmp_eq_u32 vcc, -1, %[t0]\n\t"
#define PCLOB "memory","vcc","scc"
#define UMASK 0x40074007u
DEV u32 upat(u32 tag,int k){ return ((((tag*0xea959c21u)>>(8*k))&255u)*0x04000801u)&UMASK; }
DEV u32 pkz(float a,float b){ return BC(u32,__builtin_amdgcn_cvt_pkrtz(a,b)); }
#define INV 0xffffffffu
DEV u32 nv(u32 v){ return umin_(v,0xfffeffffu); }
DEV u32x4 nv4(u32x4 v){ return u32x4{nv(v[0]),nv(v[1]),nv(v[2]),nv(v[3])}; }
#define OOR 0x40000000

#define CHSZ 36864
#define O_CH 512
#define O_CND 256
#define SLSZ 38976
#define O_R2 (O_CH+2*SLSZ)
#define O_X2 (O_R2+CHSZ)
#define O_TILE (O_X2+CHSZ)
#define XSTR 1168
#define HSTR 608
#define HT 96
#define XAD(r,b) ((r)*HSTR+(b)+((b)>=576? 8*HSTR-576:0))
#define XS1 1
#define XG1 1
#define TSTR2 1216
#define O_CAND (O_TILE+9728+1024)
#define LDS_TOTAL (O_CAND+128)
#define C_G 0
#define C_U 8
#define C_L 4
#define C_D 5
#define C_X 12
#define C_P 13
#define C_H 14
#define C_I 17
#define C_E 18
#define C_IX 19
#define C_PR 21
#define C_W 22
#define C_WIN (lb>=14? C_W:C_D)
#define O_IDX 96
#define O_EXP 144
#define O_RMX 192
#define O_RV 128
#define O_MD 400
#define O_RC (O_CAND+64)

struct C2 { float r,i; };
DEV C2 cmul(C2 a,C2 b){ C2 c; c.r=__builtin_fmaf(a.r,b.r,-(a.i*b.i)); c.i=__builtin_fmaf(a.r,b.i,a.i*b.r); return c; }
DEV h8 ldA(const lch* p){ u32x2 a=*(const volatile LAS u32x2*)p, b=*(const volatile LAS u32x2*)(p+8); return BC(h8,(u32x4{a[0],a[1],b[0],b[1]})); }
DEV bf16x8 trfrag(const lch* p0,const lch* p1){
  u32x2 b0=BC(u32x2,__builtin_amdgcn_ds_read_tr16_b64_v4i16((LAS v4s*)p0)), b1=BC(u32x2,__builtin_amdgcn_ds_read_tr16_b64_v4i16((LAS v4s*)p1));
  return BC(bf16x8,(u32x4{b0[0],b0[1],b1[0],b1[1]})); }

struct FP { u32 gg[24]; float bs[24]; int yo,y3; lch* wp; const lch* tp; };
#define D2(a,b,c) __builtin_amdgcn_fdot2_f32_bf16(BC(bf2,a),BC(bf2,b),c,false)
#define ZCH 64
#define YZ 1280
#define YG1 320
#define YG2 672
DEV void final_blk(FP& f,int z,const u32x4* xq,const u32x4* dq,f32x2 rc){
  float ea=rc[0]*-1.4426950408889634f, eb=rc[1]*-1.4426950408889634f;
  #pragma unroll
  for(int e=0;e<4;e++){ u32 xw=xq[z][e], dw=dq[z][e]; int i=8*z+2*e; float a=blo(xw), b=bhi(xw);
    float pa=__builtin_fmaf(a,rc[0],rc[1]), pb=__builtin_fmaf(b,rc[0],rc[1]);
    float ga=pa*rcpf_(1.f+__builtin_amdgcn_exp2f(__builtin_fmaf(a,ea,eb))), gb=pb*rcpf_(1.f+__builtin_amdgcn_exp2f(__builtin_fmaf(b,ea,eb)));
    f.gg[i]=pk(ga,0.f); f.gg[i+1]=pk(0.f,gb);
    f.bs[i]=__builtin_fmaf(pa*blo(dw),ga,a); f.bs[i+1]=__builtin_fmaf(pb*bhi(dw),gb,b);
    asm volatile("" : "+v"(f.gg[i]),"+v"(f.gg[i+1]),"+v"(f.bs[i]),"+v"(f.bs[i+1])); }
}
DEV void final_adr(lch* sm,FP& f,int w4){
  LANE(ln); int bid=BID, xg=bid&7, lb=bid>>3, g=ln>>4, li=ln&15, qq=li>>2, pq=li&3;
  f.yo=(144*(lb&3)+16*w4+(ln>>2))*YST+(32*(8*xg+(lb>>2))+8*(ln&3))*2; f.y3= w4==0? f.yo+2*ZCH*YST:OOR;
  lch* fb=sm+O_CH+w4*(3*YZ); f.wp=fb+(ln&1)*YG1+(ln&2)*(YG2/2)+(ln>>2)*16; f.tp=fb+(g&1)*YG1+(g&2)*(YG2/2)+(8*(pq>>1)+qq)*16+(pq&1)*8;
  asm volatile("" : "+v"(f.yo),"+v"(f.y3),"+v"(f.wp),"+v"(f.tp));
}
DEV void final_post(lch* sm,FP& f,int w4,int nch,int ob,int trow,const u32x4* xq,const u32x4* dq,f32x2 rc,u16* __restrict__ out,char* ws){
  int lb=BID>>3;
  u32x4 rs=mkrs(ws+W_Y,D_*YST); rsrc_t ro=RSRC(out,L_*D_*2);
  gwait<ESL>(sm,C_E,4*nch);
  if(AD&&lb<8) __builtin_amdgcn_s_sleep(AD);
  if(BD&&lb>=14) __builtin_amdgcn_s_sleep(BD);
  if(lb>=8&&lb<14) gwait<CSL>(sm,C_H,CHN);
  PRIO(3);
  #define LD_Y "buffer_load_dwordx4 v[112:115], %[of], %[rs], 0 offen sc1\n\tbuffer_load_dwordx4 v[116:119], %[of], %[rs], %[s1] offen sc1\n\tbuffer_load_dwordx4 v[120:123], %[o3], %[rs], 0 offen sc1\n\t"
  asm volatile(LD_Y : "+v"(rc),"+v"(trow) : [of]"v"(f.yo),[rs]"s"(rs),[s1]"s"(ZCH*YST),[o3]"v"(f.y3) : "memory");
  garrive(sm,C_PR);
  if(FS<2) final_blk(f,1,xq,dq,rc);
  if(FS<3&&w4==0) final_blk(f,2,xq,dq,rc);
  { gwait<2>(sm,C_IX,1); u32x4 iq=LD(u32x4,sm+O_IDX); u32 tt=(u32)trow*0x10001u; bool routed=false;
    #pragma unroll
    for(int q=0;q<4;q++){ u32 z=iq[q]^tt; routed|=((z&0xffffu)==0u)|((z>>16)==0u); }
    ob=routed? OOR:ob; }
  { u32 t0,cnt;
    asm volatile(POLLASM(LD_Y,"ds_write_b128 %[wp], v[112:115]\n\tds_write_b128 %[wp], v[116:119] offset:" STR(YZ) "\n\ts_cmp_lg_u32 %[w4], 0\n\ts_cbranch_scc1 8f\n\tds_write_b128 %[wp], v[120:123] offset:2*" STR(YZ) "\n8:\n\t"
      CHK_I0 MX2(116,117) MX2(118,119) MX2(120,121) MX2(122,123) CHK_IE,YSLP)
      : [t0]"=&v"(t0),[cnt]"=&s"(cnt),"+v"(ob) : [of]"v"(f.yo),[rs]"s"(rs),[s1]"s"(ZCH*YST),[o3]"v"(f.y3),[wp]"v"(f.wp),[w4]"s"(w4),"s"(ro) : PCLOB); }
  u32x4 r[3];
  #pragma unroll
  for(int z=0;z<3;z++) r[z]=BC(u32x4,trfrag(f.tp+z*YZ,f.tp+z*YZ+64));
  #pragma unroll
  for(int z=0;z<3;z++) if(z<2||w4==0){ u32x4 ov;
    #pragma unroll
    for(int e=0;e<4;e++){ u32 rw=r[z][e]; int i=8*z+2*e;
      ov[e]=pk(D2(rw,f.gg[i],f.bs[i]),D2(rw,f.gg[i+1],f.bs[i+1]));
    }
    BST128(ov,ro,ob+z*(2*ZCH),0,OAUX); SCHED(); }
}

#define TROW 544
#define O_TB 4640
#define O_DE 35664
#define O_DO (O_DE+1232)
#define O_R2K (O_DO+1152)
#define O_R3K (O_R2K+512)
#define O_DUM (O_R3K+320)
#define O_RED O_TB
#define MH(a,b,c) __builtin_amdgcn_mfma_f32_16x16x32_f16(a,b,c,0,0,0)
#define PIN(x) asm volatile("" : "+v"(x))
DEV h8 ld4w(const lch* p){ return BC(h8,(u32x4{LD(u32,p),LD(u32,p+4),LD(u32,p+8),LD(u32,p+12)})); }
DEV u32 __umul24_(u32 a,u32 b){ u32 r; asm("v_mul_u32_u24 %0, %1, %2" : "=v"(r) : "s"(b),"v"(a)); return r; }
DEV u32x4 band(u32 mask,int g,u32 val){ u32 by=__builtin_amdgcn_ubfe(mask,8*g,8), X=by|(by<<15), k=0x10001u; u32x4 r; asm volatile("" : "+s"(k),"+s"(val));
  #pragma unroll
  for(int d=0;d<4;d++) r[d]=__umul24_((X>>(2*d))&k,val);
  return r; }
DEV h8 zpack(f32x4 e,f32x4 o){ return BC(h8,(u32x4{pkh(e[0],o[0]),pkh(e[1],o[1]),pkh(e[2],o[2]),pkh(e[3],o[3])})); }
struct C3 { float r,i,n; };
DEV C3 c3(C2 a){ C3 c; c.r=a.r; c.i=a.i; PIN(c.i); c.n=-c.i; PIN(c.n); return c; }
DEV C2 cm(C2 a,C3 b){ C2 c; c.r=__builtin_fmaf(a.r,b.r,a.i*b.n); c.i=__builtin_fmaf(a.r,b.i,a.i*b.r); return c; }
DEV u32 ph(C2 a){ return pkh(a.r,a.i); }
#define W2(p,a,b) asm volatile("ds_write2_b32 %0, %1, %2 offset1:136" :: "v"(p),"v"(a),"v"(b) : "memory")
struct CP { const lch *x0,*x1,*bd; lch *p0,*p1,*p2,*wp; u32x4 Be,Bo,AE; int yo; };
template<int ROLE> DEV void lanes(CP& c,lch* cb,int ln){ int m=ln&15, g=ln>>4;
  const lch* ad=cb+((m&1)? O_DO+2*527 : O_DE+2*536)+2*(8*g-m);
  const lch* bd=cb+2*(256+16*m+8*g); lch* dum=cb+O_DUM;
  if(ROLE==0){ int l0=128*(g>>1)+8*m+4*(g&1); c.p0=cb+O_DE+(549-l0)*2; PIN(c.p0); c.p1=c.p0+6; PIN(c.p1); c.p2=c.p0+(O_DO-O_DE-18); c.x0=ad-640; c.bd=bd-64; c.x1=cb+O_R3K+2*(126-2*m+8*g)-192; PIN(c.x1);
    c.Be=band(0x1ffu<<(m+4),g,0x3c00u); PIN(c.Be); }
  if(ROLE==1){ lch* r=cb+O_R2K+(192-64*(g-2)-2*m)*2; c.p0= g<2? dum : r-66; c.p1= ln==0? cb+O_DE+(552-256)*2 : (ln<=32? dum : r); c.p2=cb+O_DE+(544-256-8*ln)*2; c.x0=ad-960; c.bd=bd-448; PIN(c.p0); PIN(c.p1); PIN(c.p2); }
  if(ROLE==2){ int i0=16*m+4*g; c.p0= m<8? cb+O_R3K+(125-i0)*2 : dum+2; c.p1= ln==0? cb+O_R2K+128 : (ln==8? cb+O_R3K : c.p0+6); c.x0=ad-960; c.bd=bd-448; PIN(c.p0); PIN(c.p1); }
  if(ROLE==3){ c.x0=cb+O_R2K+2*(188-4*m+8*g)-256; c.bd=cb+2*(248+8*m+8*g);
    c.Be=band(0x1fu<<(m+6),g,0x3c00u); c.Bo=band(0x7u<<(m+11),g,0x3c00u); PIN(c.Be); PIN(c.Bo); }
  PIN(c.x0); PIN(c.bd); }
template<int ROLE> DEV void lanes2(CP& c,lch* cb,int ln){ int m=ln&15, g=ln>>4;
  if(ROLE==0){ c.Bo=band(0x7fu<<(m+5),g,0x3c00u); c.AE=band(3u<<(m+12),g,0xffffu); PIN(c.Bo); PIN(c.AE); }
  c.wp=cb+O_RED+ln*16; c.yo=(g&1)*512+m*32+(g>>1)*16; PIN(c.wp); PIN(c.yo); }
template<int ROLE> DEV void chan2(lch* sm,lch* cb,const lch*& fa,const lch*& fb,int grp,int d,int one_,int emax,u64 tag,char* ws){
  CP c;
  const f32x4 Z4={0,0,0,0}; h8 A[12]; f32x4 acc[8]; float sc; u32x4 ry; u32 cga;
  #define YSET sc=__builtin_amdgcn_ldexpf(1.f,emax-YSH); ry=mkrs(ws+W_Y+d*YST,4096); cga=(C_G+grp)*4; PIN(sc); PIN(cga); asm volatile("" : "+s"(ry));
  #define LD_U "buffer_load_dwordx4 v[112:115], %[of], %[rs], 0 offen sc1\n\tbuffer_load_dwordx4 v[116:119], %[of], %[rs], %[s1] offen sc1\n\tbuffer_load_dwordx4 v[120:123], %[of], %[rs], %[s2] offen sc1\n\tbuffer_load_dwordx4 v[124:127], %[of], %[rs], %[s3] offen sc1\n\t"
  #define UC(t,a,b,c) "v_bitop3_b32 " t ", v" #a ", v" #b ", %[" #c "] bitop3:0x7e\n\t"
  #define UD(t,a,c) "v_bitop3_b32 " t ", " t ", v" #a ", %[" #c "] bitop3:0xf6\n\t"
  #define CHK_U UC("%[t0]",112,116,c0) UC("%[t1]",113,117,c1) UD("%[t0]",120,c0) UD("%[t1]",121,c1) UD("%[t0]",124,c0) UD("%[t1]",125,c1) UD("%[t0]",114,c2) UD("%[t1]",115,c3) UD("%[t0]",118,c2) UD("%[t1]",119,c3) \
    UD("%[t0]",122,c2) UD("%[t1]",123,c3) UD("%[t0]",126,c2) UD("%[t1]",127,c3) "v_bitop3_b32 %[t0], %[t0], %[t1], %[mk] bitop3:0xa8\n\tv_cmp_ne_u32 vcc, 0, %[t0]\n\t"
  #define UX(a,b,c,d,o) "v_xor_b32 v" #a ", %[e0], v" #a "\n\tv_xor_b32 v" #b ", %[e1], v" #b "\n\tv_xor_b32 v" #c ", %[e2], v" #c "\n\tv_xor_b32 v" #d ", %[e3], v" #d "\n\tds_write_b128 %[up], v[" #a ":" #d "] offset:" #o "\n\t"
  #define UK(n,a,b,c,d,o,T) VW(n) UX(a,b,c,d,o) UC("%[t1]",a,b,k4) UC("%[t2]",c,d,k4) T
  #define OR3 "v_or3_b32 %[t0], %[t0], %[t1], %[t2]\n\t"
  #define UPOLL { u32 t0,t1,t2,cnt; asm volatile("s_mov_b32 %[cnt], 0x20000\n1:\n\t" UK(3,112,113,114,115,544,"v_or_b32 %[t0], %[t1], %[t2]\n\t") UK(2,116,117,118,119,1056,OR3) UK(1,120,121,122,123,1568,OR3) UK(0,124,125,126,127,2080,OR3) \
      "v_and_b32 %[t0], %[mk], %[t0]\n\tv_cmp_ne_u32 vcc, 0, %[t0]\n\ts_cbranch_vccz 9f\n\ts_sleep " STR(PSLP) "\n\ts_mov_b64 exec, vcc\n\t" LD_U "s_sub_u32 %[cnt], %[cnt], 1\n\ts_cbranch_scc0 1b\n9:\n\ts_mov_b64 exec, 1\n\tds_add_u32 %[cu], %[one]\n\ts_mov_b64 exec, -1" \
      : [t0]"=&v"(t0),[t1]"=&v"(t1),[t2]"=&v"(t2),[cnt]"=&s"(cnt) : [of]"v"(c.p0),[rs]"s"(rsU),[s1]"s"((32>>TL)*GST_),[s2]"s"((64>>TL)*GST_),[s3]"s"((96>>TL)*GST_),[k4]"s"(0x40004u), \
        [e0]"s"(c0^0x40004u),[e1]"s"(c1^0x40004u),[e2]"s"(c2^0x40004u),[e3]"s"(c3_^0x40004u),[mk]"s"(UMASK),[up]"v"(c.p1),[cu]"v"((C_U+one_)*4),[one]"v"(4) : PCLOB); }
  if(ROLE<3){
    h8 a[8], b[8];
    #pragma unroll
    for(int ks=0;ks<8;ks++){ a[ks]=LD(h8,fa+ks*64); b[ks]=LD(h8,fb+ks*64); }
    SCHED();
    LANE(ln); lanes<ROLE>(c,cb,ln); float fz= ln==32? 0.f : 4.76837158203125e-07f; if(ROLE==1) PIN(fz);
    SCHED();
    f32x4 k=Z4;
    #pragma unroll
    for(int ks=0;ks<8;ks++) k=MH(a[ks],b[ks],k);
    const float f=4.76837158203125e-07f;
    float k0=k[0]*f, k1=k[1]*f, k2=k[2]*f, k3=k[3]*f;
    u32 h32=pkh(k3,k2), h10=pkh(k1,k0);
    if(ROLE!=1){
      ST(u16,c.p0)=(u16)h32; ST(u32,c.p0+2)=pkh(k2,k1); ST(u16,c.p1)=(u16)(h10>>16);
      if(ROLE==0) ST(u32x2,c.p2)=u32x2{h32,h10};
    } else {
      float n0=dppf<0x130>(k[0])*fz, z1= fz==0.f? k0:k1, z2=k[2]*fz, z3=k[3]*fz; u32 h22=pkh(z2,z2);
      ST(u16,c.p0+64)=(u16)h10; ST(u16,c.p0+2)=(u16)(h32>>16); ST(u16,c.p0)=(u16)h32; ST(u16,c.p1)=(u16)(h10>>16);
      u32x4 vo={pkh(n0,z3),h22,pkh(z2,z1),pkh(k0,k0)}, ve={pkh(n0,n0),pkh(z3,z2),h22,pkh(z1,k0)};
      asm volatile("s_mov_b32 exec_hi, 1\n\tds_write_b128 %0, %1\n\tds_write_b128 %0, %2 offset:%3\n\ts_mov_b32 exec_hi, -1" :: "v"(c.p2),"v"(ve),"v"(vo),"i"(O_DO-O_DE-16) : "memory"); }
    if(PA>=0) PRIO(PA<0? 0:PA);
    gsync(sm,C_G+grp,7);
    u32 kv;
    if(ROLE==0){ A[0]=ld4w(c.x0+640); A[1]=ld4w(c.x0+128); A[2]=ld4w(c.x1+192); A[3]=ld4w(c.x1+128); A[4]=ld4w(c.x1+64); A[5]=ld4w(c.x1); kv=LD(u16,cb+O_R2K+128); }
    else {
      #pragma unroll
      for(int r=0;r<6;r++){ A[2*r]=ld4w(c.x0+832-64*r); A[2*r+1]=ld4w(c.x0+320-64*r); } }
    SCHED(); lanes2<ROLE>(c,cb,ln); YSET SCHED();
    if(ROLE==0){ kv|=kv<<16; c.AE=u32x4{c.AE[0]&kv,c.AE[1]&kv,c.AE[2]&kv,c.AE[3]&kv}; PIN(c.AE); }
    PRIO(2); if(ROLE!=0&&BID>=P12) PRIO(1);
    gwait<1>(sm,C_U+one_,HG>=0&&BID>=112? 12:8-4*one_);
  } else {
    u32 tg=(u32)tag, c0=upat(tg,0), c1=upat(tg,1), c2=upat(tg,2), c3_=upat(tg,3); u32x4 rsU=mkrs(ws+W_U,256*UST);
    if(!one_) PRIO(2);
    LANE(ln); c.p0=(lch*)fa; c.p1=(lch*)fb;
    bool late= grp==0||BID>=112;
    if(!late) asm volatile(LD_U :: [of]"v"(c.p0),[rs]"s"(rsU),[s1]"s"((32>>TL)*GST_),[s2]"s"((64>>TL)*GST_),[s3]"s"((96>>TL)*GST_) : "memory");
    asm volatile("s_mov_b64 exec, 1\n\tglobal_store_dwordx2 %0, %1, %2 sc1\n\ts_mov_b64 exec, -1" :: "v"(W_FL+d*8),"v"(tag),"s"(ws) : "memory");
    lanes<3>(c,cb,ln); lanes2<3>(c,cb,ln); YSET
    #pragma unroll
    for(int j=2;j<8;j++){ acc[j]=Z4; PIN(acc[j]); }
    if(late){ if(UD_>=0&&BID<UDB) __builtin_amdgcn_s_sleep(UD_<0? 0:UD_); else gwait<2>(sm,C_G,UGATE); if(BID>=112) gwait<1>(sm,C_D,DN); asm volatile(LD_U :: [of]"v"(c.p0),[rs]"s"(rsU),[s1]"s"((32>>TL)*GST_),[s2]"s"((64>>TL)*GST_),[s3]"s"((96>>TL)*GST_) : "memory"); }
    if(!late) UPOLL
    gwait<1>(sm,C_G+grp,7);
    #pragma unroll
    for(int i=0;i<6;i++) A[i]=ldA(c.x0+320-64*i);
    if(late) UPOLL
    PRIO(2);
    gwait<1>(sm,C_U+one_,HG>=0&&BID>=112? 12:8-4*one_);
  }
  const lch* bd=c.bd; h8 Be=BC(h8,c.Be), Bo=BC(h8,c.Bo);
  #pragma unroll
  for(int j=0;j<(ROLE==3? 2:8);j++) acc[j]=Z4;
  constexpr int OWN= ROLE==1? 2 : ROLE==2? 1 : ROLE, NS= ROLE==0? 1:2;
  #define RW(j) { ST(f32x4,c.wp+((j)>>1)*4096+(ROLE&1)*2048+((j)&1)*1024)=acc[j]; SCHED(); }
  const lch* rp=c.wp+OWN*4096; f32x4 a[2], b[2]; u32 cv;
  #define LDV(T,p) (*(const volatile LAS T*)(p))
  #define ARR { garrive(sm,C_G+grp); SCHED(); }
  #define OPT { cv=LDV(u32,(lch*)(size_t)cga); _Pragma("unroll") for(int q=0;q<NS;q++){ a[q]=LDV(f32x4,rp+q*2048); b[q]=LDV(f32x4,rp+q*2048+1024); } SCHED(); }
  #define SUM(v) { _Pragma("unroll") for(int e=0;e<4;e++) v[0][e]=(NS==1? v[0][e] : v[0][e]+v[1][e])*sc; PIN(v[0]); SCHED(); }
  if(ROLE==1||ROLE==2){
    constexpr int NQ= ROLE==1? 5:4, LA=9; h8 B[30];
    #define QS(s) (ROLE==2? (s) : (s)<3? 5+(s) : (s))
    #define LB(i) B[i]=LD(h8,bd+320-64*((i)%6)+512*QS((i)/6));
    #pragma unroll
    for(int i=0;i<LA;i++) LB(i)
    SCHED();
    #pragma unroll
    for(int i=0;i<6*NQ;i++){ const int q=QS(i/6), r=i%6;
      if(i==6*NQ-(ROLE==1? 4:7)){ PRIO(2); OPT } if(i==6*NQ-(ROLE==1? 2:3)) SUM(a) if(i==6*NQ-1) SUM(b)
      if(!(ROLE==1&&q==3)){ acc[q]=MH(A[2*r],B[i],acc[q]); SCHED(); }
      if(q!=7&&!(ROLE==2&&q==3)){ acc[q+1]=MH(A[2*r+1],B[i],acc[q+1]); SCHED(); }
      if(i+LA<6*NQ){ LB(i+LA) SCHED(); }
      if(ROLE==2&&i==6) RW(0) if(ROLE==2&&i==12){ RW(1) ARR } if(ROLE==1&&i==13) RW(6) if(ROLE==1&&i==19){ RW(7) ARR } } }
  if(ROLE==0){
    h8 B[8], ue[4], C[8];
    #pragma unroll
    for(int kt=0;kt<4;kt++){ ue[kt]=LD(h8,bd+48+512*kt); B[kt]=LD(h8,bd+64+512*kt); }
    f32x4 ce[2],co[2]; h8 Z[2];
    ce[0]=MH(ue[0],Be,Z4); co[0]=MH(B[0],Bo,Z4); ce[1]=MH(ue[1],Be,Z4); co[1]=MH(B[1],Bo,Z4); Z[0]=zpack(ce[0],co[0]);
    #pragma unroll
    for(int kt=0;kt<4;kt++){
      SCHED();
      if(kt+2<4){ ce[kt&1]=MH(ue[kt+2],Be,Z4); co[kt&1]=MH(B[kt+2],Bo,Z4); }
      if(kt+1<4) Z[(kt+1)&1]=zpack(ce[(kt+1)&1],co[(kt+1)&1]);
      #pragma unroll
      for(int rho=0;rho+kt<4;rho++) acc[4+kt+rho]=MH(Z[kt&1],A[2+rho],acc[4+kt+rho]);
      SCHED(); if(kt>=2) C[kt-1]=LD(h8,bd+512*(kt-1)); if(kt==0) A[5]=BC(h8,c.AE); if(kt==1) A[4]=ld4w(c.x0-384); if(kt==2) A[3]=ld4w(c.x0+64); if(kt==3) A[2]=ld4w(c.x0+576); SCHED(); }
    #pragma unroll
    for(int q=0;q<9;q++){
      if(q<4){ acc[q+4]=MH(A[5],B[q],acc[q+4]); SCHED(); } if(q<6){ acc[q+2]=MH(A[4],B[q],acc[q+2]); SCHED(); }
      if(q>=1&&q<7){ acc[q+1]=MH(A[1],B[q],acc[q+1]); SCHED(); } if(q>=2&&q<8){ acc[q]=MH(A[0],B[q],acc[q]); SCHED(); }
      if(q>=4&&q<8) RW(q-2)
      if(q<4){ B[4+q]=LD(h8,bd+64+512*(4+q)); SCHED(); }
      if(q>=3){ acc[q-1]=MH(A[2],C[q-1],acc[q-1]); SCHED(); } if(q>=2&&q<8){ acc[q]=MH(A[3],C[q-1],acc[q]); SCHED(); }
      if(q>=1&&q<7){ C[(q+2)&7]=LD(h8,bd+512*((q+2)&7)); SCHED(); } }
    acc[0]=MH(A[0],B[0],acc[0]); SCHED(); RW(6) acc[1]=MH(A[1],B[0],acc[1]); SCHED(); RW(7) ARR
    OPT acc[0]=MH(A[2],C[0],acc[0]); acc[1]=MH(A[0],B[1],acc[1]); acc[1]=MH(A[2],C[1],acc[1]); acc[1]=MH(A[3],C[0],acc[1]); SCHED(); SUM(a) SUM(b) }
  if(ROLE==3){
    h8 U[12];
    #pragma unroll
    for(int kt=0;kt<6;kt++) U[kt]=LD(h8,bd+256*kt);
    f32x4 ce[2],co[2]; h8 Z[2];
    ce[0]=MH(U[0],Be,Z4); co[0]=MH(U[0],Bo,Z4); ce[1]=MH(U[1],Be,Z4); co[1]=MH(U[1],Bo,Z4); Z[0]=zpack(ce[0],co[0]);
    #pragma unroll
    for(int kt=0;kt<12;kt++){
      SCHED();
      if(kt==10) OPT if(kt==11) SUM(a)
      if(kt+2<12){ ce[kt&1]=MH(U[kt+2],Be,Z4); co[kt&1]=MH(U[kt+2],Bo,Z4); }
      if(kt>=2&&kt<=8&&!(kt&1)){ SCHED(); RW((kt+2)/2) if(kt==8) ARR }
      if(kt+1<12) Z[(kt+1)&1]=zpack(ce[(kt+1)&1],co[(kt+1)&1]);
      #pragma unroll
      for(int rho=-1;rho<5;rho++){ int j=(kt+4+rho)>>1; if(((kt+rho)&1)==0&&j>=2&&j<8) acc[j]=MH(Z[kt&1],A[rho+1],acc[j]); }
      if(kt<6){ SCHED(); U[kt+6]=LD(h8,bd+256*(kt+6)); } }
    SCHED(); SUM(b)
  }
  { float s0[4], s1[4];
    if(RFL(cv)<11u){ u32 tv,cnt;
      asm volatile("s_mov_b32 %1, 0x20000\n1:\n\tds_read_b32 %0, %6\n\tds_read_b128 %2, %7\n\tds_read_b128 %4, %7 offset:2048\n\tds_read_b128 %3, %7 offset:1024\n\tds_read_b128 %5, %7 offset:3072\n\ts_waitcnt lgkmcnt(4)\n\t"
        "v_cmp_gt_u32 vcc, 11, %0\n\ts_cbranch_vccz 2f\n\ts_sleep 1\n\ts_sub_u32 %1, %1, 1\n\ts_cbranch_scc0 1b\n2:\n\ts_waitcnt lgkmcnt(0)"
        : "=&v"(tv),"=&s"(cnt),"=&v"(a[0]),"=&v"(b[0]),"=&v"(a[1]),"=&v"(b[1]) : "v"(cga),"v"(rp) : "memory","scc","vcc");
      SUM(a) SUM(b) }
    f32x4 o0=acc[2*OWN], o1=acc[2*OWN+1];
    #pragma unroll
    for(int e=0;e<4;e++){ s0[e]=__builtin_fmaf(o0[e],sc,a[0][e]); s1[e]=__builtin_fmaf(o1[e],sc,b[0][e]); }
    auto r0=__builtin_amdgcn_permlane16_swap(nv(pk(s0[0],s0[1])),nv(pk(s1[0],s1[1])),false,false); auto r1=__builtin_amdgcn_permlane16_swap(nv(pk(s0[2],s0[3])),nv(pk(s1[2],s1[3])),false,false);
    asm volatile("buffer_store_dwordx4 %0, %1, %2, 0 offen offset:%3 sc1\n\ts_nop 1" :: "v"(u32x4{r0[0],r1[0],r0[1],r1[1]}),"v"(c.yo),"s"(ry),"i"(OWN*1024) : "memory"); }
  garrive(sm,C_E);

  asm volatile("s_endpgm" : "=v"(fa),"=v"(fb) :: "memory");
}
#define GETEP asm volatile("s_waitcnt lgkmcnt(0)\n\ts_mov_b64 %0, s[96:97]\n\ts_mov_b32 %1, s98" : "=s"(ep),"=s"(ldw));
DEV void chan(lch* sm,int grp,int w4,int d,int one_,char* ws,u32x4 q0,u32x4 q1,u32x4 q2,u32x4 q3,u32x4 q4){
  lch* cb=sm+O_CH+grp*SLSZ; int role=w4, emax;
  #define LP(v) ((lch*)(size_t)(u32)(v))
  const lch* fa=LP(q4[0]); const lch* fb=LP(q4[1]);
  u64 tag;
  { float a2,bp,br,bi,cr,ci;
    u32 fq=q3[2], fr=q3[3], one=q4[2]; float f64=BC(float,fq), f128=BC(float,fr);
    lch *t8[4]={LP(q0[0]),LP(q0[1]),LP(q0[2]),LP(q0[3])}, *tB[4]={LP(q1[0]),LP(q1[1]),LP(q1[2]),LP(q1[3])}, *t4[2]={LP(q2[0]),LP(q2[1])}, *t2[2]={LP(q2[2]),LP(q2[3])}, *t1=LP(q3[0]), *te=LP(q3[1]);
    if(BID<64){ if(grp==0) PRIO(PK&3); if(grp==1) PRIO((PK>>4)&3); if(grp==2) PRIO((PK>>8)&3); } else
    if(grp!=0) PRIO(1);
    u64 ep; u32 ldw; GETEP tag=ep+1ull;
    float dt=rb(expf_((d&1)? bhi(ldw):blo(ldw))), ca=dt*1.4426950408889634f, cb_=dt*0.15915494309189535f;
    asm volatile("s_waitcnt vmcnt(1)\n\tv_mul_f32 %2, %8, v114\n\tv_mul_f32 %3, %8, v115\n\tv_mul_f32 %0, %6, v112\n\tv_mul_f32 %1, %7, v113\n\tv_mov_b32 %4, v116\n\tv_mov_b32 %5, v117"
      : "=&v"(a2),"=&v"(bp),"=&v"(br),"=&v"(bi),"=&v"(cr),"=&v"(ci) : "v"(ca),"v"(cb_),"v"(dt) : "memory");
    C2 W; W.r=cr*br-ci*bi; W.i=cr*bi+ci*br;
    u32 mx=BC(u32,__builtin_fmaxf(__builtin_fabsf(W.r),__builtin_fabsf(W.i)));
    #define MXS(C) mx=umax_(mx,dppu<C>(mx));
    float e8=a2*8.f, eB=__builtin_fmaf(a2,f64,14.f), p8=bp*8.f, pB=bp*f64; SCHED();
    MXS(0xB1) p8=__builtin_amdgcn_fractf(p8); pB=__builtin_amdgcn_fractf(pB); e8=__builtin_amdgcn_exp2f(e8); SCHED();
    MXS(0x4E) eB=__builtin_amdgcn_exp2f(eB); float c8=__builtin_amdgcn_cosf(p8); SCHED();
    MXS(0x141) float s8=__builtin_amdgcn_sinf(p8), cB=__builtin_amdgcn_cosf(pB); SCHED();
    float sB=__builtin_amdgcn_sinf(pB); PIN(sB); MXS(0x140) float e1=__builtin_amdgcn_exp2f(a2); PIN(e1); SCHED();
    asm volatile("s_mov_b64 exec, 0x10001\n\tds_max_u32 %0, %1\n\tds_add_u32 %0, %2 offset:4\n\ts_mov_b64 exec, -1" :: "v"(te),"v"(mx),"v"(one) : "memory");
    C3 z8; z8.r=e8*c8; z8.n=e8*s8; z8.i=-z8.n; PIN(z8.i); C2 B; B.r=eB*cB; B.i=-(eB*sB);
    float p1=__builtin_amdgcn_fractf(bp), pE=__builtin_amdgcn_fractf(bp*256.f), pV=__builtin_amdgcn_fractf(bp*f128), eE=a2*256.f, eV=a2*f128, tr[8];
    #pragma unroll
    for(int k=0;k<4;k++){ SCHED(); u32 h0=ph(B); B=cm(B,z8);
      if(k==0){ tr[0]=__builtin_amdgcn_cosf(pV); tr[1]=__builtin_amdgcn_sinf(pV); } if(k==1){ tr[2]=__builtin_amdgcn_exp2f(eV); tr[3]=__builtin_amdgcn_cosf(p1); }
      if(k==2){ tr[4]=__builtin_amdgcn_sinf(p1); tr[5]=__builtin_amdgcn_exp2f(eE); } if(k==3){ tr[6]=__builtin_amdgcn_cosf(pE); tr[7]=__builtin_amdgcn_sinf(pE); }
      PIN(tr[2*k]); PIN(tr[2*k+1]);
      u32 h1=ph(B); if(k<3) B=cm(B,z8); W2(tB[k],h0,h1); }
    asm volatile("ds_read_b64 v[118:119], %0" :: "v"(te) : "memory");
    SCHED();
    C3 z1=c3(C2{e1*tr[3],e1*tr[4]}), E=c3(C2{tr[5]*tr[6],tr[5]*tr[7]}); C2 V[8]; V[0]=cmul(W,C2{tr[2]*tr[0],tr[2]*tr[1]});
    { u32 cnt,n; PIN(V[0].r); PIN(V[0].i); PIN(z1.r); PIN(E.r);
      asm volatile("s_mov_b32 %1, 0x20000\n1:\n\ts_waitcnt lgkmcnt(0)\n\tv_readfirstlane_b32 %2, v119\n\tv_readfirstlane_b32 %0, v118\n\ts_cmp_eq_u32 %2, 8\n\ts_cbranch_scc1 2f\n\tds_read_b64 v[118:119], %3\n\ts_sub_u32 %1, %1, 1\n\ts_cbranch_scc0 1b\n2:"
        : "=&s"(emax),"=&s"(cnt),"=&s"(n) : "v"(te) : "memory","scc");
      emax=RFL(emax); emax=(emax>>23)-126; }
    V[0].r=__builtin_amdgcn_ldexpf(V[0].r,14-emax); V[0].i=__builtin_amdgcn_ldexpf(V[0].i,14-emax);
    C2 Y=cm(V[0],E), Y4; u32 y0=ph(Y);
    V[1]=cm(V[0],z1); W2(t8[0],ph(V[0]),ph(V[1])); Y=cm(Y,E); u32 y1=ph(Y);
    V[2]=cm(V[1],z1); Y=cm(Y,E); u32 y2=ph(Y);
    V[3]=cm(V[2],z1); W2(t8[1],ph(V[2]),ph(V[3])); W2(t4[0],y0,ph(cm(V[2],E)));
    V[4]=cm(V[3],z1); Y=cm(Y,E); ST(u32,t1)=ph(Y);
    V[5]=cm(V[4],z1); W2(t8[2],ph(V[4]),ph(V[5])); Y4=cm(V[4],E); u32 y4=ph(Y4);
    V[6]=cm(V[5],z1); Y=cm(Y,E); ST(u32,t1+2*TROW)=ph(Y);
    V[7]=cm(V[6],z1); W2(t8[3],ph(V[6]),ph(V[7])); Y4=cm(Y4,E); W2(t2[0],y1,ph(Y4));
    W2(t4[1],y4,ph(cm(V[6],E))); Y=cm(Y,E); ST(u32,t1+4*TROW)=ph(Y);
    Y4=cm(Y4,E); W2(t2[1],y2,ph(Y4)); Y=cm(Y,E); ST(u32,t1+6*TROW)=ph(Y);
    Y=cm(Y,E); asm volatile("s_mov_b32 exec_hi, 0\n\tds_write_b32 %0, %1 offset:4352\n\ts_mov_b32 exec_hi, -1" :: "v"(t1),"v"(ph(Y)) : "memory");
  }
  if(BID<64){ if(grp==0) PRIO(PS&3); if(grp==1) PRIO((PS>>4)&3); if(grp==2) PRIO((PS>>8)&3); }
  DRAIN();
  gsync(sm,C_G+grp,4);
  if(role==0) chan2<0>(sm,cb,fa,fb,grp,d,one_,emax,tag,ws);
  if(role==1) chan2<1>(sm,cb,fa,fb,grp,d,one_,emax,tag,ws);
  if(role==2) chan2<2>(sm,cb,fa,fb,grp,d,one_,emax,tag,ws);
  chan2<3>(sm,cb,fa,fb,grp,d,one_,emax,tag,ws);
}

#define GTC(r,s,c) "v_cmp_gt_u32 %[" #s "], v" #r ", " c "\n\t"
#define GTA(s) "v_addc_co_u32 %[rk], vcc, 0, %[rk], %[" #s "]\n\t"
#define GT0(s) "v_cndmask_b32 %[rk], 0, 1, %[" #s "]\n\t"
#define GT4(a,b,c,d,k,F) GTC(a,s0,k) GTC(b,s1,k) GTC(c,s2,k) GTC(d,s3,k) F(s0) GTA(s1) GTA(s2) GTA(s3)
#define GTS [s0]"=&s"(q0),[s1]"=&s"(q1),[s2]"=&s"(q2),[s3]"=&s"(q3)
DEV void top8(lch* sm,int w4,int ln,u32 M,char* ws,u32& rank_,u32& key_){
  u32 k[8], hm=0xffff0000u, mx=1+w4*4+(ln>>4), one=1, cw=O_CND+w4*32; u64 em=0x0001000100010001ull,q0,q1,q2,q3;
  u32x4 rs=mkrs(ws+W_RG+(BID&(NRG-1))*8192,8192); int ro=w4*1024+ln*16;
  #pragma unroll
  for(int e=0;e<8;e++){ k[e]=(u32)(0xfff-(w4*256+ln*4+(e&3)+(e>>2)*1024)); PIN(k[e]); }
  lch *aW=sm+O_RMX+(w4*4+(ln>>4))*4, *aC=sm+O_RMX+(ln>>2)*4, *aO=sm+O_RMX+(ln&3)*16, *dC=sm+O_CND+(ln>>1)*4, *dO=sm+O_CND+(ln&1)*64, *cW=sm+C_W*4; PIN(aW); PIN(aC); PIN(aO); PIN(dC); PIN(dO); PIN(ro); PIN(ln); PIN(mx); PIN(one); PIN(cW);
  asm volatile("" : "+s"(hm),"+s"(em),"+s"(rs),"+s"(cw));
  gwait<2>(sm,C_G,RGATE);
  if(RD) __builtin_amdgcn_s_sleep(RD);
  { u32 cnt,t0,t1;
    #define KL(e,r) "v_lshl_or_b32 %[k" #e "], v" #r ", 16, %[k" #e "]\n\t"
    #define KH(e,r) "v_and_or_b32 %[k" #e "], v" #r ", %[hm], %[k" #e "]\n\t"
    #define CHK_R "v_bitop3_b32 %[t0], v113, v115, %[M] bitop3:0x7e\n\tv_bitop3_b32 %[t1], v117, v119, %[M] bitop3:0x7e\n\tv_or_b32 %[t0], %[t0], %[t1]\n\tv_cmp_ne_u32 vcc, 0, %[t0]\n\t"
    #define MXD(c) "v_max_u32_dpp %[mx], %[mx], %[mx] " c " row_mask:0xf bank_mask:0xf\n\t"
    asm volatile(LD_DUP2
      "s_sleep " STR(R2D) "\n\tbuffer_load_dwordx4 v[120:123], %[of], %[rs], 0 offen sc1\n\tbuffer_load_dwordx4 v[124:127], %[of], %[rs], %[so] offen sc1\n\t"
      "s_mov_b64 exec, 1\n\tds_add_u32 %[cw], %[one]\n\ts_mov_b64 exec, -1\n\ts_mov_b32 %[cnt], 0x20000\n\ts_waitcnt vmcnt(2)\n\t" CHK_R "s_cbranch_vccz 9f\n\t"
      "s_waitcnt vmcnt(0)\n\tv_mov_b64 v[112:113], v[120:121]\n\tv_mov_b64 v[114:115], v[122:123]\n\tv_mov_b64 v[116:117], v[124:125]\n\tv_mov_b64 v[118:119], v[126:127]\n\t"
      POLLASM(LD_DUP2,CHK_R,PSLP)
      "v_pk_max_u16 %[t0], v112, v114\n\tv_pk_max_u16 %[t1], v116, v118\n\tv_pk_max_u16 %[t0], %[t0], %[t1]\n\t"
      "v_max_u16_sdwa %[mx], %[t0], %[t0] dst_sel:WORD_1 dst_unused:UNUSED_PRESERVE src0_sel:WORD_0 src1_sel:WORD_1\n\t"
      KL(0,112) KH(1,112) MXD("quad_perm:[1,0,3,2]") KL(2,114) KH(3,114) MXD("quad_perm:[2,3,0,1]") KL(4,116) KH(5,116) MXD("row_half_mirror") KL(6,118) KH(7,118) MXD("row_mirror")
      "s_mov_b64 exec, %[em]\n\tds_write_b32 %[aW], %[mx]\n\ts_mov_b64 exec, -1\n\tds_read_b32 v116, %[aC]\n\tds_read_b128 v[112:115], %[aO]"
      : [k0]"+v"(k[0]),[k1]"+v"(k[1]),[k2]"+v"(k[2]),[k3]"+v"(k[3]),[k4]"+v"(k[4]),[k5]"+v"(k[5]),[k6]"+v"(k[6]),[k7]"+v"(k[7]),[mx]"+v"(mx),[cnt]"=&s"(cnt),[t0]"=&v"(t0),[t1]"=&v"(t1)
      : [of]"v"(ro),[rs]"s"(rs),[so]"s"(4096),[M]"s"(M),[hm]"s"(hm),[cw]"v"(cW),[one]"v"(one),[em]"s"(em),[aW]"v"(aW),[aC]"v"(aC),[aO]"v"(aO) : PCLOB); }
  u32 slow;
  { u32 rk,cnt,T,h,l,t,n;
    #define INS(e) "v_med3_u32 %[t], %[t], %[l], %[k" #e "]\n\tv_med3_u32 %[l], %[h], %[l], %[k" #e "]\n\tv_max_u32 %[h], %[h], %[k" #e "]\n\t"
    asm volatile("s_branch 7f\n"
      "3:\n\tv_cmp_le_u32 %[s2], %[T], %[t]\n\ts_add_u32 %[cnt], %[cnt], %[n]\n\ts_cmp_gt_u32 %[cnt], 8\n\ts_cbranch_scc1 5f\n\ts_cmp_lg_u64 %[s2], 0\n\ts_cbranch_scc1 5f\n\t"
      "s_mov_b64 exec, %[s1]\n\tv_mov_b32 %[t], %[n]\n\tv_mbcnt_lo_u32_b32 %[t], exec_lo, %[t]\n\tv_mbcnt_hi_u32_b32 %[t], exec_hi, %[t]\n\tv_lshl_add_u32 %[t], %[t], 2, %[cw]\n\tds_write_b32 %[t], %[l]\n\ts_mov_b64 exec, -1\n\ts_branch 4f\n"
      "5:\n\ts_mov_b32 %[sl], 1\n\ts_branch 6f\n7:\n\t"
      "v_max3_u32 %[h], %[k0], %[k1], %[k2]\n\tv_med3_u32 %[l], %[k0], %[k1], %[k2]\n\tv_min3_u32 %[t], %[k0], %[k1], %[k2]\n\t" INS(3) INS(4) INS(5) INS(6) INS(7)
      "s_mov_b32 %[cnt], 0x20000\n1:\n\ts_waitcnt lgkmcnt(0)\n\tv_cmp_eq_u32 vcc, 0, v116\n\ts_cbranch_vccz 2f\n\tds_read_b32 v116, %[aC]\n\tds_read_b128 v[112:115], %[aO]\n\ts_sub_u32 %[cnt], %[cnt], 1\n\ts_cbranch_scc0 1b\n2:\n\t"
      GT4(112,113,114,115,"v116",GT0) "v_and_b32 v116, %[hm], v116\n\ts_nop 0\n\tv_add_u32_dpp %[rk], %[rk], %[rk] quad_perm:[1,0,3,2] row_mask:0xf bank_mask:0xf\n\ts_mov_b32 %[sl], 0\n\ts_nop 0\n\tv_add_u32_dpp %[rk], %[rk], %[rk] quad_perm:[2,3,0,1] row_mask:0xf bank_mask:0xf\n\t"
      "v_cmp_eq_u32 vcc, 7, %[rk]\n\ts_ff1_i32_b64 %[cnt], vcc\n\ts_nop 0\n\tv_readlane_b32 %[T], v116, %[cnt]\n\ts_nop 1\n\t"
      "v_cmp_le_u32 vcc, %[T], %[h]\n\tv_cmp_le_u32 %[s1], %[T], %[l]\n\ts_bcnt1_i32_b64 %[n], vcc\n\tv_mbcnt_lo_u32_b32 %[rk], vcc_lo, 0\n\tv_mbcnt_hi_u32_b32 %[rk], vcc_hi, %[rk]\n\ts_bcnt1_i32_b64 %[cnt], %[s1]\n\ts_cbranch_scc1 3b\n\t"
      "s_cmp_gt_u32 %[n], 8\n\ts_cbranch_scc1 5b\n"
      "4:\n\tv_lshl_add_u32 %[rk], %[rk], 2, %[cw]\n\ts_mov_b64 exec, vcc\n\tds_write_b32 %[rk], %[h]\n\ts_mov_b64 exec, 1\n\tds_add_u32 %[cW], %[one] offset:4\n\ts_mov_b64 exec, -1\n6:"
      : [rk]"=&v"(rk),[cnt]"=&s"(cnt),[T]"=&s"(T),[h]"=&v"(h),[l]"=&v"(l),[t]"=&v"(t),[n]"=&s"(n),[sl]"=&s"(slow),GTS
      : [k0]"v"(k[0]),[k1]"v"(k[1]),[k2]"v"(k[2]),[k3]"v"(k[3]),[k4]"v"(k[4]),[k5]"v"(k[5]),[k6]"v"(k[6]),[k7]"v"(k[7]),[aC]"v"(aC),[aO]"v"(aO),[hm]"s"(hm),[cw]"s"(cw),[cW]"v"(cW),[one]"v"(one) : PCLOB); }
  if(__builtin_expect(RFL(slow)!=0,0)){
    u32 mine=0;
    _Pragma("unroll 1") for(int c=0;c<8;c++){ u32 lm=umax_(umax_(umax_(k[0],k[1]),umax_(k[2],k[3])),umax_(umax_(k[4],k[5]),umax_(k[6],k[7]))); u32 wm=wmax(lm); if(ln==c) mine=wm;
      #pragma unroll
      for(int e=0;e<8;e++) k[e]=(k[e]==wm)?0u:k[e]; }
    if(ln<8) ST(u32,sm+cw+ln*4)=mine;
    garrive(sm,C_W+1); }
  { u32 c,rk,cnt;
    asm volatile("s_mov_b32 %[cnt], 0x20000\n1:\n\tds_read_b32 %[rk], %[cW] offset:4\n\tds_read_b32 %[c], %[dC]\n\tds_read_b128 v[96:99], %[dO]\n\tds_read_b128 v[100:103], %[dO] offset:16\n\tds_read_b128 v[104:107], %[dO] offset:32\n\tds_read_b128 v[108:111], %[dO] offset:48\n\t"
      "s_waitcnt lgkmcnt(5)\n\tv_cmp_gt_u32 vcc, 4, %[rk]\n\ts_cbranch_vccz 2f\n\ts_sub_u32 %[cnt], %[cnt], 1\n\ts_cbranch_scc0 1b\n2:\n\t"
      "s_waitcnt lgkmcnt(3)\n\t" GT4(96,97,98,99,"%[c]",GT0) "s_waitcnt lgkmcnt(2)\n\t" GT4(100,101,102,103,"%[c]",GTA) "s_waitcnt lgkmcnt(1)\n\t" GT4(104,105,106,107,"%[c]",GTA) "s_waitcnt lgkmcnt(0)\n\t" GT4(108,109,110,111,"%[c]",GTA)
      "s_nop 1\n\tv_add_u32_dpp %[rk], %[rk], %[rk] quad_perm:[1,0,3,2] row_mask:0xf bank_mask:0xf"
      : [c]"=&v"(c),[rk]"=&v"(rk),[cnt]"=&s"(cnt),GTS : [dC]"v"(dC),[dO]"v"(dO),[cW]"v"(cW)
      : PCLOB,"v96","v97","v98","v99","v100","v101","v102","v103","v104","v105","v106","v107","v108","v109","v110","v111");
    rank_=rk; key_=c; }
}

#define FILL_(n,WAIT) "s_mov_b32 m0, %[ld]\n\ts_mov_b32 %[c], " STR(n) "-1\n3:\n\t" WAIT "buffer_load_dwordx4 %[vo], %[rs], %[so] offen" WPOL " lds\n\ts_add_u32 m0, m0, 0x1000\n\ts_add_u32 %[so], %[so], %[st]\n\t" \
  "s_sub_u32 %[c], %[c], 1\n\ts_cbranch_scc0 3b\n\ts_mov_b32 %[ld], m0\n\t"
#define FILL(n,WAIT) do{ u32 c_; asm volatile(FILL_(n,WAIT) : [c]"=&s"(c_),[so]"+s"(wso),[ld]"+s"(wld) : [vo]"v"(wvo),[rs]"s"(wrs),[st]"s"(wst) : "memory","scc","m0"); }while(0)
#define FILLD(n,WN,WW_,STK) do{ u32 c_,t_,s_=0; asm volatile(FILL_(n,STK "ds_read_b32 %[t], %[ad]\n\ts_waitcnt lgkmcnt(0)\n\tv_readfirstlane_b32 %[s], %[t]\n\ts_cmp_ge_u32 %[s], 4\n\ts_cbranch_scc1 4f\n\t" VW(WN) "4:\n\t" VW(WW_)) \
  : [c]"=&s"(c_),[so]"+s"(wso),[ld]"+s"(wld),[t]"=&v"(t_),[s]"+s"(s_) : [vo]"v"(wvo),[rs]"s"(wrs),[st]"s"(wst),[ad]"v"(C_WIN*4) : "memory","scc","m0"); wso=RFL(wso); wld=RFL(wld); }while(0)
#define STKB "s_cmp_ge_u32 %[s], 4\n\ts_cbranch_scc1 4f\n\t"

__global__ NOPK __attribute__((amdgpu_num_vgpr(56),amdgpu_num_sgpr(102))) __launch_bounds__(1024,4) void kA(const u16* __restrict__ x,char* ws,const u16* __restrict__ wr,const u16* __restrict__ Dsk,const u16* __restrict__ wg,const u16* __restrict__ wu,
                                              const u16* __restrict__ wd,u16* __restrict__ out,KP kp){
  __shared__ __attribute__((aligned(16))) char smem[LDS_TOTAL];
  lch* sm=(lch*)smem;
  u32x4 xa,xb_,wa,wb,dq[3]; u32 xc,wc;
  asm volatile(
    "s_load_dwordx16 s[64:79], %[ka], 0x0\n\ts_load_dwordx16 s[80:95], %[ka], 0x40\n\t"
    "v_readfirstlane_b32 s60, %[t]\n\ts_and_b32 s61, %[wg], 7\n\ts_lshr_b32 s62, %[wg], 3\n\tv_and_b32 v118, 31, %[t]\n\ts_bfe_u32 s63, s60, 0x20006\n\ts_cmpk_ge_u32 s60, 0x100\n\ts_cbranch_scc1 1f\n\t"
    "s_setprio 3\n\ts_lshl_b32 s56, s61, 5\n\ts_add_u32 s56, s56, s62\n\ts_mulk_i32 s56, 0x2400\n\tv_lshrrev_b32 v119, 5, %[t]\n\tv_mul_u32_u24 v119, 0x480, v119\n\tv_add_u32 v119, s56, v119\n\t"
    "v_lshl_add_u32 v116, v118, 4, v119\n\tv_lshl_add_u32 v117, v118, 2, v119\n\tv_lshlrev_b32 v114, 4, v118\n\tv_lshlrev_b32 v115, 2, v118\n\t"
    "v_lshlrev_b32 v119, 1, %[t]\n\tv_mov_b32 v118, 0\n\tds_write_b16 v119, v118\n\t"
    "s_waitcnt lgkmcnt(0)\n\t"
    "s_load_dwordx2 s[96:97], s[66:67], 0x0\n\t"
    "global_load_dwordx4 %[wa], v114, s[68:69]\n\tglobal_load_dwordx4 %[wb], v114, s[68:69] offset:512\n\tglobal_load_dword %[wc], v115, s[68:69] offset:1024\n\t"
    "global_load_dwordx4 %[xa], v116, s[64:65]\n\tglobal_load_dwordx4 %[xb], v116, s[64:65] offset:512\n\tglobal_load_dword %[xc], v117, s[64:65] offset:1024\n\t"
    "s_barrier\n\t"
    "s_branch 8f\n"
    "1:\n\ts_cmp_ge_u32 s62, " STR(CPF) "\n\ts_cbranch_scc1 6f\n\ts_getpc_b64 s[56:57]\n.Lkpc:\n\ts_and_b32 s58, s62, 3\n\ts_lshl_b32 s58, s58, 13\n\ts_sub_u32 s58, s58, 0x1000\n\tv_lshl_add_u32 v119, %[t], 4, s58\n\tv_min_u32 v119, .Lkend-.Lkpc-16, v119\n\t"
    "global_load_dwordx4 v[120:123], v119, s[56:57]\n\t.subsection 1\n.Lkend:\n\t.subsection 0\n6:\n\t"
    "v_mov_b32 v124, -1\n\tv_mov_b32 v125, -1\n\tv_mov_b32 v126, -1\n\tv_mov_b32 v127, -1\n\tv_lshlrev_b32 v119, 4, %[t]\n\tv_and_b32 v119, 0xff0, v119\n\t"
    "s_lshr_b32 s59, s60, 8\n\ts_cmp_lt_u32 s62, 8\n\ts_cselect_b32 s58, 2, 1\n\ts_cbranch_scc1 2f\n\ts_cmp_eq_u32 s59, 3\n\ts_cbranch_scc1 4f\n"
    "2:\n\ts_sub_u32 s57, s59, 2\n\ts_cmp_eq_u32 s59, 1\n\ts_cselect_b32 s58, s58, s57\n\ts_cmp_lt_u32 s62, 8\n\ts_cselect_b32 s56, 0, 8\n\ts_lshl1_add_u32 s56, s62, s56\n\ts_mulk_i32 s61, 72\n\ts_add_u32 s51, s56, s61\n\ts_sub_u32 s56, 2, s59\n\ts_sub_u32 s57, 16, s62\n\ts_cmp_eq_u32 s59, 3\n\ts_cselect_b32 s50, s57, s56\n\ts_add_u32 s56, s50, s51\n\t"
    "v_mov_b32 v112, 0\n\tv_mov_b32 v113, 0\n\tv_mov_b32 v114, 0\n\tv_mov_b32 v115, 0\n\tv_mov_b32 v116, 0\n\tv_mov_b32 v117, 0\n\ts_setprio 2\n\t"
    "s_lshl_b32 s57, s56, 2\n\ts_add_u32 s57, s57, s63\n\ts_lshl_b32 s57, s57, 6\n\tv_lshl_add_u32 v22, v118, 1, s57\n\ts_mul_i32 s57, s56, %[yst]\n\ts_add_u32 s57, s57, %[wy]\n\tv_add_u32 v23, s57, v119\n\ts_lshl_b32 s56, s56, 1\n\ts_and_b32 s56, s56, -4\n\t"
    EB1 "s_waitcnt lgkmcnt(0)\n\t"
    "global_load_short_d16_hi v112, v22, s[80:81]" QPOL "\n\tglobal_load_short_d16_hi v113, v22, s[82:83]" QPOL "\n\tglobal_load_short_d16_hi v114, v22, s[84:85]" QPOL "\n\t"
    "global_load_short_d16_hi v115, v22, s[86:87]" QPOL "\n\tglobal_load_short_d16_hi v116, v22, s[88:89]" QPOL "\n\tglobal_load_short_d16_hi v117, v22, s[90:91]" QPOL "\n\t"
    "s_load_dwordx2 s[96:97], s[66:67], 0x0\n\ts_load_dword s98, s[92:93], s56\n\t"
    EB0 IV0
    "s_mul_i32 s52, s58, %[sl]\n\ts_add_u32 s52, s52, %[tb]\n\ts_lshl_b32 s53, s63, 7\n\ts_add_u32 s53, s53, s52\n\tv_lshl_add_u32 v30, v118, 2, s53\n\tv_bfe_u32 v31, %[t], 5, 1\n\ts_movk_i32 s54, 544\n\tv_mul_u32_u24 v29, s54, v31\n\t"
    "v_lshl_add_u32 v2, v29, 3, v30\n\ts_movk_i32 s53, 1088\n\tv_add_u32 v3, s53, v2\n\tv_add_u32 v4, s53, v3\n\tv_add_u32 v5, s53, v4\n\tv_add_u32 v6, 41*544, v2\n\tv_add_u32 v7, s53, v6\n\tv_add_u32 v8, s53, v7\n\tv_add_u32 v9, s53, v8\n\t"
    "v_lshl_add_u32 v10, v29, 2, v30\n\tv_add_u32 v10, 16*544, v10\n\tv_add_u32 v11, s53, v10\n\tv_lshl_add_u32 v12, v29, 1, v30\n\tv_add_u32 v12, 24*544, v12\n\tv_add_u32 v13, 2176, v12\n\tv_add_u32 v14, v29, v30\n\tv_add_u32 v14, 32*544, v14\n\t"
    "s_lshl_b32 s53, s58, 4\n\ts_add_u32 s53, s53, %[ex]\n\tv_mov_b32 v15, s53\n\tv_cvt_f32_ubyte0 v31, v31\n\tv_mul_f32 v16, 0x42800000, v31\n\tv_add_f32 v17, v16, v16\n\tv_mov_b32 v20, 1\n\t"
    "v_and_b32 v29, 15, %[t]\n\tv_and_b32 v30, 48, %[t]\n\tv_mad_u32_u24 v30, v29, s54, v30\n\ts_lshl_b32 s55, s59, 1\n\ts_lshr_b32 s55, 0x48, s55\n\ts_add_u32 s55, s55, s63\n\ts_and_b32 s55, s55, 3\n\ts_mul_i32 s53, s55, 16*544\n\ts_movk_i32 s57, 41*544\n\ts_cmp_eq_u32 s55, 2\n\ts_cselect_b32 s53, s57, s53\n\ts_cselect_b32 s57, 32*544, s57\n\t"
    "s_add_u32 s53, s53, s52\n\ts_add_u32 s57, s57, s52\n\tv_add_u32 v18, s53, v30\n\tv_add_u32 v19, s57, v30\n\t"
    "v_min_u32 v29, 237*16, v119\n\tv_subrev_u32 v30, 204*16, v29\n\tv_add_u32 v29, %[de], v29\n\tv_min_u32 v29, v29, v30\n\ts_sub_u32 s53, s52, 4640\n\tv_add_u32 v29, s53, v29\n\tv_mov_b32 v24, 0\n\tv_mov_b32 v25, 0\n\tv_mov_b32 v26, 0\n\tv_mov_b32 v27, 0\n\tds_write_b128 v29, v[24:27]\n\t"
    IV1
    "s_cmp_lg_u32 s55, 3\n\ts_cbranch_scc1 9f\n\t"
    "v_bfe_u32 v18, %[t], 1, 5\n\tv_and_b32 v19, 1, %[t]\n\ts_cmp_lt_u32 s62, 8\n\ts_cselect_b32 s53, 2, 1\n\ts_mul_i32 s53, s53, %[sl]\n\tv_mul_u32_u24 v21, s53, v19\n\ts_sub_u32 s55, 2, s59\n\ts_lshl_b32 s55, s55, 7\n\tv_add_u32 v18, s55, v18\n\ts_cmp_lg_u32 s59, 3\n\ts_cbranch_scc1 7f\n\t"
    "v_bfe_u32 v18, %[t], 5, 1\n\tv_lshl_add_u32 v18, v18, 7, v118\n\tv_mov_b32 v19, s50\n\tv_mov_b32 v21, %[sl]\n"
    "7:\n\tv_add_u32 v19, s51, v19\n\tv_lshl_add_u32 v21, v18, 4, v21\n\tv_lshrrev_b32 v24, 3-" STR(TL) ", v19\n\tv_and_b32 v19, 7>>" STR(TL) ", v19\n\tv_lshlrev_b32 v19, 4, v19\n\tv_lshl_add_u32 v19, v24, 7, v19\n\t"
    "v_and_b32 v24, (1<<" STR(TL) ")-1, v18\n\tv_lshl_add_u32 v19, v24, 7-" STR(TL) ", v19\n\tv_lshrrev_b32 v18, " STR(TL) ", v18\n\tv_mul_u32_u24 v18, %[ust], v18\n\tv_add_u32 v18, v18, v19\n\tv_add_u32 v19, 512, v21\n\t"
    "s_branch 9f\n"
    "4:\n\t" EB1
    "v_bfe_u32 v118, %[t], 3, 5\n\tv_lshrrev_b32 v117, 1, v118\n\tv_and_b32 v117, 4, v117\n\tv_and_b32 v116, 2, v118\n\tv_and_b32 v115, 7, %[t]\n\tv_or_b32 v116, v116, v117\n\tv_xor_b32 v115, v115, v116\n\tv_lshlrev_b32 v115, 4, v115\n\t"
    "s_mul_i32 s54, s61, 9\n\ts_sub_u32 s55, s62, 14\n\ts_lshr_b32 s55, s55, 1\n\ts_add_u32 s54, s54, s55\n\ts_lshl_b32 s54, s54, 7\n\t"
    "s_mul_i32 s52, s61, 0xa2000\n\ts_mul_i32 s53, s62, 0x180\n\ts_add_u32 s52, s52, s53\n\ts_cmp_ge_u32 s62, 11\n\ts_cselect_b32 s53, 0x51000-0x480, 0\n\ts_add_u32 s52, s52, s53\n\ts_sub_u32 s52, s52, 0xc00\n\t"
    "s_movk_i32 s55, 0x480\n\ts_cmp_ge_u32 s62, 14\n\ts_cselect_b32 s54, s54, s52\n\ts_cselect_b32 s55, 0x2400, s55\n\tv_mad_u32_u24 v118, v118, s55, v115\n\ts_lshl_b32 s55, s55, 5\n\t"
    "s_lshl_b32 s52, s63, 10\n\ts_add_u32 m0, s52, %[r2]\n\ts_mov_b32 s58, 0x510000\n\ts_mov_b32 s59, 0x20000\n\ts_cmp_ge_u32 s62, 14\n\ts_cselect_b32 s53, " STR(PRE1) "-1, " STR(PRE2) "-1\n\ts_setprio 3\n\t"
    "s_waitcnt lgkmcnt(0)\n\t"
    "s_bitcmp1_b32 %[wg], 3\n\ts_cselect_b64 s[56:57], s[74:75], s[72:73]\n\ts_cmp_ge_u32 s62, 14\n\ts_cselect_b64 s[56:57], s[56:57], s[76:77]\n\ts_cselect_b32 s52, 0x180, -1\n\ts_and_b32 s57, s57, 0xffff\n\ts_add_u32 s60, s56, s54\n\ts_and_b32 s60, s60, 0x380\n\ts_cmp_eq_u32 s60, s52\n\ts_cselect_b32 s53, " STR(PRE3) "-1, s53\n\t"
    "s_load_dwordx2 s[96:97], s[66:67], 0x0\n\t"
    EB0
    "\n3:\n\t" "buffer_load_dwordx4 v118, s[56:59], s54 offen" WPOL " lds\n\ts_add_u32 m0, m0, 0x1000\n\ts_add_u32 s54, s54, s55\n\ts_sub_u32 s53, s53, 1\n\ts_cbranch_scc0 3b\n"
    "9:\n8:"
    : [xa]"=&{v[2:5]}"(xa),[xb]"=&{v[6:9]}"(xb_),[xc]"=&{v30}"(xc),[wa]"=&{v[10:13]}"(wa),[wb]"=&{v[14:17]}"(wb),[wc]"=&{v31}"(wc),[d0]"=&{v[18:21]}"(dq[0]),[d1]"=&{v[22:25]}"(dq[1]),[d2]"=&{v[26:29]}"(dq[2])
    : [ka]"s"(__builtin_amdgcn_kernarg_segment_ptr()),[wg]"s"(BID),[t]"v"(TID),[yst]"i"(YST),[wy]"i"(W_Y),[r2]"i"(O_R2),[sl]"i"(SLSZ),[tb]"i"(O_CH+O_TB),[ex]"i"(O_EXP),[de]"i"(O_DE),[ust]"i"(GST_)
    : "memory","scc","m0","v127","s50","s51","s52","s53","s54","s55","s56","s57","s58","s59","s60","s61","s62","s63","s64","s65","s66","s67","s68","s69","s70","s71","s72","s73","s74","s75","s76","s77","s78","s79",
      "s80","s81","s82","s83","s84","s85","s86","s87","s88","s89","s90","s91","s92","s93","s94","s95","s99");
  int wv=RFL(TID>>6), bid=BID, q4=wv>>2, w4=wv&3;
  int xg=bid&7, lb=bid>>3, tb=32*xg+lb, nch=2+(lb<8);
  u64 ep; u32 ldw;
  {
  }
  #define KILLIN "=v"(xa),"=v"(xb_),"=v"(xc),"=v"(wa),"=v"(wb),"=v"(wc),"=v"(dq[0]),"=v"(dq[1]),"=v"(dq[2])
  if(__builtin_expect(q4==3&&lb>=8,0)){
    int fu=18*xg+lb-14, kq=2*xg+(lb>=11), cgp=lb-8-3*(lb>=11);
  u32 wst= lb>=14? F_*64:D_*64;
  u32 wso=(lb>=14? (fu>>1)*128 : kq*(288*D_*2)+384*cgp)+(lb>=14? PRE1:PRE2)*wst, wld=(u32)(size_t)sm+O_R2+w4*1024+(lb>=14? PRE1:PRE2)*4096; u32x4 wrs=mkrs(lb>=14? ((fu&1)? wu:wg):wd,D_*F_*2);
  GETEP
  u64 tag=ep+1ull;
  #define FLANE(RS) LANE(ln); int g=ln>>4, li=ln&15, qq=li>>2, pq=li&3; u32 wvo=(u32)(8*w4+(ln>>3))*(RS)+(u32)((ln&7)^(((ln>>3)&2)|((w4&1)<<2)))*16u; \
    const lch* tp=sm+O_R2+(8*g+qq)*128+((w4^((qq>>1)|((g&1)<<1)))*32)+pq*8;
  #define WFR(img,ks) trfrag(tp+(img)*CHSZ+(ks)*4096,tp+(img)*CHSZ+(ks)*4096+512)
  const u32x4 I4={INV,INV,INV,INV};
  if(lb>=14){
    FLANE(F_*2)
    int mt=fu&1;
    const lch* xp=sm+O_TILE+li*HSTR+g*16; int of=(mt*288+(fu>>1)*4+w4)*256+(li&7)*32+g*8+(li>>3)*4;
    const lch* mp=sm+O_RC+(li&7)*4;
    asm volatile("" : "+v"(xp),"+v"(of),"+v"(mp));
    rsrc_t rsG=RSRC(ws+W_GU,2*8*F_*2);
    if(w4==0) BST128(I4,rsG,(mt*288+(fu>>1)*4)*256+ln*16,0,SC1);
    bool slow=(((u32)wrs[0]+wso)&0x380u)==0x180u;
    if(slow){ wso-=(PRE1-PRE3)*wst; wld-=(PRE1-PRE3)*4096; FILL(5+PRE1-PRE3,VW(W3)); } else FILLD(5,W1,W1B,"");
    if(NA){ VMW(11-NA); garrive(sm,C_P); }
    if(w4==0){ VMW(5); if(ln==0) GST(ws+W_FL+(576+fu)*8,tag); }
    if(slow) FILLD(13-PRE1,W3,W1B,STKB); else FILLD(13-PRE1,W1,W1B,STKB);
    bf16x8 fB[18]; f32x4 cs={0,0,0,0}; const bf16x8 on=BC(bf16x8,(u32x4{0x3f803f80u,0x3f803f80u,0x3f803f80u,0x3f803f80u}));
    const lch* tq=tp+CHSZ; PIN(tq);
    #define RDF(a,b) _Pragma("unroll") for(int ks=a;ks<b;ks++) fB[ks]= ks<9? WFR(0,ks) : trfrag(tq+(ks-9)*4096,tq+(ks-9)*4096+512);
    #define CSF(a,b) SCHED(); _Pragma("unroll") for(int ks=a;ks<b;ks++) cs=__builtin_amdgcn_mfma_f32_16x16x32_bf16(fB[ks],on,cs,0,0,0); SCHED();
    gwait<0>(sm,C_P,4); RDF(0,NA)
    VMW(18-NB); garrive(sm,C_P); CSF(0,NA) gwait<0>(sm,C_P,8); RDF(NA,NB)
    VMW(LAST1); garrive(sm,C_P); CSF(NA,NB) gwait<0>(sm,C_P,12); RDF(NB,18-LAST1)
    DRAIN(); garrive(sm,C_P); CSF(NB,18-LAST1) gwait<0>(sm,C_P,16);
    float cA,cB,ri,ce,p0,p1,q0,q1; u32 mdw;
    { bf16x8 xb[9];
      RDF(18-LAST1,18) SCHED();
      u32 cx=*(const volatile LAS u32*)(sm+C_X*4);
      #pragma unroll
      for(int ks=0;ks<4;ks++) xb[ks]=*(const volatile LAS bf16x8*)(xp+ks*64);
      CSF(18-LAST1,18)
      if(RFL(cx)<4u){ gwait<XS1>(sm,C_X,XG1); gwait4<64>(C_X,4,xp,xb[0],xb[1],xb[2],xb[3]); }
      mdw=LD(u32,mp); xb[4]=LD(bf16x8,xp+256); xb[5]=LD(bf16x8,xp+320);
      SCHED();
      #pragma unroll
      for(int ks=0;ks<8;ks++){
        if(ks==0) asm volatile("v_mfma_f32_16x16x32_bf16 v[112:115], %0, %2, 0\n\tv_mfma_f32_16x16x32_bf16 v[116:119], %1, %2, 0" :: "v"(fB[0]),"v"(fB[9]),"v"(xb[0]));
        else asm volatile("v_mfma_f32_16x16x32_bf16 v[112:115], %0, %2, v[112:115]\n\tv_mfma_f32_16x16x32_bf16 v[116:119], %1, %2, v[116:119]" :: "v"(fB[ks]),"v"(fB[ks+9]),"v"(xb[ks]));
        SCHED();
        if(ks<3) xb[ks+6]=LD(bf16x8,xp+(ks+6)*64); if(ks==HG) garrive(sm,C_U);
        if(ks==2){ PIN(cs); cA= li<8? cs[0]:cs[2]; cB= li<8? cs[1]:cs[3]; PIN(cA); PIN(cB); }
        if(ks==3){ PIN(mdw); ri=rcpf_(bhi(mdw)); float nm=-blo(mdw); cA*=nm; cB*=nm; PIN(cA); PIN(cB); } if(ks==4){ ce=ri*-1.4426950408889634f; p0=cA*ri; p1=cB*ri; PIN(p0); PIN(p1); } if(ks==5){ q0=cA*ce; q1=cB*ce; PIN(q0); PIN(q1); }
        SCHED(); }
      #define RR8 " row_ror:8 row_mask:0xf bank_mask:"
      float v0,v1,s0,s1;
      asm volatile("v_mfma_f32_16x16x32_bf16 v[112:115], %4, %6, v[112:115]\n\tv_mfma_f32_16x16x32_bf16 v[116:119], %5, %6, v[116:119]\n\ts_nop 7\n\tv_add_f32_dpp %0, v116, v112" RR8 "0x3\n\tv_add_f32_dpp %1, v117, v113" RR8 "0x3\n\tv_add_f32_dpp %0, v114, v118" RR8 "0xc\n\tv_add_f32_dpp %1, v115, v119" RR8 "0xc\n\t"
        "v_fmac_f32 %2, %0, %8\n\tv_fmac_f32 %3, %1, %8\n\tv_fma_f32 %0, %0, %7, %9\n\tv_fma_f32 %1, %1, %7, %10"
        : "=&v"(v0),"=&v"(v1),"+v"(q0),"+v"(q1) : "v"(fB[8]),"v"(fB[17]),"v"(xb[8]),"v"(ri),"v"(ce),"v"(p0),"v"(p1));
      s0=q0; s1=q1;
      if(mt==0){ v0*=rcpf_(1.f+__builtin_amdgcn_exp2f(s0)); v1*=rcpf_(1.f+__builtin_amdgcn_exp2f(s1)); }
      __builtin_amdgcn_raw_buffer_store_b32(nv(pk(v0,v1)),rsG,of,0,SC1); garrive(sm,C_H); }
    if(mt==0&&w4==0){
    LANE(l2); ln=l2; li=ln&15; g=ln>>4;
    int c9=(lb-14)>>1, tk=(xg&6)+((li>>2)&1), t=(int)LD(u16,sm+O_IDX+tk*2), c=64*c9+32*(xg&1)+16*(li>>3)+4*(li&3)+g; f32x4 pr;
    { u32 u=LD(u16,sm+O_IDX+16+tk*2)^0x8000u, xb=LD(u16,sm+O_TILE+XAD(tk,c*2)), sv=LD(u32,sm+O_RC+tk*4); float ds=b2f(Dsk[c]); u^=(u32)(((int)(u<<16))>>31)&0x7fffu;
      poll1<0>(ws+W_FL+(64*c9+ln)*8,tag);
 poll1<0>(ws+W_FL+(720+(ln<48? ln:47))*8,tag);
      float xn=(b2f(xb)-blo(sv))*rcpf_(bhi(sv)); pr=f32x4{b2f(xb),xn,xn*ds,sigm_f(b2f(u))}; }
    gwait<2>(sm,C_PR,PRN);
    { u32x4 rs=mkrs(ws+W_PART+(xg*9+c9)*4096,4096), ry=mkrs(ws+W_Y,D_*YST);
      int yo=c*YST+(t&~1)*2, po=ln*16; u32 a[4], t0,cnt,yb; u16* op=out+t*D_+c; u32 ysl=0x3f80u<<((t&1)*16); PIN(ysl); f32x2 pe={pr[1]*-1.4426950408889634f,pr[3]*-1.4426950408889634f};
      #define LD_P "buffer_load_dwordx4 v[112:115], %[of], %[rs], 0 offen sc1\n\tbuffer_load_dwordx4 v[116:119], %[of], %[rs], 0 offen offset:1024 sc1\n\tbuffer_load_dwordx4 v[120:123], %[of], %[rs], 0 offen offset:2048 sc1\n\tbuffer_load_dwordx4 v[124:127], %[of], %[rs], 0 offen offset:3072 sc1\n\t"
      #define LD_YB "buffer_load_dword %[yb], %[yo], %[ry], 0 offen sc1\n\t"
      #define AD4(o,a,b,c,d) "v_add_f32 %[t0], v" #a ", v" #b "\n\tv_add_f32 %[" #o "], v" #c ", v" #d "\n\tv_add_f32 %[" #o "], %[t0], %[" #o "]\n\t"
      u64 ym;
      #define PGAP "s_sleep " STR(PSL) "\n\t"
      u64 nm;
      asm volatile(LD_YB PGAP LD_P "s_mov_b32 %[cnt], 0x20000\n1:\n\ts_waitcnt vmcnt(0)\n\t" AD4(a0,112,116,120,124) AD4(a1,113,117,121,125) "v_cmp_eq_u32 %[ym], -1, %[yb]\n\t" AD4(a2,114,118,122,126) "v_cmp_u_f32 %[nm], %[a0], %[a1]\n\t" AD4(a3,115,119,123,127)
        "s_or_b64 %[nm], %[nm], %[ym]\n\tv_cmp_u_f32 vcc, %[a2], %[a3]\n\ts_or_b64 vcc, vcc, %[nm]\n\ts_cbranch_scc0 9f\n\t"
        CHK_I0 MX2(116,117) MX2(118,119) MX2(120,121) MX2(122,123) MX2(124,125) MX2(126,127) CHK_IE
        "s_or_b64 exec, vcc, %[ym]\n\ts_cbranch_scc0 8f\n\ts_sleep " STR(QSLP) "\n\ts_mov_b64 exec, %[ym]\n\ts_cbranch_execz 2f\n\t" LD_YB "2:\n\ts_mov_b64 exec, vcc\n\ts_cbranch_execz 3f\n\t" LD_P
        "3:\n\ts_mov_b64 exec, -1\n\ts_sub_u32 %[cnt], %[cnt], 1\n\ts_cbranch_scc0 1b\n8:\n\ts_mov_b64 exec, -1\n9:"
        : [t0]"=&v"(t0),[cnt]"=&s"(cnt),[a0]"=&v"(a[0]),[a1]"=&v"(a[1]),[a2]"=&v"(a[2]),[a3]"=&v"(a[3]),[yb]"=&v"(yb),[ym]"=&s"(ym),[nm]"=&s"(nm) : [of]"v"(po),[rs]"s"(rs),[yo]"v"(yo),[ry]"s"(ry),"v"(op),"v"(pr),"v"(pe) : PCLOB);
      auto s0=__builtin_amdgcn_permlane32_swap(a[0],a[2],false,false); auto s1=__builtin_amdgcn_permlane32_swap(a[1],a[3],false,false);
      u32 x0=s0[0],x1=s0[1],y0=s1[0],y1=s1[1]; u32 p0=BC(u32,BC(float,x0)+BC(float,x1)), p1=BC(u32,BC(float,y0)+BC(float,y1));
      auto s2=__builtin_amdgcn_permlane16_swap(p0,p1,false,false); u32 z0=s2[0], z1=s2[1]; float s=BC(float,z0)+BC(float,z1);
      float gq=__builtin_fmaf(s,pr[3],pr[1])*rcpf_(1.f+__builtin_amdgcn_exp2f(__builtin_fmaf(s,pe[1],pe[0])));
      float sy=D2(yb,ysl,pr[2]);
      float o=__builtin_fmaf(sy,gq,pr[0]);
      *op=(u16)(pk(o,o)&0xffffu);
      if(bid==112&&ln==0) GST(ws+W_EPOCH,tag); }
    }
  } else
  {
    FLANE(D_*2)
    bf16x8 fr[27];
    rsrc_t rsP=RSRC(ws+W_PART+cgp*12288+kq*256+PWB,8*9*4096-2*12288);
    #pragma unroll
    for(int i=0;i<3;i++) BST128(I4,rsP,li<8? PLO+g*16+i*4096 : OOR,0,SC1);
    if(PRE2<9) FILLD(9-PRE2,W2_,W2B,"");
    wso+=128u-9u*wst;
    FILLD(9,W2_,W2B,"");
    VMW(9); gsync(sm,C_P,4);
    #pragma unroll
    for(int ks=0;ks<9;ks++) fr[ks]=WFR(0,ks);
    gsync(sm,C_P,8);
    if(w4==0&&ln==0) GST(ws+W_FL+(720+6*xg+lb-8)*8,tag);
    wso+=128u-9u*wst; wld-=2*CHSZ;
    FILL(9,VW(W2B));
    int fo=W_FL+(576+((9*kq)&~1)+(ln<9? ln:9))*8;
    if(w4==0) asm volatile("global_load_dwordx2 v[112:113], %0, %1 sc1\n\ts_waitcnt vmcnt(10)" :: "v"(fo),"s"(ws) : "memory"); else VMW(9);
    gsync(sm,C_P,12);
    #pragma unroll
    for(int ks=0;ks<9;ks++) fr[9+ks]=WFR(1,ks);
    { int t2=TID; asm volatile("" : "+v"(t2)); ln=t2&63; li=ln&15; g=ln>>4; }
    const lch* hp=sm+O_TILE+(li&7)*HT+g*16; int po= li<8? PLO+g*16+4096 : OOR; PIN(hp); PIN(po);
    u32x4 rs=mkrs(ws+W_GU,2*8*F_*2);
    int of=((ln>>5)*288+18*kq+2*w4)*256+(ln&31)*16; lch* hd=sm+O_TILE+w4*(8*HT)+((ln>>1)&7)*HT+((ln>>4)&1)*32+(ln&1)*16+(ln>>5)*8; PIN(of); PIN(hd); asm volatile("" : "+s"(rs));
    u32x4 rp=mkrs(ws+W_PART+cgp*12288+kq*256+PWB-4096,8*9*4096); u32 ch=C_H*4, one=1; int o3= w4==0? of+4096:OOR; PIN(ch); PIN(one); PIN(o3); asm volatile("" : "+s"(rp));
    DRAIN();
    if(w4==0){ u64 bad; asm volatile("v_cmp_ne_u64 %0, %1, v[112:113]" : "=s"(bad) : "s"(tag) : "memory");
      if(bad!=0ull) poll1<0>(ws+fo,tag); }
    __builtin_amdgcn_s_waitcnt(0x0f70);
    gsync(sm,C_P,16);
    { u32 t0,t1,cnt;
      #define LD_G "buffer_load_dwordx4 v[112:115], %[of], %[rs], 0 offen sc1\n\tbuffer_load_dwordx4 v[116:119], %[of], %[rs], 0 offen offset:2048 sc1\n\tbuffer_load_dwordx4 v[120:123], %[o3], %[rs], 0 offen sc1\n\t"
      #define HM1(a,b) "v_and_b32 %[t0], %[lm], v" #b "\n\tv_and_b32 v" #b ", %[hm], v" #b "\n\tv_dot2_f32_bf16 %[t0], v" #a ", %[t0], 0\n\tv_dot2_f32_bf16 v" #a ", v" #a ", v" #b ", 0\n\t"
      #define HM2(a,b) "v_and_b32 %[t1], %[lm], v" #b "\n\tv_and_b32 v" #b ", %[hm], v" #b "\n\tv_dot2_f32_bf16 %[t1], v" #a ", %[t1], 0\n\tv_dot2_f32_bf16 v" #a ", v" #a ", v" #b ", 0\n\t"
      #define HMUL(a,b,c,d,o) "v_permlane32_swap_b32 v" #a ", v" #c "\n\tv_permlane32_swap_b32 v" #b ", v" #d "\n\t" HM1(a,c) HM2(b,d) "v_cvt_pk_bf16_f32 v" #a ", %[t0], v" #a "\n\ts_nop 1\n\tv_cvt_pk_bf16_f32 v" #b ", %[t1], v" #b "\n\tds_write_b64 %[hd], v[" #a ":" #b "] offset:" STR(o) "\n\t"
      if(GD) __builtin_amdgcn_s_sleep(GD);
      asm volatile(LD_G :: [of]"v"(of),[rs]"s"(rs),[o3]"v"(o3) : "memory");
      #pragma unroll
      for(int ks=0;ks<G2;ks++) fr[18+ks]=WFR(0,ks);
      #pragma unroll
      for(int ks=0;ks<G2;ks++) PIN(fr[18+ks]);
      asm volatile(POLLASM(LD_G,CHK_I0 MX2(116,117) MX2(118,119) MX2(120,121) MX2(122,123) CHK_IE,GSLP)
        HMUL(112,113,114,115,0) HMUL(116,117,118,119,32*HT) "s_cmp_lg_u32 %[w4], 0\n\ts_cbranch_scc1 8f\n\t" HMUL(120,121,122,123,64*HT) "8:\n\ts_mov_b64 exec, 1\n\tds_add_u32 %[ch], %[one]\n\ts_mov_b64 exec, -1\n\t"
        "s_mov_b32 %[cnt], 0x20000\n6:\n\tds_read_b32 %[t0], %[ch]\n\tds_read_b128 v[112:115], %[hp]\n\tds_read_b128 v[116:119], %[hp] offset:8*" STR(HT) "\n\tds_read_b128 v[120:123], %[hp] offset:16*" STR(HT) "\n\tds_read_b128 v[124:127], %[hp] offset:24*" STR(HT) "\n\ts_waitcnt lgkmcnt(4)\n\t"
        "v_cmp_gt_u32 vcc, 4, %[t0]\n\ts_cbranch_vccz 7f\n\ts_sleep 1\n\ts_sub_u32 %[cnt], %[cnt], 1\n\ts_cbranch_scc0 6b\n7:"
        : [t0]"=&v"(t0),[t1]"=&v"(t1),[cnt]"=&s"(cnt) : [of]"v"(of),[rs]"s"(rs),[o3]"v"(o3),[w4]"s"(w4),[hd]"v"(hd),[hm]"s"(0xffff0000u),[lm]"s"(0xffffu),[ch]"v"(ch),[one]"v"(one),[hp]"v"(hp),"v"(po) : PCLOB); }
    { bf16x8 hb[9];
      #define MF "v_mfma_f32_16x16x32_bf16 "
      #define MF3(h) MF "%0, %3, " h ", %0\n\t" MF "%1, %4, " h ", %1\n\t" MF "%2, %5, " h ", %2"
      #define ACC "+v"(fr[0]),"+v"(fr[9]),"+v"(fr[18])
      #define FRS(k) "v"(fr[k]),"v"(fr[9+(k)]),"v"(fr[18+(k)])
      asm volatile("s_waitcnt lgkmcnt(3)\n\t" MF "%0, %0, v[112:115], 0\n\t" MF "%1, %1, v[112:115], 0\n\t" MF "%2, %2, v[112:115], 0\n\ts_waitcnt lgkmcnt(0)\n\t" MF3("v[116:119]") : ACC : FRS(1));
      hb[4]=LD(bf16x8,hp+4*8*HT); hb[5]=LD(bf16x8,hp+5*8*HT); hb[6]=LD(bf16x8,hp+6*8*HT); SCHED();
      asm volatile(MF3("v[120:123]") : ACC : FRS(2));
      #pragma unroll
      for(int ks=G2;ks<9;ks++) fr[18+ks]=WFR(0,ks);
      SCHED();
      asm volatile(MF3("v[124:127]") : ACC : FRS(3));
      hb[7]=LD(bf16x8,hp+7*8*HT); hb[8]=LD(bf16x8,hp+8*8*HT); SCHED();
      #pragma unroll
      for(int ks=4;ks<8;ks++){ asm volatile(MF3("%6") : ACC : FRS(ks),"v"(hb[ks])); SCHED(); }
      asm volatile(MF3("%6") "\n\ts_nop 5\n\tbuffer_store_dwordx4 %0, %7, %8, 0 offen sc1\n\tbuffer_store_dwordx4 %1, %7, %8, %9 offen sc1\n\tbuffer_store_dwordx4 %2, %7, %8, %10 offen sc1\n\ts_mov_b64 exec, 1\n\tds_add_u32 %11, %12\n\ts_mov_b64 exec, -1"
        : ACC : FRS(8),"v"(hb[8]),"v"(po),"s"(rp),"s"(4096),"s"(8192),"v"(ch),"v"(one) : "memory"); }

  }
  asm volatile("s_endpgm" : KILLIN :: "memory"); }
  {
  if(__builtin_expect(q4!=0,0)){ int grp= q4==1? nch-1 : q4-2;
    chan(sm,grp,(w4+(0x48>>(2*q4)))&3,72*xg+(q4==3? 16+lb : 2*lb+(lb<8? 0:8)+2-q4),(q4+1)>>2,ws,xa,xb_,wa,wb,dq[0]);
    asm volatile("s_endpgm" : KILLIN :: "memory"); }
  u32x4 xq[3]; f32x2 rc; int xr_,xr2_;
  {
    LANE(ln); int g=ln>>4, li=ln&15, qq=li>>2, pq=li&3, j=ln&31, rl=2*w4+(ln>>5);
    lch* tile=sm+O_TILE; u32 Tr=(ln>>1)&16;
    int uo=W_U+(tb>>TL)*GST_+(tb&((1<<TL)-1))*(128>>TL)+(w4*1024<<TL)+(ln>>(3-TL))*128+(ln&(7>>TL))*16, uo1=uo+(4096<<TL), uo2=uo+(8192<<TL); PIN(uo); PIN(uo1); PIN(uo2);
    lch* tw=tile+rl*TSTR2+j*16; lch* tw2=tw+1024-j*12; u32 one=0x3f803f80u; const lch* tp=tile+qq*TSTR2+(w4*64+g*16+pq*4)*2;
    int go= ln<32? W_RG+(ln>>2)*8192+(tb*4+(ln&3))*8 : W_ST+(tb*8+(ln&7))*8; lch* kw=sm+O_RV+rl*2; const lch* kr=sm+(ln<32? O_RV+(ln&3)*4 : O_MD+(ln&7)*4); PIN(kw); PIN(kr);
    int xr=(32*(tb>>2)+8*g+(li&7))*(D_*2)+288*(lb&3)+32*w4+(ln&8)*2, xr2= w4==0? xr+256:OOR;
    rsrc_t rx=RSRC(x,L_*D_*2); u64 gm=((1ull<<(4*NRG))-1ull)|0xff00000000ull; u32 um=~UMASK, km=0x80008000u; asm volatile("" : "+s"(gm),"+s"(um),"+s"(km),"+s"(ws));
    asm volatile("s_waitcnt vmcnt(3)" : "+v"(wa),"+v"(wb),"+v"(wc),"+v"(Tr),"+v"(tw),"+v"(tw2),"+v"(tp),"+v"(one),"+v"(go),"+v"(xr),"+v"(xr2) :: "memory");
    u32 wq[9]={wa[0],wa[1],wa[2],wa[3],wb[0],wb[1],wb[2],wb[3],wc}; float sw;
    { float a[3];
      #pragma unroll
      for(int i=0;i<9;i++) a[i%3]=D2(wq[i],one,i<3? 0.f:a[i%3]);
      sw=add16x(sum16((a[0]+a[1])+a[2])); }
    GETEP u32 tg=(u32)ep+1u; Tr=((upat(tg,w4)>>Tr)&0xffffu)*0x10001u;
    float s1=0.f, s2=0.f, s3=0.f, i576=1.f/576.f; u32 hm=0xffff0000u, zero=0, c1=1; u64 wm=0x0001000000010000ull; lch* kw2=sm+O_MD+rl*4; asm volatile("" : "+s"(hm));
    asm volatile("v_mov_b32 v119, %12\n\ts_waitcnt vmcnt(0)" : "+v"(xa),"+v"(xb_),"+v"(xc),"+v"(Tr),"+v"(sw),"+v"(s1),"+v"(s2),"+v"(s3),"+v"(zero),"+v"(c1),"+v"(kw2),"+s"(wm) : "s"(tg),"s"(i576) : "memory");
    u32 v[9]={xa[0],xa[1],xa[2],xa[3],xb_[0],xb_[1],xb_[2],xb_[3],xc};
    { float xf[18];
      #pragma unroll
      for(int i=0;i<9;i++){ s1=D2(v[i],one,s1); SCHED(); s2=D2(v[i],v[i],s2); SCHED(); s3=D2(v[i],wq[i],s3); SCHED(); }
      float m1,m2;
      #define AD_(r,c) "v_add_f32_dpp %[" #r "], %[" #r "], %[" #r "] " c " bank_mask:0xf\n\t"
      #define UL_(i) "v_lshlrev_b32 %[f" #i "], 16, %[v" #i "]\n\t"
      #define RM " row_mask:0xf"
      asm volatile(UL_(0) UL_(1) "v_permlane16_swap_b32 %[a], %[b]\n\t" AD_(c,"quad_perm:[1,0,3,2]" RM) "v_add_f32 %[a], %[a], %[b]\n\t" UL_(2) AD_(c,"quad_perm:[2,3,0,1]" RM) AD_(a,"quad_perm:[1,0,3,2]" RM) UL_(3)
        AD_(c,"row_half_mirror" RM) AD_(a,"quad_perm:[2,3,0,1]" RM) UL_(4) AD_(c,"row_mirror" RM) AD_(a,"row_half_mirror" RM) UL_(5) AD_(c,"row_bcast:15 row_mask:0xa") AD_(a,"row_mirror" RM)
        "v_mul_f32 %[d], %[k], %[a]\n\tv_mul_f32 %[e], %[k], %[a]\n\t" UL_(6) UL_(7) "v_permlane16_swap_b32 %[d], %[e]"
        : [a]"+v"(s1),[b]"+v"(s2),[c]"+v"(s3),[d]"=&v"(m1),[e]"=&v"(m2),[f0]"=&v"(xf[0]),[f1]"=&v"(xf[2]),[f2]"=&v"(xf[4]),[f3]"=&v"(xf[6]),[f4]"=&v"(xf[8]),[f5]"=&v"(xf[10]),[f6]"=&v"(xf[12]),[f7]"=&v"(xf[14])
        : [v0]"v"(v[0]),[v1]"v"(v[1]),[v2]"v"(v[2]),[v3]"v"(v[3]),[v4]"v"(v[4]),[v5]"v"(v[5]),[v6]"v"(v[6]),[v7]"v"(v[7]),[k]"s"(i576));
      s1=m1; float mm=s1*s1, var=m2-mm;
      if(__builtin_expect(BALLOT(!(mm<=64.f*var))!=0ull,0)){
        float q=0.f;
        #pragma unroll
        for(int i=0;i<9;i++){ u32 t=v[i]; PIN(t); float d0=blo(t)-s1, d1=bhi(t)-s1; q=__builtin_fmaf(d0,d0,q); q=__builtin_fmaf(d1,d1,q); }
        var=add16x(sum16(q))*(1.f/576.f); }
      u32 vb=pk(var,s1);
      float den=blo(vb)+1e-5f, m=BC(float,vb&hm);
      vb=pk(den,den); den=__builtin_amdgcn_sqrtf(BC(float,vb&hm)); SCHED();
      s3=__builtin_fmaf(-m,sw,s3); PIN(s3); SCHED();
      u32 md=pk(m,den);
      float ri=rcpf_(BC(float,md&hm)); SCHED(); xf[16]=blo(v[8]); PIN(xf[16]); SCHED();
      s3*=ri;
      u32 u=pk(s3,s3);
      { u32 sg=(u32)((int)u>>31); u^=sg|km; }
      asm volatile("s_mov_b64 exec, %6\n\tds_write_b16 %0, %1\n\tds_write_b32 %2, %3\n\ts_mov_b32 exec_hi, 0\n\tds_add_u32 %4, %5 offset:%7\n\ts_mov_b64 exec, -1\n\t"
        "s_cmp_lg_u32 %8, 3\n\ts_cbranch_scc1 1f\n\tds_read_b32 v117, %4 offset:%7\n\tds_read_b32 v118, %9\n1:" :: "v"(kw),"v"(u),"v"(kw2),"v"(md),"v"(zero),"v"(c1),"s"(wm),"i"(C_I*4),"s"(w4),"v"(kr) : "memory","scc");
      SCHED();
      float rs_=ri*0.0625f, cs_=-m*rs_;
      #define UA_(i) "v_and_b32 %" #i ", %11, %" #i "\n\t"
      asm volatile(UA_(0) UA_(1) UA_(2) UA_(3) UA_(4) UA_(5) UA_(6) UA_(7) UA_(8) : "+v"(v[0]),"+v"(v[1]),"+v"(v[2]),"+v"(v[3]),"+v"(v[4]),"+v"(v[5]),"+v"(v[6]),"+v"(v[7]),"+v"(v[8]),"+v"(rs_),"+v"(cs_) : "s"(hm));
      #pragma unroll
      for(int i=0;i<9;i++) xf[2*i+1]=BC(float,v[i]);
      u32 h[9];
      #define UH(i) h[i]=(pkz(__builtin_fmaf(xf[2*(i)],rs_,cs_),__builtin_fmaf(xf[2*(i)+1],rs_,cs_))&um)|Tr;
      if(RPH>0){ UH(0) PIN(h[0]); } if(RPH>1){ UH(1) PIN(h[1]); } SCHED();
      { u32 cnt;
        asm volatile("s_cmp_lg_u32 %[w4], 3\n\ts_cbranch_scc1 3f\n\ts_mov_b32 %[cnt], 0x20000\n1:\n\ts_waitcnt lgkmcnt(0)\n\tv_cmp_gt_u32 vcc, 4, v117\n\ts_cbranch_vccz 2f\n\tds_read_b32 v117, %[z] offset:%[ci]\n\tds_read_b32 v118, %[kr]\n\ts_sub_u32 %[cnt], %[cnt], 1\n\ts_cbranch_scc0 1b\n"
          "2:\n\ts_mov_b64 exec, %[gm]\n\tglobal_store_dwordx2 %[go], v[118:119], %[ws] sc1\n\ts_mov_b64 exec, -1\n3:" : [cnt]"=&s"(cnt) : [w4]"s"(w4),[z]"v"(zero),[ci]"i"(C_I*4),[kr]"v"(kr),[gm]"s"(gm),[go]"v"(go),[ws]"s"(ws) : PCLOB); }
      SCHED();
      if(RPH<1){ UH(0) } if(RPH<2){ UH(1) } UH(2) UH(3) ST(u32x4,tw)=u32x4{h[0],h[1],h[2],h[3]}; UH(4) UH(5) UH(6) UH(7) ST(u32x4,tw+512)=u32x4{h[4],h[5],h[6],h[7]};
      UH(8) ST(u32,tw2)=h[8]; }
    { u32 tv,cnt;
      asm volatile("s_mov_b64 exec, 1\n\tds_add_u32 %[z], %[c1] offset:%[cl]\n\ts_mov_b64 exec, -1\n\ts_mov_b32 %[cnt], 0x20000\n1:\n\tds_read_b32 %[tv], %[z] offset:%[cl]\n\t"
        "ds_read_b64_tr_b16 v[96:97], %[tp]\n\tds_read_b64_tr_b16 v[98:99], %[tp] offset:4*" STR(TSTR2) "\n\tds_read_b64_tr_b16 v[100:101], %[tp] offset:512\n\tds_read_b64_tr_b16 v[102:103], %[tp] offset:512+4*" STR(TSTR2) "\n\t"
        "ds_read_b64_tr_b16 v[104:105], %[tp] offset:1024\n\tds_read_b64_tr_b16 v[106:107], %[tp] offset:1024+4*" STR(TSTR2) "\n\ts_waitcnt lgkmcnt(6)\n\tv_cmp_gt_u32 vcc, 4, %[tv]\n\ts_cbranch_vccz 2f\n\ts_sub_u32 %[cnt], %[cnt], 1\n\ts_cbranch_scc0 1b\n"
        "2:\n\ts_waitcnt lgkmcnt(4)\n\tglobal_store_dwordx4 %[u0], v[96:99], %[ws] sc1\n\ts_waitcnt lgkmcnt(2)\n\tglobal_store_dwordx4 %[u1], v[100:103], %[ws] sc1\n\ts_waitcnt lgkmcnt(0)\n\t"
        "s_cmp_lg_u32 %[w4], 0\n\ts_cbranch_scc1 3f\n\tglobal_store_dwordx4 %[u2], v[104:107], %[ws] sc1\n3:\n\ts_nop 1"
        : [tv]"=&v"(tv),[cnt]"=&s"(cnt) : [z]"v"(zero),[c1]"v"(c1),[cl]"i"(C_L*4),[tp]"v"(tp),[u0]"v"(uo),[u1]"v"(uo1),[u2]"v"(uo2),[ws]"s"(ws),[w4]"s"(w4)
        : PCLOB,"v96","v97","v98","v99","v100","v101","v102","v103","v104","v105","v106","v107");
      xr_=xr; xr2_=xr2;
      #define XQ { rsrc_t rx_=RSRC(x,L_*D_*2); xq[0]=BLD128(rx_,xr_,0,XAUX); xq[1]=BLD128(rx_,xr_+128,0,XAUX); xq[2]=BLD128(rx_,xr2_,0,XAUX); }
    }
    if(lb>=2&&lb<8){
      const u16* wm=(lb&1)? wu:wg; int co=(3-(int)((u64)wm>>7)-xg)&7, wi=((lb>>1)-1)*4+w4, r_=576-12*PFN+wi+12*j;
      int po= j<PFN? r_*(F_*2)+(9*xg+co)*128+(ln>>5)*1024 : OOR;
      if(ln>=32&&co!=0){ int c8=j&7; po= PX? (575-wi*4-(j>>3))*(F_*2)+(9*xg+c8+(c8>=co))*128 : OOR; }
      asm volatile("buffer_load_dword v120, %0, %1, 0 offen" PPOL :: "v"(po),"s"(mkrs(wm,D_*F_*2)) : "memory");
      if(PX>1){ int c8=ln&7; asm volatile("buffer_load_dword v120, %0, %1, 0 offen" PPOL :: "v"(ln<PX? (527-wi-12*(ln>>3))*(F_*2)+(9*xg+c8+(c8>=co))*128 : OOR),"s"(mkrs(wm,D_*F_*2)) : "memory"); } }
    if(lb>=8&&lb<14) garrive(sm,C_D);
  }
  FP fp; int trow;
  { LANE(ln); int g=ln>>4, li=ln&15; u64 tag=ep+1ull;
    const char* sp_=ws+W_ST+(32*(tb>>2)+8*g+(li&7))*8; u64 fs, ff; const char* fp_=ws+W_FL+(144*(lb&3)+16*w4+64*(g<2||w4==0? (g<3? g:2):0)+li)*8;
    if(lb==CTLB||lb>=14){
      u32 M=(u32)tag, rank,key; u32x4 rx=mkrs(x,L_*D_*2);
      int j=ln&31, rr=2*w4+(ln>>5), o16=2047*1152+j*16, o4=2047*1152+j*4, so=W_ST+2047*8, sh=(ln>>5)*16, m9=-1152, r0=2*w4, r1=2*w4+1;
      lch *xw=sm+O_TILE+XAD(rr,j*16), *xw1=sm+O_TILE+XAD(rr,j*16+512), *xw2=sm+O_TILE+XAD(rr,j*4+1024), *rw=sm+O_RC+rr*4; u32 zero=0, one=1;
      PIN(o16); PIN(o4); PIN(so); PIN(sh); PIN(xw); PIN(xw1); PIN(xw2); PIN(rw); PIN(zero); PIN(one); asm volatile("" : "+s"(rx),"+s"(m9),"+s"(r0),"+s"(r1));
      top8(sm,w4,ln,M,ws,rank,key);
      {
        u64 q; u32 a,b;
        asm volatile("v_cmp_eq_u32 vcc, %[r0], %[rk]\n\tv_cmp_eq_u32 %[q], %[r1], %[rk]\n\ts_ff1_i32_b64 %[a], vcc\n\ts_ff1_i32_b64 %[b], %[q]\n\tv_readlane_b32 %[a], %[key], %[a]\n\tv_readlane_b32 %[b], %[key], %[b]\n\ts_pack_ll_b32_b16 %[a], %[a], %[b]\n\t"
          "v_bfe_u32 %[sh], %[a], %[sh], 11\n\tv_mad_i32_i24 %[o16], %[sh], %[m9], %[o16]\n\tv_mad_i32_i24 %[o4], %[sh], %[m9], %[o4]\n\t"
          "buffer_load_dwordx4 v[112:115], %[o16], %[rx], 0 offen\n\tv_mad_i32_i24 %[so], %[sh], -8, %[so]\n\tbuffer_load_dwordx4 v[116:119], %[o16], %[rx], 0 offen offset:512\n\tbuffer_load_dword v120, %[o4], %[rx], 0 offen offset:1024\n\tglobal_load_dwordx2 v[122:123], %[so], %[ws] sc1\n\t"
          "s_mov_b64 exec, 1\n\tds_add_u32 %[z], %[one] offset:%[cd]\n\ts_mov_b64 exec, -1"
          : [q]"=&s"(q),[a]"=&s"(a),[b]"=&s"(b),[sh]"+v"(sh),[o16]"+v"(o16),[o4]"+v"(o4),[so]"+v"(so) : [r0]"s"(r0),[r1]"s"(r1),[rk]"v"(rank),[key]"v"(key),[m9]"s"(m9),[rx]"s"(rx),[ws]"s"(ws),[z]"v"(zero),[one]"v"(one),[cd]"i"(C_D*4) : PCLOB);
        if(lb==CTLB&&rank<8&&!(ln&1)) GST(ws+W_IDX+xg*1024+w4*128+rank*8,(tag<<16)|(~key&2047u));
        if(w4==0){ if(rank<8) ST(u16,sm+O_IDX+(ln&1)*16+rank*2)=(u16)((ln&1)? key>>16 : ~key&2047u); garrive(sm,C_IX); }
        u64 bad; asm volatile("s_waitcnt vmcnt(2)\n\tds_write_b128 %2, v[112:115]\n\ts_waitcnt vmcnt(0)\n\tds_write_b128 %3, v[116:119]\n\tv_cmp_ne_u32 %0, %1, v123\n\tds_write_b32 %4, v120" : "=s"(bad) : "s"(M),"v"(xw),"v"(xw1),"v"(xw2) : "memory");
        if(__builtin_expect(bad!=0ull,0)){ u32 f1=(u32)poll1<32>(ws+so,tag<<32); asm volatile("v_mov_b32 v122, %0" :: "v"(f1) : "memory"); }
        asm volatile("ds_write_b32 %0, v122\n\ts_mov_b64 exec, 1\n\tds_add_u32 %1, %2 offset:%3\n\ts_mov_b64 exec, -1" :: "v"(rw),"v"(zero),"v"(one),"i"(C_X*4) : "memory");
      }
    }
    PRIO(0);
    if(lb<14) gwait<8>(sm,C_U,8); else gwait<HSL>(sm,C_H,HN);
    fs=GLD(sp_); ff=GLD(fp_); XQ
    { rsrc_t rd=RSRC(Dsk,D_*2); int do_=288*(lb&3)+32*w4+(ln&8)*2; dq[0]=BLD128(rd,do_,0,0); dq[1]=BLD128(rd,do_+128,0,0); dq[2]=BLD128(rd,do_+256,0,0); }
    final_adr(sm,fp,w4); trow=32*(tb>>2)+8*g+(li&7); PIN(trow);
    {
      if(BALLOT((u32)(fs>>32)!=(u32)tag)!=0ull) fs=poll1<32>(sp_,tag<<32);
      float ri=rcpf_(bhi((u32)fs)); rc=f32x2{ri,-blo((u32)fs)*ri}; }
    asm volatile("" : "+v"(rc),"+v"(xq[0]),"+v"(xq[1]),"+v"(xq[2]),"+v"(dq[0]),"+v"(dq[1]),"+v"(dq[2]));
    bool ix= w4==3&&lb<14&&lb!=CTLB; u64 f; const char* p=ws+W_IDX+(xg*8+(lb&3))*128+(ln&7)*8; if(ix) f=GLD(p);
    final_blk(fp,0,xq,dq,rc); if(FS>=2) final_blk(fp,1,xq,dq,rc); if(FS>=3&&w4==0) final_blk(fp,2,xq,dq,rc);
    if(BALLOT(ff!=tag)!=0ull) poll1<0>(fp_,tag);
    if(ix){ if(BALLOT(((f^(tag<<16))>>16)!=0ull)!=0ull) f=poll1<16>(p,tag<<16);
      if(ln<8) ST(u16,sm+O_IDX+ln*2)=(u16)f; garrive(sm,C_IX); }
  }
  final_post(sm,fp,w4,nch,xr_,trow,xq,dq,rc,out,ws);
  }
}

extern "C" void kernel_launch(void* const* d_in, const int* in_sizes, int n_in, void* d_out, int out_size, void* d_ws, size_t ws_size, hipStream_t stream) {
  const u16** I=(const u16**)d_in;
  KP kp; kp.Are=I[1]; kp.Aim=I[2]; kp.Bre=I[3]; kp.Bim=I[4]; kp.Cre=I[5]; kp.Cim=I[6]; kp.ldt=I[7]; kp.pad=I[7];
  kA<<<256,1024,0,stream>>>(I[0],(char*)d_ws+WSOFF,I[10],I[8],I[11],I[12],I[13],(u16*)d_out,kp);
}
```

```cpp
#include <hip/hip_runtime.h>
#include <hip/hip_bf16.h>
#include <stdint.h>
#if defined(__HIP_DEVICE_COMPILE__)
#define NOPK __attribute__((target("no-packed-fp32-ops")))
#else
#define NOPK
#endif
#define DEV __device__ NOPK __forceinline__
typedef unsigned short u16;
typedef unsigned int u32;
typedef unsigned long long u64;
typedef short bf16x8 __attribute__((ext_vector_type(8)));
typedef _Float16 h8 __attribute__((ext_vector_type(8)));
typedef _Float16 h2 __attribute__((ext_vector_type(2)));
typedef float f32x4 __attribute__((ext_vector_type(4)));
typedef float f32x2 __attribute__((ext_vector_type(2)));
typedef u32 u32x4 __attribute__((ext_vector_type(4)));
typedef u32 u32x2 __attribute__((ext_vector_type(2)));
typedef __bf16 bf2 __attribute__((ext_vector_type(2)));
typedef short v4s __attribute__((ext_vector_type(4)));
typedef __amdgpu_buffer_rsrc_t rsrc_t;
#define GAS __attribute__((address_space(1)))
#define LAS __attribute__((address_space(3)))
typedef LAS char lch;
#define L_ 2048
#define D_ 576
#define F_ 4608
#define BC(T,v) __builtin_bit_cast(T,v)
#define TID ((int)__builtin_amdgcn_workitem_id_x())
#define BID ((int)__builtin_amdgcn_workgroup_id_x())
#define RFL(x) __builtin_amdgcn_readfirstlane(x)
#define RLX_AGENT __ATOMIC_RELAXED, __HIP_MEMORY_SCOPE_AGENT
#define LD(T,p) (*(const LAS T*)(p))
#define ST(T,p) (*(LAS T*)(p))
#define SPINCAP (1<<17)
#define DRAIN() asm volatile("s_waitcnt vmcnt(0)" ::: "memory")
#define GLD(p) __hip_atomic_load((const GAS u64*)(p),RLX_AGENT)
#define GST(p,v) __hip_atomic_store((GAS u64*)(p),(u64)(v),RLX_AGENT)
#define BALLOT(c) __builtin_amdgcn_ballot_w64(c)
#define SC1 16
#define LANE(l) int l; { int t2_=TID; asm volatile("" : "+v"(t2_)); l=t2_&63; }
#define SCHED() __builtin_amdgcn_sched_barrier(0)
#define RSRC(p,n) __builtin_amdgcn_make_buffer_rsrc((void*)(p),0,(n),0x00020000)
#define BLD128 __builtin_amdgcn_raw_buffer_load_b128
#define BLD32 __builtin_amdgcn_raw_buffer_load_b32
#define BST128 __builtin_amdgcn_raw_buffer_store_b128
#define PRIO(n) __builtin_amdgcn_s_setprio(n)

DEV float b2f(u32 v){ return BC(float,v<<16); }
DEV float blo(u32 v){ return BC(float,v<<16); }
DEV float bhi(u32 v){ return BC(float,v&0xffff0000u); }
DEV u32 pk(float a,float b){ f32x2 v={a,b}; bf2 r=__builtin_convertvector(v,bf2); return BC(u32,r); }
DEV u32 pkh(float a,float b){ f32x2 v={a,b}; h2 r=__builtin_convertvector(v,h2); return BC(u32,r); }
DEV float rb(float f){ return BC(float,pk(f,f)<<16); }
DEV float rcpf_(float x){ return __builtin_amdgcn_rcpf(x); }
DEV float expf_(float x){
  const float L2E=1.4426950216293335f, L2El=1.9259629911266175e-8f; float h=x*L2E; float l=__builtin_fmaf(x,L2E,-h)+x*L2El;
  float e=__builtin_amdgcn_exp2f(h); return __builtin_fmaf(e*l,0.6931471805599453f,e); }
DEV float sigm_f(float x){ return rcpf_(1.f+__builtin_amdgcn_exp2f(-x*1.4426950408889634f)); }
DEV float silu_f(float x){ return x*sigm_f(x); }
DEV u32 umax_(u32 a,u32 b){ return a>b?a:b; }
DEV u32 umin_(u32 a,u32 b){ return a<b?a:b; }

#define WSOFF 0
#define G2 7
#define PSL 2
#define NA 5
#define NB 11
#define GD 3
#define AD 14
#define DN 1
#define ECS 528
#define PK 0x112
#define PS 0x030
#define RD 0
#define PD 7
#define P12 64
#define ESL 4
#define CHN 8
#define CSL 2
#define PWB (w4>>1)*(9*4096)+(w4&1)*128
#define PLO (li>>1)*(18*4096)+(li&1)*64
#define YSH 3
#define PRE1 6
#define PRE3 4
#define PRE2 5
#define W1 5
#define W3 3
#define W2_ 4
#define W1B 20
#define W2B 12
#define LAST1 3
#define RGATE 3
#define UGATE 7
#define R2D 12
#define PFN 32
#define CPF 8
#define NRG 8
#define W_EPOCH 0
#define W_RG 16384
#define W_ST (W_RG+32*8192)
#define W_U (W_ST+32768)
#define UST (D_*16+128)
#define YST (L_*2+128)
#define TL 1
#define GST_ ((D_*16<<TL)+128)
#define W_Y (W_U+256*UST)
#define W_PART (W_Y+D_*YST)
#define W_GU (W_PART+8*9*16*256)
#define W_FL 9216
#define W_IDX 1024
#define CTLB 0
struct KP { const u16 *Are,*Aim,*Bre,*Bim,*Cre,*Cim,*ldt,*pad; };

template<int CTRL> DEV float dppf(float v){ return BC(float,__builtin_amdgcn_update_dpp(0u,BC(u32,v),CTRL,0xf,0xf,false)); }
template<int CTRL> DEV u32 dppu(u32 v){ return (u32)__builtin_amdgcn_update_dpp(0u,v,CTRL,0xf,0xf,false); }
DEV float sum16(float v){ v+=dppf<0xB1>(v); v+=dppf<0x4E>(v); v+=dppf<0x141>(v); v+=dppf<0x140>(v); return v; }
DEV float add16x(float v){ auto r=__builtin_amdgcn_permlane16_swap(BC(u32,v),BC(u32,v),false,false); u32 a=r[0], b=r[1]; return BC(float,a)+BC(float,b); }
DEV u32 wmax(u32 v){
  v=umax_(v,(u32)__builtin_amdgcn_update_dpp(0u,v,0x111,0xf,0xf,false));
  v=umax_(v,(u32)__builtin_amdgcn_update_dpp(0u,v,0x112,0xf,0xf,false));
  v=umax_(v,(u32)__builtin_amdgcn_update_dpp(0u,v,0x114,0xf,0xf,false));
  v=umax_(v,(u32)__builtin_amdgcn_update_dpp(0u,v,0x118,0xf,0xf,false));
  v=umax_(v,(u32)__builtin_amdgcn_update_dpp(0u,v,0x142,0xa,0xf,false));
  v=umax_(v,(u32)__builtin_amdgcn_update_dpp(0u,v,0x143,0xc,0xf,false));
  return (u32)__builtin_amdgcn_readlane((int)v,63); }

DEV void garrive(lch* sm,int c){ u32 ad=(u32)(size_t)sm+c*4, one=1;
  asm volatile("s_mov_b64 exec, 1\n\tds_add_u32 %0, %1\n\ts_mov_b64 exec, -1" :: "v"(ad),"v"(one) : "memory"); }
template<int SLP> DEV void gwait(lch* sm,int c,u32 target){ u32 ad=(u32)(size_t)sm+c*4, tv, cnt, sv; target=RFL(target);
  asm volatile("s_mov_b32 %1, 0x20000\n"
    "1:\n\tds_read_b32 %0, %3\n\ts_waitcnt lgkmcnt(0)\n\tv_readfirstlane_b32 %2, %0\n\ts_cmp_ge_u32 %2, %4\n\ts_cbranch_scc1 2f\n\ts_sleep %5\n\ts_sub_u32 %1, %1, 1\n\ts_cbranch_scc0 1b\n"
    "2:" : "=&v"(tv),"=&s"(cnt),"=&s"(sv) : "v"(ad),"s"(target),"i"(SLP) : "memory","scc"); }
template<int ST_,class T> DEV void gwait4(int c,u32 target,const lch* p,T& a,T& b,T& d,T& e){ u32 tv,cnt,sv;
  asm volatile("s_mov_b32 %1, 0x20000\n1:\n\tds_read_b32 %0, %7\n\tds_read_b128 %3, %8\n\tds_read_b128 %4, %8 offset:%10\n\tds_read_b128 %5, %8 offset:%11\n\tds_read_b128 %6, %8 offset:%12\n\ts_waitcnt lgkmcnt(4)\n\t"
    "v_readfirstlane_b32 %2, %0\n\ts_cmp_ge_u32 %2, %9\n\ts_cbranch_scc1 2f\n\ts_sleep 1\n\ts_sub_u32 %1, %1, 1\n\ts_cbranch_scc0 1b\n2:\n\ts_waitcnt lgkmcnt(0)"
    : "=&v"(tv),"=&s"(cnt),"=&s"(sv),"=&v"(a),"=&v"(b),"=&v"(d),"=&v"(e) : "v"(c*4),"v"(p),"i"(target),"i"(ST_),"i"(2*ST_),"i"(3*ST_) : "memory","scc"); }
DEV void gsync(lch* sm,int c,u32 target){ garrive(sm,c); gwait<0>(sm,c,target); }
template<int SH> DEV u64 poll1(const char* p,u64 want){ u64 f=GLD(p);
  _Pragma("unroll 1") for(int sp=0;sp<SPINCAP;sp++){ bool bad=((f^want)>>SH)!=0ull; if(BALLOT(bad)==0ull) break; __builtin_amdgcn_s_sleep(1); if(bad) f=GLD(p); }
  __builtin_amdgcn_fence(__ATOMIC_ACQUIRE,"wavefront"); return f; }
#define STR_(x) #x
#define STR(x) STR_(x)
DEV u32x4 mkrs(const void* p,u32 n){ u64 a=(u64)p; return u32x4{(u32)RFL((int)(u32)a),(u32)RFL((int)((u32)(a>>32)&0xffffu)),n,0x00020000u}; }
#define VMW(n) asm volatile("s_waitcnt vmcnt(%0)" :: "i"(n) : "memory")
#define VW(n) "s_waitcnt vmcnt(" STR(n) ")\n\t"
#define YSLP 1
#define GSLP 1
#define QSLP 1
#define PSLP 3
#define POLLASM(LOADS,CHECK,K) "s_mov_b32 %[cnt], 0x20000\n1:\n\ts_waitcnt vmcnt(0)\n\t" CHECK "s_cbranch_vccz 9f\n\ts_sleep " STR(K) "\n\ts_mov_b64 exec, vcc\n\t" LOADS \
  "s_mov_b64 exec, -1\n\ts_sub_u32 %[cnt], %[cnt], 1\n\ts_cbranch_scc0 1b\n9:\n\t"
#define LD_DUP2 "buffer_load_dwordx4 v[112:115], %[of], %[rs], 0 offen sc1\n\tbuffer_load_dwordx4 v[116:119], %[of], %[rs], %[so] offen sc1\n\t"
#define MX2(a,b) "v_max3_u32 %[t0], %[t0], v" #a ", v" #b "\n\t"
#define CHK_I0 "v_max_u32 %[t0], v112, v113\n\t" MX2(114,115)
#define CHK_IE "v_cmp_eq_u32 vcc, -1, %[t0]\n\t"
#define PCLOB "memory","vcc","scc"
#define UMASK 0x40074007u
DEV u32 upat(u32 tag,int k){ return ((((tag*0xea959c21u)>>(8*k))&255u)*0x04000801u)&UMASK; }
#define YMASK 0x70007000u
DEV u32 ycls(u32 tag,int j){ return ((tag*0x9e35u)>>(2*j))&3u; }
DEV u32 ypat(u32 tag,int k){ return (((((tag*0x9e35u)>>(4*k))&15u)*0x04001000u)&0x30003000u)+0x20002000u; }
DEV u32 pkz(float a,float b){ return BC(u32,__builtin_amdgcn_cvt_pkrtz(a,b)); }
#define INV 0xffffffffu
DEV u32 nv(u32 v){ return umin_(v,0xfffeffffu); }
DEV u32x4 nv4(u32x4 v){ return u32x4{nv(v[0]),nv(v[1]),nv(v[2]),nv(v[3])}; }
#define OOR 0x40000000

#define CHSZ 36864
#define O_CH 512
#define O_CND 256
#define SLSZ 38976
#define O_R2 (O_CH+2*SLSZ)
#define O_X2 (O_R2+CHSZ)
#define O_TILE (O_X2+CHSZ)
#define XSTR 1168
#define HSTR 608
#define HT 96
#define XAD(r,b) ((r)*HSTR+(b)+((b)>=576? 8*HSTR-576:0))
#define XS1 1
#define XG1 1
#define TSTR2 1216
#define O_CAND (O_TILE+9728+1024)
#define LDS_TOTAL (O_CAND+128)
#define C_G 0
#define C_U 8
#define C_L 4
#define C_D 5
#define C_X 12
#define C_P 13
#define C_H 14
#define C_I 17
#define C_E 18
#define C_IX 19
#define C_PR 21
#define C_W 22
#define C_WIN (lb>=14? C_W:C_D)
#define O_IDX 96
#define O_EXP 144
#define O_RMX 192
#define O_RV 128
#define O_MD 400
#define O_RC (O_CAND+64)

struct C2 { float r,i; };
DEV C2 cmul(C2 a,C2 b){ C2 c; c.r=__builtin_fmaf(a.r,b.r,-(a.i*b.i)); c.i=__builtin_fmaf(a.r,b.i,a.i*b.r); return c; }
DEV h8 ldA(const lch* p){ u32x2 a=*(const volatile LAS u32x2*)p, b=*(const volatile LAS u32x2*)(p+8); return BC(h8,(u32x4{a[0],a[1],b[0],b[1]})); }
DEV bf16x8 trfrag(const lch* p0,const lch* p1){
  u32x2 b0=BC(u32x2,__builtin_amdgcn_ds_read_tr16_b64_v4i16((LAS v4s*)p0)), b1=BC(u32x2,__builtin_amdgcn_ds_read_tr16_b64_v4i16((LAS v4s*)p1));
  return BC(bf16x8,(u32x4{b0[0],b0[1],b1[0],b1[1]})); }

struct FP { u32 gg[24]; float bs[24]; int yo,y3; lch* wp; const lch* tp; };
#define D2(a,b,c) __builtin_amdgcn_fdot2_f32_bf16(BC(bf2,a),BC(bf2,b),c,false)
#define ZCH 64
#define YZ 1280
#define YG1 320
#define YG2 672
DEV void final_blk(FP& f,int z,const u32x4* xq,f32x4 rc,const lch* ep){
  f32x4 q[4];
  #pragma unroll
  for(int e=0;e<4;e++) q[e]=LD(f32x4,ep+z*128+e*16);
  #pragma unroll
  for(int e=0;e<4;e++){ u32 xw=xq[z][e]; int i=8*z+2*e; float a=blo(xw), b=bhi(xw);
    float pa=__builtin_fmaf(a,rc[0],rc[1]), pb=__builtin_fmaf(b,rc[0],rc[1]);
    float ga=pa*rcpf_(__builtin_fmaf(__builtin_amdgcn_exp2f(__builtin_fmaf(a,rc[2],rc[3])),q[e][0],q[e][0])), gb=pb*rcpf_(__builtin_fmaf(__builtin_amdgcn_exp2f(__builtin_fmaf(b,rc[2],rc[3])),q[e][2],q[e][2]));
    f.gg[i]=pk(ga,0.f); f.gg[i+1]=pk(0.f,gb);
    f.bs[i]=__builtin_fmaf(pa*q[e][1],ga,a); f.bs[i+1]=__builtin_fmaf(pb*q[e][3],gb,b);
    asm volatile("" : "+v"(f.gg[i]),"+v"(f.gg[i+1]),"+v"(f.bs[i]),"+v"(f.bs[i+1])); }
}
struct FL { u64 fs,ff; u32 dk; u32x4 xq[3]; int so,fo,xr,xr2; const char* ws; const u16* D; u32x4 rx; };
DEV void final_ld(FL& l){
  asm volatile("global_load_dwordx2 %0, %6, %10 sc1\n\tglobal_load_dwordx2 %1, %7, %10 sc1\n\tv_lshrrev_b32 %2, 2, %7\n\tglobal_load_ushort %2, %2, %11 offset:%13\n\t"
    "buffer_load_dwordx4 %3, %8, %12, 0 offen\n\tbuffer_load_dwordx4 %4, %8, %12, 0 offen offset:128\n\tbuffer_load_dwordx4 %5, %9, %12, 0 offen"
    : "=&{v[80:81]}"(l.fs),"=&{v[82:83]}"(l.ff),"=&{v79}"(l.dk),"=&{v[84:87]}"(l.xq[0]),"=&{v[88:91]}"(l.xq[1]),"=&{v[92:95]}"(l.xq[2])
    : "v"(l.so),"v"(l.fo),"v"(l.xr),"v"(l.xr2),"s"(l.ws),"s"(l.D),"s"(l.rx),"i"(-W_FL/4) : "memory"); }
DEV void final_adr(lch* sm,FP& f,int w4){
  LANE(ln); int bid=BID, xg=bid&7, lb=bid>>3, g=ln>>4, li=ln&15, qq=li>>2, pq=li&3;
  f.yo=(144*(lb&3)+16*w4+(ln>>2))*YST+(32*(8*xg+(lb>>2))+8*(ln&3))*2; f.y3= w4==0? f.yo+2*ZCH*YST:OOR;
  lch* fb=sm+O_CH+w4*(3*YZ); f.wp=fb+(ln&1)*YG1+(ln&2)*(YG2/2)+(ln>>2)*16; f.tp=fb+(g&1)*YG1+(g&2)*(YG2/2)+(8*(pq>>1)+qq)*16+(pq&1)*8;
  asm volatile("" : "+v"(f.yo),"+v"(f.y3),"+v"(f.wp),"+v"(f.tp));
}
DEV void final_post(lch* sm,FP& f,int w4,int nch,int ob,int trow,u32 tg,u16* __restrict__ out,char* ws){
  int lb=BID>>3;
  u32x4 rs=mkrs(ws+W_Y,D_*YST); rsrc_t ro=RSRC(out,L_*D_*2);
  gwait<ESL>(sm,C_E,4*nch);
  if(AD&&lb<8) __builtin_amdgcn_s_sleep(AD);
  if(lb>=8&&lb<14) gwait<CSL>(sm,C_H,CHN);
  PRIO(3);
  #define LD_Y "buffer_load_dwordx4 v[112:115], %[of], %[rs], 0 offen sc1\n\tbuffer_load_dwordx4 v[116:119], %[of], %[rs], %[s1] offen sc1\n\tbuffer_load_dwordx4 v[120:123], %[o3], %[rs], 0 offen sc1\n\t"
  asm volatile(LD_Y : "+v"(trow) : [of]"v"(f.yo),[rs]"s"(rs),[s1]"s"(ZCH*YST),[o3]"v"(f.y3) : "memory");
  garrive(sm,C_PR);
  { gwait<2>(sm,C_IX,1); u32x4 iq=LD(u32x4,sm+O_IDX); u32 tt=(u32)trow*0x10001u; bool routed=false;
    #pragma unroll
    for(int q=0;q<4;q++){ u32 z=iq[q]^tt; routed|=((z&0xffffu)==0u)|((z>>16)==0u); }
    ob=routed? OOR:ob; }
  { u32 t0,t1,t2,cnt;
    #define UC(t,a,b,c) "v_bitop3_b32 " t ", v" #a ", v" #b ", %[" #c "] bitop3:0x7e\n\t"
    #define UD(t,a,c) "v_bitop3_b32 " t ", " t ", v" #a ", %[" #c "] bitop3:0xf6\n\t"
    asm volatile("s_mov_b32 %[cnt], 0x20000\n1:\n\ts_waitcnt vmcnt(0)\n\tds_write_b128 %[wp], v[112:115]\n\tds_write_b128 %[wp], v[116:119] offset:" STR(YZ) "\n\t" UC("%[t0]",112,116,c0) "s_cmp_lg_u32 %[w4], 0\n\ts_cbranch_scc1 8f\n\tds_write_b128 %[wp], v[120:123] offset:2*" STR(YZ) "\n\t"
      UD("%[t0]",120,c0) UD("%[t0]",121,c1) UD("%[t0]",122,c2) UD("%[t0]",123,c3) "8:\n\t" UC("%[t1]",113,117,c1) UC("%[t2]",114,118,c2) "v_or3_b32 %[t0], %[t0], %[t1], %[t2]\n\t" UC("%[t1]",115,119,c3)
      "v_bitop3_b32 %[t0], %[t0], %[t1], %[mk] bitop3:0xa8\n\tv_cmp_ne_u32 vcc, 0, %[t0]\n\ts_cbranch_vccz 9f\n\ts_sleep " STR(YSLP) "\n\ts_wqm_b64 exec, vcc\n\t" LD_Y "s_mov_b64 exec, -1\n\ts_sub_u32 %[cnt], %[cnt], 1\n\ts_cbranch_scc0 1b\n9:"
      : [t0]"=&v"(t0),[t1]"=&v"(t1),[t2]"=&v"(t2),[cnt]"=&s"(cnt),"+v"(ob) : [of]"v"(f.yo),[rs]"s"(rs),[s1]"s"(ZCH*YST),[o3]"v"(f.y3),[wp]"v"(f.wp),[w4]"s"(w4),"s"(ro),
        [c0]"s"(ypat(tg,0)),[c1]"s"(ypat(tg,1)),[c2]"s"(ypat(tg,2)),[c3]"s"(ypat(tg,3)),[mk]"s"(YMASK) : PCLOB); }
  u32x4 r[3];
  #pragma unroll
  for(int z=0;z<3;z++) r[z]=BC(u32x4,trfrag(f.tp+z*YZ,f.tp+z*YZ+64));
  #pragma unroll
  for(int z=0;z<3;z++) if(z<2||w4==0){ u32x4 ov;
    #pragma unroll
    for(int e=0;e<4;e++){ u32 rw=r[z][e]; int i=8*z+2*e;
      ov[e]=pk(D2(rw,f.gg[i],f.bs[i]),D2(rw,f.gg[i+1],f.bs[i+1]));
    }
    BST128(ov,ro,ob+z*(2*ZCH),0,0); SCHED(); }
}

#define TROW 544
#define O_TB 4640
#define O_DE 35664
#define O_DO (O_DE+1232)
#define O_R2K (O_DO+1152)
#define O_R3K (O_R2K+512)
#define O_DUM (O_R3K+320)
#define O_RED O_TB
#define MH(a,b,c) __builtin_amdgcn_mfma_f32_16x16x32_f16(a,b,c,0,0,0)
#define PIN(x) asm volatile("" : "+v"(x))
DEV h8 ld4w(const lch* p){ return BC(h8,(u32x4{LD(u32,p),LD(u32,p+4),LD(u32,p+8),LD(u32,p+12)})); }
DEV u32 __umul24_(u32 a,u32 b){ u32 r; asm("v_mul_u32_u24 %0, %1, %2" : "=v"(r) : "s"(b),"v"(a)); return r; }
DEV u32x4 band(u32 mask,int g,u32 val){ u32 by=__builtin_amdgcn_ubfe(mask,8*g,8), X=by|(by<<15), k=0x10001u; u32x4 r; asm volatile("" : "+s"(k),"+s"(val));
  #pragma unroll
  for(int d=0;d<4;d++) r[d]=__umul24_((X>>(2*d))&k,val);
  return r; }
DEV h8 zpack(f32x4 e,f32x4 o){ return BC(h8,(u32x4{pkh(e[0],o[0]),pkh(e[1],o[1]),pkh(e[2],o[2]),pkh(e[3],o[3])})); }
struct C3 { float r,i,n; };
DEV C3 c3(C2 a){ C3 c; c.r=a.r; c.i=a.i; PIN(c.i); c.n=-c.i; PIN(c.n); return c; }
DEV C2 cm(C2 a,C3 b){ C2 c; c.r=__builtin_fmaf(a.r,b.r,a.i*b.n); c.i=__builtin_fmaf(a.r,b.i,a.i*b.r); return c; }
DEV u32 ph(C2 a){ return pkh(a.r,a.i); }
#define W2(p,a,b) asm volatile("ds_write2_b32 %0, %1, %2 offset1:136" :: "v"(p),"v"(a),"v"(b) : "memory")
struct CP { const lch *x0,*x1,*bd; lch *p0,*p1,*p2,*wp; u32x4 Be,Bo,AE; int yo; };
template<int ROLE> DEV void lanes(CP& c,lch* cb,int ln){ int m=ln&15, g=ln>>4;
  const lch* ad=cb+((m&1)? O_DO+2*527 : O_DE+2*536)+2*(8*g-m);
  const lch* bd=cb+2*(256+16*m+8*g); lch* dum=cb+O_DUM;
  if(ROLE==0){ int l0=128*(g>>1)+8*m+4*(g&1); c.p0=cb+O_DE+(549-l0)*2; PIN(c.p0); c.p1=c.p0+6; PIN(c.p1); c.p2=c.p0+(O_DO-O_DE-18); c.x0=ad-640; c.bd=bd-64; c.x1=cb+O_R3K+2*(126-2*m+8*g)-192; PIN(c.x1);
    c.Be=band(0x1ffu<<(m+4),g,0x3c00u); PIN(c.Be); }
  if(ROLE==1){ lch* r=cb+O_R2K+(192-64*(g-2)-2*m)*2; c.p0= g<2? dum : r-66; c.p1= ln==0? cb+O_DE+(552-256)*2 : (ln<=32? dum : r); c.p2=cb+O_DE+(544-256-8*ln)*2; c.x0=ad-960; c.bd=bd-448; PIN(c.p0); PIN(c.p1); PIN(c.p2); }
  if(ROLE==2){ int i0=16*m+4*g; c.p0= m<8? cb+O_R3K+(125-i0)*2 : dum+2; c.p1= ln==0? cb+O_R2K+128 : (ln==8? cb+O_R3K : c.p0+6); c.x0=ad-960; c.bd=bd-448; PIN(c.p0); PIN(c.p1); }
  if(ROLE==3){ c.x0=cb+O_R2K+2*(188-4*m+8*g)-256; c.bd=cb+2*(248+8*m+8*g);
    c.Be=band(0x1fu<<(m+6),g,0x3c00u); c.Bo=band(0x7u<<(m+11),g,0x3c00u); PIN(c.Be); PIN(c.Bo); }
  PIN(c.x0); PIN(c.bd); }
template<int ROLE> DEV void lanes2(CP& c,lch* cb,int ln){ int m=ln&15, g=ln>>4;
  if(ROLE==0){ c.Bo=band(0x7fu<<(m+5),g,0x3c00u); c.AE=band(3u<<(m+12),g,0xffffu); PIN(c.Bo); PIN(c.AE); }
  c.wp=cb+O_RED+ln*16; c.yo=(g&1)*512+m*32+(g>>1)*16; PIN(c.wp); PIN(c.yo); }
template<int ROLE> DEV void chan2(lch* sm,lch* cb,const lch*& fa,const lch*& fb,int grp,int d,int one_,int emax,u64 tag,char* ws){
  CP c;
  const f32x4 Z4={0,0,0,0}; h8 A[12]; f32x4 acc[8]; float sc; u32x4 ry; u32 cga,ym,yp[4];
  #define YSET sc=BC(float,5u<<23); ry=mkrs(ws+W_Y+d*YST,4096); cga=(C_G+grp)*4; ym=~YMASK; PIN(sc); PIN(cga); PIN(ym); _Pragma("unroll") for(int k=0;k<4;k++) yp[k]=ypat((u32)tag,k); asm volatile("" : "+s"(ry),"+s"(yp[0]),"+s"(yp[1]),"+s"(yp[2]),"+s"(yp[3]));
  #define LD_U "buffer_load_dwordx4 v[112:115], %[of], %[rs], 0 offen sc1\n\tbuffer_load_dwordx4 v[116:119], %[of], %[rs], %[s1] offen sc1\n\tbuffer_load_dwordx4 v[120:123], %[of], %[rs], %[s2] offen sc1\n\tbuffer_load_dwordx4 v[124:127], %[of], %[rs], %[s3] offen sc1\n\t"
  #define CHK_U UC("%[t0]",112,116,c0) UC("%[t1]",113,117,c1) UD("%[t0]",120,c0) UD("%[t1]",121,c1) UD("%[t0]",124,c0) UD("%[t1]",125,c1) UD("%[t0]",114,c2) UD("%[t1]",115,c3) UD("%[t0]",118,c2) UD("%[t1]",119,c3) \
    UD("%[t0]",122,c2) UD("%[t1]",123,c3) UD("%[t0]",126,c2) UD("%[t1]",127,c3) "v_bitop3_b32 %[t0], %[t0], %[t1], %[mk] bitop3:0xa8\n\tv_cmp_ne_u32 vcc, 0, %[t0]\n\t"
  #define UX(a,b,c,d,o) "v_xor_b32 v" #a ", %[e0], v" #a "\n\tv_xor_b32 v" #b ", %[e1], v" #b "\n\tv_xor_b32 v" #c ", %[e2], v" #c "\n\tv_xor_b32 v" #d ", %[e3], v" #d "\n\tds_write_b128 %[up], v[" #a ":" #d "] offset:" #o "\n\t"
  #define UK(n,a,b,c,d,o,T) VW(n) UX(a,b,c,d,o) UC("%[t1]",a,b,k4) UC("%[t2]",c,d,k4) T
  #define OR3 "v_or3_b32 %[t0], %[t0], %[t1], %[t2]\n\t"
  #define UPOLL { u32 t0,t1,t2,cnt; asm volatile("s_mov_b32 %[cnt], 0x20000\n1:\n\t" UK(3,112,113,114,115,544,"v_or_b32 %[t0], %[t1], %[t2]\n\t") UK(2,116,117,118,119,1056,OR3) UK(1,120,121,122,123,1568,OR3) UK(0,124,125,126,127,2080,OR3) \
      "v_and_b32 %[t0], %[mk], %[t0]\n\tv_cmp_ne_u32 vcc, 0, %[t0]\n\ts_cbranch_vccz 9f\n\ts_sleep " STR(PSLP) "\n\ts_mov_b64 exec, vcc\n\t" LD_U "s_sub_u32 %[cnt], %[cnt], 1\n\ts_cbranch_scc0 1b\n9:\n\ts_mov_b64 exec, 1\n\tds_add_u32 %[cu], %[one]\n\ts_mov_b64 exec, -1" \
      : [t0]"=&v"(t0),[t1]"=&v"(t1),[t2]"=&v"(t2),[cnt]"=&s"(cnt) : [of]"v"(c.p0),[rs]"s"(rsU),[s1]"s"((32>>TL)*GST_),[s2]"s"((64>>TL)*GST_),[s3]"s"((96>>TL)*GST_),[k4]"s"(0x40004u), \
        [e0]"s"(c0^0x40004u),[e1]"s"(c1^0x40004u),[e2]"s"(c2^0x40004u),[e3]"s"(c3_^0x40004u),[mk]"s"(UMASK),[up]"v"(c.p1),[cu]"v"((C_U+one_)*4),[one]"v"(4) : PCLOB); }
  if(ROLE<3){
    h8 a[8], b[8];
    #pragma unroll
    for(int ks=0;ks<8;ks++){ a[ks]=LD(h8,fa+ks*64); b[ks]=LD(h8,fb+ks*64); }
    SCHED();
    LANE(ln); lanes<ROLE>(c,cb,ln); float fz= ln==32? 0.f : 4.76837158203125e-07f; if(ROLE==1) PIN(fz);
    SCHED();
    f32x4 k=Z4;
    #pragma unroll
    for(int ks=0;ks<8;ks++) k=MH(a[ks],b[ks],k);
    const float f=4.76837158203125e-07f;
    float k0=k[0]*f, k1=k[1]*f, k2=k[2]*f, k3=k[3]*f;
    u32 h32=pkh(k3,k2), h10=pkh(k1,k0);
    if(ROLE!=1){
      ST(u16,c.p0)=(u16)h32; ST(u32,c.p0+2)=pkh(k2,k1); ST(u16,c.p1)=(u16)(h10>>16);
      if(ROLE==0) ST(u32x2,c.p2)=u32x2{h32,h10};
    } else {
      float n0=dppf<0x130>(k[0])*fz, z1= fz==0.f? k0:k1, z2=k[2]*fz, z3=k[3]*fz; u32 h22=pkh(z2,z2);
      ST(u16,c.p0+64)=(u16)h10; ST(u16,c.p0+2)=(u16)(h32>>16); ST(u16,c.p0)=(u16)h32; ST(u16,c.p1)=(u16)(h10>>16);
      u32x4 vo={pkh(n0,z3),h22,pkh(z2,z1),pkh(k0,k0)}, ve={pkh(n0,n0),pkh(z3,z2),h22,pkh(z1,k0)};
      asm volatile("s_mov_b32 exec_hi, 1\n\tds_write_b128 %0, %1\n\tds_write_b128 %0, %2 offset:%3\n\ts_mov_b32 exec_hi, -1" :: "v"(c.p2),"v"(ve),"v"(vo),"i"(O_DO-O_DE-16) : "memory"); }
    gsync(sm,C_G+grp,7);
    u32 kv;
    if(ROLE==0){ A[0]=ld4w(c.x0+640); A[1]=ld4w(c.x0+128); A[2]=ld4w(c.x1+192); A[3]=ld4w(c.x1+128); A[4]=ld4w(c.x1+64); A[5]=ld4w(c.x1); kv=LD(u16,cb+O_R2K+128); }
    else {
      #pragma unroll
      for(int r=0;r<6;r++){ A[2*r]=ld4w(c.x0+832-64*r); A[2*r+1]=ld4w(c.x0+320-64*r); } }
    SCHED(); lanes2<ROLE>(c,cb,ln); YSET SCHED();
    if(ROLE==0){ kv|=kv<<16; c.AE=u32x4{c.AE[0]&kv,c.AE[1]&kv,c.AE[2]&kv,c.AE[3]&kv}; PIN(c.AE); }
    PRIO(2); if(ROLE!=0&&BID>=P12) PRIO(1);
    gwait<1>(sm,C_U+one_,8-4*one_);
  } else {
    u32 tg=(u32)tag, c0=upat(tg,0), c1=upat(tg,1), c2=upat(tg,2), c3_=upat(tg,3); u32x4 rsU=mkrs(ws+W_U,256*UST);
    if(!one_) PRIO(2);
    LANE(ln); c.p0=(lch*)fa; c.p1=(lch*)fb;
    bool late= grp==0||BID>=112;
    if(!late) asm volatile(LD_U :: [of]"v"(c.p0),[rs]"s"(rsU),[s1]"s"((32>>TL)*GST_),[s2]"s"((64>>TL)*GST_),[s3]"s"((96>>TL)*GST_) : "memory");
    lanes<3>(c,cb,ln); lanes2<3>(c,cb,ln); YSET
    #pragma unroll
    for(int j=2;j<8;j++){ acc[j]=Z4; PIN(acc[j]); }
    if(late){ gwait<2>(sm,C_G,UGATE); if(BID>=112) gwait<1>(sm,C_D,DN); asm volatile(LD_U :: [of]"v"(c.p0),[rs]"s"(rsU),[s1]"s"((32>>TL)*GST_),[s2]"s"((64>>TL)*GST_),[s3]"s"((96>>TL)*GST_) : "memory"); }
    if(!late) UPOLL
    gwait<1>(sm,C_G+grp,7);
    #pragma unroll
    for(int i=0;i<6;i++) A[i]=ldA(c.x0+320-64*i);
    if(late) UPOLL
    PRIO(2);
    gwait<1>(sm,C_U+one_,8-4*one_);
  }
  const lch* bd=c.bd; h8 Be=BC(h8,c.Be), Bo=BC(h8,c.Bo);
  #pragma unroll
  for(int j=0;j<(ROLE==3? 2:8);j++) acc[j]=Z4;
  constexpr int OWN= ROLE==1? 2 : ROLE==2? 1 : ROLE, NS= ROLE==0? 1:2;
  #define RW(j) { ST(f32x4,c.wp+((j)>>1)*4096+(ROLE&1)*2048+((j)&1)*1024)=acc[j]; SCHED(); }
  const lch* rp=c.wp+OWN*4096; f32x4 a[2], b[2]; u32 cv;
  #define LDV(T,p) (*(const volatile LAS T*)(p))
  #define ARR { garrive(sm,C_G+grp); SCHED(); }
  #define OPT { cv=LDV(u32,(lch*)(size_t)cga); _Pragma("unroll") for(int q=0;q<NS;q++){ a[q]=LDV(f32x4,rp+q*2048); b[q]=LDV(f32x4,rp+q*2048+1024); } SCHED(); }
  #define SUM(v) { _Pragma("unroll") for(int e=0;e<4;e++) v[0][e]=(NS==1? v[0][e] : v[0][e]+v[1][e])*sc; PIN(v[0]); SCHED(); }
  if(ROLE==1||ROLE==2){
    constexpr int NQ= ROLE==1? 5:4, LA=9; h8 B[30];
    #define QS(s) (ROLE==2? (s) : (s)<3? 5+(s) : (s))
    #define LB(i) B[i]=LD(h8,bd+320-64*((i)%6)+512*QS((i)/6));
    #pragma unroll
    for(int i=0;i<LA;i++) LB(i)
    SCHED();
    #pragma unroll
    for(int i=0;i<6*NQ;i++){ const int q=QS(i/6), r=i%6;
      if(i==6*NQ-(ROLE==1? 4:7)){ PRIO(2); OPT } if(i==6*NQ-(ROLE==1? 2:3)) SUM(a) if(i==6*NQ-1) SUM(b)
      if(!(ROLE==1&&q==3)){ acc[q]=MH(A[2*r],B[i],acc[q]); SCHED(); }
      if(q!=7&&!(ROLE==2&&q==3)){ acc[q+1]=MH(A[2*r+1],B[i],acc[q+1]); SCHED(); }
      if(i+LA<6*NQ){ LB(i+LA) SCHED(); }
      if(ROLE==2&&i==6) RW(0) if(ROLE==2&&i==12){ RW(1) ARR } if(ROLE==1&&i==13) RW(6) if(ROLE==1&&i==19){ RW(7) ARR } } }
  if(ROLE==0){
    h8 B[8], ue[4], C[8];
    #pragma unroll
    for(int kt=0;kt<4;kt++){ ue[kt]=LD(h8,bd+48+512*kt); B[kt]=LD(h8,bd+64+512*kt); }
    f32x4 ce[2],co[2]; h8 Z[2];
    ce[0]=MH(ue[0],Be,Z4); co[0]=MH(B[0],Bo,Z4); ce[1]=MH(ue[1],Be,Z4); co[1]=MH(B[1],Bo,Z4); Z[0]=zpack(ce[0],co[0]);
    #pragma unroll
    for(int kt=0;kt<4;kt++){
      SCHED();
      if(kt+2<4){ ce[kt&1]=MH(ue[kt+2],Be,Z4); co[kt&1]=MH(B[kt+2],Bo,Z4); }
      if(kt+1<4) Z[(kt+1)&1]=zpack(ce[(kt+1)&1],co[(kt+1)&1]);
      #pragma unroll
      for(int rho=0;rho+kt<4;rho++) acc[4+kt+rho]=MH(Z[kt&1],A[2+rho],acc[4+kt+rho]);
      SCHED(); if(kt>=2) C[kt-1]=LD(h8,bd+512*(kt-1)); if(kt==0) A[5]=BC(h8,c.AE); if(kt==1) A[4]=ld4w(c.x0-384); if(kt==2) A[3]=ld4w(c.x0+64); if(kt==3) A[2]=ld4w(c.x0+576); SCHED(); }
    #pragma unroll
    for(int q=0;q<9;q++){
      if(q<4){ acc[q+4]=MH(A[5],B[q],acc[q+4]); SCHED(); } if(q<6){ acc[q+2]=MH(A[4],B[q],acc[q+2]); SCHED(); }
      if(q>=1&&q<7){ acc[q+1]=MH(A[1],B[q],acc[q+1]); SCHED(); } if(q>=2&&q<8){ acc[q]=MH(A[0],B[q],acc[q]); SCHED(); }
      if(q>=4&&q<8) RW(q-2)
      if(q<4){ B[4+q]=LD(h8,bd+64+512*(4+q)); SCHED(); }
      if(q>=3){ acc[q-1]=MH(A[2],C[q-1],acc[q-1]); SCHED(); } if(q>=2&&q<8){ acc[q]=MH(A[3],C[q-1],acc[q]); SCHED(); }
      if(q>=1&&q<7){ C[(q+2)&7]=LD(h8,bd+512*((q+2)&7)); SCHED(); } }
    acc[0]=MH(A[0],B[0],acc[0]); SCHED(); RW(6) acc[1]=MH(A[1],B[0],acc[1]); SCHED(); RW(7) ARR
    OPT acc[0]=MH(A[2],C[0],acc[0]); acc[1]=MH(A[0],B[1],acc[1]); acc[1]=MH(A[2],C[1],acc[1]); acc[1]=MH(A[3],C[0],acc[1]); SCHED(); SUM(a) SUM(b) }
  if(ROLE==3){
    h8 U[12];
    #pragma unroll
    for(int kt=0;kt<6;kt++) U[kt]=LD(h8,bd+256*kt);
    f32x4 ce[2],co[2]; h8 Z[2];
    ce[0]=MH(U[0],Be,Z4); co[0]=MH(U[0],Bo,Z4); ce[1]=MH(U[1],Be,Z4); co[1]=MH(U[1],Bo,Z4); Z[0]=zpack(ce[0],co[0]);
    #pragma unroll
    for(int kt=0;kt<12;kt++){
      SCHED();
      if(kt==10) OPT if(kt==11) SUM(a)
      if(kt+2<12){ ce[kt&1]=MH(U[kt+2],Be,Z4); co[kt&1]=MH(U[kt+2],Bo,Z4); }
      if(kt>=2&&kt<=8&&!(kt&1)){ SCHED(); RW((kt+2)/2) if(kt==8) ARR }
      if(kt+1<12) Z[(kt+1)&1]=zpack(ce[(kt+1)&1],co[(kt+1)&1]);
      #pragma unroll
      for(int rho=-1;rho<5;rho++){ int j=(kt+4+rho)>>1; if(((kt+rho)&1)==0&&j>=2&&j<8) acc[j]=MH(Z[kt&1],A[rho+1],acc[j]); }
      if(kt<6){ SCHED(); U[kt+6]=LD(h8,bd+256*(kt+6)); } }
    SCHED(); SUM(b)
  }
  { float s0[4], s1[4];
    if(RFL(cv)<11u){ u32 tv,cnt;
      asm volatile("s_mov_b32 %1, 0x20000\n1:\n\tds_read_b32 %0, %6\n\tds_read_b128 %2, %7\n\tds_read_b128 %4, %7 offset:2048\n\tds_read_b128 %3, %7 offset:1024\n\tds_read_b128 %5, %7 offset:3072\n\ts_waitcnt lgkmcnt(4)\n\t"
        "v_cmp_gt_u32 vcc, 11, %0\n\ts_cbranch_vccz 2f\n\ts_sleep 1\n\ts_sub_u32 %1, %1, 1\n\ts_cbranch_scc0 1b\n2:\n\ts_waitcnt lgkmcnt(0)"
        : "=&v"(tv),"=&s"(cnt),"=&v"(a[0]),"=&v"(b[0]),"=&v"(a[1]),"=&v"(b[1]) : "v"(cga),"v"(rp) : "memory","scc","vcc");
      SUM(a) SUM(b) }
    f32x4 o0=acc[2*OWN], o1=acc[2*OWN+1];
    #pragma unroll
    for(int e=0;e<4;e++){ s0[e]=__builtin_fmaf(o0[e],sc,a[0][e]); s1[e]=__builtin_fmaf(o1[e],sc,b[0][e]); }
    auto r0=__builtin_amdgcn_permlane16_swap(pk(s0[0],s0[1]),pk(s1[0],s1[1]),false,false); auto r1=__builtin_amdgcn_permlane16_swap(pk(s0[2],s0[3]),pk(s1[2],s1[3]),false,false);
    asm volatile("buffer_store_dwordx4 %0, %1, %2, 0 offen offset:%3 sc1\n\ts_nop 1" :: "v"(u32x4{(r0[0]&ym)|yp[0],(r1[0]&ym)|yp[1],(r0[1]&ym)|yp[2],(r1[1]&ym)|yp[3]}),"v"(c.yo),"s"(ry),"i"(OWN*1024) : "memory"); }
  garrive(sm,C_E);

  asm volatile("s_endpgm" : "=v"(fa),"=v"(fb) :: "memory");
}
#define GETEP asm volatile("s_waitcnt lgkmcnt(0)\n\ts_mov_b64 %0, s[96:97]\n\ts_mov_b32 %1, s98" : "=s"(ep),"=s"(ldw));
DEV void chan(lch* sm,int grp,int w4,int d,int one_,char* ws,u32x4 q0,u32x4 q1,u32x4 q2,u32x4 q3,u32x4 q4){
  lch* cb=sm+O_CH+grp*SLSZ; int role=w4, emax;
  #define LP(v) ((lch*)(size_t)(u32)(v))
  const lch* fa=LP(q4[0]); const lch* fb=LP(q4[1]);
  u64 tag;
  { float a2,bp,br,bi,cr,ci;
    u32 fq=q3[2], fr=q3[3], one=q4[2]; float f64=BC(float,fq), f128=BC(float,fr);
    lch *t8[4]={LP(q0[0]),LP(q0[1]),LP(q0[2]),LP(q0[3])}, *tB[4]={LP(q1[0]),LP(q1[1]),LP(q1[2]),LP(q1[3])}, *t4[2]={LP(q2[0]),LP(q2[1])}, *t2[2]={LP(q2[2]),LP(q2[3])}, *t1=LP(q3[0]), *te=LP(q3[1]);
    if(BID<64){ if(grp==0) PRIO(PK&3); if(grp==1) PRIO((PK>>4)&3); if(grp==2) PRIO((PK>>8)&3); } else
    if(grp!=0) PRIO(1);
    u64 ep; u32 ldw; GETEP tag=ep+1ull;
    float dt=rb(expf_((d&1)? bhi(ldw):blo(ldw))), ca=dt*1.4426950408889634f, cb_=dt*0.15915494309189535f;
    asm volatile("s_waitcnt vmcnt(0)\n\tv_mul_f32 %2, %8, v114\n\tv_mul_f32 %3, %8, v115\n\tv_mul_f32 %0, %6, v112\n\tv_mul_f32 %1, %7, v113\n\tv_mov_b32 %4, v116\n\tv_mov_b32 %5, v117"
      : "=&v"(a2),"=&v"(bp),"=&v"(br),"=&v"(bi),"=&v"(cr),"=&v"(ci) : "v"(ca),"v"(cb_),"v"(dt) : "memory");
    C2 W; W.r=cr*br-ci*bi; W.i=cr*bi+ci*br;
    u32 mx=BC(u32,__builtin_fmaxf(__builtin_fabsf(W.r),__builtin_fabsf(W.i)));
    #define MXS(C) mx=umax_(mx,dppu<C>(mx));
    float e8=a2*8.f, eB=__builtin_fmaf(a2,f64,14.f), p8=bp*8.f, pB=bp*f64; SCHED();
    MXS(0xB1) p8=__builtin_amdgcn_fractf(p8); pB=__builtin_amdgcn_fractf(pB); e8=__builtin_amdgcn_exp2f(e8); SCHED();
    MXS(0x4E) eB=__builtin_amdgcn_exp2f(eB); float c8=__builtin_amdgcn_cosf(p8); SCHED();
    MXS(0x141) float s8=__builtin_amdgcn_sinf(p8), cB=__builtin_amdgcn_cosf(pB); SCHED();
    float sB=__builtin_amdgcn_sinf(pB); PIN(sB); MXS(0x140) float e1=__builtin_amdgcn_exp2f(a2); PIN(e1); SCHED();
    asm volatile("s_mov_b64 exec, 0x10001\n\tds_max_u32 %0, %1\n\tds_add_u32 %0, %2 offset:4\n\ts_mov_b64 exec, -1" :: "v"(te),"v"(mx),"v"(one) : "memory");
    C3 z8; z8.r=e8*c8; z8.n=e8*s8; z8.i=-z8.n; PIN(z8.i); C2 B; B.r=eB*cB; B.i=-(eB*sB);
    float p1=__builtin_amdgcn_fractf(bp), pE=__builtin_amdgcn_fractf(bp*256.f), pV=__builtin_amdgcn_fractf(bp*f128), eE=a2*256.f, eV=a2*f128, tr[8];
    #pragma unroll
    for(int k=0;k<4;k++){ SCHED(); u32 h0=ph(B); B=cm(B,z8);
      if(k==0){ tr[0]=__builtin_amdgcn_cosf(pV); tr[1]=__builtin_amdgcn_sinf(pV); } if(k==1){ tr[2]=__builtin_amdgcn_exp2f(eV); tr[3]=__builtin_amdgcn_cosf(p1); }
      if(k==2){ tr[4]=__builtin_amdgcn_sinf(p1); tr[5]=__builtin_amdgcn_exp2f(eE); } if(k==3){ tr[6]=__builtin_amdgcn_cosf(pE); tr[7]=__builtin_amdgcn_sinf(pE); }
      PIN(tr[2*k]); PIN(tr[2*k+1]);
      u32 h1=ph(B); if(k<3) B=cm(B,z8); W2(tB[k],h0,h1); }
    asm volatile("ds_read_b64 v[118:119], %0" :: "v"(te) : "memory");
    SCHED();
    C3 z1=c3(C2{e1*tr[3],e1*tr[4]}), E=c3(C2{tr[5]*tr[6],tr[5]*tr[7]}); C2 V[8]; V[0]=cmul(W,C2{tr[2]*tr[0],tr[2]*tr[1]});
    { u32 cnt,n; PIN(V[0].r); PIN(V[0].i); PIN(z1.r); PIN(E.r);
      asm volatile("s_mov_b32 %1, 0x20000\n1:\n\ts_waitcnt lgkmcnt(0)\n\tv_readfirstlane_b32 %2, v119\n\tv_readfirstlane_b32 %0, v118\n\ts_cmp_eq_u32 %2, 8\n\ts_cbranch_scc1 2f\n\tds_read_b64 v[118:119], %3\n\ts_sub_u32 %1, %1, 1\n\ts_cbranch_scc0 1b\n2:"
        : "=&s"(emax),"=&s"(cnt),"=&s"(n) : "v"(te) : "memory","scc");
      emax=RFL(emax); emax=(emax>>23)-126; }
    if(role==3) asm volatile("s_mov_b64 exec, 1\n\tglobal_store_dwordx2 %0, %1, %2 sc1\n\ts_mov_b64 exec, -1" :: "v"(W_FL+d*8),"v"(tag<<32|(u32)(emax+10-YSH)),"s"(ws) : "memory");
    V[0].r=__builtin_amdgcn_ldexpf(V[0].r,14-emax); V[0].i=__builtin_amdgcn_ldexpf(V[0].i,14-emax);
    C2 Y=cm(V[0],E), Y4; u32 y0=ph(Y);
    V[1]=cm(V[0],z1); W2(t8[0],ph(V[0]),ph(V[1])); Y=cm(Y,E); u32 y1=ph(Y);
    V[2]=cm(V[1],z1); Y=cm(Y,E); u32 y2=ph(Y);
    V[3]=cm(V[2],z1); W2(t8[1],ph(V[2]),ph(V[3])); W2(t4[0],y0,ph(cm(V[2],E)));
    V[4]=cm(V[3],z1); Y=cm(Y,E); ST(u32,t1)=ph(Y);
    V[5]=cm(V[4],z1); W2(t8[2],ph(V[4]),ph(V[5])); Y4=cm(V[4],E); u32 y4=ph(Y4);
    V[6]=cm(V[5],z1); Y=cm(Y,E); ST(u32,t1+2*TROW)=ph(Y);
    V[7]=cm(V[6],z1); W2(t8[3],ph(V[6]),ph(V[7])); Y4=cm(Y4,E); W2(t2[0],y1,ph(Y4));
    W2(t4[1],y4,ph(cm(V[6],E))); Y=cm(Y,E); ST(u32,t1+4*TROW)=ph(Y);
    Y4=cm(Y4,E); W2(t2[1],y2,ph(Y4)); Y=cm(Y,E); ST(u32,t1+6*TROW)=ph(Y);
    Y=cm(Y,E); asm volatile("s_mov_b32 exec_hi, 0\n\tds_write_b32 %0, %1 offset:4352\n\ts_mov_b32 exec_hi, -1" :: "v"(t1),"v"(ph(Y)) : "memory");
  }
  if(BID<64){ if(grp==0) PRIO(PS&3); if(grp==1) PRIO((PS>>4)&3); if(grp==2) PRIO((PS>>8)&3); }
  gsync(sm,C_G+grp,4);
  if(role==0) chan2<0>(sm,cb,fa,fb,grp,d,one_,emax,tag,ws);
  if(role==1) chan2<1>(sm,cb,fa,fb,grp,d,one_,emax,tag,ws);
  if(role==2) chan2<2>(sm,cb,fa,fb,grp,d,one_,emax,tag,ws);
  chan2<3>(sm,cb,fa,fb,grp,d,one_,emax,tag,ws);
}

#define GTC(r,s,c) "v_cmp_gt_u32 %[" #s "], v" #r ", " c "\n\t"
#define GTA(s) "v_addc_co_u32 %[rk], vcc, 0, %[rk], %[" #s "]\n\t"
#define GT0(s) "v_cndmask_b32 %[rk], 0, 1, %[" #s "]\n\t"
#define GT4(a,b,c,d,k,F) GTC(a,s0,k) GTC(b,s1,k) GTC(c,s2,k) GTC(d,s3,k) F(s0) GTA(s1) GTA(s2) GTA(s3)
#define GTS [s0]"=&s"(q0),[s1]"=&s"(q1),[s2]"=&s"(q2),[s3]"=&s"(q3)
DEV void top8(lch* sm,int w4,int ln,u32 M,char* ws,u32& rank_,u32& key_){
  u32 k[8], hm=0xffff0000u, mx=1+w4*4+(ln>>4), one=1, cw=O_CND+w4*32; u64 em=0x0001000100010001ull,q0,q1,q2,q3;
  u32x4 rs=mkrs(ws+W_RG+(BID&(NRG-1))*8192,8192); int ro=w4*1024+ln*16;
  #pragma unroll
  for(int e=0;e<8;e++){ k[e]=(u32)(0xfff-(w4*256+ln*4+(e&3)+(e>>2)*1024)); PIN(k[e]); }
  lch *aW=sm+O_RMX+(w4*4+(ln>>4))*4, *aC=sm+O_RMX+(ln>>2)*4, *aO=sm+O_RMX+(ln&3)*16, *dC=sm+O_CND+(ln>>1)*4, *dO=sm+O_CND+(ln&1)*64, *cW=sm+C_W*4; PIN(aW); PIN(aC); PIN(aO); PIN(dC); PIN(dO); PIN(ro); PIN(ln); PIN(mx); PIN(one); PIN(cW);
  asm volatile("" : "+s"(hm),"+s"(em),"+s"(rs),"+s"(cw));
  PRIO(3);
  gwait<2>(sm,C_G,RGATE);
  if(RD) __builtin_amdgcn_s_sleep(RD);
  { u32 cnt,t0,t1;
    #define KL(e,r) "v_lshl_or_b32 %[k" #e "], v" #r ", 16, %[k" #e "]\n\t"
    #define KH(e,r) "v_and_or_b32 %[k" #e "], v" #r ", %[hm], %[k" #e "]\n\t"
    #define CHK_R "v_bitop3_b32 %[t0], v113, v115, %[M] bitop3:0x7e\n\tv_bitop3_b32 %[t1], v117, v119, %[M] bitop3:0x7e\n\tv_or_b32 %[t0], %[t0], %[t1]\n\tv_cmp_ne_u32 vcc, 0, %[t0]\n\t"
    #define MXD(c) "v_max_u32_dpp %[mx], %[mx], %[mx] " c " row_mask:0xf bank_mask:0xf\n\t"
    asm volatile(LD_DUP2
      "s_sleep " STR(R2D) "\n\tbuffer_load_dwordx4 v[120:123], %[of], %[rs], 0 offen sc1\n\tbuffer_load_dwordx4 v[124:127], %[of], %[rs], %[so] offen sc1\n\t"
      "s_mov_b64 exec, 1\n\tds_add_u32 %[cw], %[one]\n\ts_mov_b64 exec, -1\n\ts_mov_b32 %[cnt], 0x20000\n\ts_waitcnt vmcnt(2)\n\t" CHK_R "s_cbranch_vccz 9f\n\t"
      "s_waitcnt vmcnt(0)\n\tv_mov_b64 v[112:113], v[120:121]\n\tv_mov_b64 v[114:115], v[122:123]\n\tv_mov_b64 v[116:117], v[124:125]\n\tv_mov_b64 v[118:119], v[126:127]\n\t"
      POLLASM(LD_DUP2,CHK_R,PSLP)
      "v_pk_max_u16 %[t0], v112, v114\n\tv_pk_max_u16 %[t1], v116, v118\n\tv_pk_max_u16 %[t0], %[t0], %[t1]\n\t"
      "v_max_u16_sdwa %[mx], %[t0], %[t0] dst_sel:WORD_1 dst_unused:UNUSED_PRESERVE src0_sel:WORD_0 src1_sel:WORD_1\n\t"
      KL(0,112) KH(1,112) MXD("quad_perm:[1,0,3,2]") KL(2,114) KH(3,114) MXD("quad_perm:[2,3,0,1]") KL(4,116) KH(5,116) MXD("row_half_mirror") KL(6,118) KH(7,118) MXD("row_mirror")
      "s_mov_b64 exec, %[em]\n\tds_write_b32 %[aW], %[mx]\n\ts_mov_b64 exec, -1\n\tds_read_b32 v116, %[aC]\n\tds_read_b128 v[112:115], %[aO]"
      : [k0]"+v"(k[0]),[k1]"+v"(k[1]),[k2]"+v"(k[2]),[k3]"+v"(k[3]),[k4]"+v"(k[4]),[k5]"+v"(k[5]),[k6]"+v"(k[6]),[k7]"+v"(k[7]),[mx]"+v"(mx),[cnt]"=&s"(cnt),[t0]"=&v"(t0),[t1]"=&v"(t1)
      : [of]"v"(ro),[rs]"s"(rs),[so]"s"(4096),[M]"s"(M),[hm]"s"(hm),[cw]"v"(cW),[one]"v"(one),[em]"s"(em),[aW]"v"(aW),[aC]"v"(aC),[aO]"v"(aO) : PCLOB); }
  u32 slow;
  { u32 rk,cnt,T,h,l,t,n;
    #define INS(e) "v_med3_u32 %[t], %[t], %[l], %[k" #e "]\n\tv_med3_u32 %[l], %[h], %[l], %[k" #e "]\n\tv_max_u32 %[h], %[h], %[k" #e "]\n\t"
    asm volatile("s_branch 7f\n"
      "3:\n\tv_cmp_le_u32 %[s2], %[T], %[t]\n\ts_add_u32 %[cnt], %[cnt], %[n]\n\ts_cmp_gt_u32 %[cnt], 8\n\ts_cbranch_scc1 5f\n\ts_cmp_lg_u64 %[s2], 0\n\ts_cbranch_scc1 5f\n\t"
      "s_mov_b64 exec, %[s1]\n\tv_mov_b32 %[t], %[n]\n\tv_mbcnt_lo_u32_b32 %[t], exec_lo, %[t]\n\tv_mbcnt_hi_u32_b32 %[t], exec_hi, %[t]\n\tv_lshl_add_u32 %[t], %[t], 2, %[cw]\n\tds_write_b32 %[t], %[l]\n\ts_mov_b64 exec, -1\n\ts_branch 4f\n"
      "5:\n\ts_mov_b32 %[sl], 1\n\ts_branch 6f\n7:\n\t"
      "v_max3_u32 %[h], %[k0], %[k1], %[k2]\n\tv_med3_u32 %[l], %[k0], %[k1], %[k2]\n\tv_min3_u32 %[t], %[k0], %[k1], %[k2]\n\t" INS(3) INS(4) INS(5) INS(6) INS(7)
      "s_mov_b32 %[cnt], 0x20000\n1:\n\ts_waitcnt lgkmcnt(0)\n\tv_cmp_eq_u32 vcc, 0, v116\n\ts_cbranch_vccz 2f\n\tds_read_b32 v116, %[aC]\n\tds_read_b128 v[112:115], %[aO]\n\ts_sub_u32 %[cnt], %[cnt], 1\n\ts_cbranch_scc0 1b\n2:\n\t"
      GT4(112,113,114,115,"v116",GT0) "v_and_b32 v116, %[hm], v116\n\ts_nop 0\n\tv_add_u32_dpp %[rk], %[rk], %[rk] quad_perm:[1,0,3,2] row_mask:0xf bank_mask:0xf\n\ts_mov_b32 %[sl], 0\n\ts_nop 0\n\tv_add_u32_dpp %[rk], %[rk], %[rk] quad_perm:[2,3,0,1] row_mask:0xf bank_mask:0xf\n\t"
      "v_cmp_eq_u32 vcc, 7, %[rk]\n\ts_ff1_i32_b64 %[cnt], vcc\n\ts_nop 0\n\tv_readlane_b32 %[T], v116, %[cnt]\n\ts_nop 1\n\t"
      "v_cmp_le_u32 vcc, %[T], %[h]\n\tv_cmp_le_u32 %[s1], %[T], %[l]\n\ts_bcnt1_i32_b64 %[n], vcc\n\tv_mbcnt_lo_u32_b32 %[rk], vcc_lo, 0\n\tv_mbcnt_hi_u32_b32 %[rk], vcc_hi, %[rk]\n\ts_bcnt1_i32_b64 %[cnt], %[s1]\n\ts_cbranch_scc1 3b\n\t"
      "s_cmp_gt_u32 %[n], 8\n\ts_cbranch_scc1 5b\n"
      "4:\n\tv_lshl_add_u32 %[rk], %[rk], 2, %[cw]\n\ts_mov_b64 exec, vcc\n\tds_write_b32 %[rk], %[h]\n\ts_mov_b64 exec, 1\n\tds_add_u32 %[cW], %[one] offset:4\n\ts_mov_b64 exec, -1\n6:"
      : [rk]"=&v"(rk),[cnt]"=&s"(cnt),[T]"=&s"(T),[h]"=&v"(h),[l]"=&v"(l),[t]"=&v"(t),[n]"=&s"(n),[sl]"=&s"(slow),GTS
      : [k0]"v"(k[0]),[k1]"v"(k[1]),[k2]"v"(k[2]),[k3]"v"(k[3]),[k4]"v"(k[4]),[k5]"v"(k[5]),[k6]"v"(k[6]),[k7]"v"(k[7]),[aC]"v"(aC),[aO]"v"(aO),[hm]"s"(hm),[cw]"s"(cw),[cW]"v"(cW),[one]"v"(one) : PCLOB); }
  if(__builtin_expect(RFL(slow)!=0,0)){
    u32 mine=0;
    _Pragma("unroll 1") for(int c=0;c<8;c++){ u32 lm=umax_(umax_(umax_(k[0],k[1]),umax_(k[2],k[3])),umax_(umax_(k[4],k[5]),umax_(k[6],k[7]))); u32 wm=wmax(lm); if(ln==c) mine=wm;
      #pragma unroll
      for(int e=0;e<8;e++) k[e]=(k[e]==wm)?0u:k[e]; }
    if(ln<8) ST(u32,sm+cw+ln*4)=mine;
    garrive(sm,C_W+1); }
  { u32 c,rk,cnt;
    asm volatile("s_mov_b32 %[cnt], 0x20000\n1:\n\tds_read_b32 %[rk], %[cW] offset:4\n\tds_read_b32 %[c], %[dC]\n\tds_read_b128 v[96:99], %[dO]\n\tds_read_b128 v[100:103], %[dO] offset:16\n\tds_read_b128 v[104:107], %[dO] offset:32\n\tds_read_b128 v[108:111], %[dO] offset:48\n\t"
      "s_waitcnt lgkmcnt(5)\n\tv_cmp_gt_u32 vcc, 4, %[rk]\n\ts_cbranch_vccz 2f\n\ts_sub_u32 %[cnt], %[cnt], 1\n\ts_cbranch_scc0 1b\n2:\n\t"
      "s_waitcnt lgkmcnt(3)\n\t" GT4(96,97,98,99,"%[c]",GT0) "s_waitcnt lgkmcnt(2)\n\t" GT4(100,101,102,103,"%[c]",GTA) "s_waitcnt lgkmcnt(1)\n\t" GT4(104,105,106,107,"%[c]",GTA) "s_waitcnt lgkmcnt(0)\n\t" GT4(108,109,110,111,"%[c]",GTA)
      "s_nop 1\n\tv_add_u32_dpp %[rk], %[rk], %[rk] quad_perm:[1,0,3,2] row_mask:0xf bank_mask:0xf"
      : [c]"=&v"(c),[rk]"=&v"(rk),[cnt]"=&s"(cnt),GTS : [dC]"v"(dC),[dO]"v"(dO),[cW]"v"(cW)
      : PCLOB,"v96","v97","v98","v99","v100","v101","v102","v103","v104","v105","v106","v107","v108","v109","v110","v111");
    rank_=rk; key_=c; }
}

#define FILL_(n,WAIT) "s_mov_b32 m0, %[ld]\n\ts_mov_b32 %[c], " STR(n) "-1\n3:\n\t" WAIT "buffer_load_dwordx4 %[vo], %[rs], %[so] offen nt lds\n\ts_add_u32 m0, m0, 0x1000\n\ts_add_u32 %[so], %[so], %[st]\n\t" \
  "s_sub_u32 %[c], %[c], 1\n\ts_cbranch_scc0 3b\n\ts_mov_b32 %[ld], m0\n\t"
#define FILL(n,WAIT) do{ u32 c_; asm volatile(FILL_(n,WAIT) : [c]"=&s"(c_),[so]"+s"(wso),[ld]"+s"(wld) : [vo]"v"(wvo),[rs]"s"(wrs),[st]"s"(wst) : "memory","scc","m0"); }while(0)
#define FILLD(n,WN,WW_,STK) do{ u32 c_,t_,s_=0; asm volatile(FILL_(n,STK "ds_read_b32 %[t], %[ad]\n\ts_waitcnt lgkmcnt(0)\n\tv_readfirstlane_b32 %[s], %[t]\n\ts_cmp_ge_u32 %[s], 4\n\ts_cbranch_scc1 4f\n\t" VW(WN) "4:\n\t" VW(WW_)) \
  : [c]"=&s"(c_),[so]"+s"(wso),[ld]"+s"(wld),[t]"=&v"(t_),[s]"+s"(s_) : [vo]"v"(wvo),[rs]"s"(wrs),[st]"s"(wst),[ad]"v"(C_WIN*4) : "memory","scc","m0"); wso=RFL(wso); wld=RFL(wld); }while(0)
#define STKB "s_cmp_ge_u32 %[s], 4\n\ts_cbranch_scc1 4f\n\t"

__global__ NOPK __attribute__((amdgpu_num_vgpr(56),amdgpu_num_sgpr(102))) __launch_bounds__(1024,4) void kA(const u16* x_,char* ws_,const u16* wr_,const u16* Dsk_,const u16* wg_,const u16* wu_,const u16* wd_,u16* out_,KP kp){
  __shared__ __attribute__((aligned(16))) char smem[LDS_TOTAL];
  lch* sm=(lch*)smem;
  u32x4 xa,xb_,wa,wb,dq[3]; u32 xc,wc; u64 p_[7];
  asm volatile(
    "s_load_dwordx16 s[64:79], %[ka], 0x0\n\ts_load_dwordx16 s[80:95], %[ka], 0x40\n\t"
    "v_readfirstlane_b32 s60, %[t]\n\ts_and_b32 s61, %[wg], 7\n\ts_lshr_b32 s62, %[wg], 3\n\tv_and_b32 v118, 31, %[t]\n\ts_bfe_u32 s63, s60, 0x20006\n\ts_cmpk_ge_u32 s60, 0x100\n\ts_cbranch_scc1 1f\n\t"
    "s_setprio 3\n\ts_lshl_b32 s56, s61, 5\n\ts_add_u32 s56, s56, s62\n\ts_mulk_i32 s56, 0x2400\n\tv_lshrrev_b32 v119, 5, %[t]\n\tv_mul_u32_u24 v119, 0x480, v119\n\tv_add_u32 v119, s56, v119\n\t"
    "v_lshl_add_u32 v116, v118, 4, v119\n\tv_lshl_add_u32 v117, v118, 2, v119\n\tv_lshlrev_b32 v114, 4, v118\n\tv_lshlrev_b32 v115, 2, v118\n\t"
    "v_lshlrev_b32 v119, 1, %[t]\n\tv_mov_b32 v118, 0\n\tds_write_b16 v119, v118\n\t"
    "s_waitcnt lgkmcnt(0)\n\t"
    "s_load_dwordx2 s[96:97], s[66:67], 0x0\n\t"
    "global_load_dwordx4 %[wa], v114, s[68:69]\n\tglobal_load_dwordx4 %[wb], v114, s[68:69] offset:512\n\tglobal_load_dword %[wc], v115, s[68:69] offset:1024\n\t"
    "global_load_dwordx4 %[xa], v116, s[64:65]\n\tglobal_load_dwordx4 %[xb], v116, s[64:65] offset:512\n\tglobal_load_dword %[xc], v117, s[64:65] offset:1024\n\t"
    "s_barrier\n\t"
    "s_branch 8f\n"
    "1:\n\ts_cmp_ge_u32 s62, " STR(CPF) "\n\ts_cbranch_scc1 6f\n\ts_getpc_b64 s[56:57]\n.Lkpc:\n\ts_and_b32 s58, s62, 3\n\ts_lshl_b32 s58, s58, 13\n\ts_sub_u32 s58, s58, 0x1000\n\tv_lshl_add_u32 v119, %[t], 4, s58\n\tv_min_u32 v119, .Lkend-.Lkpc-16, v119\n\t"
    "global_load_dwordx4 v[120:123], v119, s[56:57]\n\t.subsection 1\n.Lkend:\n\t.subsection 0\n6:\n\t"
    "v_lshlrev_b32 v119, 4, %[t]\n\tv_and_b32 v119, 0xff0, v119\n\t"
    "s_lshr_b32 s59, s60, 8\n\ts_cmp_lt_u32 s62, 8\n\ts_cselect_b32 s58, 2, 1\n\ts_cbranch_scc1 2f\n\ts_cmp_eq_u32 s59, 3\n\ts_cbranch_scc1 4f\n"
    "2:\n\ts_sub_u32 s57, s59, 2\n\ts_cmp_eq_u32 s59, 1\n\ts_cselect_b32 s58, s58, s57\n\ts_cmp_lt_u32 s62, 8\n\ts_cselect_b32 s56, 0, 8\n\ts_lshl1_add_u32 s56, s62, s56\n\ts_mulk_i32 s61, 72\n\ts_add_u32 s51, s56, s61\n\ts_sub_u32 s56, 2, s59\n\ts_sub_u32 s57, 16, s62\n\ts_cmp_eq_u32 s59, 3\n\ts_cselect_b32 s50, s57, s56\n\ts_add_u32 s56, s50, s51\n\t"
    "v_mov_b32 v112, 0\n\tv_mov_b32 v113, 0\n\tv_mov_b32 v114, 0\n\tv_mov_b32 v115, 0\n\tv_mov_b32 v116, 0\n\tv_mov_b32 v117, 0\n\ts_setprio 2\n\t"
    "s_lshl_b32 s57, s56, 2\n\ts_add_u32 s57, s57, s63\n\ts_lshl_b32 s57, s57, 6\n\tv_lshl_add_u32 v22, v118, 1, s57\n\ts_lshl_b32 s56, s56, 1\n\ts_and_b32 s56, s56, -4\n\t"
    "s_waitcnt lgkmcnt(0)\n\t"
    "global_load_short_d16_hi v112, v22, s[80:81] nt\n\tglobal_load_short_d16_hi v113, v22, s[82:83] nt\n\tglobal_load_short_d16_hi v114, v22, s[84:85] nt\n\t"
    "global_load_short_d16_hi v115, v22, s[86:87] nt\n\tglobal_load_short_d16_hi v116, v22, s[88:89] nt\n\tglobal_load_short_d16_hi v117, v22, s[90:91] nt\n\t"
    "s_load_dwordx2 s[96:97], s[66:67], 0x0\n\ts_load_dword s98, s[92:93], s56\n\t"
    "s_barrier\n\t"
    "s_mul_i32 s52, s58, %[sl]\n\ts_add_u32 s52, s52, %[tb]\n\ts_lshl_b32 s53, s63, 7\n\ts_add_u32 s53, s53, s52\n\tv_lshl_add_u32 v30, v118, 2, s53\n\tv_bfe_u32 v31, %[t], 5, 1\n\ts_movk_i32 s54, 544\n\tv_mul_u32_u24 v29, s54, v31\n\t"
    "v_lshl_add_u32 v2, v29, 3, v30\n\ts_movk_i32 s53, 1088\n\tv_add_u32 v3, s53, v2\n\tv_add_u32 v4, s53, v3\n\tv_add_u32 v5, s53, v4\n\tv_add_u32 v6, 41*544, v2\n\tv_add_u32 v7, s53, v6\n\tv_add_u32 v8, s53, v7\n\tv_add_u32 v9, s53, v8\n\t"
    "v_lshl_add_u32 v10, v29, 2, v30\n\tv_add_u32 v10, 16*544, v10\n\tv_add_u32 v11, s53, v10\n\tv_lshl_add_u32 v12, v29, 1, v30\n\tv_add_u32 v12, 24*544, v12\n\tv_add_u32 v13, 2176, v12\n\tv_add_u32 v14, v29, v30\n\tv_add_u32 v14, 32*544, v14\n\t"
    "s_lshl_b32 s53, s58, 4\n\ts_add_u32 s53, s53, %[ex]\n\tv_mov_b32 v15, s53\n\tv_cvt_f32_ubyte0 v31, v31\n\tv_mul_f32 v16, 0x42800000, v31\n\tv_add_f32 v17, v16, v16\n\tv_mov_b32 v20, 1\n\t"
    "v_and_b32 v29, 15, %[t]\n\tv_and_b32 v30, 48, %[t]\n\tv_mad_u32_u24 v30, v29, s54, v30\n\ts_lshl_b32 s55, s59, 1\n\ts_lshr_b32 s55, 0x48, s55\n\ts_add_u32 s55, s55, s63\n\ts_and_b32 s55, s55, 3\n\ts_mul_i32 s53, s55, 16*544\n\ts_movk_i32 s57, 41*544\n\ts_cmp_eq_u32 s55, 2\n\ts_cselect_b32 s53, s57, s53\n\ts_cselect_b32 s57, 32*544, s57\n\t"
    "s_add_u32 s53, s53, s52\n\ts_add_u32 s57, s57, s52\n\tv_add_u32 v18, s53, v30\n\tv_add_u32 v19, s57, v30\n\t"
    "v_min_u32 v29, 237*16, v119\n\tv_subrev_u32 v30, 204*16, v29\n\tv_add_u32 v29, %[de], v29\n\tv_min_u32 v29, v29, v30\n\ts_sub_u32 s53, s52, 4640\n\tv_add_u32 v29, s53, v29\n\tv_mov_b32 v24, 0\n\tv_mov_b32 v25, 0\n\tv_mov_b32 v26, 0\n\tv_mov_b32 v27, 0\n\tds_write_b128 v29, v[24:27]\n\t"
    "s_cmp_lg_u32 s55, 3\n\ts_cbranch_scc1 9f\n\t"
    "v_bfe_u32 v18, %[t], 1, 5\n\tv_and_b32 v19, 1, %[t]\n\ts_cmp_lt_u32 s62, 8\n\ts_cselect_b32 s53, 2, 1\n\ts_mul_i32 s53, s53, %[sl]\n\tv_mul_u32_u24 v21, s53, v19\n\ts_sub_u32 s55, 2, s59\n\ts_lshl_b32 s55, s55, 7\n\tv_add_u32 v18, s55, v18\n\ts_cmp_lg_u32 s59, 3\n\ts_cbranch_scc1 7f\n\t"
    "v_bfe_u32 v18, %[t], 5, 1\n\tv_lshl_add_u32 v18, v18, 7, v118\n\tv_mov_b32 v19, s50\n\tv_mov_b32 v21, %[sl]\n"
    "7:\n\tv_add_u32 v19, s51, v19\n\tv_lshl_add_u32 v21, v18, 4, v21\n\tv_lshrrev_b32 v24, 3-" STR(TL) ", v19\n\tv_and_b32 v19, 7>>" STR(TL) ", v19\n\tv_lshlrev_b32 v19, 4, v19\n\tv_lshl_add_u32 v19, v24, 7, v19\n\t"
    "v_and_b32 v24, (1<<" STR(TL) ")-1, v18\n\tv_lshl_add_u32 v19, v24, 7-" STR(TL) ", v19\n\tv_lshrrev_b32 v18, " STR(TL) ", v18\n\tv_mul_u32_u24 v18, %[ust], v18\n\tv_add_u32 v18, v18, v19\n\tv_add_u32 v19, 512, v21\n\t"
    "s_branch 9f\n"
    "4:\n\t"
    "v_bfe_u32 v118, %[t], 3, 5\n\tv_lshrrev_b32 v117, 1, v118\n\tv_and_b32 v117, 4, v117\n\tv_and_b32 v116, 2, v118\n\tv_and_b32 v115, 7, %[t]\n\tv_or_b32 v116, v116, v117\n\tv_xor_b32 v115, v115, v116\n\tv_lshlrev_b32 v115, 4, v115\n\t"
    "s_mul_i32 s54, s61, 9\n\ts_sub_u32 s55, s62, 14\n\ts_lshr_b32 s55, s55, 1\n\ts_add_u32 s54, s54, s55\n\ts_lshl_b32 s54, s54, 7\n\t"
    "s_mul_i32 s52, s61, 0xa2000\n\ts_mul_i32 s53, s62, 0x180\n\ts_add_u32 s52, s52, s53\n\ts_cmp_ge_u32 s62, 11\n\ts_cselect_b32 s53, 0x51000-0x480, 0\n\ts_add_u32 s52, s52, s53\n\ts_sub_u32 s52, s52, 0xc00\n\t"
    "s_movk_i32 s55, 0x480\n\ts_cmp_ge_u32 s62, 14\n\ts_cselect_b32 s54, s54, s52\n\ts_cselect_b32 s55, 0x2400, s55\n\tv_mad_u32_u24 v118, v118, s55, v115\n\ts_lshl_b32 s55, s55, 5\n\t"
    "s_lshl_b32 s52, s63, 10\n\ts_add_u32 m0, s52, %[r2]\n\ts_mov_b32 s58, 0x510000\n\ts_mov_b32 s59, 0x20000\n\ts_cmp_ge_u32 s62, 14\n\ts_cselect_b32 s53, " STR(PRE1) "-1, " STR(PRE2) "-1\n\ts_setprio 3\n\t"
    "s_waitcnt lgkmcnt(0)\n\t"
    "s_bitcmp1_b32 %[wg], 3\n\ts_cselect_b64 s[56:57], s[74:75], s[72:73]\n\ts_cmp_ge_u32 s62, 14\n\ts_cselect_b64 s[56:57], s[56:57], s[76:77]\n\ts_cselect_b32 s52, 0x180, -1\n\ts_and_b32 s57, s57, 0xffff\n\ts_add_u32 s60, s56, s54\n\ts_and_b32 s60, s60, 0x380\n\ts_cmp_eq_u32 s60, s52\n\ts_cselect_b32 s53, " STR(PRE3) "-1, s53\n\t"
    "s_load_dwordx2 s[96:97], s[66:67], 0x0\n\t"
    "s_barrier\n\t"
    "s_sleep " STR(PD) "\n\t"
    "\n3:\n\t" "buffer_load_dwordx4 v118, s[56:59], s54 offen nt lds\n\ts_add_u32 m0, m0, 0x1000\n\ts_add_u32 s54, s54, s55\n\ts_sub_u32 s53, s53, 1\n\ts_cbranch_scc0 3b\n"
    "9:\n8:"
    : [xa]"=&{v[2:5]}"(xa),[xb]"=&{v[6:9]}"(xb_),[xc]"=&{v30}"(xc),[wa]"=&{v[10:13]}"(wa),[wb]"=&{v[14:17]}"(wb),[wc]"=&{v31}"(wc),[d0]"=&{v[18:21]}"(dq[0]),[d1]"=&{v[22:25]}"(dq[1]),[d2]"=&{v[26:29]}"(dq[2]),
      "=&{s[64:65]}"(p_[0]),"=&{s[66:67]}"(p_[1]),"=&{s[70:71]}"(p_[2]),"=&{s[72:73]}"(p_[3]),"=&{s[74:75]}"(p_[4]),"=&{s[76:77]}"(p_[5]),"=&{s[78:79]}"(p_[6])
    : [ka]"s"(__builtin_amdgcn_kernarg_segment_ptr()),[wg]"s"(BID),[t]"v"(TID),[r2]"i"(O_R2),[sl]"i"(SLSZ),[tb]"i"(O_CH+O_TB),[ex]"i"(O_EXP),[de]"i"(O_DE),[ust]"i"(GST_)
    : "memory","scc","m0","v127","s50","s51","s52","s53","s54","s55","s56","s57","s58","s59","s60","s61","s62","s63","s68","s69",
      "s80","s81","s82","s83","s84","s85","s86","s87","s88","s89","s90","s91","s92","s93","s94","s95","s99");
  SCHED();
  #define GP(T,i) ((T*)(GAS T*)p_[i])
  const u16 *x=GP(const u16,0), *Dsk=GP(const u16,2), *wg=GP(const u16,3), *wu=GP(const u16,4), *wd=GP(const u16,5); char* ws=GP(char,1); u16* out=GP(u16,6);
  int wv=RFL(TID>>6), bid=BID, q4=wv>>2, w4=wv&3;
  int xg=bid&7, lb=bid>>3, tb=32*xg+lb, nch=2+(lb<8);
  u64 ep; u32 ldw;
  {
  }
  #define KILLIN "=v"(xa),"=v"(xb_),"=v"(xc),"=v"(wa),"=v"(wb),"=v"(wc),"=v"(dq[0]),"=v"(dq[1]),"=v"(dq[2])
  if(__builtin_expect(q4==3&&lb>=8,0)){
    int fu=18*xg+lb-14, kq=2*xg+(lb>=11), cgp=lb-8-3*(lb>=11);
  u32 wst= lb>=14? F_*64:D_*64;
  u32 wso=(lb>=14? (fu>>1)*128 : kq*(288*D_*2)+384*cgp)+(lb>=14? PRE1:PRE2)*wst, wld=(u32)(size_t)sm+O_R2+w4*1024+(lb>=14? PRE1:PRE2)*4096; u32x4 wrs=mkrs(lb>=14? ((fu&1)? wu:wg):wd,D_*F_*2);
  GETEP
  u64 tag=ep+1ull;
  #define FLANE(RS) LANE(ln); int g=ln>>4, li=ln&15, qq=li>>2, pq=li&3; u32 wvo=(u32)(8*w4+(ln>>3))*(RS)+(u32)((ln&7)^(((ln>>3)&2)|((w4&1)<<2)))*16u; \
    const lch* tp=sm+O_R2+(8*g+qq)*128+((w4^((qq>>1)|((g&1)<<1)))*32)+pq*8;
  #define WFR(img,ks) trfrag(tp+(img)*CHSZ+(ks)*4096,tp+(img)*CHSZ+(ks)*4096+512)
  const u32x4 I4={INV,INV,INV,INV};
  if(lb>=14){
    FLANE(F_*2)
    int mt=fu&1;
    const lch* xp=sm+O_TILE+li*HSTR+g*16; int of=(mt*288+(fu>>1)*4+w4)*256+(li&7)*32+g*8+(li>>3)*4;
    const lch* mp=sm+O_RC+(li&7)*4;
    asm volatile("" : "+v"(xp),"+v"(of),"+v"(mp));
    rsrc_t rsG=RSRC(ws+W_GU,2*8*F_*2);
    if(w4==0) BST128(I4,rsG,(mt*288+(fu>>1)*4)*256+ln*16,0,SC1);
    bool slow=(((u32)wrs[0]+wso)&0x380u)==0x180u;
    if(slow){ wso-=(PRE1-PRE3)*wst; wld-=(PRE1-PRE3)*4096; FILL(5+PRE1-PRE3,VW(W3)); } else FILLD(5,W1,W1B,"");
    if(NA){ VMW(11-NA); garrive(sm,C_P); }
    if(w4==0){ VMW(5); if(ln==0) GST(ws+W_FL+(576+fu)*8,tag); }
    if(slow) FILLD(13-PRE1,W3,W1B,STKB); else FILLD(13-PRE1,W1,W1B,STKB);
    bf16x8 fB[18]; f32x4 cs={0,0,0,0}; const bf16x8 on=BC(bf16x8,(u32x4{0x3f803f80u,0x3f803f80u,0x3f803f80u,0x3f803f80u}));
    const lch* tq=tp+CHSZ; PIN(tq);
    #define RDF(a,b) _Pragma("unroll") for(int ks=a;ks<b;ks++) fB[ks]= ks<9? WFR(0,ks) : trfrag(tq+(ks-9)*4096,tq+(ks-9)*4096+512);
    #define CSF(a,b) SCHED(); _Pragma("unroll") for(int ks=a;ks<b;ks++) cs=__builtin_amdgcn_mfma_f32_16x16x32_bf16(fB[ks],on,cs,0,0,0); SCHED();
    gwait<0>(sm,C_P,4); RDF(0,NA)
    VMW(18-NB); garrive(sm,C_P); CSF(0,NA) gwait<0>(sm,C_P,8); RDF(NA,NB)
    VMW(LAST1); garrive(sm,C_P); CSF(NA,NB) gwait<0>(sm,C_P,12); RDF(NB,18-LAST1)
    DRAIN(); garrive(sm,C_P); CSF(NB,18-LAST1) gwait<0>(sm,C_P,16);
    float cA,cB,ri,ce,p0,p1,q0,q1; u32 mdw;
    { bf16x8 xb[9];
      RDF(18-LAST1,18) SCHED();
      u32 cx=*(const volatile LAS u32*)(sm+C_X*4);
      #pragma unroll
      for(int ks=0;ks<4;ks++) xb[ks]=*(const volatile LAS bf16x8*)(xp+ks*64);
      CSF(18-LAST1,18)
      if(RFL(cx)<4u){ gwait<XS1>(sm,C_X,XG1); gwait4<64>(C_X,4,xp,xb[0],xb[1],xb[2],xb[3]); }
      mdw=LD(u32,mp); xb[4]=LD(bf16x8,xp+256); xb[5]=LD(bf16x8,xp+320);
      SCHED();
      #pragma unroll
      for(int ks=0;ks<8;ks++){
        if(ks==0) asm volatile("v_mfma_f32_16x16x32_bf16 v[112:115], %0, %2, 0\n\tv_mfma_f32_16x16x32_bf16 v[116:119], %1, %2, 0" :: "v"(fB[0]),"v"(fB[9]),"v"(xb[0]));
        else asm volatile("v_mfma_f32_16x16x32_bf16 v[112:115], %0, %2, v[112:115]\n\tv_mfma_f32_16x16x32_bf16 v[116:119], %1, %2, v[116:119]" :: "v"(fB[ks]),"v"(fB[ks+9]),"v"(xb[ks]));
        SCHED();
        if(ks<3) xb[ks+6]=LD(bf16x8,xp+(ks+6)*64);
        if(ks==2){ PIN(cs); cA= li<8? cs[0]:cs[2]; cB= li<8? cs[1]:cs[3]; PIN(cA); PIN(cB); }
        if(ks==3){ PIN(mdw); ri=rcpf_(bhi(mdw)); float nm=-blo(mdw); cA*=nm; cB*=nm; PIN(cA); PIN(cB); } if(ks==4){ ce=ri*-1.4426950408889634f; p0=cA*ri; p1=cB*ri; PIN(p0); PIN(p1); } if(ks==5){ q0=cA*ce; q1=cB*ce; PIN(q0); PIN(q1); }
        SCHED(); }
      #define RR8 " row_ror:8 row_mask:0xf bank_mask:"
      float v0,v1,s0,s1;
      asm volatile("v_mfma_f32_16x16x32_bf16 v[112:115], %4, %6, v[112:115]\n\tv_mfma_f32_16x16x32_bf16 v[116:119], %5, %6, v[116:119]\n\ts_nop 7\n\tv_add_f32_dpp %0, v116, v112" RR8 "0x3\n\tv_add_f32_dpp %1, v117, v113" RR8 "0x3\n\tv_add_f32_dpp %0, v114, v118" RR8 "0xc\n\tv_add_f32_dpp %1, v115, v119" RR8 "0xc\n\t"
        "v_fmac_f32 %2, %0, %8\n\tv_fmac_f32 %3, %1, %8\n\tv_fma_f32 %0, %0, %7, %9\n\tv_fma_f32 %1, %1, %7, %10"
        : "=&v"(v0),"=&v"(v1),"+v"(q0),"+v"(q1) : "v"(fB[8]),"v"(fB[17]),"v"(xb[8]),"v"(ri),"v"(ce),"v"(p0),"v"(p1));
      s0=q0; s1=q1;
      if(mt==0){ v0*=rcpf_(1.f+__builtin_amdgcn_exp2f(s0)); v1*=rcpf_(1.f+__builtin_amdgcn_exp2f(s1)); }
      __builtin_amdgcn_raw_buffer_store_b32(nv(pk(v0,v1)),rsG,of,0,SC1); garrive(sm,C_H); }
    if(mt==0&&w4==0){
    LANE(l2); ln=l2; li=ln&15; g=ln>>4;
    int c9=(lb-14)>>1, tk=(xg&6)+((li>>2)&1), t=(int)LD(u16,sm+O_IDX+tk*2), c=64*c9+32*(xg&1)+16*(li>>3)+4*(li&3)+g; f32x4 pr; u32x4 ysl;
    { u32 u=LD(u16,sm+O_IDX+16+tk*2)^0x8000u, xb=LD(u16,sm+O_TILE+XAD(tk,c*2)), sv=LD(u32,sm+O_RC+tk*4); float ds=b2f(((const GAS u16*)Dsk)[c]); u^=(u32)(((int)(u<<16))>>31)&0x7fffu;
      int ye=(int)(u32)poll1<32>(ws+W_FL+c*8,tag<<32)+175-32*(int)ycls((u32)tag,t&7); ye= ye<0? 0 : ye>254? 254:ye;
      #pragma unroll
      for(int k=0;k<4;k++) ysl[k]= ((t>>1)&3)==k? (u32)ye<<(7+(t&1)*16) : 0u;
      poll1<0>(ws+W_FL+(720+(ln<48? ln:47))*8,tag);
      float xn=(b2f(xb)-blo(sv))*rcpf_(bhi(sv)); pr=f32x4{b2f(xb),xn,xn*ds,sigm_f(b2f(u))}; }
    gwait<2>(sm,C_PR,1);
    { u32x4 rs=mkrs(ws+W_PART+(xg*9+c9)*4096,4096), ry=mkrs(ws+W_Y,D_*YST);
      int yo=c*YST+(t&~7)*2, y2=yo^16, po=ln*16; u32 a[4], t0,cnt; u32x4 yb; GAS u16* op=(GAS u16*)out+t*D_+c; PIN(ysl); f32x2 pe={pr[1]*-1.4426950408889634f,pr[3]*-1.4426950408889634f};
      #define LD_P "buffer_load_dwordx4 v[112:115], %[of], %[rs], 0 offen sc1\n\tbuffer_load_dwordx4 v[116:119], %[of], %[rs], 0 offen offset:1024 sc1\n\tbuffer_load_dwordx4 v[120:123], %[of], %[rs], 0 offen offset:2048 sc1\n\tbuffer_load_dwordx4 v[124:127], %[of], %[rs], 0 offen offset:3072 sc1\n\t"
      #define LD_YB "buffer_load_dwordx4 v[96:99], %[yo], %[ry], 0 offen sc1\n\tbuffer_load_dwordx4 v[100:103], %[y2], %[ry], 0 offen sc1\n\t"
      #define CK_YB UC("%[t0]",96,100,c0) UC("%[a0]",97,101,c1) UC("%[a1]",98,102,c2) UC("%[a2]",99,103,c3) "v_or3_b32 %[t0], %[t0], %[a0], %[a1]\n\tv_bitop3_b32 %[t0], %[t0], %[a2], %[mk] bitop3:0xa8\n\tv_cmp_ne_u32 %[ym], 0, %[t0]\n\t"
      #define AD4(o,a,b,c,d) "v_add_f32 %[t0], v" #a ", v" #b "\n\tv_add_f32 %[" #o "], v" #c ", v" #d "\n\tv_add_f32 %[" #o "], %[t0], %[" #o "]\n\t"
      u64 ym;
      #define PGAP "s_sleep " STR(PSL) "\n\t"
      u64 nm;
      asm volatile(LD_YB PGAP LD_P "s_mov_b32 %[cnt], 0x20000\n\ts_waitcnt vmcnt(4)\n1:\n\t" CK_YB "s_waitcnt vmcnt(0)\n\t" AD4(a0,112,116,120,124) AD4(a1,113,117,121,125) AD4(a2,114,118,122,126) "v_cmp_u_f32 %[nm], %[a0], %[a1]\n\t" AD4(a3,115,119,123,127)
        "s_or_b64 %[nm], %[nm], %[ym]\n\tv_cmp_u_f32 vcc, %[a2], %[a3]\n\ts_or_b64 vcc, vcc, %[nm]\n\ts_cbranch_scc0 9f\n\t"
        CHK_I0 MX2(116,117) MX2(118,119) MX2(120,121) MX2(122,123) MX2(124,125) MX2(126,127) CHK_IE
        "s_or_b64 exec, vcc, %[ym]\n\ts_cbranch_scc0 8f\n\ts_sleep " STR(QSLP) "\n\ts_mov_b64 exec, %[ym]\n\ts_cbranch_execz 2f\n\t" LD_YB "2:\n\ts_mov_b64 exec, vcc\n\ts_cbranch_execz 3f\n\t" LD_P
        "s_mov_b64 exec, -1\n\ts_waitcnt vmcnt(4)\n\ts_branch 4f\n3:\n\ts_mov_b64 exec, -1\n\ts_waitcnt vmcnt(0)\n4:\n\ts_sub_u32 %[cnt], %[cnt], 1\n\ts_cbranch_scc0 1b\n8:\n\ts_mov_b64 exec, -1\n9:"
        : [t0]"=&v"(t0),[cnt]"=&s"(cnt),[a0]"=&v"(a[0]),[a1]"=&v"(a[1]),[a2]"=&v"(a[2]),[a3]"=&v"(a[3]),"=&{v[96:99]}"(yb),[ym]"=&s"(ym),[nm]"=&s"(nm) : [of]"v"(po),[rs]"s"(rs),[yo]"v"(yo),[y2]"v"(y2),[ry]"s"(ry),"v"(op),"v"(pr),"v"(pe),
          [c0]"s"(ypat((u32)tag,0)),[c1]"s"(ypat((u32)tag,1)),[c2]"s"(ypat((u32)tag,2)),[c3]"s"(ypat((u32)tag,3)),[mk]"s"(YMASK) : PCLOB,"v100","v101","v102","v103");
      auto s0=__builtin_amdgcn_permlane32_swap(a[0],a[2],false,false); auto s1=__builtin_amdgcn_permlane32_swap(a[1],a[3],false,false);
      u32 x0=s0[0],x1=s0[1],y0=s1[0],y1=s1[1]; u32 p0=BC(u32,BC(float,x0)+BC(float,x1)), p1=BC(u32,BC(float,y0)+BC(float,y1));
      auto s2=__builtin_amdgcn_permlane16_swap(p0,p1,false,false); u32 z0=s2[0], z1=s2[1]; float s=BC(float,z0)+BC(float,z1);
      float gq=__builtin_fmaf(s,pr[3],pr[1])*rcpf_(1.f+__builtin_amdgcn_exp2f(__builtin_fmaf(s,pe[1],pe[0])));
      float sy=pr[2];
      #pragma unroll
      for(int k=0;k<4;k++){ u32 w=yb[k], q=ysl[k]; sy=D2(w,q,sy); }
      float o=__builtin_fmaf(sy,gq,pr[0]);
      *op=(u16)(pk(o,o)&0xffffu);
      if(bid==112&&ln==0) GST(ws+W_EPOCH,tag); }
    }
  } else
  {
    FLANE(D_*2)
    bf16x8 fr[27];
    rsrc_t rsP=RSRC(ws+W_PART+cgp*12288+kq*256+PWB,8*9*4096-2*12288);
    #pragma unroll
    for(int i=0;i<3;i++) BST128(I4,rsP,li<8? PLO+g*16+i*4096 : OOR,0,SC1);
    if(PRE2<9) FILLD(9-PRE2,W2_,W2B,"");
    wso+=128u-9u*wst;
    FILLD(9,W2_,W2B,"");
    VMW(9); gsync(sm,C_P,4);
    #pragma unroll
    for(int ks=0;ks<9;ks++) fr[ks]=WFR(0,ks);
    gsync(sm,C_P,8);
    if(w4==0&&ln==0) GST(ws+W_FL+(720+6*xg+lb-8)*8,tag);
    wso+=128u-9u*wst; wld-=2*CHSZ;
    FILL(9,VW(W2B));
    int fo=W_FL+(576+((9*kq)&~1)+(ln<9? ln:9))*8;
    if(w4==0) asm volatile("global_load_dwordx2 v[112:113], %0, %1 sc1\n\ts_waitcnt vmcnt(10)" :: "v"(fo),"s"(ws) : "memory"); else VMW(9);
    gsync(sm,C_P,12);
    #pragma unroll
    for(int ks=0;ks<9;ks++) fr[9+ks]=WFR(1,ks);
    { int t2=TID; asm volatile("" : "+v"(t2)); ln=t2&63; li=ln&15; g=ln>>4; }
    const lch* hp=sm+O_TILE+(li&7)*HT+g*16; int po= li<8? PLO+g*16+4096 : OOR; PIN(hp); PIN(po);
    u32x4 rs=mkrs(ws+W_GU,2*8*F_*2);
    int of=((ln>>5)*288+18*kq+2*w4)*256+(ln&31)*16; lch* hd=sm+O_TILE+w4*(8*HT)+((ln>>1)&7)*HT+((ln>>4)&1)*32+(ln&1)*16+(ln>>5)*8; PIN(of); PIN(hd); asm volatile("" : "+s"(rs));
    u32x4 rp=mkrs(ws+W_PART+cgp*12288+kq*256+PWB-4096,8*9*4096); u32 ch=C_H*4, one=1; int o3= w4==0? of+4096:OOR; PIN(ch); PIN(one); PIN(o3); asm volatile("" : "+s"(rp));
    DRAIN();
    if(w4==0){ u64 bad; asm volatile("v_cmp_ne_u64 %0, %1, v[112:113]" : "=s"(bad) : "s"(tag) : "memory");
      if(bad!=0ull) poll1<0>(ws+fo,tag); }
    __builtin_amdgcn_s_waitcnt(0x0f70);
    gsync(sm,C_P,16);
    { u32 t0,t1,cnt;
      #define LD_G "buffer_load_dwordx4 v[112:115], %[of], %[rs], 0 offen sc1\n\tbuffer_load_dwordx4 v[116:119], %[of], %[rs], 0 offen offset:2048 sc1\n\tbuffer_load_dwordx4 v[120:123], %[o3], %[rs], 0 offen sc1\n\t"
      #define HM1(a,b) "v_and_b32 %[t0], %[lm], v" #b "\n\tv_and_b32 v" #b ", %[hm], v" #b "\n\tv_dot2_f32_bf16 %[t0], v" #a ", %[t0], 0\n\tv_dot2_f32_bf16 v" #a ", v" #a ", v" #b ", 0\n\t"
      #define HM2(a,b) "v_and_b32 %[t1], %[lm], v" #b "\n\tv_and_b32 v" #b ", %[hm], v" #b "\n\tv_dot2_f32_bf16 %[t1], v" #a ", %[t1], 0\n\tv_dot2_f32_bf16 v" #a ", v" #a ", v" #b ", 0\n\t"
      #define HMUL(a,b,c,d,o) "v_permlane32_swap_b32 v" #a ", v" #c "\n\tv_permlane32_swap_b32 v" #b ", v" #d "\n\t" HM1(a,c) HM2(b,d) "v_cvt_pk_bf16_f32 v" #a ", %[t0], v" #a "\n\ts_nop 1\n\tv_cvt_pk_bf16_f32 v" #b ", %[t1], v" #b "\n\tds_write_b64 %[hd], v[" #a ":" #b "] offset:" STR(o) "\n\t"
      if(GD) __builtin_amdgcn_s_sleep(GD);
      asm volatile(LD_G :: [of]"v"(of),[rs]"s"(rs),[o3]"v"(o3) : "memory");
      #pragma unroll
      for(int ks=0;ks<G2;ks++) fr[18+ks]=WFR(0,ks);
      #pragma unroll
      for(int ks=0;ks<G2;ks++) PIN(fr[18+ks]);
      asm volatile(POLLASM(LD_G,CHK_I0 MX2(116,117) MX2(118,119) MX2(120,121) MX2(122,123) CHK_IE,GSLP)
        HMUL(112,113,114,115,0) HMUL(116,117,118,119,32*HT) "s_cmp_lg_u32 %[w4], 0\n\ts_cbranch_scc1 8f\n\t" HMUL(120,121,122,123,64*HT) "8:\n\ts_mov_b64 exec, 1\n\tds_add_u32 %[ch], %[one]\n\ts_mov_b64 exec, -1\n\t"
        "s_mov_b32 %[cnt], 0x20000\n6:\n\tds_read_b32 %[t0], %[ch]\n\tds_read_b128 v[112:115], %[hp]\n\tds_read_b128 v[116:119], %[hp] offset:8*" STR(HT) "\n\tds_read_b128 v[120:123], %[hp] offset:16*" STR(HT) "\n\tds_read_b128 v[124:127], %[hp] offset:24*" STR(HT) "\n\ts_waitcnt lgkmcnt(4)\n\t"
        "v_cmp_gt_u32 vcc, 4, %[t0]\n\ts_cbranch_vccz 7f\n\ts_sleep 1\n\ts_sub_u32 %[cnt], %[cnt], 1\n\ts_cbranch_scc0 6b\n7:"
        : [t0]"=&v"(t0),[t1]"=&v"(t1),[cnt]"=&s"(cnt) : [of]"v"(of),[rs]"s"(rs),[o3]"v"(o3),[w4]"s"(w4),[hd]"v"(hd),[hm]"s"(0xffff0000u),[lm]"s"(0xffffu),[ch]"v"(ch),[one]"v"(one),[hp]"v"(hp),"v"(po) : PCLOB); }
    { bf16x8 hb[9];
      #define MF "v_mfma_f32_16x16x32_bf16 "
      #define MF3(h) MF "%0, %3, " h ", %0\n\t" MF "%1, %4, " h ", %1\n\t" MF "%2, %5, " h ", %2"
      #define ACC "+v"(fr[0]),"+v"(fr[9]),"+v"(fr[18])
      #define FRS(k) "v"(fr[k]),"v"(fr[9+(k)]),"v"(fr[18+(k)])
      asm volatile("s_waitcnt lgkmcnt(3)\n\t" MF "%0, %0, v[112:115], 0\n\t" MF "%1, %1, v[112:115], 0\n\t" MF "%2, %2, v[112:115], 0\n\ts_waitcnt lgkmcnt(0)\n\t" MF3("v[116:119]") : ACC : FRS(1));
      hb[4]=LD(bf16x8,hp+4*8*HT); hb[5]=LD(bf16x8,hp+5*8*HT); hb[6]=LD(bf16x8,hp+6*8*HT); SCHED();
      asm volatile(MF3("v[120:123]") : ACC : FRS(2));
      #pragma unroll
      for(int ks=G2;ks<9;ks++) fr[18+ks]=WFR(0,ks);
      SCHED();
      asm volatile(MF3("v[124:127]") : ACC : FRS(3));
      hb[7]=LD(bf16x8,hp+7*8*HT); hb[8]=LD(bf16x8,hp+8*8*HT); SCHED();
      #pragma unroll
      for(int ks=4;ks<8;ks++){ asm volatile(MF3("%6") : ACC : FRS(ks),"v"(hb[ks])); SCHED(); }
      asm volatile(MF3("%6") "\n\ts_nop 5\n\tbuffer_store_dwordx4 %0, %7, %8, 0 offen sc1\n\tbuffer_store_dwordx4 %1, %7, %8, %9 offen sc1\n\tbuffer_store_dwordx4 %2, %7, %8, %10 offen sc1\n\ts_mov_b64 exec, 1\n\tds_add_u32 %11, %12\n\ts_mov_b64 exec, -1"
        : ACC : FRS(8),"v"(hb[8]),"v"(po),"s"(rp),"s"(4096),"s"(8192),"v"(ch),"v"(one) : "memory"); }

  }
  asm volatile("s_endpgm" : KILLIN :: "memory"); }
  {
  if(__builtin_expect(q4!=0,0)){ int grp= q4==1? nch-1 : q4-2;
    chan(sm,grp,(w4+(0x48>>(2*q4)))&3,72*xg+(q4==3? 16+lb : 2*lb+(lb<8? 0:8)+2-q4),(q4+1)>>2,ws,xa,xb_,wa,wb,dq[0]);
    asm volatile("s_endpgm" : KILLIN :: "memory"); }
  int xr_,xr2_;
  {
    LANE(ln); int g=ln>>4, li=ln&15, qq=li>>2, pq=li&3, j=ln&31, rl=2*w4+(ln>>5);
    lch* tile=sm+O_TILE; u32 Tr=(ln>>1)&16;
    int uo=W_U+(tb>>TL)*GST_+(tb&((1<<TL)-1))*(128>>TL)+(w4*1024<<TL)+(ln>>(3-TL))*128+(ln&(7>>TL))*16, uo1=uo+(4096<<TL), uo2=uo+(8192<<TL); PIN(uo); PIN(uo1); PIN(uo2);
    lch* tw=tile+rl*TSTR2+j*16; lch* tw2=tw+1024-j*12; u32 one=0x3f803f80u; const lch* tp=tile+qq*TSTR2+(w4*64+g*16+pq*4)*2;
    int go= ln<32? W_RG+(ln>>2)*8192+(tb*4+(ln&3))*8 : W_ST+(tb*8+(ln&7))*8; lch* kw=sm+O_RV+rl*2; const lch* kr=sm+(ln<32? O_RV+(ln&3)*4 : O_MD+(ln&7)*4); PIN(kw); PIN(kr);
    int xr=(32*(tb>>2)+8*g+(li&7))*(D_*2)+288*(lb&3)+32*w4+(ln&8)*2, xr2= w4==0? xr+256:OOR;
    rsrc_t rx=RSRC(x,L_*D_*2); u64 gm=((1ull<<(4*NRG))-1ull)|0xff00000000ull; u32 um=~UMASK, km=0x80008000u; asm volatile("" : "+s"(gm),"+s"(um),"+s"(km),"+s"(ws));
    float s1=0.f, s2=0.f, s3=0.f, sw=0.f, i576=1.f/576.f; u32 hm=0xffff0000u, zero=0, c1=1; u64 wm=0x0001000000010000ull; lch* kw2=sm+O_MD+rl*4;
    asm volatile("" : "+v"(s1),"+v"(s2),"+v"(s3),"+v"(sw),"+v"(zero),"+v"(c1),"+v"(kw2),"+s"(wm),"+s"(hm),"+s"(i576));
    asm volatile("s_waitcnt vmcnt(3)" : "+v"(wa),"+v"(wb),"+v"(wc),"+v"(Tr),"+v"(tw),"+v"(tw2),"+v"(tp),"+v"(one),"+v"(go),"+v"(xr),"+v"(xr2) :: "memory");
    u32 wq[9]={wa[0],wa[1],wa[2],wa[3],wb[0],wb[1],wb[2],wb[3],wc};
    #pragma unroll
    for(int i=0;i<9;i++) sw=D2(wq[i],one,sw);
    SCHED();
    asm volatile("s_waitcnt vmcnt(0)" : "+v"(xa),"+v"(xb_),"+v"(xc),"+v"(sw) :: "memory");
    u32 v[9]={xa[0],xa[1],xa[2],xa[3],xb_[0],xb_[1],xb_[2],xb_[3],xc};
    { float xf[18];
      #pragma unroll
      for(int i=0;i<9;i++){ s1=D2(v[i],one,s1); SCHED(); s2=D2(v[i],v[i],s2); SCHED(); s3=D2(v[i],wq[i],s3); SCHED(); }
      float m1,m2;
      #define AD_(r,c) "v_add_f32_dpp %[" #r "], %[" #r "], %[" #r "] " c " bank_mask:0xf\n\t"
      #define UL_(i) "v_lshlrev_b32 %[f" #i "], 16, %[v" #i "]\n\t"
      #define RM " row_mask:0xf"
      asm volatile(UL_(0) UL_(1) "v_permlane16_swap_b32 %[a], %[b]\n\tv_permlane16_swap_b32 %[c], %[w]\n\tv_add_f32 %[a], %[a], %[b]\n\tv_add_f32 %[w], %[w], %[c]\n\t" UL_(2) AD_(a,"quad_perm:[1,0,3,2]" RM) AD_(w,"quad_perm:[1,0,3,2]" RM) UL_(3)
        AD_(a,"quad_perm:[2,3,0,1]" RM) AD_(w,"quad_perm:[2,3,0,1]" RM) UL_(4) AD_(a,"row_half_mirror" RM) AD_(w,"row_half_mirror" RM) UL_(5) AD_(a,"row_mirror" RM) AD_(w,"row_mirror" RM)
        "v_mul_f32 %[d], %[k], %[a]\n\tv_mul_f32 %[e], %[k], %[a]\n\t" "v_mov_b32_dpp %[c], %[w] row_bcast:15 row_mask:0xa bank_mask:0xf\n\t" UL_(6) "v_permlane16_swap_b32 %[d], %[e]\n\t" UL_(7)
        : [a]"+v"(s1),[b]"+v"(s2),[c]"+v"(s3),[w]"+v"(sw),[d]"=&v"(m1),[e]"=&v"(m2),[f0]"=&v"(xf[0]),[f1]"=&v"(xf[2]),[f2]"=&v"(xf[4]),[f3]"=&v"(xf[6]),[f4]"=&v"(xf[8]),[f5]"=&v"(xf[10]),[f6]"=&v"(xf[12]),[f7]"=&v"(xf[14])
        : [v0]"v"(v[0]),[v1]"v"(v[1]),[v2]"v"(v[2]),[v3]"v"(v[3]),[v4]"v"(v[4]),[v5]"v"(v[5]),[v6]"v"(v[6]),[v7]"v"(v[7]),[k]"s"(i576));
      s1=m1; float mm=s1*s1, var=m2-mm;
      if(__builtin_expect(BALLOT(!(mm<=64.f*var))!=0ull,0)){
        float q=0.f;
        #pragma unroll
        for(int i=0;i<9;i++){ u32 t=v[i]; PIN(t); float d0=blo(t)-s1, d1=bhi(t)-s1; q=__builtin_fmaf(d0,d0,q); q=__builtin_fmaf(d1,d1,q); }
        var=add16x(sum16(q))*(1.f/576.f); }
      u32 vb=pk(var,s1);
      float den=blo(vb)+1e-5f, m=BC(float,vb&hm);
      vb=pk(den,den); den=__builtin_amdgcn_sqrtf(BC(float,vb&hm)); SCHED();
      s3=__builtin_fmaf(-m,sw,s3); PIN(s3); SCHED();
      u32 md=pk(m,den);
      float ri=rcpf_(BC(float,md&hm)); SCHED(); xf[16]=blo(v[8]); PIN(xf[16]); SCHED();
      s3*=ri;
      u32 u=pk(s3,s3);
      { u32 sg=(u32)((int)u>>31); u^=sg|km; }
      asm volatile("s_waitcnt lgkmcnt(0)\n\ts_mov_b64 exec, %6\n\tds_write_b16 %0, %1\n\tds_write_b32 %2, %3\n\ts_mov_b32 exec_hi, 0\n\tds_add_u32 %4, %5 offset:%7\n\ts_mov_b64 exec, -1\n\t"
        "s_cmp_lg_u32 %8, 3\n\ts_cbranch_scc1 1f\n\tds_read_b32 v117, %4 offset:%7\n\tds_read_b32 v118, %9\n1:" :: "v"(kw),"v"(u),"v"(kw2),"v"(md),"v"(zero),"v"(c1),"s"(wm),"i"(C_I*4),"s"(w4),"v"(kr) : "memory","scc");
      SCHED();
      { asm volatile("s_mov_b64 %0, s[96:97]" : "=s"(ep)); u32 tg=(u32)ep+1u; Tr=((upat(tg,w4)>>Tr)&0xffffu)*0x10001u; asm volatile("v_mov_b32 v119, %1" : "+v"(Tr) : "s"(tg)); }
      float rs_=ri*0.0625f, cs_=-m*rs_;
      #define UA_(i) "v_and_b32 %" #i ", %11, %" #i "\n\t"
      asm volatile(UA_(0) UA_(1) UA_(2) UA_(3) UA_(4) UA_(5) UA_(6) UA_(7) UA_(8) : "+v"(v[0]),"+v"(v[1]),"+v"(v[2]),"+v"(v[3]),"+v"(v[4]),"+v"(v[5]),"+v"(v[6]),"+v"(v[7]),"+v"(v[8]),"+v"(rs_),"+v"(cs_) : "s"(hm));
      #pragma unroll
      for(int i=0;i<9;i++) xf[2*i+1]=BC(float,v[i]);
      u32 h[9];
      #define UH(i) h[i]=(pkz(__builtin_fmaf(xf[2*(i)],rs_,cs_),__builtin_fmaf(xf[2*(i)+1],rs_,cs_))&um)|Tr;
      SCHED();
      { u32 cnt;
        asm volatile("s_cmp_lg_u32 %[w4], 3\n\ts_cbranch_scc1 3f\n\ts_mov_b32 %[cnt], 0x20000\n1:\n\ts_waitcnt lgkmcnt(0)\n\tv_cmp_gt_u32 vcc, 4, v117\n\ts_cbranch_vccz 2f\n\tds_read_b32 v117, %[z] offset:%[ci]\n\tds_read_b32 v118, %[kr]\n\ts_sub_u32 %[cnt], %[cnt], 1\n\ts_cbranch_scc0 1b\n"
          "2:\n\ts_mov_b64 exec, %[gm]\n\tglobal_store_dwordx2 %[go], v[118:119], %[ws] sc1\n\ts_mov_b64 exec, -1\n3:" : [cnt]"=&s"(cnt) : [w4]"s"(w4),[z]"v"(zero),[ci]"i"(C_I*4),[kr]"v"(kr),[gm]"s"(gm),[go]"v"(go),[ws]"s"(ws) : PCLOB); }
      SCHED();
      UH(0) UH(1) UH(2) UH(3) ST(u32x4,tw)=u32x4{h[0],h[1],h[2],h[3]}; UH(4) UH(5) UH(6) UH(7) ST(u32x4,tw+512)=u32x4{h[4],h[5],h[6],h[7]};
      UH(8) ST(u32,tw2)=h[8]; }
    { u32 tv,cnt;
      asm volatile("s_mov_b64 exec, 1\n\tds_add_u32 %[z], %[c1] offset:%[cl]\n\ts_mov_b64 exec, -1\n\ts_mov_b32 %[cnt], 0x20000\n1:\n\tds_read_b32 %[tv], %[z] offset:%[cl]\n\t"
        "ds_read_b64_tr_b16 v[96:97], %[tp]\n\tds_read_b64_tr_b16 v[98:99], %[tp] offset:4*" STR(TSTR2) "\n\tds_read_b64_tr_b16 v[100:101], %[tp] offset:512\n\tds_read_b64_tr_b16 v[102:103], %[tp] offset:512+4*" STR(TSTR2) "\n\t"
        "ds_read_b64_tr_b16 v[104:105], %[tp] offset:1024\n\tds_read_b64_tr_b16 v[106:107], %[tp] offset:1024+4*" STR(TSTR2) "\n\ts_waitcnt lgkmcnt(6)\n\tv_cmp_gt_u32 vcc, 4, %[tv]\n\ts_cbranch_vccz 2f\n\ts_sub_u32 %[cnt], %[cnt], 1\n\ts_cbranch_scc0 1b\n"
        "2:\n\ts_waitcnt lgkmcnt(4)\n\tglobal_store_dwordx4 %[u0], v[96:99], %[ws] sc1\n\ts_waitcnt lgkmcnt(2)\n\tglobal_store_dwordx4 %[u1], v[100:103], %[ws] sc1\n\ts_waitcnt lgkmcnt(0)\n\t"
        "s_cmp_lg_u32 %[w4], 0\n\ts_cbranch_scc1 3f\n\tglobal_store_dwordx4 %[u2], v[104:107], %[ws] sc1\n3:\n\ts_nop 1"
        : [tv]"=&v"(tv),[cnt]"=&s"(cnt) : [z]"v"(zero),[c1]"v"(c1),[cl]"i"(C_L*4),[tp]"v"(tp),[u0]"v"(uo),[u1]"v"(uo1),[u2]"v"(uo2),[ws]"s"(ws),[w4]"s"(w4)
        : PCLOB,"v96","v97","v98","v99","v100","v101","v102","v103","v104","v105","v106","v107");
      xr_=xr; xr2_=xr2;
    }
    if(lb>=2&&lb<8){
      const u16* wm=(lb&1)? wu:wg; int co=(3-(int)((u64)wm>>7)-xg)&7, wi=((lb>>1)-1)*4+w4, r_=576-12*PFN+wi+12*j;
      asm volatile("buffer_load_dword v120, %0, %1, 0 offen" :: "v"(j<PFN&&(ln<32||co==0)? r_*(F_*2)+(9*xg+co)*128+(ln>>5)*1024 : OOR),"s"(mkrs(wm,D_*F_*2)) : "memory"); }
    if(lb>=8&&lb<14) garrive(sm,C_D);
  }
  FP fp; int trow;
  { LANE(ln); int g=ln>>4, li=ln&15; u64 tag=ep+1ull;
    PRIO(0);
    FL fl; fl.so=W_ST+(32*(tb>>2)+8*g+(li&7))*8; fl.fo=W_FL+(144*(lb&3)+16*w4+64*(g<2||w4==0? (g<3? g:2):0)+li)*8; fl.xr=xr_; fl.xr2=xr2_; fl.ws=ws; fl.D=Dsk; fl.rx=mkrs(x,L_*D_*2); PIN(fl.so); PIN(fl.fo);
    final_adr(sm,fp,w4); trow=32*(tb>>2)+8*g+(li&7); PIN(trow);
    lch *tw=sm+O_CH+O_TB+16384+w4*(4*ECS)+ln*8, *et=tw-ln*8+ycls((u32)tag,li&7)*ECS+(ln&8)*8; PIN(tw); PIN(et);
    if(lb==CTLB||lb>=14){
      u32 M=(u32)tag, rank,key; u32x4 rx=fl.rx;
      int j=ln&31, rr=2*w4+(ln>>5), o16=2047*1152+j*16, o4=2047*1152+j*4, so=W_ST+2047*8, sh=(ln>>5)*16, m9=-1152, r0=2*w4, r1=2*w4+1;
      lch *xw=sm+O_TILE+XAD(rr,j*16), *xw1=sm+O_TILE+XAD(rr,j*16+512), *xw2=sm+O_TILE+XAD(rr,j*4+1024), *rw=sm+O_RC+rr*4; u32 zero=0, one=1;
      PIN(o16); PIN(o4); PIN(so); PIN(sh); PIN(xw); PIN(xw1); PIN(xw2); PIN(rw); PIN(zero); PIN(one); asm volatile("" : "+s"(rx),"+s"(m9),"+s"(r0),"+s"(r1));
      top8(sm,w4,ln,M,ws,rank,key);
      {
        u64 q; u32 a,b;
        asm volatile("v_cmp_eq_u32 vcc, %[r0], %[rk]\n\tv_cmp_eq_u32 %[q], %[r1], %[rk]\n\ts_ff1_i32_b64 %[a], vcc\n\ts_ff1_i32_b64 %[b], %[q]\n\tv_readlane_b32 %[a], %[key], %[a]\n\tv_readlane_b32 %[b], %[key], %[b]\n\ts_pack_ll_b32_b16 %[a], %[a], %[b]\n\t"
          "v_bfe_u32 %[sh], %[a], %[sh], 11\n\tv_mad_i32_i24 %[o16], %[sh], %[m9], %[o16]\n\tv_mad_i32_i24 %[o4], %[sh], %[m9], %[o4]\n\t"
          "buffer_load_dwordx4 v[112:115], %[o16], %[rx], 0 offen\n\tv_mad_i32_i24 %[so], %[sh], -8, %[so]\n\tbuffer_load_dwordx4 v[116:119], %[o16], %[rx], 0 offen offset:512\n\tbuffer_load_dword v120, %[o4], %[rx], 0 offen offset:1024\n\tglobal_load_dwordx2 v[122:123], %[so], %[ws] sc1\n\t"
          "s_mov_b64 exec, 1\n\tds_add_u32 %[z], %[one] offset:%[cd]\n\ts_mov_b64 exec, -1"
          : [q]"=&s"(q),[a]"=&s"(a),[b]"=&s"(b),[sh]"+v"(sh),[o16]"+v"(o16),[o4]"+v"(o4),[so]"+v"(so) : [r0]"s"(r0),[r1]"s"(r1),[rk]"v"(rank),[key]"v"(key),[m9]"s"(m9),[rx]"s"(rx),[ws]"s"(ws),[z]"v"(zero),[one]"v"(one),[cd]"i"(C_D*4) : PCLOB);
        if(lb==CTLB&&rank<8&&!(ln&1)) GST(ws+W_IDX+xg*1024+w4*128+rank*8,(tag<<16)|(~key&2047u));
        if(w4==0){ if(rank<8) ST(u16,sm+O_IDX+(ln&1)*16+rank*2)=(u16)((ln&1)? key>>16 : ~key&2047u); garrive(sm,C_IX); }
        u64 bad; asm volatile("s_waitcnt vmcnt(2)\n\tds_write_b128 %2, v[112:115]\n\ts_waitcnt vmcnt(0)\n\tds_write_b128 %3, v[116:119]\n\tv_cmp_ne_u32 %0, %1, v123\n\tds_write_b32 %4, v120" : "=s"(bad) : "s"(M),"v"(xw),"v"(xw1),"v"(xw2) : "memory");
        if(__builtin_expect(bad!=0ull,0)){ u32 f1=(u32)poll1<32>(ws+so,tag<<32); asm volatile("v_mov_b32 v122, %0" :: "v"(f1) : "memory"); }
        asm volatile("ds_write_b32 %0, v122\n\ts_mov_b64 exec, 1\n\tds_add_u32 %1, %2 offset:%3\n\ts_mov_b64 exec, -1" :: "v"(rw),"v"(zero),"v"(one),"i"(C_X*4) : "memory");
      }
    }
    PRIO(0);
    if(lb<14) gwait<8>(sm,C_U,8); else gwait<4>(sm,C_H,4);
    final_ld(fl); asm volatile("s_waitcnt vmcnt(0)" : "+v"(fl.fs),"+v"(fl.ff),"+v"(fl.dk),"+v"(fl.xq[0]),"+v"(fl.xq[1]),"+v"(fl.xq[2]) :: "memory");
    gwait<2>(sm,C_G,7);
    f32x4 rc;
    { u64 fs=fl.fs, ff=fl.ff;
      if(__builtin_expect(BALLOT((u32)(fs>>32)!=(u32)tag)!=0ull,0)) fs=poll1<32>(ws+fl.so,tag<<32);
      if(__builtin_expect(BALLOT((u32)(ff>>32)!=(u32)tag)!=0ull,0)) ff=poll1<32>(ws+fl.fo,tag<<32);
      int e0=79-(int)(u32)ff; e0= e0<1? 1 : e0>158? 158:e0; float is=BC(float,(u32)e0<<23), di=b2f(fl.dk)*is;
      #pragma unroll
      for(int c=0;c<4;c++){ ST(f32x2,tw+c*ECS)=f32x2{is,di}; is*=4294967296.f; di*=4294967296.f; }
      float ri=rcpf_(bhi((u32)fs)), nm=-blo((u32)fs)*ri; rc=f32x4{ri,nm,ri*-1.4426950408889634f,nm*-1.4426950408889634f}; }
    asm volatile("" : "+v"(rc) :: "memory");
    bool ix= w4==3&&lb<14&&lb!=CTLB; u64 f; const char* p=ws+W_IDX+(xg*8+(lb&3))*128+(ln&7)*8; if(ix) f=GLD(p);
    final_blk(fp,0,fl.xq,rc,et); final_blk(fp,1,fl.xq,rc,et); if(w4==0) final_blk(fp,2,fl.xq,rc,et);
    if(ix){ if(BALLOT(((f^(tag<<16))>>16)!=0ull)!=0ull) f=poll1<16>(p,tag<<16);
      if(ln<8) ST(u16,sm+O_IDX+ln*2)=(u16)f; garrive(sm,C_IX); }
  }
  final_post(sm,fp,w4,nch,xr_,trow,(u32)ep+1u,out,ws);
  }
}

extern "C" void kernel_launch(void* const* d_in, const int* in_sizes, int n_in, void* d_out, int out_size, void* d_ws, size_t ws_size, hipStream_t stream) {
  const u16** I=(const u16**)d_in;
  KP kp; kp.Are=I[1]; kp.Aim=I[2]; kp.Bre=I[3]; kp.Bim=I[4]; kp.Cre=I[5]; kp.Cim=I[6]; kp.ldt=I[7]; kp.pad=I[7];
  kA<<<256,1024,0,stream>>>(I[0],(char*)d_ws+WSOFF,I[10],I[8],I[11],I[12],I[13],(u16*)d_out,kp);
}
```

```cpp
#include <hip/hip_runtime.h>
#include <hip/hip_bf16.h>
#include <stdint.h>
#if defined(__HIP_DEVICE_COMPILE__)
#define NOPK __attribute__((target("no-packed-fp32-ops")))
#else
#define NOPK
#endif
#define DEV __device__ NOPK __forceinline__
typedef unsigned short u16;
typedef unsigned int u32;
typedef unsigned long long u64;
typedef short bf16x8 __attribute__((ext_vector_type(8)));
typedef _Float16 h8 __attribute__((ext_vector_type(8)));
typedef _Float16 h2 __attribute__((ext_vector_type(2)));
typedef float f32x4 __attribute__((ext_vector_type(4)));
typedef float f32x2 __attribute__((ext_vector_type(2)));
typedef u32 u32x4 __attribute__((ext_vector_type(4)));
typedef u32 u32x2 __attribute__((ext_vector_type(2)));
typedef __bf16 bf2 __attribute__((ext_vector_type(2)));
typedef short v4s __attribute__((ext_vector_type(4)));
typedef __amdgpu_buffer_rsrc_t rsrc_t;
#define GAS __attribute__((address_space(1)))
#define LAS __attribute__((address_space(3)))
typedef LAS char lch;
#define L_ 2048
#define D_ 576
#define F_ 4608
#define BC(T,v) __builtin_bit_cast(T,v)
#define TID ((int)__builtin_amdgcn_workitem_id_x())
#define BID ((int)__builtin_amdgcn_workgroup_id_x())
#define RFL(x) __builtin_amdgcn_readfirstlane(x)
#define RLX_AGENT __ATOMIC_RELAXED, __HIP_MEMORY_SCOPE_AGENT
#define LD(T,p) (*(const LAS T*)(p))
#define ST(T,p) (*(LAS T*)(p))
#define SPINCAP (1<<17)
#define DRAIN() asm volatile("s_waitcnt vmcnt(0)" ::: "memory")
#define GLD(p) __hip_atomic_load((const GAS u64*)(p),RLX_AGENT)
#define GST(p,v) __hip_atomic_store((GAS u64*)(p),(u64)(v),RLX_AGENT)
#define BALLOT(c) __builtin_amdgcn_ballot_w64(c)
#define SC1 16
#define LANE(l) int l; { int t2_=TID; asm volatile("" : "+v"(t2_)); l=t2_&63; }
#define SCHED() __builtin_amdgcn_sched_barrier(0)
#define RSRC(p,n) __builtin_amdgcn_make_buffer_rsrc((void*)(p),0,(n),0x00020000)
#define BLD128 __builtin_amdgcn_raw_buffer_load_b128
#define BLD32 __builtin_amdgcn_raw_buffer_load_b32
#define BST128 __builtin_amdgcn_raw_buffer_store_b128
#define PRIO(n) __builtin_amdgcn_s_setprio(n)

DEV float b2f(u32 v){ return BC(float,v<<16); }
DEV float blo(u32 v){ return BC(float,v<<16); }
DEV float bhi(u32 v){ return BC(float,v&0xffff0000u); }
DEV u32 pk(float a,float b){ f32x2 v={a,b}; bf2 r=__builtin_convertvector(v,bf2); return BC(u32,r); }
DEV u32 pkh(float a,float b){ f32x2 v={a,b}; h2 r=__builtin_convertvector(v,h2); return BC(u32,r); }
DEV float rb(float f){ return BC(float,pk(f,f)<<16); }
DEV float rcpf_(float x){ return __builtin_amdgcn_rcpf(x); }
DEV float expf_(float x){
  const float L2E=1.4426950216293335f, L2El=1.9259629911266175e-8f; float h=x*L2E; float l=__builtin_fmaf(x,L2E,-h)+x*L2El;
  float e=__builtin_amdgcn_exp2f(h); return __builtin_fmaf(e*l,0.6931471805599453f,e); }
DEV float sigm_f(float x){ return rcpf_(1.f+__builtin_amdgcn_exp2f(-x*1.4426950408889634f)); }
DEV float silu_f(float x){ return x*sigm_f(x); }
DEV u32 umax_(u32 a,u32 b){ return a>b?a:b; }
DEV u32 umin_(u32 a,u32 b){ return a<b?a:b; }

#define WSOFF 0
#define G2 7
#define PSL 1
#define NA 5
#define NB 11
#define GD 3
#define AD 14
#define DN 1
#define ECS 528
#define ED 8
#define PK 0x112
#define PS 0x030
#define RD 0
#define PD 9
#define P12 64
#define ESL 4
#define CHN 8
#define CSL 2
#define PWB (w4>>1)*(9*4096)+(w4&1)*128
#define PLO (li>>1)*(18*4096)+(li&1)*64
#define YSH 3
#define PRE1 6
#define PRE3 4
#define PRE2 5
#define W1 5
#define W3 3
#define W2_ 4
#define W1B 20
#define W2B 12
#define LAST1 3
#define RGATE 3
#define UGATE 7
#define R2D 12
#define PFN 32
#define CPF 8
#define NRG 8
#define W_EPOCH 0
#define W_RG 16384
#define W_ST (W_RG+32*8192)
#define W_U (W_ST+32768)
#define UST (D_*16+128)
#define YST (L_*2+128)
#define TL 1
#define GST_ ((D_*16<<TL)+128)
#define W_Y (W_U+256*UST)
#define W_PART (W_Y+D_*YST)
#define W_GU (W_PART+8*9*16*256)
#define W_FL 9216
#define W_IDX 1024
#define CTLB 0
struct KP { const u16 *Are,*Aim,*Bre,*Bim,*Cre,*Cim,*ldt,*pad; };

template<int CTRL> DEV float dppf(float v){ return BC(float,__builtin_amdgcn_update_dpp(0u,BC(u32,v),CTRL,0xf,0xf,false)); }
template<int CTRL> DEV u32 dppu(u32 v){ return (u32)__builtin_amdgcn_update_dpp(0u,v,CTRL,0xf,0xf,false); }
DEV float sum16(float v){ v+=dppf<0xB1>(v); v+=dppf<0x4E>(v); v+=dppf<0x141>(v); v+=dppf<0x140>(v); return v; }
DEV float add16x(float v){ auto r=__builtin_amdgcn_permlane16_swap(BC(u32,v),BC(u32,v),false,false); u32 a=r[0], b=r[1]; return BC(float,a)+BC(float,b); }
DEV u32 wmax(u32 v){
  v=umax_(v,(u32)__builtin_amdgcn_update_dpp(0u,v,0x111,0xf,0xf,false));
  v=umax_(v,(u32)__builtin_amdgcn_update_dpp(0u,v,0x112,0xf,0xf,false));
  v=umax_(v,(u32)__builtin_amdgcn_update_dpp(0u,v,0x114,0xf,0xf,false));
  v=umax_(v,(u32)__builtin_amdgcn_update_dpp(0u,v,0x118,0xf,0xf,false));
  v=umax_(v,(u32)__builtin_amdgcn_update_dpp(0u,v,0x142,0xa,0xf,false));
  v=umax_(v,(u32)__builtin_amdgcn_update_dpp(0u,v,0x143,0xc,0xf,false));
  return (u32)__builtin_amdgcn_readlane((int)v,63); }

DEV void garrive(lch* sm,int c){ u32 ad=(u32)(size_t)sm+c*4, one=1;
  asm volatile("s_mov_b64 exec, 1\n\tds_add_u32 %0, %1\n\ts_mov_b64 exec, -1" :: "v"(ad),"v"(one) : "memory"); }
template<int SLP> DEV void gwait(lch* sm,int c,u32 target){ u32 ad=(u32)(size_t)sm+c*4, tv, cnt, sv; target=RFL(target);
  asm volatile("s_mov_b32 %1, 0x20000\n"
    "1:\n\tds_read_b32 %0, %3\n\ts_waitcnt lgkmcnt(0)\n\tv_readfirstlane_b32 %2, %0\n\ts_cmp_ge_u32 %2, %4\n\ts_cbranch_scc1 2f\n\ts_sleep %5\n\ts_sub_u32 %1, %1, 1\n\ts_cbranch_scc0 1b\n"
    "2:" : "=&v"(tv),"=&s"(cnt),"=&s"(sv) : "v"(ad),"s"(target),"i"(SLP) : "memory","scc"); }
template<int ST_,class T> DEV void gwait4(int c,u32 target,const lch* p,T& a,T& b,T& d,T& e){ u32 tv,cnt,sv;
  asm volatile("s_mov_b32 %1, 0x20000\n1:\n\tds_read_b32 %0, %7\n\tds_read_b128 %3, %8\n\tds_read_b128 %4, %8 offset:%10\n\tds_read_b128 %5, %8 offset:%11\n\tds_read_b128 %6, %8 offset:%12\n\ts_waitcnt lgkmcnt(4)\n\t"
    "v_readfirstlane_b32 %2, %0\n\ts_cmp_ge_u32 %2, %9\n\ts_cbranch_scc1 2f\n\ts_sleep 1\n\ts_sub_u32 %1, %1, 1\n\ts_cbranch_scc0 1b\n2:\n\ts_waitcnt lgkmcnt(0)"
    : "=&v"(tv),"=&s"(cnt),"=&s"(sv),"=&v"(a),"=&v"(b),"=&v"(d),"=&v"(e) : "v"(c*4),"v"(p),"i"(target),"i"(ST_),"i"(2*ST_),"i"(3*ST_) : "memory","scc"); }
DEV void gsync(lch* sm,int c,u32 target){ garrive(sm,c); gwait<0>(sm,c,target); }
template<int SH> DEV u64 poll1(const char* p,u64 want){ u64 f=GLD(p);
  _Pragma("unroll 1") for(int sp=0;sp<SPINCAP;sp++){ bool bad=((f^want)>>SH)!=0ull; if(BALLOT(bad)==0ull) break; __builtin_amdgcn_s_sleep(1); if(bad) f=GLD(p); }
  __builtin_amdgcn_fence(__ATOMIC_ACQUIRE,"wavefront"); return f; }
#define STR_(x) #x
#define STR(x) STR_(x)
DEV u32x4 mkrs(const void* p,u32 n){ u64 a=(u64)p; return u32x4{(u32)RFL((int)(u32)a),(u32)RFL((int)((u32)(a>>32)&0xffffu)),n,0x00020000u}; }
#define VMW(n) asm volatile("s_waitcnt vmcnt(%0)" :: "i"(n) : "memory")
#define VW(n) "s_waitcnt vmcnt(" STR(n) ")\n\t"
#define YSLP 1
#define GSLP 1
#define QSLP 1
#define PSLP 3
#define POLLASM(LOADS,CHECK,K) "s_mov_b32 %[cnt], 0x20000\n1:\n\ts_waitcnt vmcnt(0)\n\t" CHECK "s_cbranch_vccz 9f\n\ts_sleep " STR(K) "\n\ts_mov_b64 exec, vcc\n\t" LOADS \
  "s_mov_b64 exec, -1\n\ts_sub_u32 %[cnt], %[cnt], 1\n\ts_cbranch_scc0 1b\n9:\n\t"
#define LD_DUP2 "buffer_load_dwordx4 v[112:115], %[of], %[rs], 0 offen sc1\n\tbuffer_load_dwordx4 v[116:119], %[of], %[rs], %[so] offen sc1\n\t"
#define MX2(a,b) "v_max3_u32 %[t0], %[t0], v" #a ", v" #b "\n\t"
#define CHK_I0 "v_max_u32 %[t0], v112, v113\n\t" MX2(114,115)
#define CHK_IE "v_cmp_eq_u32 vcc, -1, %[t0]\n\t"
#define PCLOB "memory","vcc","scc"
#define UMASK 0x40074007u
DEV u32 upat(u32 tag,int k){ return ((((tag*0xea959c21u)>>(8*k))&255u)*0x04000801u)&UMASK; }
#define YMASK 0x70007000u
DEV u32 ycls(u32 tag,int j){ return ((tag*0x9e35u)>>(2*j))&3u; }
DEV u32 ypat(u32 tag,int k){ return (((((tag*0x9e35u)>>(4*k))&15u)*0x04001000u)&0x30003000u)+0x20002000u; }
DEV u32 pkz(float a,float b){ return BC(u32,__builtin_amdgcn_cvt_pkrtz(a,b)); }
#define INV 0xffffffffu
DEV u32 nv(u32 v){ return umin_(v,0xfffeffffu); }
DEV u32x4 nv4(u32x4 v){ return u32x4{nv(v[0]),nv(v[1]),nv(v[2]),nv(v[3])}; }
#define OOR 0x40000000

#define CHSZ 36864
#define O_CH 512
#define O_CND 256
#define SLSZ 38976
#define O_R2 (O_CH+2*SLSZ)
#define O_X2 (O_R2+CHSZ)
#define O_TILE (O_X2+CHSZ)
#define XSTR 1168
#define HSTR 608
#define HT 96
#define XAD(r,b) ((r)*HSTR+(b)+((b)>=576? 8*HSTR-576:0))
#define XS1 1
#define XG1 1
#define TSTR2 1216
#define O_CAND (O_TILE+9728+1024)
#define LDS_TOTAL (O_CAND+128)
#define C_G 0
#define C_U 8
#define C_L 4
#define C_D 5
#define C_X 12
#define C_P 13
#define C_H 14
#define C_I 17
#define C_E 18
#define C_IX 19
#define C_PR 21
#define C_W 22
#define C_WIN (lb>=14? C_W:C_D)
#define O_IDX 96
#define O_EXP 144
#define O_RMX 192
#define O_RV 128
#define O_MD 400
#define O_RC (O_CAND+64)

struct C2 { float r,i; };
DEV C2 cmul(C2 a,C2 b){ C2 c; c.r=__builtin_fmaf(a.r,b.r,-(a.i*b.i)); c.i=__builtin_fmaf(a.r,b.i,a.i*b.r); return c; }
DEV h8 ldA(const lch* p){ u32x2 a=*(const volatile LAS u32x2*)p, b=*(const volatile LAS u32x2*)(p+8); return BC(h8,(u32x4{a[0],a[1],b[0],b[1]})); }
DEV bf16x8 trfrag(const lch* p0,const lch* p1){
  u32x2 b0=BC(u32x2,__builtin_amdgcn_ds_read_tr16_b64_v4i16((LAS v4s*)p0)), b1=BC(u32x2,__builtin_amdgcn_ds_read_tr16_b64_v4i16((LAS v4s*)p1));
  return BC(bf16x8,(u32x4{b0[0],b0[1],b1[0],b1[1]})); }

struct FP { u32 gg[24]; float bs[24]; int yo,y3; lch* wp; const lch* tp; };
#define D2(a,b,c) __builtin_amdgcn_fdot2_f32_bf16(BC(bf2,a),BC(bf2,b),c,false)
#define ZCH 64
#define YZ 1280
#define YG1 320
#define YG2 672
DEV void final_blk(FP& f,int z,const u32x4* xq,f32x4 rc,const lch* ep){
  f32x4 q[4];
  #pragma unroll
  for(int e=0;e<4;e++) q[e]=LD(f32x4,ep+z*128+e*16);
  #pragma unroll
  for(int e=0;e<4;e++){ u32 xw=xq[z][e]; int i=8*z+2*e; float a=blo(xw), b=bhi(xw);
    float pa=__builtin_fmaf(a,rc[0],rc[1]), pb=__builtin_fmaf(b,rc[0],rc[1]);
    float ga=pa*rcpf_(__builtin_fmaf(__builtin_amdgcn_exp2f(__builtin_fmaf(a,rc[2],rc[3])),q[e][0],q[e][0])), gb=pb*rcpf_(__builtin_fmaf(__builtin_amdgcn_exp2f(__builtin_fmaf(b,rc[2],rc[3])),q[e][2],q[e][2]));
    f.gg[i]=pk(ga,0.f); f.gg[i+1]=pk(0.f,gb);
    f.bs[i]=__builtin_fmaf(pa*q[e][1],ga,a); f.bs[i+1]=__builtin_fmaf(pb*q[e][3],gb,b);
    asm volatile("" : "+v"(f.gg[i]),"+v"(f.gg[i+1]),"+v"(f.bs[i]),"+v"(f.bs[i+1])); }
}
struct FL { u64 fs,ff; u32 dk; u32x4 xq[3]; int so,fo,xr,xr2; const char* ws; const u16* D; u32x4 rx; };
DEV void final_ld(FL& l){
  asm volatile("global_load_dwordx2 %0, %6, %10 sc1\n\tglobal_load_dwordx2 %1, %7, %10 sc1\n\tv_lshrrev_b32 %2, 2, %7\n\tglobal_load_ushort %2, %2, %11 offset:%13\n\t"
    "buffer_load_dwordx4 %3, %8, %12, 0 offen\n\tbuffer_load_dwordx4 %4, %8, %12, 0 offen offset:128\n\tbuffer_load_dwordx4 %5, %9, %12, 0 offen"
    : "=&{v[80:81]}"(l.fs),"=&{v[82:83]}"(l.ff),"=&{v79}"(l.dk),"=&{v[84:87]}"(l.xq[0]),"=&{v[88:91]}"(l.xq[1]),"=&{v[92:95]}"(l.xq[2])
    : "v"(l.so),"v"(l.fo),"v"(l.xr),"v"(l.xr2),"s"(l.ws),"s"(l.D),"s"(l.rx),"i"(-W_FL/4) : "memory"); }
DEV void final_adr(lch* sm,FP& f,int w4){
  LANE(ln); int bid=BID, xg=bid&7, lb=bid>>3, g=ln>>4, li=ln&15, qq=li>>2, pq=li&3;
  f.yo=(144*(lb&3)+16*w4+(ln>>2))*YST+(32*(8*xg+(lb>>2))+8*(ln&3))*2; f.y3= w4==0? f.yo+2*ZCH*YST:OOR;
  lch* fb=sm+O_CH+w4*(3*YZ); f.wp=fb+(ln&1)*YG1+(ln&2)*(YG2/2)+(ln>>2)*16; f.tp=fb+(g&1)*YG1+(g&2)*(YG2/2)+(8*(pq>>1)+qq)*16+(pq&1)*8;
  asm volatile("" : "+v"(f.yo),"+v"(f.y3),"+v"(f.wp),"+v"(f.tp));
}
DEV void final_post(lch* sm,FP& f,int w4,int nch,int ob,int trow,u32 tg,u16* __restrict__ out,char* ws){
  int lb=BID>>3;
  u32x4 rs=mkrs(ws+W_Y,D_*YST); rsrc_t ro=RSRC(out,L_*D_*2);
  gwait<ESL>(sm,C_E,4*nch);
  if(AD&&lb<8) __builtin_amdgcn_s_sleep(AD);
  if(lb>=8&&lb<14) gwait<CSL>(sm,C_H,CHN);
  PRIO(3);
  #define LD_Y "buffer_load_dwordx4 v[112:115], %[of], %[rs], 0 offen sc1\n\tbuffer_load_dwordx4 v[116:119], %[of], %[rs], %[s1] offen sc1\n\tbuffer_load_dwordx4 v[120:123], %[o3], %[rs], 0 offen sc1\n\t"
  asm volatile(LD_Y : "+v"(trow) : [of]"v"(f.yo),[rs]"s"(rs),[s1]"s"(ZCH*YST),[o3]"v"(f.y3) : "memory");
  garrive(sm,C_PR);
  { gwait<2>(sm,C_IX,1); u32x4 iq=LD(u32x4,sm+O_IDX); u32 tt=(u32)trow*0x10001u; bool routed=false;
    #pragma unroll
    for(int q=0;q<4;q++){ u32 z=iq[q]^tt; routed|=((z&0xffffu)==0u)|((z>>16)==0u); }
    ob=routed? OOR:ob; }
  { u32 t0,t1,t2,cnt;
    #define UC(t,a,b,c) "v_bitop3_b32 " t ", v" #a ", v" #b ", %[" #c "] bitop3:0x7e\n\t"
    #define UD(t,a,c) "v_bitop3_b32 " t ", " t ", v" #a ", %[" #c "] bitop3:0xf6\n\t"
    asm volatile("s_mov_b32 %[cnt], 0x20000\n1:\n\ts_waitcnt vmcnt(0)\n\tds_write_b128 %[wp], v[112:115]\n\tds_write_b128 %[wp], v[116:119] offset:" STR(YZ) "\n\t" UC("%[t0]",112,116,c0) "s_cmp_lg_u32 %[w4], 0\n\ts_cbranch_scc1 8f\n\tds_write_b128 %[wp], v[120:123] offset:2*" STR(YZ) "\n\t"
      UD("%[t0]",120,c0) UD("%[t0]",121,c1) UD("%[t0]",122,c2) UD("%[t0]",123,c3) "8:\n\t" UC("%[t1]",113,117,c1) UC("%[t2]",114,118,c2) "v_or3_b32 %[t0], %[t0], %[t1], %[t2]\n\t" UC("%[t1]",115,119,c3)
      "v_bitop3_b32 %[t0], %[t0], %[t1], %[mk] bitop3:0xa8\n\tv_cmp_ne_u32 vcc, 0, %[t0]\n\ts_cbranch_vccz 9f\n\ts_sleep " STR(YSLP) "\n\ts_wqm_b64 exec, vcc\n\t" LD_Y "s_mov_b64 exec, -1\n\ts_sub_u32 %[cnt], %[cnt], 1\n\ts_cbranch_scc0 1b\n9:"
      : [t0]"=&v"(t0),[t1]"=&v"(t1),[t2]"=&v"(t2),[cnt]"=&s"(cnt),"+v"(ob) : [of]"v"(f.yo),[rs]"s"(rs),[s1]"s"(ZCH*YST),[o3]"v"(f.y3),[wp]"v"(f.wp),[w4]"s"(w4),"s"(ro),
        [c0]"s"(ypat(tg,0)),[c1]"s"(ypat(tg,1)),[c2]"s"(ypat(tg,2)),[c3]"s"(ypat(tg,3)),[mk]"s"(YMASK) : PCLOB); }
  u32x4 r[3];
  #pragma unroll
  for(int z=0;z<3;z++) r[z]=BC(u32x4,trfrag(f.tp+z*YZ,f.tp+z*YZ+64));
  #pragma unroll
  for(int z=0;z<3;z++) if(z<2||w4==0){ u32x4 ov;
    #pragma unroll
    for(int e=0;e<4;e++){ u32 rw=r[z][e]; int i=8*z+2*e;
      ov[e]=pk(D2(rw,f.gg[i],f.bs[i]),D2(rw,f.gg[i+1],f.bs[i+1]));
    }
    BST128(ov,ro,ob+z*(2*ZCH),0,0); SCHED(); }
}

#define TROW 544
#define O_TB 4640
#define O_DE 35664
#define O_DO (O_DE+1232)
#define O_R2K (O_DO+1152)
#define O_R3K (O_R2K+512)
#define O_DUM (O_R3K+320)
#define O_RED O_TB
#define MH(a,b,c) __builtin_amdgcn_mfma_f32_16x16x32_f16(a,b,c,0,0,0)
#define PIN(x) asm volatile("" : "+v"(x))
DEV h8 ld4w(const lch* p){ return BC(h8,(u32x4{LD(u32,p),LD(u32,p+4),LD(u32,p+8),LD(u32,p+12)})); }
DEV u32 __umul24_(u32 a,u32 b){ u32 r; asm("v_mul_u32_u24 %0, %1, %2" : "=v"(r) : "s"(b),"v"(a)); return r; }
DEV u32x4 band(u32 mask,int g,u32 val){ u32 by=__builtin_amdgcn_ubfe(mask,8*g,8), X=by|(by<<15), k=0x10001u; u32x4 r; asm volatile("" : "+s"(k),"+s"(val));
  #pragma unroll
  for(int d=0;d<4;d++) r[d]=__umul24_((X>>(2*d))&k,val);
  return r; }
DEV h8 zpack(f32x4 e,f32x4 o){ return BC(h8,(u32x4{pkh(e[0],o[0]),pkh(e[1],o[1]),pkh(e[2],o[2]),pkh(e[3],o[3])})); }
struct C3 { float r,i,n; };
DEV C3 c3(C2 a){ C3 c; c.r=a.r; c.i=a.i; PIN(c.i); c.n=-c.i; PIN(c.n); return c; }
DEV C2 cm(C2 a,C3 b){ C2 c; c.r=__builtin_fmaf(a.r,b.r,a.i*b.n); c.i=__builtin_fmaf(a.r,b.i,a.i*b.r); return c; }
DEV u32 ph(C2 a){ return pkh(a.r,a.i); }
#define W2(p,a,b) asm volatile("ds_write2_b32 %0, %1, %2 offset1:136" :: "v"(p),"v"(a),"v"(b) : "memory")
struct CP { const lch *x0,*x1,*bd; lch *p0,*p1,*p2,*wp; u32x4 Be,Bo,AE; int yo; };
template<int ROLE> DEV void lanes(CP& c,lch* cb,int ln){ int m=ln&15, g=ln>>4;
  const lch* ad=cb+((m&1)? O_DO+2*527 : O_DE+2*536)+2*(8*g-m);
  const lch* bd=cb+2*(256+16*m+8*g); lch* dum=cb+O_DUM;
  if(ROLE==0){ int l0=128*(g>>1)+8*m+4*(g&1); c.p0=cb+O_DE+(549-l0)*2; PIN(c.p0); c.p1=c.p0+6; PIN(c.p1); c.p2=c.p0+(O_DO-O_DE-18); c.x0=ad-640; c.bd=bd-64; c.x1=cb+O_R3K+2*(126-2*m+8*g)-192; PIN(c.x1);
    c.Be=band(0x1ffu<<(m+4),g,0x3c00u); PIN(c.Be); }
  if(ROLE==1){ lch* r=cb+O_R2K+(192-64*(g-2)-2*m)*2; c.p0= g<2? dum : r-66; c.p1= ln==0? cb+O_DE+(552-256)*2 : (ln<=32? dum : r); c.p2=cb+O_DE+(544-256-8*ln)*2; c.x0=ad-960; c.bd=bd-448; PIN(c.p0); PIN(c.p1); PIN(c.p2); }
  if(ROLE==2){ int i0=16*m+4*g; c.p0= m<8? cb+O_R3K+(125-i0)*2 : dum+2; c.p1= ln==0? cb+O_R2K+128 : (ln==8? cb+O_R3K : c.p0+6); c.x0=ad-960; c.bd=bd-448; PIN(c.p0); PIN(c.p1); }
  if(ROLE==3){ c.x0=cb+O_R2K+2*(188-4*m+8*g)-256; c.bd=cb+2*(248+8*m+8*g);
    c.Be=band(0x1fu<<(m+6),g,0x3c00u); c.Bo=band(0x7u<<(m+11),g,0x3c00u); PIN(c.Be); PIN(c.Bo); }
  PIN(c.x0); PIN(c.bd); }
template<int ROLE> DEV void lanes2(CP& c,lch* cb,int ln){ int m=ln&15, g=ln>>4;
  if(ROLE==0){ c.Bo=band(0x7fu<<(m+5),g,0x3c00u); c.AE=band(3u<<(m+12),g,0xffffu); PIN(c.Bo); PIN(c.AE); }
  c.wp=cb+O_RED+ln*16; c.yo=(g&1)*512+m*32+(g>>1)*16; PIN(c.wp); PIN(c.yo); }
template<int ROLE> DEV void chan2(lch* sm,lch* cb,const lch*& fa,const lch*& fb,int grp,int d,int one_,int emax,u64 tag,char* ws){
  CP c;
  const f32x4 Z4={0,0,0,0}; h8 A[12]; f32x4 acc[8]; float sc; u32x4 ry; u32 cga,ym,yp[4];
  #define YSET sc=BC(float,5u<<23); ry=mkrs(ws+W_Y+d*YST,4096); cga=(C_G+grp)*4; ym=~YMASK; PIN(sc); PIN(cga); PIN(ym); _Pragma("unroll") for(int k=0;k<4;k++) yp[k]=ypat((u32)tag,k); asm volatile("" : "+s"(ry),"+s"(yp[0]),"+s"(yp[1]),"+s"(yp[2]),"+s"(yp[3]));
  #define LD_U "buffer_load_dwordx4 v[112:115], %[of], %[rs], 0 offen sc1\n\tbuffer_load_dwordx4 v[116:119], %[of], %[rs], %[s1] offen sc1\n\tbuffer_load_dwordx4 v[120:123], %[of], %[rs], %[s2] offen sc1\n\tbuffer_load_dwordx4 v[124:127], %[of], %[rs], %[s3] offen sc1\n\t"
  #define CHK_U UC("%[t0]",112,116,c0) UC("%[t1]",113,117,c1) UD("%[t0]",120,c0) UD("%[t1]",121,c1) UD("%[t0]",124,c0) UD("%[t1]",125,c1) UD("%[t0]",114,c2) UD("%[t1]",115,c3) UD("%[t0]",118,c2) UD("%[t1]",119,c3) \
    UD("%[t0]",122,c2) UD("%[t1]",123,c3) UD("%[t0]",126,c2) UD("%[t1]",127,c3) "v_bitop3_b32 %[t0], %[t0], %[t1], %[mk] bitop3:0xa8\n\tv_cmp_ne_u32 vcc, 0, %[t0]\n\t"
  #define UX(a,b,c,d,o) "v_xor_b32 v" #a ", %[e0], v" #a "\n\tv_xor_b32 v" #b ", %[e1], v" #b "\n\tv_xor_b32 v" #c ", %[e2], v" #c "\n\tv_xor_b32 v" #d ", %[e3], v" #d "\n\tds_write_b128 %[up], v[" #a ":" #d "] offset:" #o "\n\t"
  #define UK(n,a,b,c,d,o,T) VW(n) UX(a,b,c,d,o) UC("%[t1]",a,b,k4) UC("%[t2]",c,d,k4) T
  #define OR3 "v_or3_b32 %[t0], %[t0], %[t1], %[t2]\n\t"
  #define UPOLL { u32 t0,t1,t2,cnt; asm volatile("s_mov_b32 %[cnt], 0x20000\n1:\n\t" UK(3,112,113,114,115,544,"v_or_b32 %[t0], %[t1], %[t2]\n\t") UK(2,116,117,118,119,1056,OR3) UK(1,120,121,122,123,1568,OR3) UK(0,124,125,126,127,2080,OR3) \
      "v_and_b32 %[t0], %[mk], %[t0]\n\tv_cmp_ne_u32 vcc, 0, %[t0]\n\ts_cbranch_vccz 9f\n\ts_sleep " STR(PSLP) "\n\ts_mov_b64 exec, vcc\n\t" LD_U "s_sub_u32 %[cnt], %[cnt], 1\n\ts_cbranch_scc0 1b\n9:\n\ts_mov_b64 exec, 1\n\tds_add_u32 %[cu], %[one]\n\ts_mov_b64 exec, -1" \
      : [t0]"=&v"(t0),[t1]"=&v"(t1),[t2]"=&v"(t2),[cnt]"=&s"(cnt) : [of]"v"(c.p0),[rs]"s"(rsU),[s1]"s"((32>>TL)*GST_),[s2]"s"((64>>TL)*GST_),[s3]"s"((96>>TL)*GST_),[k4]"s"(0x40004u), \
        [e0]"s"(c0^0x40004u),[e1]"s"(c1^0x40004u),[e2]"s"(c2^0x40004u),[e3]"s"(c3_^0x40004u),[mk]"s"(UMASK),[up]"v"(c.p1),[cu]"v"((C_U+one_)*4),[one]"v"(4) : PCLOB); }
  if(ROLE<3){
    h8 a[8], b[8];
    #pragma unroll
    for(int ks=0;ks<8;ks++){ a[ks]=LD(h8,fa+ks*64); b[ks]=LD(h8,fb+ks*64); }
    SCHED();
    LANE(ln); lanes<ROLE>(c,cb,ln); float fz= ln==32? 0.f : 4.76837158203125e-07f; if(ROLE==1) PIN(fz);
    SCHED();
    f32x4 k=Z4;
    #pragma unroll
    for(int ks=0;ks<8;ks++) k=MH(a[ks],b[ks],k);
    const float f=4.76837158203125e-07f;
    float k0=k[0]*f, k1=k[1]*f, k2=k[2]*f, k3=k[3]*f;
    u32 h32=pkh(k3,k2), h10=pkh(k1,k0);
    if(ROLE!=1){
      ST(u16,c.p0)=(u16)h32; ST(u32,c.p0+2)=pkh(k2,k1); ST(u16,c.p1)=(u16)(h10>>16);
      if(ROLE==0) ST(u32x2,c.p2)=u32x2{h32,h10};
    } else {
      float n0=dppf<0x130>(k[0])*fz, z1= fz==0.f? k0:k1, z2=k[2]*fz, z3=k[3]*fz; u32 h22=pkh(z2,z2);
      ST(u16,c.p0+64)=(u16)h10; ST(u16,c.p0+2)=(u16)(h32>>16); ST(u16,c.p0)=(u16)h32; ST(u16,c.p1)=(u16)(h10>>16);
      u32x4 vo={pkh(n0,z3),h22,pkh(z2,z1),pkh(k0,k0)}, ve={pkh(n0,n0),pkh(z3,z2),h22,pkh(z1,k0)};
      asm volatile("s_mov_b32 exec_hi, 1\n\tds_write_b128 %0, %1\n\tds_write_b128 %0, %2 offset:%3\n\ts_mov_b32 exec_hi, -1" :: "v"(c.p2),"v"(ve),"v"(vo),"i"(O_DO-O_DE-16) : "memory"); }
    gsync(sm,C_G+grp,7);
    u32 kv;
    if(ROLE==0){ A[0]=ld4w(c.x0+640); A[1]=ld4w(c.x0+128); A[2]=ld4w(c.x1+192); A[3]=ld4w(c.x1+128); A[4]=ld4w(c.x1+64); A[5]=ld4w(c.x1); kv=LD(u16,cb+O_R2K+128); }
    else {
      #pragma unroll
      for(int r=0;r<6;r++){ A[2*r]=ld4w(c.x0+832-64*r); A[2*r+1]=ld4w(c.x0+320-64*r); } }
    SCHED(); lanes2<ROLE>(c,cb,ln); YSET SCHED();
    if(ROLE==0){ kv|=kv<<16; c.AE=u32x4{c.AE[0]&kv,c.AE[1]&kv,c.AE[2]&kv,c.AE[3]&kv}; PIN(c.AE); }
    PRIO(2); if(ROLE!=0&&BID>=P12) PRIO(1);
    gwait<1>(sm,C_U+one_,8-4*one_);
  } else {
    u32 tg=(u32)tag, c0=upat(tg,0), c1=upat(tg,1), c2=upat(tg,2), c3_=upat(tg,3); u32x4 rsU=mkrs(ws+W_U,256*UST);
    if(!one_) PRIO(2);
    LANE(ln); c.p0=(lch*)fa; c.p1=(lch*)fb;
    bool late= grp==0||BID>=112;
    if(!late&&!one_&&ED) __builtin_amdgcn_s_sleep(ED);
    if(!late) asm volatile(LD_U :: [of]"v"(c.p0),[rs]"s"(rsU),[s1]"s"((32>>TL)*GST_),[s2]"s"((64>>TL)*GST_),[s3]"s"((96>>TL)*GST_) : "memory");
    lanes<3>(c,cb,ln); lanes2<3>(c,cb,ln); YSET
    #pragma unroll
    for(int j=2;j<8;j++){ acc[j]=Z4; PIN(acc[j]); }
    if(late){ gwait<2>(sm,C_G,UGATE); if(BID>=112) gwait<1>(sm,C_D,DN); asm volatile(LD_U :: [of]"v"(c.p0),[rs]"s"(rsU),[s1]"s"((32>>TL)*GST_),[s2]"s"((64>>TL)*GST_),[s3]"s"((96>>TL)*GST_) : "memory"); }
    if(!late) UPOLL
    gwait<1>(sm,C_G+grp,7);
    #pragma unroll
    for(int i=0;i<6;i++) A[i]=ldA(c.x0+320-64*i);
    if(late) UPOLL
    PRIO(2);
    gwait<1>(sm,C_U+one_,8-4*one_);
  }
  const lch* bd=c.bd; h8 Be=BC(h8,c.Be), Bo=BC(h8,c.Bo);
  #pragma unroll
  for(int j=0;j<(ROLE==3? 2:8);j++) acc[j]=Z4;
  constexpr int OWN= ROLE==1? 2 : ROLE==2? 1 : ROLE, NS= ROLE==0? 1:2;
  #define RW(j) { ST(f32x4,c.wp+((j)>>1)*4096+(ROLE&1)*2048+((j)&1)*1024)=acc[j]; SCHED(); }
  const lch* rp=c.wp+OWN*4096; f32x4 a[2], b[2]; u32 cv;
  #define LDV(T,p) (*(const volatile LAS T*)(p))
  #define ARR { garrive(sm,C_G+grp); SCHED(); }
  #define OPT { cv=LDV(u32,(lch*)(size_t)cga); _Pragma("unroll") for(int q=0;q<NS;q++){ a[q]=LDV(f32x4,rp+q*2048); b[q]=LDV(f32x4,rp+q*2048+1024); } SCHED(); }
  #define SUM(v) { _Pragma("unroll") for(int e=0;e<4;e++) v[0][e]=(NS==1? v[0][e] : v[0][e]+v[1][e])*sc; PIN(v[0]); SCHED(); }
  if(ROLE==1||ROLE==2){
    constexpr int NQ= ROLE==1? 5:4, LA=9; h8 B[30];
    #define QS(s) (ROLE==2? (s) : (s)<3? 5+(s) : (s))
    #define LB(i) B[i]=LD(h8,bd+320-64*((i)%6)+512*QS((i)/6));
    #pragma unroll
    for(int i=0;i<LA;i++) LB(i)
    SCHED();
    #pragma unroll
    for(int i=0;i<6*NQ;i++){ const int q=QS(i/6), r=i%6;
      if(i==6*NQ-(ROLE==1? 4:7)){ PRIO(2); OPT } if(i==6*NQ-(ROLE==1? 2:3)) SUM(a) if(i==6*NQ-1) SUM(b)
      if(!(ROLE==1&&q==3)){ acc[q]=MH(A[2*r],B[i],acc[q]); SCHED(); }
      if(q!=7&&!(ROLE==2&&q==3)){ acc[q+1]=MH(A[2*r+1],B[i],acc[q+1]); SCHED(); }
      if(i+LA<6*NQ){ LB(i+LA) SCHED(); }
      if(ROLE==2&&i==6) RW(0) if(ROLE==2&&i==12){ RW(1) ARR } if(ROLE==1&&i==13) RW(6) if(ROLE==1&&i==19){ RW(7) ARR } } }
  if(ROLE==0){
    h8 B[8], ue[4], C[8];
    #pragma unroll
    for(int kt=0;kt<4;kt++){ ue[kt]=LD(h8,bd+48+512*kt); B[kt]=LD(h8,bd+64+512*kt); }
    f32x4 ce[2],co[2]; h8 Z[2];
    ce[0]=MH(ue[0],Be,Z4); co[0]=MH(B[0],Bo,Z4); ce[1]=MH(ue[1],Be,Z4); co[1]=MH(B[1],Bo,Z4); Z[0]=zpack(ce[0],co[0]);
    #pragma unroll
    for(int kt=0;kt<4;kt++){
      SCHED();
      if(kt+2<4){ ce[kt&1]=MH(ue[kt+2],Be,Z4); co[kt&1]=MH(B[kt+2],Bo,Z4); }
      if(kt+1<4) Z[(kt+1)&1]=zpack(ce[(kt+1)&1],co[(kt+1)&1]);
      #pragma unroll
      for(int rho=0;rho+kt<4;rho++) acc[4+kt+rho]=MH(Z[kt&1],A[2+rho],acc[4+kt+rho]);
      SCHED(); if(kt>=2) C[kt-1]=LD(h8,bd+512*(kt-1)); if(kt==0) A[5]=BC(h8,c.AE); if(kt==1) A[4]=ld4w(c.x0-384); if(kt==2) A[3]=ld4w(c.x0+64); if(kt==3) A[2]=ld4w(c.x0+576); SCHED(); }
    #pragma unroll
    for(int q=0;q<9;q++){
      if(q<4){ acc[q+4]=MH(A[5],B[q],acc[q+4]); SCHED(); } if(q<6){ acc[q+2]=MH(A[4],B[q],acc[q+2]); SCHED(); }
      if(q>=1&&q<7){ acc[q+1]=MH(A[1],B[q],acc[q+1]); SCHED(); } if(q>=2&&q<8){ acc[q]=MH(A[0],B[q],acc[q]); SCHED(); }
      if(q>=4&&q<8) RW(q-2)
      if(q<4){ B[4+q]=LD(h8,bd+64+512*(4+q)); SCHED(); }
      if(q>=3){ acc[q-1]=MH(A[2],C[q-1],acc[q-1]); SCHED(); } if(q>=2&&q<8){ acc[q]=MH(A[3],C[q-1],acc[q]); SCHED(); }
      if(q>=1&&q<7){ C[(q+2)&7]=LD(h8,bd+512*((q+2)&7)); SCHED(); } }
    acc[0]=MH(A[0],B[0],acc[0]); SCHED(); RW(6) acc[1]=MH(A[1],B[0],acc[1]); SCHED(); RW(7) ARR
    OPT acc[0]=MH(A[2],C[0],acc[0]); acc[1]=MH(A[0],B[1],acc[1]); acc[1]=MH(A[2],C[1],acc[1]); acc[1]=MH(A[3],C[0],acc[1]); SCHED(); SUM(a) SUM(b) }
  if(ROLE==3){
    h8 U[12];
    #pragma unroll
    for(int kt=0;kt<6;kt++) U[kt]=LD(h8,bd+256*kt);
    f32x4 ce[2],co[2]; h8 Z[2];
    ce[0]=MH(U[0],Be,Z4); co[0]=MH(U[0],Bo,Z4); ce[1]=MH(U[1],Be,Z4); co[1]=MH(U[1],Bo,Z4); Z[0]=zpack(ce[0],co[0]);
    #pragma unroll
    for(int kt=0;kt<12;kt++){
      SCHED();
      if(kt==10) OPT if(kt==11) SUM(a)
      if(kt+2<12){ ce[kt&1]=MH(U[kt+2],Be,Z4); co[kt&1]=MH(U[kt+2],Bo,Z4); }
      if(kt>=2&&kt<=8&&!(kt&1)){ SCHED(); RW((kt+2)/2) if(kt==8) ARR }
      if(kt+1<12) Z[(kt+1)&1]=zpack(ce[(kt+1)&1],co[(kt+1)&1]);
      #pragma unroll
      for(int rho=-1;rho<5;rho++){ int j=(kt+4+rho)>>1; if(((kt+rho)&1)==0&&j>=2&&j<8) acc[j]=MH(Z[kt&1],A[rho+1],acc[j]); }
      if(kt<6){ SCHED(); U[kt+6]=LD(h8,bd+256*(kt+6)); } }
    SCHED(); SUM(b)
  }
  { float s0[4], s1[4];
    if(RFL(cv)<11u){ u32 tv,cnt;
      asm volatile("s_mov_b32 %1, 0x20000\n1:\n\tds_read_b32 %0, %6\n\tds_read_b128 %2, %7\n\tds_read_b128 %4, %7 offset:2048\n\tds_read_b128 %3, %7 offset:1024\n\tds_read_b128 %5, %7 offset:3072\n\ts_waitcnt lgkmcnt(4)\n\t"
        "v_cmp_gt_u32 vcc, 11, %0\n\ts_cbranch_vccz 2f\n\ts_sleep 1\n\ts_sub_u32 %1, %1, 1\n\ts_cbranch_scc0 1b\n2:\n\ts_waitcnt lgkmcnt(0)"
        : "=&v"(tv),"=&s"(cnt),"=&v"(a[0]),"=&v"(b[0]),"=&v"(a[1]),"=&v"(b[1]) : "v"(cga),"v"(rp) : "memory","scc","vcc");
      SUM(a) SUM(b) }
    f32x4 o0=acc[2*OWN], o1=acc[2*OWN+1];
    #pragma unroll
    for(int e=0;e<4;e++){ s0[e]=__builtin_fmaf(o0[e],sc,a[0][e]); s1[e]=__builtin_fmaf(o1[e],sc,b[0][e]); }
    auto r0=__builtin_amdgcn_permlane16_swap(pk(s0[0],s0[1]),pk(s1[0],s1[1]),false,false); auto r1=__builtin_amdgcn_permlane16_swap(pk(s0[2],s0[3]),pk(s1[2],s1[3]),false,false);
    asm volatile("buffer_store_dwordx4 %0, %1, %2, 0 offen offset:%3 sc1\n\ts_nop 1" :: "v"(u32x4{(r0[0]&ym)|yp[0],(r1[0]&ym)|yp[1],(r0[1]&ym)|yp[2],(r1[1]&ym)|yp[3]}),"v"(c.yo),"s"(ry),"i"(OWN*1024) : "memory"); }
  garrive(sm,C_E);

  asm volatile("s_endpgm" : "=v"(fa),"=v"(fb) :: "memory");
}
#define GETEP asm volatile("s_waitcnt lgkmcnt(0)\n\ts_mov_b64 %0, s[96:97]\n\ts_mov_b32 %1, s98" : "=s"(ep),"=s"(ldw));
DEV void chan(lch* sm,int grp,int w4,int d,int one_,char* ws,u32x4 q0,u32x4 q1,u32x4 q2,u32x4 q3,u32x4 q4){
  lch* cb=sm+O_CH+grp*SLSZ; int role=w4, emax;
  #define LP(v) ((lch*)(size_t)(u32)(v))
  const lch* fa=LP(q4[0]); const lch* fb=LP(q4[1]);
  u64 tag;
  { float a2,bp,br,bi,cr,ci;
    u32 fq=q3[2], fr=q3[3], one=q4[2]; float f64=BC(float,fq), f128=BC(float,fr);
    lch *t8[4]={LP(q0[0]),LP(q0[1]),LP(q0[2]),LP(q0[3])}, *tB[4]={LP(q1[0]),LP(q1[1]),LP(q1[2]),LP(q1[3])}, *t4[2]={LP(q2[0]),LP(q2[1])}, *t2[2]={LP(q2[2]),LP(q2[3])}, *t1=LP(q3[0]), *te=LP(q3[1]);
    if(BID<64){ if(grp==0) PRIO(PK&3); if(grp==1) PRIO((PK>>4)&3); if(grp==2) PRIO((PK>>8)&3); } else
    if(grp!=0) PRIO(1);
    u64 ep; u32 ldw; GETEP tag=ep+1ull;
    float dt=rb(expf_((d&1)? bhi(ldw):blo(ldw))), ca=dt*1.4426950408889634f, cb_=dt*0.15915494309189535f;
    asm volatile("s_waitcnt vmcnt(0)\n\tv_mul_f32 %2, %8, v114\n\tv_mul_f32 %3, %8, v115\n\tv_mul_f32 %0, %6, v112\n\tv_mul_f32 %1, %7, v113\n\tv_mov_b32 %4, v116\n\tv_mov_b32 %5, v117"
      : "=&v"(a2),"=&v"(bp),"=&v"(br),"=&v"(bi),"=&v"(cr),"=&v"(ci) : "v"(ca),"v"(cb_),"v"(dt) : "memory");
    C2 W; W.r=cr*br-ci*bi; W.i=cr*bi+ci*br;
    u32 mx=BC(u32,__builtin_fmaxf(__builtin_fabsf(W.r),__builtin_fabsf(W.i)));
    #define MXS(C) mx=umax_(mx,dppu<C>(mx));
    float e8=a2*8.f, eB=__builtin_fmaf(a2,f64,14.f), p8=bp*8.f, pB=bp*f64; SCHED();
    MXS(0xB1) p8=__builtin_amdgcn_fractf(p8); pB=__builtin_amdgcn_fractf(pB); e8=__builtin_amdgcn_exp2f(e8); SCHED();
    MXS(0x4E) eB=__builtin_amdgcn_exp2f(eB); float c8=__builtin_amdgcn_cosf(p8); SCHED();
    MXS(0x141) float s8=__builtin_amdgcn_sinf(p8), cB=__builtin_amdgcn_cosf(pB); SCHED();
    float sB=__builtin_amdgcn_sinf(pB); PIN(sB); MXS(0x140) float e1=__builtin_amdgcn_exp2f(a2); PIN(e1); SCHED();
    asm volatile("s_mov_b64 exec, 0x10001\n\tds_max_u32 %0, %1\n\tds_add_u32 %0, %2 offset:4\n\ts_mov_b64 exec, -1" :: "v"(te),"v"(mx),"v"(one) : "memory");
    C3 z8; z8.r=e8*c8; z8.n=e8*s8; z8.i=-z8.n; PIN(z8.i); C2 B; B.r=eB*cB; B.i=-(eB*sB);
    float p1=__builtin_amdgcn_fractf(bp), pE=__builtin_amdgcn_fractf(bp*256.f), pV=__builtin_amdgcn_fractf(bp*f128), eE=a2*256.f, eV=a2*f128, tr[8];
    #pragma unroll
    for(int k=0;k<4;k++){ SCHED(); u32 h0=ph(B); B=cm(B,z8);
      if(k==0){ tr[0]=__builtin_amdgcn_cosf(pV); tr[1]=__builtin_amdgcn_sinf(pV); } if(k==1){ tr[2]=__builtin_amdgcn_exp2f(eV); tr[3]=__builtin_amdgcn_cosf(p1); }
      if(k==2){ tr[4]=__builtin_amdgcn_sinf(p1); tr[5]=__builtin_amdgcn_exp2f(eE); } if(k==3){ tr[6]=__builtin_amdgcn_cosf(pE); tr[7]=__builtin_amdgcn_sinf(pE); }
      PIN(tr[2*k]); PIN(tr[2*k+1]);
      u32 h1=ph(B); if(k<3) B=cm(B,z8); W2(tB[k],h0,h1); }
    asm volatile("ds_read_b64 v[118:119], %0" :: "v"(te) : "memory");
    SCHED();
    C3 z1=c3(C2{e1*tr[3],e1*tr[4]}), E=c3(C2{tr[5]*tr[6],tr[5]*tr[7]}); C2 V[8]; V[0]=cmul(W,C2{tr[2]*tr[0],tr[2]*tr[1]});
    { u32 cnt,n; PIN(V[0].r); PIN(V[0].i); PIN(z1.r); PIN(E.r);
      asm volatile("s_mov_b32 %1, 0x20000\n1:\n\ts_waitcnt lgkmcnt(0)\n\tv_readfirstlane_b32 %2, v119\n\tv_readfirstlane_b32 %0, v118\n\ts_cmp_eq_u32 %2, 8\n\ts_cbranch_scc1 2f\n\tds_read_b64 v[118:119], %3\n\ts_sub_u32 %1, %1, 1\n\ts_cbranch_scc0 1b\n2:"
        : "=&s"(emax),"=&s"(cnt),"=&s"(n) : "v"(te) : "memory","scc");
      emax=RFL(emax); emax=(emax>>23)-126; }
    if(role==3) asm volatile("s_mov_b64 exec, 1\n\tglobal_store_dwordx2 %0, %1, %2 sc1\n\ts_mov_b64 exec, -1" :: "v"(W_FL+d*8),"v"(tag<<32|(u32)(emax+10-YSH)),"s"(ws) : "memory");
    V[0].r=__builtin_amdgcn_ldexpf(V[0].r,14-emax); V[0].i=__builtin_amdgcn_ldexpf(V[0].i,14-emax);
    C2 Y=cm(V[0],E), Y4; u32 y0=ph(Y);
    V[1]=cm(V[0],z1); W2(t8[0],ph(V[0]),ph(V[1])); Y=cm(Y,E); u32 y1=ph(Y);
    V[2]=cm(V[1],z1); Y=cm(Y,E); u32 y2=ph(Y);
    V[3]=cm(V[2],z1); W2(t8[1],ph(V[2]),ph(V[3])); W2(t4[0],y0,ph(cm(V[2],E)));
    V[4]=cm(V[3],z1); Y=cm(Y,E); ST(u32,t1)=ph(Y);
    V[5]=cm(V[4],z1); W2(t8[2],ph(V[4]),ph(V[5])); Y4=cm(V[4],E); u32 y4=ph(Y4);
    V[6]=cm(V[5],z1); Y=cm(Y,E); ST(u32,t1+2*TROW)=ph(Y);
    V[7]=cm(V[6],z1); W2(t8[3],ph(V[6]),ph(V[7])); Y4=cm(Y4,E); W2(t2[0],y1,ph(Y4));
    W2(t4[1],y4,ph(cm(V[6],E))); Y=cm(Y,E); ST(u32,t1+4*TROW)=ph(Y);
    Y4=cm(Y4,E); W2(t2[1],y2,ph(Y4)); Y=cm(Y,E); ST(u32,t1+6*TROW)=ph(Y);
    Y=cm(Y,E); asm volatile("s_mov_b32 exec_hi, 0\n\tds_write_b32 %0, %1 offset:4352\n\ts_mov_b32 exec_hi, -1" :: "v"(t1),"v"(ph(Y)) : "memory");
  }
  if(BID<64){ if(grp==0) PRIO(PS&3); if(grp==1) PRIO((PS>>4)&3); if(grp==2) PRIO((PS>>8)&3); }
  gsync(sm,C_G+grp,4);
  if(role==0) chan2<0>(sm,cb,fa,fb,grp,d,one_,emax,tag,ws);
  if(role==1) chan2<1>(sm,cb,fa,fb,grp,d,one_,emax,tag,ws);
  if(role==2) chan2<2>(sm,cb,fa,fb,grp,d,one_,emax,tag,ws);
  chan2<3>(sm,cb,fa,fb,grp,d,one_,emax,tag,ws);
}

#define GTC(r,s,c) "v_cmp_gt_u32 %[" #s "], v" #r ", " c "\n\t"
#define GTA(s) "v_addc_co_u32 %[rk], vcc, 0, %[rk], %[" #s "]\n\t"
#define GT0(s) "v_cndmask_b32 %[rk], 0, 1, %[" #s "]\n\t"
#define GT4(a,b,c,d,k,F) GTC(a,s0,k) GTC(b,s1,k) GTC(c,s2,k) GTC(d,s3,k) F(s0) GTA(s1) GTA(s2) GTA(s3)
#define GTS [s0]"=&s"(q0),[s1]"=&s"(q1),[s2]"=&s"(q2),[s3]"=&s"(q3)
DEV void top8(lch* sm,int w4,int ln,u32 M,char* ws,u32& rank_,u32& key_){
  u32 k[8], hm=0xffff0000u, mx=1+w4*4+(ln>>4), one=1, cw=O_CND+w4*32; u64 em=0x0001000100010001ull,q0,q1,q2,q3;
  u32x4 rs=mkrs(ws+W_RG+(BID&(NRG-1))*8192,8192); int ro=w4*1024+ln*16;
  #pragma unroll
  for(int e=0;e<8;e++){ k[e]=(u32)(0xfff-(w4*256+ln*4+(e&3)+(e>>2)*1024)); PIN(k[e]); }
  lch *aW=sm+O_RMX+(w4*4+(ln>>4))*4, *aC=sm+O_RMX+(ln>>2)*4, *aO=sm+O_RMX+(ln&3)*16, *dC=sm+O_CND+(ln>>1)*4, *dO=sm+O_CND+(ln&1)*64, *cW=sm+C_W*4; PIN(aW); PIN(aC); PIN(aO); PIN(dC); PIN(dO); PIN(ro); PIN(ln); PIN(mx); PIN(one); PIN(cW);
  asm volatile("" : "+s"(hm),"+s"(em),"+s"(rs),"+s"(cw));
  PRIO(3);
  gwait<2>(sm,C_G,RGATE);
  if(RD) __builtin_amdgcn_s_sleep(RD);
  { u32 cnt,t0,t1;
    #define KL(e,r) "v_lshl_or_b32 %[k" #e "], v" #r ", 16, %[k" #e "]\n\t"
    #define KH(e,r) "v_and_or_b32 %[k" #e "], v" #r ", %[hm], %[k" #e "]\n\t"
    #define CHK_R "v_bitop3_b32 %[t0], v113, v115, %[M] bitop3:0x7e\n\tv_bitop3_b32 %[t1], v117, v119, %[M] bitop3:0x7e\n\tv_or_b32 %[t0], %[t0], %[t1]\n\tv_cmp_ne_u32 vcc, 0, %[t0]\n\t"
    #define MXD(c) "v_max_u32_dpp %[mx], %[mx], %[mx] " c " row_mask:0xf bank_mask:0xf\n\t"
    asm volatile(LD_DUP2
      "s_sleep " STR(R2D) "\n\tbuffer_load_dwordx4 v[120:123], %[of], %[rs], 0 offen sc1\n\tbuffer_load_dwordx4 v[124:127], %[of], %[rs], %[so] offen sc1\n\t"
      "s_mov_b64 exec, 1\n\tds_add_u32 %[cw], %[one]\n\ts_mov_b64 exec, -1\n\ts_mov_b32 %[cnt], 0x20000\n\ts_waitcnt vmcnt(2)\n\t" CHK_R "s_cbranch_vccz 9f\n\t"
      "s_waitcnt vmcnt(0)\n\tv_mov_b64 v[112:113], v[120:121]\n\tv_mov_b64 v[114:115], v[122:123]\n\tv_mov_b64 v[116:117], v[124:125]\n\tv_mov_b64 v[118:119], v[126:127]\n\t"
      POLLASM(LD_DUP2,CHK_R,PSLP)
      "v_pk_max_u16 %[t0], v112, v114\n\tv_pk_max_u16 %[t1], v116, v118\n\tv_pk_max_u16 %[t0], %[t0], %[t1]\n\t"
      "v_max_u16_sdwa %[mx], %[t0], %[t0] dst_sel:WORD_1 dst_unused:UNUSED_PRESERVE src0_sel:WORD_0 src1_sel:WORD_1\n\t"
      KL(0,112) KH(1,112) MXD("quad_perm:[1,0,3,2]") KL(2,114) KH(3,114) MXD("quad_perm:[2,3,0,1]") KL(4,116) KH(5,116) MXD("row_half_mirror") KL(6,118) KH(7,118) MXD("row_mirror")
      "s_mov_b64 exec, %[em]\n\tds_write_b32 %[aW], %[mx]\n\ts_mov_b64 exec, -1\n\tds_read_b32 v116, %[aC]\n\tds_read_b128 v[112:115], %[aO]"
      : [k0]"+v"(k[0]),[k1]"+v"(k[1]),[k2]"+v"(k[2]),[k3]"+v"(k[3]),[k4]"+v"(k[4]),[k5]"+v"(k[5]),[k6]"+v"(k[6]),[k7]"+v"(k[7]),[mx]"+v"(mx),[cnt]"=&s"(cnt),[t0]"=&v"(t0),[t1]"=&v"(t1)
      : [of]"v"(ro),[rs]"s"(rs),[so]"s"(4096),[M]"s"(M),[hm]"s"(hm),[cw]"v"(cW),[one]"v"(one),[em]"s"(em),[aW]"v"(aW),[aC]"v"(aC),[aO]"v"(aO) : PCLOB); }
  u32 slow;
  { u32 rk,cnt,T,h,l,t,n;
    #define INS(e) "v_med3_u32 %[t], %[t], %[l], %[k" #e "]\n\tv_med3_u32 %[l], %[h], %[l], %[k" #e "]\n\tv_max_u32 %[h], %[h], %[k" #e "]\n\t"
    asm volatile("s_branch 7f\n"
      "3:\n\tv_cmp_le_u32 %[s2], %[T], %[t]\n\ts_add_u32 %[cnt], %[cnt], %[n]\n\ts_cmp_gt_u32 %[cnt], 8\n\ts_cbranch_scc1 5f\n\ts_cmp_lg_u64 %[s2], 0\n\ts_cbranch_scc1 5f\n\t"
      "s_mov_b64 exec, %[s1]\n\tv_mov_b32 %[t], %[n]\n\tv_mbcnt_lo_u32_b32 %[t], exec_lo, %[t]\n\tv_mbcnt_hi_u32_b32 %[t], exec_hi, %[t]\n\tv_lshl_add_u32 %[t], %[t], 2, %[cw]\n\tds_write_b32 %[t], %[l]\n\ts_mov_b64 exec, -1\n\ts_branch 4f\n"
      "5:\n\ts_mov_b32 %[sl], 1\n\ts_branch 6f\n7:\n\t"
      "v_max3_u32 %[h], %[k0], %[k1], %[k2]\n\tv_med3_u32 %[l], %[k0], %[k1], %[k2]\n\tv_min3_u32 %[t], %[k0], %[k1], %[k2]\n\t" INS(3) INS(4) INS(5) INS(6) INS(7)
      "s_mov_b32 %[cnt], 0x20000\n1:\n\ts_waitcnt lgkmcnt(0)\n\tv_cmp_eq_u32 vcc, 0, v116\n\ts_cbranch_vccz 2f\n\tds_read_b32 v116, %[aC]\n\tds_read_b128 v[112:115], %[aO]\n\ts_sub_u32 %[cnt], %[cnt], 1\n\ts_cbranch_scc0 1b\n2:\n\t"
      GT4(112,113,114,115,"v116",GT0) "v_and_b32 v116, %[hm], v116\n\ts_nop 0\n\tv_add_u32_dpp %[rk], %[rk], %[rk] quad_perm:[1,0,3,2] row_mask:0xf bank_mask:0xf\n\ts_mov_b32 %[sl], 0\n\ts_nop 0\n\tv_add_u32_dpp %[rk], %[rk], %[rk] quad_perm:[2,3,0,1] row_mask:0xf bank_mask:0xf\n\t"
      "v_cmp_eq_u32 vcc, 7, %[rk]\n\ts_ff1_i32_b64 %[cnt], vcc\n\ts_nop 0\n\tv_readlane_b32 %[T], v116, %[cnt]\n\ts_nop 1\n\t"
      "v_cmp_le_u32 vcc, %[T], %[h]\n\tv_cmp_le_u32 %[s1], %[T], %[l]\n\ts_bcnt1_i32_b64 %[n], vcc\n\tv_mbcnt_lo_u32_b32 %[rk], vcc_lo, 0\n\tv_mbcnt_hi_u32_b32 %[rk], vcc_hi, %[rk]\n\ts_bcnt1_i32_b64 %[cnt], %[s1]\n\ts_cbranch_scc1 3b\n\t"
      "s_cmp_gt_u32 %[n], 8\n\ts_cbranch_scc1 5b\n"
      "4:\n\tv_lshl_add_u32 %[rk], %[rk], 2, %[cw]\n\ts_mov_b64 exec, vcc\n\tds_write_b32 %[rk], %[h]\n\ts_mov_b64 exec, 1\n\tds_add_u32 %[cW], %[one] offset:4\n\ts_mov_b64 exec, -1\n6:"
      : [rk]"=&v"(rk),[cnt]"=&s"(cnt),[T]"=&s"(T),[h]"=&v"(h),[l]"=&v"(l),[t]"=&v"(t),[n]"=&s"(n),[sl]"=&s"(slow),GTS
      : [k0]"v"(k[0]),[k1]"v"(k[1]),[k2]"v"(k[2]),[k3]"v"(k[3]),[k4]"v"(k[4]),[k5]"v"(k[5]),[k6]"v"(k[6]),[k7]"v"(k[7]),[aC]"v"(aC),[aO]"v"(aO),[hm]"s"(hm),[cw]"s"(cw),[cW]"v"(cW),[one]"v"(one) : PCLOB); }
  if(__builtin_expect(RFL(slow)!=0,0)){
    u32 mine=0;
    _Pragma("unroll 1") for(int c=0;c<8;c++){ u32 lm=umax_(umax_(umax_(k[0],k[1]),umax_(k[2],k[3])),umax_(umax_(k[4],k[5]),umax_(k[6],k[7]))); u32 wm=wmax(lm); if(ln==c) mine=wm;
      #pragma unroll
      for(int e=0;e<8;e++) k[e]=(k[e]==wm)?0u:k[e]; }
    if(ln<8) ST(u32,sm+cw+ln*4)=mine;
    garrive(sm,C_W+1); }
  { u32 c,rk,cnt;
    asm volatile("s_mov_b32 %[cnt], 0x20000\n1:\n\tds_read_b32 %[rk], %[cW] offset:4\n\tds_read_b32 %[c], %[dC]\n\tds_read_b128 v[96:99], %[dO]\n\tds_read_b128 v[100:103], %[dO] offset:16\n\tds_read_b128 v[104:107], %[dO] offset:32\n\tds_read_b128 v[108:111], %[dO] offset:48\n\t"
      "s_waitcnt lgkmcnt(5)\n\tv_cmp_gt_u32 vcc, 4, %[rk]\n\ts_cbranch_vccz 2f\n\ts_sub_u32 %[cnt], %[cnt], 1\n\ts_cbranch_scc0 1b\n2:\n\t"
      "s_waitcnt lgkmcnt(3)\n\t" GT4(96,97,98,99,"%[c]",GT0) "s_waitcnt lgkmcnt(2)\n\t" GT4(100,101,102,103,"%[c]",GTA) "s_waitcnt lgkmcnt(1)\n\t" GT4(104,105,106,107,"%[c]",GTA) "s_waitcnt lgkmcnt(0)\n\t" GT4(108,109,110,111,"%[c]",GTA)
      "s_nop 1\n\tv_add_u32_dpp %[rk], %[rk], %[rk] quad_perm:[1,0,3,2] row_mask:0xf bank_mask:0xf"
      : [c]"=&v"(c),[rk]"=&v"(rk),[cnt]"=&s"(cnt),GTS : [dC]"v"(dC),[dO]"v"(dO),[cW]"v"(cW)
      : PCLOB,"v96","v97","v98","v99","v100","v101","v102","v103","v104","v105","v106","v107","v108","v109","v110","v111");
    rank_=rk; key_=c; }
}

#define FILL_(n,WAIT) "s_mov_b32 m0, %[ld]\n\ts_mov_b32 %[c], " STR(n) "-1\n3:\n\t" WAIT "buffer_load_dwordx4 %[vo], %[rs], %[so] offen nt lds\n\ts_add_u32 m0, m0, 0x1000\n\ts_add_u32 %[so], %[so], %[st]\n\t" \
  "s_sub_u32 %[c], %[c], 1\n\ts_cbranch_scc0 3b\n\ts_mov_b32 %[ld], m0\n\t"
#define FILL(n,WAIT) do{ u32 c_; asm volatile(FILL_(n,WAIT) : [c]"=&s"(c_),[so]"+s"(wso),[ld]"+s"(wld) : [vo]"v"(wvo),[rs]"s"(wrs),[st]"s"(wst) : "memory","scc","m0"); }while(0)
#define FILLD(n,WN,WW_,STK) do{ u32 c_,t_,s_=0; asm volatile(FILL_(n,STK "ds_read_b32 %[t], %[ad]\n\ts_waitcnt lgkmcnt(0)\n\tv_readfirstlane_b32 %[s], %[t]\n\ts_cmp_ge_u32 %[s], 4\n\ts_cbranch_scc1 4f\n\t" VW(WN) "4:\n\t" VW(WW_)) \
  : [c]"=&s"(c_),[so]"+s"(wso),[ld]"+s"(wld),[t]"=&v"(t_),[s]"+s"(s_) : [vo]"v"(wvo),[rs]"s"(wrs),[st]"s"(wst),[ad]"v"(C_WIN*4) : "memory","scc","m0"); wso=RFL(wso); wld=RFL(wld); }while(0)
#define STKB "s_cmp_ge_u32 %[s], 4\n\ts_cbranch_scc1 4f\n\t"

__global__ NOPK __attribute__((amdgpu_num_vgpr(56),amdgpu_num_sgpr(102))) __launch_bounds__(1024,4) void kA(const u16* x_,char* ws_,const u16* wr_,const u16* Dsk_,const u16* wg_,const u16* wu_,const u16* wd_,u16* out_,KP kp){
  __shared__ __attribute__((aligned(16))) char smem[LDS_TOTAL];
  lch* sm=(lch*)smem;
  u32x4 xa,xb_,wa,wb,dq[3]; u32 xc,wc; u64 p_[7];
  asm volatile(
    "s_load_dwordx16 s[64:79], %[ka], 0x0\n\ts_load_dwordx16 s[80:95], %[ka], 0x40\n\t"
    "v_readfirstlane_b32 s60, %[t]\n\ts_and_b32 s61, %[wg], 7\n\ts_lshr_b32 s62, %[wg], 3\n\tv_and_b32 v118, 31, %[t]\n\ts_bfe_u32 s63, s60, 0x20006\n\ts_cmpk_ge_u32 s60, 0x100\n\ts_cbranch_scc1 1f\n\t"
    "s_setprio 3\n\ts_lshl_b32 s56, s61, 5\n\ts_add_u32 s56, s56, s62\n\ts_mulk_i32 s56, 0x2400\n\tv_lshrrev_b32 v119, 5, %[t]\n\tv_mul_u32_u24 v119, 0x480, v119\n\tv_add_u32 v119, s56, v119\n\t"
    "v_lshl_add_u32 v116, v118, 4, v119\n\tv_lshl_add_u32 v117, v118, 2, v119\n\tv_lshlrev_b32 v114, 4, v118\n\tv_lshlrev_b32 v115, 2, v118\n\t"
    "v_lshlrev_b32 v119, 1, %[t]\n\tv_mov_b32 v118, 0\n\tds_write_b16 v119, v118\n\t"
    "s_waitcnt lgkmcnt(0)\n\t"
    "s_load_dwordx2 s[96:97], s[66:67], 0x0\n\t"
    "global_load_dwordx4 %[wa], v114, s[68:69]\n\tglobal_load_dwordx4 %[wb], v114, s[68:69] offset:512\n\tglobal_load_dword %[wc], v115, s[68:69] offset:1024\n\t"
    "global_load_dwordx4 %[xa], v116, s[64:65]\n\tglobal_load_dwordx4 %[xb], v116, s[64:65] offset:512\n\tglobal_load_dword %[xc], v117, s[64:65] offset:1024\n\t"
    "s_barrier\n\t"
    "s_branch 8f\n"
    "1:\n\ts_cmp_ge_u32 s62, " STR(CPF) "\n\ts_cbranch_scc1 6f\n\ts_getpc_b64 s[56:57]\n.Lkpc:\n\ts_and_b32 s58, s62, 3\n\ts_lshl_b32 s58, s58, 13\n\ts_sub_u32 s58, s58, 0x1000\n\tv_lshl_add_u32 v119, %[t], 4, s58\n\tv_min_u32 v119, .Lkend-.Lkpc-16, v119\n\t"
    "global_load_dwordx4 v[120:123], v119, s[56:57]\n\t.subsection 1\n.Lkend:\n\t.subsection 0\n6:\n\t"
    "v_lshlrev_b32 v119, 4, %[t]\n\tv_and_b32 v119, 0xff0, v119\n\t"
    "s_lshr_b32 s59, s60, 8\n\ts_cmp_lt_u32 s62, 8\n\ts_cselect_b32 s58, 2, 1\n\ts_cbranch_scc1 2f\n\ts_cmp_eq_u32 s59, 3\n\ts_cbranch_scc1 4f\n"
    "2:\n\ts_sub_u32 s57, s59, 2\n\ts_cmp_eq_u32 s59, 1\n\ts_cselect_b32 s58, s58, s57\n\ts_cmp_lt_u32 s62, 8\n\ts_cselect_b32 s56, 0, 8\n\ts_lshl1_add_u32 s56, s62, s56\n\ts_mulk_i32 s61, 72\n\ts_add_u32 s51, s56, s61\n\ts_sub_u32 s56, 2, s59\n\ts_sub_u32 s57, 16, s62\n\ts_cmp_eq_u32 s59, 3\n\ts_cselect_b32 s50, s57, s56\n\ts_add_u32 s56, s50, s51\n\t"
    "v_mov_b32 v112, 0\n\tv_mov_b32 v113, 0\n\tv_mov_b32 v114, 0\n\tv_mov_b32 v115, 0\n\tv_mov_b32 v116, 0\n\tv_mov_b32 v117, 0\n\ts_setprio 2\n\t"
    "s_lshl_b32 s57, s56, 2\n\ts_add_u32 s57, s57, s63\n\ts_lshl_b32 s57, s57, 6\n\tv_lshl_add_u32 v22, v118, 1, s57\n\ts_lshl_b32 s56, s56, 1\n\ts_and_b32 s56, s56, -4\n\t"
    "s_waitcnt lgkmcnt(0)\n\t"
    "global_load_short_d16_hi v112, v22, s[80:81] nt\n\tglobal_load_short_d16_hi v113, v22, s[82:83] nt\n\tglobal_load_short_d16_hi v114, v22, s[84:85] nt\n\t"
    "global_load_short_d16_hi v115, v22, s[86:87] nt\n\tglobal_load_short_d16_hi v116, v22, s[88:89] nt\n\tglobal_load_short_d16_hi v117, v22, s[90:91] nt\n\t"
    "s_load_dwordx2 s[96:97], s[66:67], 0x0\n\ts_load_dword s98, s[92:93], s56\n\t"
    "s_barrier\n\t"
    "s_mul_i32 s52, s58, %[sl]\n\ts_add_u32 s52, s52, %[tb]\n\ts_lshl_b32 s53, s63, 7\n\ts_add_u32 s53, s53, s52\n\tv_lshl_add_u32 v30, v118, 2, s53\n\tv_bfe_u32 v31, %[t], 5, 1\n\ts_movk_i32 s54, 544\n\tv_mul_u32_u24 v29, s54, v31\n\t"
    "v_lshl_add_u32 v2, v29, 3, v30\n\ts_movk_i32 s53, 1088\n\tv_add_u32 v3, s53, v2\n\tv_add_u32 v4, s53, v3\n\tv_add_u32 v5, s53, v4\n\tv_add_u32 v6, 41*544, v2\n\tv_add_u32 v7, s53, v6\n\tv_add_u32 v8, s53, v7\n\tv_add_u32 v9, s53, v8\n\t"
    "v_lshl_add_u32 v10, v29, 2, v30\n\tv_add_u32 v10, 16*544, v10\n\tv_add_u32 v11, s53, v10\n\tv_lshl_add_u32 v12, v29, 1, v30\n\tv_add_u32 v12, 24*544, v12\n\tv_add_u32 v13, 2176, v12\n\tv_add_u32 v14, v29, v30\n\tv_add_u32 v14, 32*544, v14\n\t"
    "s_lshl_b32 s53, s58, 4\n\ts_add_u32 s53, s53, %[ex]\n\tv_mov_b32 v15, s53\n\tv_cvt_f32_ubyte0 v31, v31\n\tv_mul_f32 v16, 0x42800000, v31\n\tv_add_f32 v17, v16, v16\n\tv_mov_b32 v20, 1\n\t"
    "v_and_b32 v29, 15, %[t]\n\tv_and_b32 v30, 48, %[t]\n\tv_mad_u32_u24 v30, v29, s54, v30\n\ts_lshl_b32 s55, s59, 1\n\ts_lshr_b32 s55, 0x48, s55\n\ts_add_u32 s55, s55, s63\n\ts_and_b32 s55, s55, 3\n\ts_mul_i32 s53, s55, 16*544\n\ts_movk_i32 s57, 41*544\n\ts_cmp_eq_u32 s55, 2\n\ts_cselect_b32 s53, s57, s53\n\ts_cselect_b32 s57, 32*544, s57\n\t"
    "s_add_u32 s53, s53, s52\n\ts_add_u32 s57, s57, s52\n\tv_add_u32 v18, s53, v30\n\tv_add_u32 v19, s57, v30\n\t"
    "v_min_u32 v29, 237*16, v119\n\tv_subrev_u32 v30, 204*16, v29\n\tv_add_u32 v29, %[de], v29\n\tv_min_u32 v29, v29, v30\n\ts_sub_u32 s53, s52, 4640\n\tv_add_u32 v29, s53, v29\n\tv_mov_b32 v24, 0\n\tv_mov_b32 v25, 0\n\tv_mov_b32 v26, 0\n\tv_mov_b32 v27, 0\n\tds_write_b128 v29, v[24:27]\n\t"
    "s_cmp_lg_u32 s55, 3\n\ts_cbranch_scc1 9f\n\t"
    "v_bfe_u32 v18, %[t], 1, 5\n\tv_and_b32 v19, 1, %[t]\n\ts_cmp_lt_u32 s62, 8\n\ts_cselect_b32 s53, 2, 1\n\ts_mul_i32 s53, s53, %[sl]\n\tv_mul_u32_u24 v21, s53, v19\n\ts_sub_u32 s55, 2, s59\n\ts_lshl_b32 s55, s55, 7\n\tv_add_u32 v18, s55, v18\n\ts_cmp_lg_u32 s59, 3\n\ts_cbranch_scc1 7f\n\t"
    "v_bfe_u32 v18, %[t], 5, 1\n\tv_lshl_add_u32 v18, v18, 7, v118\n\tv_mov_b32 v19, s50\n\tv_mov_b32 v21, %[sl]\n"
    "7:\n\tv_add_u32 v19, s51, v19\n\tv_lshl_add_u32 v21, v18, 4, v21\n\tv_lshrrev_b32 v24, 3-" STR(TL) ", v19\n\tv_and_b32 v19, 7>>" STR(TL) ", v19\n\tv_lshlrev_b32 v19, 4, v19\n\tv_lshl_add_u32 v19, v24, 7, v19\n\t"
    "v_and_b32 v24, (1<<" STR(TL) ")-1, v18\n\tv_lshl_add_u32 v19, v24, 7-" STR(TL) ", v19\n\tv_lshrrev_b32 v18, " STR(TL) ", v18\n\tv_mul_u32_u24 v18, %[ust], v18\n\tv_add_u32 v18, v18, v19\n\tv_add_u32 v19, 512, v21\n\t"
    "s_branch 9f\n"
    "4:\n\t"
    "v_bfe_u32 v118, %[t], 3, 5\n\tv_lshrrev_b32 v117, 1, v118\n\tv_and_b32 v117, 4, v117\n\tv_and_b32 v116, 2, v118\n\tv_and_b32 v115, 7, %[t]\n\tv_or_b32 v116, v116, v117\n\tv_xor_b32 v115, v115, v116\n\tv_lshlrev_b32 v115, 4, v115\n\t"
    "s_mul_i32 s54, s61, 9\n\ts_sub_u32 s55, s62, 14\n\ts_lshr_b32 s55, s55, 1\n\ts_add_u32 s54, s54, s55\n\ts_lshl_b32 s54, s54, 7\n\t"
    "s_mul_i32 s52, s61, 0xa2000\n\ts_mul_i32 s53, s62, 0x180\n\ts_add_u32 s52, s52, s53\n\ts_cmp_ge_u32 s62, 11\n\ts_cselect_b32 s53, 0x51000-0x480, 0\n\ts_add_u32 s52, s52, s53\n\ts_sub_u32 s52, s52, 0xc00\n\t"
    "s_movk_i32 s55, 0x480\n\ts_cmp_ge_u32 s62, 14\n\ts_cselect_b32 s54, s54, s52\n\ts_cselect_b32 s55, 0x2400, s55\n\tv_mad_u32_u24 v118, v118, s55, v115\n\ts_lshl_b32 s55, s55, 5\n\t"
    "s_lshl_b32 s52, s63, 10\n\ts_add_u32 m0, s52, %[r2]\n\ts_mov_b32 s58, 0x510000\n\ts_mov_b32 s59, 0x20000\n\ts_cmp_ge_u32 s62, 14\n\ts_cselect_b32 s53, " STR(PRE1) "-1, " STR(PRE2) "-1\n\ts_setprio 3\n\t"
    "s_waitcnt lgkmcnt(0)\n\t"
    "s_bitcmp1_b32 %[wg], 3\n\ts_cselect_b64 s[56:57], s[74:75], s[72:73]\n\ts_cmp_ge_u32 s62, 14\n\ts_cselect_b64 s[56:57], s[56:57], s[76:77]\n\ts_cselect_b32 s52, 0x180, -1\n\ts_and_b32 s57, s57, 0xffff\n\ts_add_u32 s60, s56, s54\n\ts_and_b32 s60, s60, 0x380\n\ts_cmp_eq_u32 s60, s52\n\ts_cselect_b32 s53, " STR(PRE3) "-1, s53\n\t"
    "s_load_dwordx2 s[96:97], s[66:67], 0x0\n\t"
    "s_barrier\n\t"
    "s_sleep " STR(PD) "\n\t"
    "\n3:\n\t" "buffer_load_dwordx4 v118, s[56:59], s54 offen nt lds\n\ts_add_u32 m0, m0, 0x1000\n\ts_add_u32 s54, s54, s55\n\ts_sub_u32 s53, s53, 1\n\ts_cbranch_scc0 3b\n"
    "9:\n8:"
    : [xa]"=&{v[2:5]}"(xa),[xb]"=&{v[6:9]}"(xb_),[xc]"=&{v30}"(xc),[wa]"=&{v[10:13]}"(wa),[wb]"=&{v[14:17]}"(wb),[wc]"=&{v31}"(wc),[d0]"=&{v[18:21]}"(dq[0]),[d1]"=&{v[22:25]}"(dq[1]),[d2]"=&{v[26:29]}"(dq[2]),
      "=&{s[64:65]}"(p_[0]),"=&{s[66:67]}"(p_[1]),"=&{s[70:71]}"(p_[2]),"=&{s[72:73]}"(p_[3]),"=&{s[74:75]}"(p_[4]),"=&{s[76:77]}"(p_[5]),"=&{s[78:79]}"(p_[6])
    : [ka]"s"(__builtin_amdgcn_kernarg_segment_ptr()),[wg]"s"(BID),[t]"v"(TID),[r2]"i"(O_R2),[sl]"i"(SLSZ),[tb]"i"(O_CH+O_TB),[ex]"i"(O_EXP),[de]"i"(O_DE),[ust]"i"(GST_)
    : "memory","scc","m0","v127","s50","s51","s52","s53","s54","s55","s56","s57","s58","s59","s60","s61","s62","s63","s68","s69",
      "s80","s81","s82","s83","s84","s85","s86","s87","s88","s89","s90","s91","s92","s93","s94","s95","s99");
  SCHED();
  #define GP(T,i) ((T*)(GAS T*)p_[i])
  const u16 *x=GP(const u16,0), *Dsk=GP(const u16,2), *wg=GP(const u16,3), *wu=GP(const u16,4), *wd=GP(const u16,5); char* ws=GP(char,1); u16* out=GP(u16,6);
  int wv=RFL(TID>>6), bid=BID, q4=wv>>2, w4=wv&3;
  int xg=bid&7, lb=bid>>3, tb=32*xg+lb, nch=2+(lb<8);
  u64 ep; u32 ldw;
  {
  }
  #define KILLIN "=v"(xa),"=v"(xb_),"=v"(xc),"=v"(wa),"=v"(wb),"=v"(wc),"=v"(dq[0]),"=v"(dq[1]),"=v"(dq[2])
  if(__builtin_expect(q4==3&&lb>=8,0)){
    int fu=18*xg+lb-14, kq=2*xg+(lb>=11), cgp=lb-8-3*(lb>=11);
  u32 wst= lb>=14? F_*64:D_*64;
  u32 wso=(lb>=14? (fu>>1)*128 : kq*(288*D_*2)+384*cgp)+(lb>=14? PRE1:PRE2)*wst, wld=(u32)(size_t)sm+O_R2+w4*1024+(lb>=14? PRE1:PRE2)*4096; u32x4 wrs=mkrs(lb>=14? ((fu&1)? wu:wg):wd,D_*F_*2);
  GETEP
  u64 tag=ep+1ull;
  #define FLANE(RS) LANE(ln); int g=ln>>4, li=ln&15, qq=li>>2, pq=li&3; u32 wvo=(u32)(8*w4+(ln>>3))*(RS)+(u32)((ln&7)^(((ln>>3)&2)|((w4&1)<<2)))*16u; \
    const lch* tp=sm+O_R2+(8*g+qq)*128+((w4^((qq>>1)|((g&1)<<1)))*32)+pq*8;
  #define WFR(img,ks) trfrag(tp+(img)*CHSZ+(ks)*4096,tp+(img)*CHSZ+(ks)*4096+512)
  const u32x4 I4={INV,INV,INV,INV};
  if(lb>=14){
    FLANE(F_*2)
    int mt=fu&1;
    const lch* xp=sm+O_TILE+li*HSTR+g*16; int of=(mt*288+(fu>>1)*4+w4)*256+(li&7)*32+g*8+(li>>3)*4;
    const lch* mp=sm+O_RC+(li&7)*4;
    asm volatile("" : "+v"(xp),"+v"(of),"+v"(mp));
    rsrc_t rsG=RSRC(ws+W_GU,2*8*F_*2);
    if(w4==0) BST128(I4,rsG,(mt*288+(fu>>1)*4)*256+ln*16,0,SC1);
    bool slow=(((u32)wrs[0]+wso)&0x380u)==0x180u;
    if(slow){ wso-=(PRE1-PRE3)*wst; wld-=(PRE1-PRE3)*4096; FILL(5+PRE1-PRE3,VW(W3)); } else FILLD(5,W1,W1B,"");
    if(NA){ VMW(11-NA); garrive(sm,C_P); }
    if(w4==0){ VMW(5); if(ln==0) GST(ws+W_FL+(576+fu)*8,tag); }
    if(slow) FILLD(13-PRE1,W3,W1B,STKB); else FILLD(13-PRE1,W1,W1B,STKB);
    bf16x8 fB[18]; f32x4 cs={0,0,0,0}; const bf16x8 on=BC(bf16x8,(u32x4{0x3f803f80u,0x3f803f80u,0x3f803f80u,0x3f803f80u}));
    const lch* tq=tp+CHSZ; PIN(tq);
    #define RDF(a,b) _Pragma("unroll") for(int ks=a;ks<b;ks++) fB[ks]= ks<9? WFR(0,ks) : trfrag(tq+(ks-9)*4096,tq+(ks-9)*4096+512);
    #define CSF(a,b) SCHED(); _Pragma("unroll") for(int ks=a;ks<b;ks++) cs=__builtin_amdgcn_mfma_f32_16x16x32_bf16(fB[ks],on,cs,0,0,0); SCHED();
    gwait<0>(sm,C_P,4); RDF(0,NA)
    VMW(18-NB); garrive(sm,C_P); CSF(0,NA) gwait<0>(sm,C_P,8); RDF(NA,NB)
    VMW(LAST1); garrive(sm,C_P); CSF(NA,NB) gwait<0>(sm,C_P,12); RDF(NB,18-LAST1)
    DRAIN(); garrive(sm,C_P); CSF(NB,18-LAST1) gwait<0>(sm,C_P,16);
    float cA,cB,ri,ce,p0,p1,q0,q1; u32 mdw;
    { bf16x8 xb[9];
      RDF(18-LAST1,18) SCHED();
      u32 cx=*(const volatile LAS u32*)(sm+C_X*4);
      #pragma unroll
      for(int ks=0;ks<4;ks++) xb[ks]=*(const volatile LAS bf16x8*)(xp+ks*64);
      CSF(18-LAST1,18)
      if(RFL(cx)<4u){ gwait<XS1>(sm,C_X,XG1); gwait4<64>(C_X,4,xp,xb[0],xb[1],xb[2],xb[3]); }
      mdw=LD(u32,mp); xb[4]=LD(bf16x8,xp+256); xb[5]=LD(bf16x8,xp+320);
      SCHED();
      #pragma unroll
      for(int ks=0;ks<8;ks++){
        if(ks==0) asm volatile("v_mfma_f32_16x16x32_bf16 v[112:115], %0, %2, 0\n\tv_mfma_f32_16x16x32_bf16 v[116:119], %1, %2, 0" :: "v"(fB[0]),"v"(fB[9]),"v"(xb[0]));
        else asm volatile("v_mfma_f32_16x16x32_bf16 v[112:115], %0, %2, v[112:115]\n\tv_mfma_f32_16x16x32_bf16 v[116:119], %1, %2, v[116:119]" :: "v"(fB[ks]),"v"(fB[ks+9]),"v"(xb[ks]));
        SCHED();
        if(ks<3) xb[ks+6]=LD(bf16x8,xp+(ks+6)*64);
        if(ks==2){ PIN(cs); cA= li<8? cs[0]:cs[2]; cB= li<8? cs[1]:cs[3]; PIN(cA); PIN(cB); }
        if(ks==3){ PIN(mdw); ri=rcpf_(bhi(mdw)); float nm=-blo(mdw); cA*=nm; cB*=nm; PIN(cA); PIN(cB); } if(ks==4){ ce=ri*-1.4426950408889634f; p0=cA*ri; p1=cB*ri; PIN(p0); PIN(p1); } if(ks==5){ q0=cA*ce; q1=cB*ce; PIN(q0); PIN(q1); }
        SCHED(); }
      #define RR8 " row_ror:8 row_mask:0xf bank_mask:"
      float v0,v1,s0,s1;
      asm volatile("v_mfma_f32_16x16x32_bf16 v[112:115], %4, %6, v[112:115]\n\tv_mfma_f32_16x16x32_bf16 v[116:119], %5, %6, v[116:119]\n\ts_nop 7\n\tv_add_f32_dpp %0, v116, v112" RR8 "0x3\n\tv_add_f32_dpp %1, v117, v113" RR8 "0x3\n\tv_add_f32_dpp %0, v114, v118" RR8 "0xc\n\tv_add_f32_dpp %1, v115, v119" RR8 "0xc\n\t"
        "v_fmac_f32 %2, %0, %8\n\tv_fmac_f32 %3, %1, %8\n\tv_fma_f32 %0, %0, %7, %9\n\tv_fma_f32 %1, %1, %7, %10"
        : "=&v"(v0),"=&v"(v1),"+v"(q0),"+v"(q1) : "v"(fB[8]),"v"(fB[17]),"v"(xb[8]),"v"(ri),"v"(ce),"v"(p0),"v"(p1));
      s0=q0; s1=q1;
      if(mt==0){ v0*=rcpf_(1.f+__builtin_amdgcn_exp2f(s0)); v1*=rcpf_(1.f+__builtin_amdgcn_exp2f(s1)); }
      __builtin_amdgcn_raw_buffer_store_b32(nv(pk(v0,v1)),rsG,of,0,SC1); garrive(sm,C_H); }
    if(mt==0&&w4==0){
    LANE(l2); ln=l2; li=ln&15; g=ln>>4;
    int c9=(lb-14)>>1, tk=(xg&6)+((li>>2)&1), t=(int)LD(u16,sm+O_IDX+tk*2), c=64*c9+32*(xg&1)+16*(li>>3)+4*(li&3)+g; f32x4 pr; u32x4 ysl;
    { u32 u=LD(u16,sm+O_IDX+16+tk*2)^0x8000u, xb=LD(u16,sm+O_TILE+XAD(tk,c*2)), sv=LD(u32,sm+O_RC+tk*4); float ds=b2f(((const GAS u16*)Dsk)[c]); u^=(u32)(((int)(u<<16))>>31)&0x7fffu;
      int ye=(int)(u32)poll1<32>(ws+W_FL+c*8,tag<<32)+175-32*(int)ycls((u32)tag,t&7); ye= ye<0? 0 : ye>254? 254:ye;
      #pragma unroll
      for(int k=0;k<4;k++) ysl[k]= ((t>>1)&3)==k? (u32)ye<<(7+(t&1)*16) : 0u;
      poll1<0>(ws+W_FL+(720+(ln<48? ln:47))*8,tag);
      float xn=(b2f(xb)-blo(sv))*rcpf_(bhi(sv)); pr=f32x4{b2f(xb),xn,xn*ds,sigm_f(b2f(u))}; }
    gwait<2>(sm,C_PR,1);
    { u32x4 rs=mkrs(ws+W_PART+(xg*9+c9)*4096,4096), ry=mkrs(ws+W_Y,D_*YST);
      int yw=c*YST+(t&~7)*2, yn=(int)dppu<0xB1>((u32)yw)^16, yo=(ln&1)? yn:yw, y2=(ln&1)? yw:yn, po=ln*16;
      u32 a[4], t0,cnt; u32x4 yb; GAS u16* op=(GAS u16*)out+t*D_+c; PIN(ysl); f32x2 pe={pr[1]*-1.4426950408889634f,pr[3]*-1.4426950408889634f};
      #define LD_P "buffer_load_dwordx4 v[112:115], %[of], %[rs], 0 offen sc1\n\tbuffer_load_dwordx4 v[116:119], %[of], %[rs], 0 offen offset:1024 sc1\n\tbuffer_load_dwordx4 v[120:123], %[of], %[rs], 0 offen offset:2048 sc1\n\tbuffer_load_dwordx4 v[124:127], %[of], %[rs], 0 offen offset:3072 sc1\n\t"
      #define LD_YB "buffer_load_dwordx4 v[96:99], %[yo], %[ry], 0 offen sc1\n\tbuffer_load_dwordx4 v[100:103], %[y2], %[ry], 0 offen sc1\n\t"
      #define CK_YB UC("%[t0]",96,100,c0) UC("%[a0]",97,101,c1) UC("%[a1]",98,102,c2) UC("%[a2]",99,103,c3) "v_or3_b32 %[t0], %[t0], %[a0], %[a1]\n\tv_bitop3_b32 %[t0], %[t0], %[a2], %[mk] bitop3:0xa8\n\tv_cmp_ne_u32 %[ym], 0, %[t0]\n\t" \
        "v_cndmask_b32 v104, v96, v100, %[od]\n\tv_cndmask_b32 v105, v97, v101, %[od]\n\tv_cndmask_b32 v106, v98, v102, %[od]\n\tv_cndmask_b32 v107, v99, v103, %[od]\n\t"
      #define AD4(o,a,b,c,d) "v_add_f32 %[t0], v" #a ", v" #b "\n\tv_add_f32 %[" #o "], v" #c ", v" #d "\n\tv_add_f32 %[" #o "], %[t0], %[" #o "]\n\t"
      u64 ym;
      #define PGAP "s_sleep " STR(PSL) "\n\t"
      u64 nm;
      asm volatile(LD_YB PGAP LD_P "s_mov_b32 %[cnt], 0x20000\n\ts_waitcnt vmcnt(4)\n1:\n\t" CK_YB "s_waitcnt vmcnt(0)\n\t" AD4(a0,112,116,120,124) AD4(a1,113,117,121,125) AD4(a2,114,118,122,126) "v_cmp_u_f32 %[nm], %[a0], %[a1]\n\t" AD4(a3,115,119,123,127)
        "s_or_b64 %[nm], %[nm], %[ym]\n\tv_cmp_u_f32 vcc, %[a2], %[a3]\n\ts_or_b64 vcc, vcc, %[nm]\n\ts_cbranch_scc0 9f\n\t"
        CHK_I0 MX2(116,117) MX2(118,119) MX2(120,121) MX2(122,123) MX2(124,125) MX2(126,127) CHK_IE
        "s_or_b64 exec, vcc, %[ym]\n\ts_cbranch_scc0 8f\n\ts_sleep " STR(QSLP) "\n\ts_wqm_b64 exec, %[ym]\n\ts_cbranch_execz 2f\n\t" LD_YB "2:\n\ts_mov_b64 exec, vcc\n\ts_cbranch_execz 3f\n\t" LD_P
        "s_mov_b64 exec, -1\n\ts_waitcnt vmcnt(4)\n\ts_branch 4f\n3:\n\ts_mov_b64 exec, -1\n\ts_waitcnt vmcnt(0)\n4:\n\ts_sub_u32 %[cnt], %[cnt], 1\n\ts_cbranch_scc0 1b\n8:\n\ts_mov_b64 exec, -1\n9:"
        : [t0]"=&v"(t0),[cnt]"=&s"(cnt),[a0]"=&v"(a[0]),[a1]"=&v"(a[1]),[a2]"=&v"(a[2]),[a3]"=&v"(a[3]),"=&{v[104:107]}"(yb),[ym]"=&s"(ym),[nm]"=&s"(nm) : [of]"v"(po),[rs]"s"(rs),[yo]"v"(yo),[y2]"v"(y2),[ry]"s"(ry),"v"(op),"v"(pr),"v"(pe),
          [c0]"s"(ypat((u32)tag,0)),[c1]"s"(ypat((u32)tag,1)),[c2]"s"(ypat((u32)tag,2)),[c3]"s"(ypat((u32)tag,3)),[mk]"s"(YMASK),[od]"s"(0xaaaaaaaaaaaaaaaaull) : PCLOB,"v96","v97","v98","v99","v100","v101","v102","v103");
      auto s0=__builtin_amdgcn_permlane32_swap(a[0],a[2],false,false); auto s1=__builtin_amdgcn_permlane32_swap(a[1],a[3],false,false);
      u32 x0=s0[0],x1=s0[1],y0=s1[0],y1=s1[1]; u32 p0=BC(u32,BC(float,x0)+BC(float,x1)), p1=BC(u32,BC(float,y0)+BC(float,y1));
      auto s2=__builtin_amdgcn_permlane16_swap(p0,p1,false,false); u32 z0=s2[0], z1=s2[1]; float s=BC(float,z0)+BC(float,z1);
      float gq=__builtin_fmaf(s,pr[3],pr[1])*rcpf_(1.f+__builtin_amdgcn_exp2f(__builtin_fmaf(s,pe[1],pe[0])));
      float sy=pr[2];
      #pragma unroll
      for(int k=0;k<4;k++){ u32 w=yb[k], q=ysl[k]; sy=D2(w,q,sy); }
      float o=__builtin_fmaf(sy,gq,pr[0]);
      *op=(u16)(pk(o,o)&0xffffu);
      if(bid==112&&ln==0) GST(ws+W_EPOCH,tag); }
    }
  } else
  {
    FLANE(D_*2)
    bf16x8 fr[27];
    rsrc_t rsP=RSRC(ws+W_PART+cgp*12288+kq*256+PWB,8*9*4096-2*12288);
    #pragma unroll
    for(int i=0;i<3;i++) BST128(I4,rsP,li<8? PLO+g*16+i*4096 : OOR,0,SC1);
    if(PRE2<9) FILLD(9-PRE2,W2_,W2B,"");
    wso+=128u-9u*wst;
    FILLD(9,W2_,W2B,"");
    VMW(9); gsync(sm,C_P,4);
    #pragma unroll
    for(int ks=0;ks<9;ks++) fr[ks]=WFR(0,ks);
    gsync(sm,C_P,8);
    if(w4==0&&ln==0) GST(ws+W_FL+(720+6*xg+lb-8)*8,tag);
    wso+=128u-9u*wst; wld-=2*CHSZ;
    FILL(9,VW(W2B));
    gwait<2>(sm,C_D,4);
    int fo=W_FL+(576+((9*kq)&~1)+(ln<9? ln:9))*8;
    if(w4==0) asm volatile("global_load_dwordx2 v[112:113], %0, %1 sc1\n\ts_waitcnt vmcnt(10)" :: "v"(fo),"s"(ws) : "memory"); else VMW(9);
    gsync(sm,C_P,12);
    #pragma unroll
    for(int ks=0;ks<9;ks++) fr[9+ks]=WFR(1,ks);
    { int t2=TID; asm volatile("" : "+v"(t2)); ln=t2&63; li=ln&15; g=ln>>4; }
    const lch* hp=sm+O_TILE+(li&7)*HT+g*16; int po= li<8? PLO+g*16+4096 : OOR; PIN(hp); PIN(po);
    u32x4 rs=mkrs(ws+W_GU,2*8*F_*2);
    int of=((ln>>5)*288+18*kq+2*w4)*256+(ln&31)*16; lch* hd=sm+O_TILE+w4*(8*HT)+((ln>>1)&7)*HT+((ln>>4)&1)*32+(ln&1)*16+(ln>>5)*8; PIN(of); PIN(hd); asm volatile("" : "+s"(rs));
    u32x4 rp=mkrs(ws+W_PART+cgp*12288+kq*256+PWB-4096,8*9*4096); u32 ch=C_H*4, one=1; int o3= w4==0? of+4096:OOR; PIN(ch); PIN(one); PIN(o3); asm volatile("" : "+s"(rp));
    DRAIN();
    if(w4==0){ u64 bad; asm volatile("v_cmp_ne_u64 %0, %1, v[112:113]" : "=s"(bad) : "s"(tag) : "memory");
      if(bad!=0ull) poll1<0>(ws+fo,tag); }
    __builtin_amdgcn_s_waitcnt(0x0f70);
    gsync(sm,C_P,16);
    { u32 t0,t1,cnt;
      #define LD_G "buffer_load_dwordx4 v[112:115], %[of], %[rs], 0 offen sc1\n\tbuffer_load_dwordx4 v[116:119], %[of], %[rs], 0 offen offset:2048 sc1\n\tbuffer_load_dwordx4 v[120:123], %[o3], %[rs], 0 offen sc1\n\t"
      #define HM1(a,b) "v_and_b32 %[t0], %[lm], v" #b "\n\tv_and_b32 v" #b ", %[hm], v" #b "\n\tv_dot2_f32_bf16 %[t0], v" #a ", %[t0], 0\n\tv_dot2_f32_bf16 v" #a ", v" #a ", v" #b ", 0\n\t"
      #define HM2(a,b) "v_and_b32 %[t1], %[lm], v" #b "\n\tv_and_b32 v" #b ", %[hm], v" #b "\n\tv_dot2_f32_bf16 %[t1], v" #a ", %[t1], 0\n\tv_dot2_f32_bf16 v" #a ", v" #a ", v" #b ", 0\n\t"
      #define HMUL(a,b,c,d,o) "v_permlane32_swap_b32 v" #a ", v" #c "\n\tv_permlane32_swap_b32 v" #b ", v" #d "\n\t" HM1(a,c) HM2(b,d) "v_cvt_pk_bf16_f32 v" #a ", %[t0], v" #a "\n\ts_nop 1\n\tv_cvt_pk_bf16_f32 v" #b ", %[t1], v" #b "\n\tds_write_b64 %[hd], v[" #a ":" #b "] offset:" STR(o) "\n\t"
      if(GD) __builtin_amdgcn_s_sleep(GD);
      asm volatile(LD_G :: [of]"v"(of),[rs]"s"(rs),[o3]"v"(o3) : "memory");
      #pragma unroll
      for(int ks=0;ks<G2;ks++) fr[18+ks]=WFR(0,ks);
      #pragma unroll
      for(int ks=0;ks<G2;ks++) PIN(fr[18+ks]);
      asm volatile(POLLASM(LD_G,CHK_I0 MX2(116,117) MX2(118,119) MX2(120,121) MX2(122,123) CHK_IE,GSLP)
        HMUL(112,113,114,115,0) HMUL(116,117,118,119,32*HT) "s_cmp_lg_u32 %[w4], 0\n\ts_cbranch_scc1 8f\n\t" HMUL(120,121,122,123,64*HT) "8:\n\ts_mov_b64 exec, 1\n\tds_add_u32 %[ch], %[one]\n\ts_mov_b64 exec, -1\n\t"
        "s_mov_b32 %[cnt], 0x20000\n6:\n\tds_read_b32 %[t0], %[ch]\n\tds_read_b128 v[112:115], %[hp]\n\tds_read_b128 v[116:119], %[hp] offset:8*" STR(HT) "\n\tds_read_b128 v[120:123], %[hp] offset:16*" STR(HT) "\n\tds_read_b128 v[124:127], %[hp] offset:24*" STR(HT) "\n\ts_waitcnt lgkmcnt(4)\n\t"
        "v_cmp_gt_u32 vcc, 4, %[t0]\n\ts_cbranch_vccz 7f\n\ts_sleep 1\n\ts_sub_u32 %[cnt], %[cnt], 1\n\ts_cbranch_scc0 6b\n7:"
        : [t0]"=&v"(t0),[t1]"=&v"(t1),[cnt]"=&s"(cnt) : [of]"v"(of),[rs]"s"(rs),[o3]"v"(o3),[w4]"s"(w4),[hd]"v"(hd),[hm]"s"(0xffff0000u),[lm]"s"(0xffffu),[ch]"v"(ch),[one]"v"(one),[hp]"v"(hp),"v"(po) : PCLOB); }
    { bf16x8 hb[9];
      #define MF "v_mfma_f32_16x16x32_bf16 "
      #define MF3(h) MF "%0, %3, " h ", %0\n\t" MF "%1, %4, " h ", %1\n\t" MF "%2, %5, " h ", %2"
      #define ACC "+v"(fr[0]),"+v"(fr[9]),"+v"(fr[18])
      #define FRS(k) "v"(fr[k]),"v"(fr[9+(k)]),"v"(fr[18+(k)])
      asm volatile("s_waitcnt lgkmcnt(3)\n\t" MF "%0, %0, v[112:115], 0\n\t" MF "%1, %1, v[112:115], 0\n\t" MF "%2, %2, v[112:115], 0\n\ts_waitcnt lgkmcnt(0)\n\t" MF3("v[116:119]") : ACC : FRS(1));
      hb[4]=LD(bf16x8,hp+4*8*HT); hb[5]=LD(bf16x8,hp+5*8*HT); hb[6]=LD(bf16x8,hp+6*8*HT); SCHED();
      asm volatile(MF3("v[120:123]") : ACC : FRS(2));
      #pragma unroll
      for(int ks=G2;ks<9;ks++) fr[18+ks]=WFR(0,ks);
      SCHED();
      asm volatile(MF3("v[124:127]") : ACC : FRS(3));
      hb[7]=LD(bf16x8,hp+7*8*HT); hb[8]=LD(bf16x8,hp+8*8*HT); SCHED();
      #pragma unroll
      for(int ks=4;ks<8;ks++){ asm volatile(MF3("%6") : ACC : FRS(ks),"v"(hb[ks])); SCHED(); }
      asm volatile(MF3("%6") "\n\ts_nop 5\n\tbuffer_store_dwordx4 %0, %7, %8, 0 offen sc1\n\tbuffer_store_dwordx4 %1, %7, %8, %9 offen sc1\n\tbuffer_store_dwordx4 %2, %7, %8, %10 offen sc1\n\ts_mov_b64 exec, 1\n\tds_add_u32 %11, %12\n\ts_mov_b64 exec, -1"
        : ACC : FRS(8),"v"(hb[8]),"v"(po),"s"(rp),"s"(4096),"s"(8192),"v"(ch),"v"(one) : "memory"); }

  }
  asm volatile("s_endpgm" : KILLIN :: "memory"); }
  {
  if(__builtin_expect(q4!=0,0)){ int grp= q4==1? nch-1 : q4-2;
    chan(sm,grp,(w4+(0x48>>(2*q4)))&3,72*xg+(q4==3? 16+lb : 2*lb+(lb<8? 0:8)+2-q4),(q4+1)>>2,ws,xa,xb_,wa,wb,dq[0]);
    asm volatile("s_endpgm" : KILLIN :: "memory"); }
  int xr_,xr2_;
  {
    LANE(ln); int g=ln>>4, li=ln&15, qq=li>>2, pq=li&3, j=ln&31, rl=2*w4+(ln>>5);
    lch* tile=sm+O_TILE; u32 Tr=(ln>>1)&16;
    int uo=W_U+(tb>>TL)*GST_+(tb&((1<<TL)-1))*(128>>TL)+(w4*1024<<TL)+(ln>>(3-TL))*128+(ln&(7>>TL))*16, uo1=uo+(4096<<TL), uo2=uo+(8192<<TL); PIN(uo); PIN(uo1); PIN(uo2);
    lch* tw=tile+rl*TSTR2+j*16; lch* tw2=tw+1024-j*12; u32 one=0x3f803f80u; const lch* tp=tile+qq*TSTR2+(w4*64+g*16+pq*4)*2;
    int go= ln<32? W_RG+(ln>>2)*8192+(tb*4+(ln&3))*8 : W_ST+(tb*8+(ln&7))*8; lch* kw=sm+O_RV+rl*2; const lch* kr=sm+(ln<32? O_RV+(ln&3)*4 : O_MD+(ln&7)*4); PIN(kw); PIN(kr);
    int xr=(32*(tb>>2)+8*g+(li&7))*(D_*2)+288*(lb&3)+32*w4+(ln&8)*2, xr2= w4==0? xr+256:OOR;
    rsrc_t rx=RSRC(x,L_*D_*2); u64 gm=((1ull<<(4*NRG))-1ull)|0xff00000000ull; u32 um=~UMASK, km=0x80008000u; asm volatile("" : "+s"(gm),"+s"(um),"+s"(km),"+s"(ws));
    float s1=0.f, s2=0.f, s3=0.f, sw=0.f, i576=1.f/576.f; u32 hm=0xffff0000u, zero=0, c1=1; u64 wm=0x0001000000010000ull; lch* kw2=sm+O_MD+rl*4;
    asm volatile("" : "+v"(s1),"+v"(s2),"+v"(s3),"+v"(sw),"+v"(zero),"+v"(c1),"+v"(kw2),"+s"(wm),"+s"(hm),"+s"(i576));
    asm volatile("s_waitcnt vmcnt(3)" : "+v"(wa),"+v"(wb),"+v"(wc),"+v"(Tr),"+v"(tw),"+v"(tw2),"+v"(tp),"+v"(one),"+v"(go),"+v"(xr),"+v"(xr2) :: "memory");
    u32 wq[9]={wa[0],wa[1],wa[2],wa[3],wb[0],wb[1],wb[2],wb[3],wc};
    #pragma unroll
    for(int i=0;i<9;i++) sw=D2(wq[i],one,sw);
    SCHED();
    asm volatile("s_waitcnt vmcnt(0)" : "+v"(xa),"+v"(xb_),"+v"(xc),"+v"(sw) :: "memory");
    u32 v[9]={xa[0],xa[1],xa[2],xa[3],xb_[0],xb_[1],xb_[2],xb_[3],xc};
    { float xf[18];
      #pragma unroll
      for(int i=0;i<9;i++){ s1=D2(v[i],one,s1); SCHED(); s2=D2(v[i],v[i],s2); SCHED(); s3=D2(v[i],wq[i],s3); SCHED(); }
      float m1,m2;
      #define AD_(r,c) "v_add_f32_dpp %[" #r "], %[" #r "], %[" #r "] " c " bank_mask:0xf\n\t"
      #define UL_(i) "v_lshlrev_b32 %[f" #i "], 16, %[v" #i "]\n\t"
      #define RM " row_mask:0xf"
      asm volatile(UL_(0) UL_(1) "v_permlane16_swap_b32 %[a], %[b]\n\tv_permlane16_swap_b32 %[c], %[w]\n\tv_add_f32 %[a], %[a], %[b]\n\tv_add_f32 %[w], %[w], %[c]\n\t" UL_(2) AD_(a,"quad_perm:[1,0,3,2]" RM) AD_(w,"quad_perm:[1,0,3,2]" RM) UL_(3)
        AD_(a,"quad_perm:[2,3,0,1]" RM) AD_(w,"quad_perm:[2,3,0,1]" RM) UL_(4) AD_(a,"row_half_mirror" RM) AD_(w,"row_half_mirror" RM) UL_(5) AD_(a,"row_mirror" RM) AD_(w,"row_mirror" RM)
        "v_mul_f32 %[d], %[k], %[a]\n\tv_mul_f32 %[e], %[k], %[a]\n\t" "v_mov_b32_dpp %[c], %[w] row_bcast:15 row_mask:0xa bank_mask:0xf\n\t" UL_(6) "v_permlane16_swap_b32 %[d], %[e]\n\t" UL_(7)
        : [a]"+v"(s1),[b]"+v"(s2),[c]"+v"(s3),[w]"+v"(sw),[d]"=&v"(m1),[e]"=&v"(m2),[f0]"=&v"(xf[0]),[f1]"=&v"(xf[2]),[f2]"=&v"(xf[4]),[f3]"=&v"(xf[6]),[f4]"=&v"(xf[8]),[f5]"=&v"(xf[10]),[f6]"=&v"(xf[12]),[f7]"=&v"(xf[14])
        : [v0]"v"(v[0]),[v1]"v"(v[1]),[v2]"v"(v[2]),[v3]"v"(v[3]),[v4]"v"(v[4]),[v5]"v"(v[5]),[v6]"v"(v[6]),[v7]"v"(v[7]),[k]"s"(i576));
      s1=m1; float mm=s1*s1, var=m2-mm;
      if(__builtin_expect(BALLOT(!(mm<=64.f*var))!=0ull,0)){
        float q=0.f;
        #pragma unroll
        for(int i=0;i<9;i++){ u32 t=v[i]; PIN(t); float d0=blo(t)-s1, d1=bhi(t)-s1; q=__builtin_fmaf(d0,d0,q); q=__builtin_fmaf(d1,d1,q); }
        var=add16x(sum16(q))*(1.f/576.f); }
      u32 vb=pk(var,s1);
      float den=blo(vb)+1e-5f, m=BC(float,vb&hm);
      vb=pk(den,den); den=__builtin_amdgcn_sqrtf(BC(float,vb&hm)); SCHED();
      s3=__builtin_fmaf(-m,sw,s3); PIN(s3); SCHED();
      u32 md=pk(m,den);
      float ri=rcpf_(BC(float,md&hm)); SCHED(); xf[16]=blo(v[8]); PIN(xf[16]); SCHED();
      s3*=ri;
      u32 u=pk(s3,s3);
      { u32 sg=(u32)((int)u>>31); u^=sg|km; }
      asm volatile("s_waitcnt lgkmcnt(0)\n\ts_mov_b64 exec, %6\n\tds_write_b16 %0, %1\n\tds_write_b32 %2, %3\n\ts_mov_b32 exec_hi, 0\n\tds_add_u32 %4, %5 offset:%7\n\ts_mov_b64 exec, -1\n\t"
        "s_cmp_lg_u32 %8, 3\n\ts_cbranch_scc1 1f\n\tds_read_b32 v117, %4 offset:%7\n\tds_read_b32 v118, %9\n1:" :: "v"(kw),"v"(u),"v"(kw2),"v"(md),"v"(zero),"v"(c1),"s"(wm),"i"(C_I*4),"s"(w4),"v"(kr) : "memory","scc");
      SCHED();
      { asm volatile("s_mov_b64 %0, s[96:97]" : "=s"(ep)); u32 tg=(u32)ep+1u; Tr=((upat(tg,w4)>>Tr)&0xffffu)*0x10001u; asm volatile("v_mov_b32 v119, %1" : "+v"(Tr) : "s"(tg)); }
      float rs_=ri*0.0625f, cs_=-m*rs_;
      #define UA_(i) "v_and_b32 %" #i ", %11, %" #i "\n\t"
      asm volatile(UA_(0) UA_(1) UA_(2) UA_(3) UA_(4) UA_(5) UA_(6) UA_(7) UA_(8) : "+v"(v[0]),"+v"(v[1]),"+v"(v[2]),"+v"(v[3]),"+v"(v[4]),"+v"(v[5]),"+v"(v[6]),"+v"(v[7]),"+v"(v[8]),"+v"(rs_),"+v"(cs_) : "s"(hm));
      #pragma unroll
      for(int i=0;i<9;i++) xf[2*i+1]=BC(float,v[i]);
      u32 h[9];
      #define UH(i) h[i]=(pkz(__builtin_fmaf(xf[2*(i)],rs_,cs_),__builtin_fmaf(xf[2*(i)+1],rs_,cs_))&um)|Tr;
      SCHED();
      { u32 cnt;
        asm volatile("s_cmp_lg_u32 %[w4], 3\n\ts_cbranch_scc1 3f\n\ts_mov_b32 %[cnt], 0x20000\n1:\n\ts_waitcnt lgkmcnt(0)\n\tv_cmp_gt_u32 vcc, 4, v117\n\ts_cbranch_vccz 2f\n\tds_read_b32 v117, %[z] offset:%[ci]\n\tds_read_b32 v118, %[kr]\n\ts_sub_u32 %[cnt], %[cnt], 1\n\ts_cbranch_scc0 1b\n"
          "2:\n\ts_mov_b64 exec, %[gm]\n\tglobal_store_dwordx2 %[go], v[118:119], %[ws] sc1\n\ts_mov_b64 exec, -1\n3:" : [cnt]"=&s"(cnt) : [w4]"s"(w4),[z]"v"(zero),[ci]"i"(C_I*4),[kr]"v"(kr),[gm]"s"(gm),[go]"v"(go),[ws]"s"(ws) : PCLOB); }
      SCHED();
      UH(0) UH(1) UH(2) UH(3) ST(u32x4,tw)=u32x4{h[0],h[1],h[2],h[3]}; UH(4) UH(5) UH(6) UH(7) ST(u32x4,tw+512)=u32x4{h[4],h[5],h[6],h[7]};
      UH(8) ST(u32,tw2)=h[8]; }
    { u32 tv,cnt;
      asm volatile("s_mov_b64 exec, 1\n\tds_add_u32 %[z], %[c1] offset:%[cl]\n\ts_mov_b64 exec, -1\n\ts_mov_b32 %[cnt], 0x20000\n1:\n\tds_read_b32 %[tv], %[z] offset:%[cl]\n\t"
        "ds_read_b64_tr_b16 v[96:97], %[tp]\n\tds_read_b64_tr_b16 v[98:99], %[tp] offset:4*" STR(TSTR2) "\n\tds_read_b64_tr_b16 v[100:101], %[tp] offset:512\n\tds_read_b64_tr_b16 v[102:103], %[tp] offset:512+4*" STR(TSTR2) "\n\t"
        "ds_read_b64_tr_b16 v[104:105], %[tp] offset:1024\n\tds_read_b64_tr_b16 v[106:107], %[tp] offset:1024+4*" STR(TSTR2) "\n\ts_waitcnt lgkmcnt(6)\n\tv_cmp_gt_u32 vcc, 4, %[tv]\n\ts_cbranch_vccz 2f\n\ts_sub_u32 %[cnt], %[cnt], 1\n\ts_cbranch_scc0 1b\n"
        "2:\n\ts_waitcnt lgkmcnt(4)\n\tglobal_store_dwordx4 %[u0], v[96:99], %[ws] sc1\n\ts_waitcnt lgkmcnt(2)\n\tglobal_store_dwordx4 %[u1], v[100:103], %[ws] sc1\n\ts_waitcnt lgkmcnt(0)\n\t"
        "s_cmp_lg_u32 %[w4], 0\n\ts_cbranch_scc1 3f\n\tglobal_store_dwordx4 %[u2], v[104:107], %[ws] sc1\n3:\n\ts_nop 1"
        : [tv]"=&v"(tv),[cnt]"=&s"(cnt) : [z]"v"(zero),[c1]"v"(c1),[cl]"i"(C_L*4),[tp]"v"(tp),[u0]"v"(uo),[u1]"v"(uo1),[u2]"v"(uo2),[ws]"s"(ws),[w4]"s"(w4)
        : PCLOB,"v96","v97","v98","v99","v100","v101","v102","v103","v104","v105","v106","v107");
      xr_=xr; xr2_=xr2;
    }
    if(lb>=2&&lb<8){
      const u16* wm=(lb&1)? wu:wg; int co=(3-(int)((u64)wm>>7)-xg)&7, wi=((lb>>1)-1)*4+w4, r_=576-12*PFN+wi+12*j;
      asm volatile("buffer_load_dword v120, %0, %1, 0 offen" :: "v"(j<PFN&&(ln<32||co==0)? r_*(F_*2)+(9*xg+co)*128+(ln>>5)*1024 : OOR),"s"(mkrs(wm,D_*F_*2)) : "memory"); }
    if(lb>=8&&lb<14) garrive(sm,C_D);
  }
  FP fp; int trow;
  { LANE(ln); int g=ln>>4, li=ln&15; u64 tag=ep+1ull;
    PRIO(0);
    FL fl; fl.so=W_ST+(32*(tb>>2)+8*g+(li&7))*8; fl.fo=W_FL+(144*(lb&3)+16*w4+64*(g<2||w4==0? (g<3? g:2):0)+li)*8; fl.xr=xr_; fl.xr2=xr2_; fl.ws=ws; fl.D=Dsk; fl.rx=mkrs(x,L_*D_*2); PIN(fl.so); PIN(fl.fo);
    final_adr(sm,fp,w4); trow=32*(tb>>2)+8*g+(li&7); PIN(trow);
    lch *tw=sm+O_CH+O_TB+16384+w4*(4*ECS)+ln*8, *et=tw-ln*8+ycls((u32)tag,li&7)*ECS+(ln&8)*8; PIN(tw); PIN(et);
    if(lb==CTLB||lb>=14){
      u32 M=(u32)tag, rank,key; u32x4 rx=fl.rx;
      int j=ln&31, rr=2*w4+(ln>>5), o16=2047*1152+j*16, o4=2047*1152+j*4, so=W_ST+2047*8, sh=(ln>>5)*16, m9=-1152, r0=2*w4, r1=2*w4+1;
      lch *xw=sm+O_TILE+XAD(rr,j*16), *xw1=sm+O_TILE+XAD(rr,j*16+512), *xw2=sm+O_TILE+XAD(rr,j*4+1024), *rw=sm+O_RC+rr*4; u32 zero=0, one=1;
      PIN(o16); PIN(o4); PIN(so); PIN(sh); PIN(xw); PIN(xw1); PIN(xw2); PIN(rw); PIN(zero); PIN(one); asm volatile("" : "+s"(rx),"+s"(m9),"+s"(r0),"+s"(r1));
      top8(sm,w4,ln,M,ws,rank,key);
      {
        u64 q; u32 a,b;
        asm volatile("v_cmp_eq_u32 vcc, %[r0], %[rk]\n\tv_cmp_eq_u32 %[q], %[r1], %[rk]\n\ts_ff1_i32_b64 %[a], vcc\n\ts_ff1_i32_b64 %[b], %[q]\n\tv_readlane_b32 %[a], %[key], %[a]\n\tv_readlane_b32 %[b], %[key], %[b]\n\ts_pack_ll_b32_b16 %[a], %[a], %[b]\n\t"
          "v_bfe_u32 %[sh], %[a], %[sh], 11\n\tv_mad_i32_i24 %[o16], %[sh], %[m9], %[o16]\n\tv_mad_i32_i24 %[o4], %[sh], %[m9], %[o4]\n\t"
          "buffer_load_dwordx4 v[112:115], %[o16], %[rx], 0 offen\n\tv_mad_i32_i24 %[so], %[sh], -8, %[so]\n\tbuffer_load_dwordx4 v[116:119], %[o16], %[rx], 0 offen offset:512\n\tbuffer_load_dword v120, %[o4], %[rx], 0 offen offset:1024\n\tglobal_load_dwordx2 v[122:123], %[so], %[ws] sc1\n\t"
          "s_mov_b64 exec, 1\n\tds_add_u32 %[z], %[one] offset:%[cd]\n\ts_mov_b64 exec, -1"
          : [q]"=&s"(q),[a]"=&s"(a),[b]"=&s"(b),[sh]"+v"(sh),[o16]"+v"(o16),[o4]"+v"(o4),[so]"+v"(so) : [r0]"s"(r0),[r1]"s"(r1),[rk]"v"(rank),[key]"v"(key),[m9]"s"(m9),[rx]"s"(rx),[ws]"s"(ws),[z]"v"(zero),[one]"v"(one),[cd]"i"(C_D*4) : PCLOB);
        if(lb==CTLB&&rank<8&&!(ln&1)) GST(ws+W_IDX+xg*1024+w4*128+rank*8,(tag<<16)|(~key&2047u));
        if(w4==0){ if(rank<8) ST(u16,sm+O_IDX+(ln&1)*16+rank*2)=(u16)((ln&1)? key>>16 : ~key&2047u); garrive(sm,C_IX); }
        u64 bad; asm volatile("s_waitcnt vmcnt(2)\n\tds_write_b128 %2, v[112:115]\n\ts_waitcnt vmcnt(0)\n\tds_write_b128 %3, v[116:119]\n\tv_cmp_ne_u32 %0, %1, v123\n\tds_write_b32 %4, v120" : "=s"(bad) : "s"(M),"v"(xw),"v"(xw1),"v"(xw2) : "memory");
        if(__builtin_expect(bad!=0ull,0)){ u32 f1=(u32)poll1<32>(ws+so,tag<<32); asm volatile("v_mov_b32 v122, %0" :: "v"(f1) : "memory"); }
        asm volatile("ds_write_b32 %0, v122\n\ts_mov_b64 exec, 1\n\tds_add_u32 %1, %2 offset:%3\n\ts_mov_b64 exec, -1" :: "v"(rw),"v"(zero),"v"(one),"i"(C_X*4) : "memory");
      }
    }
    PRIO(0);
    if(lb<14) gwait<8>(sm,C_U,8); else gwait<4>(sm,C_H,4);
    final_ld(fl); asm volatile("s_waitcnt vmcnt(0)" : "+v"(fl.fs),"+v"(fl.ff),"+v"(fl.dk),"+v"(fl.xq[0]),"+v"(fl.xq[1]),"+v"(fl.xq[2]) :: "memory");
    gwait<2>(sm,C_G,7);
    f32x4 rc;
    { u64 fs=fl.fs, ff=fl.ff;
      if(__builtin_expect(BALLOT((u32)(fs>>32)!=(u32)tag)!=0ull,0)) fs=poll1<32>(ws+fl.so,tag<<32);
      if(__builtin_expect(BALLOT((u32)(ff>>32)!=(u32)tag)!=0ull,0)) ff=poll1<32>(ws+fl.fo,tag<<32);
      int e0=79-(int)(u32)ff; e0= e0<24? 24 : e0>121? 121:e0; float is=BC(float,(u32)e0<<23), di=b2f(fl.dk)*is;
      #pragma unroll
      for(int c=0;c<4;c++){ ST(f32x2,tw+c*ECS)=f32x2{is,di}; is*=4294967296.f; di*=4294967296.f; }
      float ri=rcpf_(bhi((u32)fs)), nm=-blo((u32)fs)*ri; rc=f32x4{ri,nm,ri*-1.4426950408889634f,nm*-1.4426950408889634f}; }
    asm volatile("" : "+v"(rc) :: "memory");
    bool ix= w4==3&&lb<14&&lb!=CTLB; u64 f; const char* p=ws+W_IDX+(xg*8+(lb&3))*128+(ln&7)*8; if(ix) f=GLD(p);
    final_blk(fp,0,fl.xq,rc,et); final_blk(fp,1,fl.xq,rc,et); if(w4==0) final_blk(fp,2,fl.xq,rc,et);
    if(ix){ if(BALLOT(((f^(tag<<16))>>16)!=0ull)!=0ull) f=poll1<16>(p,tag<<16);
      if(ln<8) ST(u16,sm+O_IDX+ln*2)=(u16)f; garrive(sm,C_IX); }
  }
  final_post(sm,fp,w4,nch,xr_,trow,(u32)ep+1u,out,ws);
  }
}

extern "C" void kernel_launch(void* const* d_in, const int* in_sizes, int n_in, void* d_out, int out_size, void* d_ws, size_t ws_size, hipStream_t stream) {
  const u16** I=(const u16**)d_in;
  KP kp; kp.Are=I[1]; kp.Aim=I[2]; kp.Bre=I[3]; kp.Bim=I[4]; kp.Cre=I[5]; kp.Cim=I[6]; kp.ldt=I[7]; kp.pad=I[7];
  kA<<<256,1024,0,stream>>>(I[0],(char*)d_ws+WSOFF,I[10],I[8],I[11],I[12],I[13],(u16*)d_out,kp);
}
```
